```python
import torch

def _softmax(x, axis=-1):
    return torch.softmax(x, dim=axis)
def _gelu(x):
    return torch.nn.functional.gelu(x, approximate="tanh")
def _silu(x):
    return torch.nn.functional.silu(x)
def _sigmoid(x):
    return torch.sigmoid(x)
def _layernorm(x, axis=-1, eps=1e-5):
    m = x.mean(dim=axis, keepdim=True)
    v = x.var(dim=axis, keepdim=True, unbiased=False)
    return (x - m) / torch.sqrt(v + eps)
def _scatter_add(tgt, idx, src, axis=0):
    return tgt.clone().index_add_(axis, idx, src)
def _segment_sum(src, seg, n):
    out = torch.zeros((n,) + src.shape[1:], dtype=src.dtype)
    return out.index_add_(0, seg, src)
def _pad(x, pw):
    flat = tuple(v for pair in reversed(pw) for v in pair)
    return torch.nn.functional.pad(x, flat)

kernel_name = 'concat_heads__mha_block_cached__gqa_block_cached__near_ridge'

def setup_inputs():

    _w = (lambda a: (lambda t: t.to(torch.bfloat16) if t.is_floating_point() else t)(torch.as_tensor(a)))
    def _A_setup():
        B, Sq, D = 8, 240, 512
        H, Sk, Dh = 32, 512, 128
        return {
            "x": _w((lambda *s: torch.randn(*s, dtype=torch.bfloat16) * 0.8)(B, Sq, D)),
            "k_cache": _w((lambda *s: torch.randn(*s, dtype=torch.bfloat16) * 0.8)(B, H, Sk, Dh)),
            "v_cache": _w((lambda *s: torch.randn(*s, dtype=torch.bfloat16) * 0.05)(B, H, Sk, Dh)),
            "wq": _w((lambda *s: torch.randn(*s, dtype=torch.bfloat16) * 0.8)(D, H * Dh) * (D ** -0.5)),
            "wo": _w((lambda *s: torch.randn(*s, dtype=torch.bfloat16) * 0.05)(H * Dh, D) * ((H * Dh) ** -0.5)),
        }
    _A_in = _A_setup()

    def _B_setup():
        B, Sq, D = 8, 240, 512
        Hq, Hkv, Sk, Dh = 32, 32, 512, 128
        assert Hq % Hkv == 0, (Hq, Hkv)
        return {
            "x": _w((lambda *s: torch.randn(*s, dtype=torch.bfloat16) * 0.8)(B, Sq, D)),
            "k_cache": _w((lambda *s: torch.randn(*s, dtype=torch.bfloat16) * 0.8)(B, Hkv, Sk, Dh)),
            "v_cache": _w((lambda *s: torch.randn(*s, dtype=torch.bfloat16) * 0.05)(B, Hkv, Sk, Dh)),
            "wq": _w((lambda *s: torch.randn(*s, dtype=torch.bfloat16) * 0.8)(D, Hq * Dh) * (D ** -0.5)),
            "wo": _w((lambda *s: torch.randn(*s, dtype=torch.bfloat16) * 0.05)(Hq * Dh, D) * ((Hq * Dh) ** -0.5)),
        }
    _B_in = _B_setup()

    return {
        "A_x": _A_in["x"],
        "A_k_cache": _A_in["k_cache"],
        "A_v_cache": _A_in["v_cache"],
        "A_wq": _A_in["wq"],
        "A_wo": _A_in["wo"],
        "B_x": _B_in["x"],
        "B_k_cache": _B_in["k_cache"],
        "B_v_cache": _B_in["v_cache"],
        "B_wq": _B_in["wq"],
        "B_wo": _B_in["wo"],
    }

def reference(A_x, A_k_cache, A_v_cache, A_wq, A_wo, B_x, B_k_cache, B_v_cache, B_wq, B_wo):
    def _A_ref(x, k_cache, v_cache, wq, wo):
        B, Sq, D = x.shape
        H, Dh = 32, 128
        q = (lambda x, *d: x.permute(*d))((x @ wq).reshape(B, Sq, H, Dh), 0, 2, 1, 3)
        scores = torch.einsum("bhqd,bhkd->bhqk", q, k_cache) * (Dh ** -0.5)
        p = _softmax(scores, axis=-1)
        o = torch.einsum("bhqk,bhkd->bhqd", p, v_cache)
        return (lambda x, *d: x.permute(*d))(o, 0, 2, 1, 3).reshape(B, Sq, H * Dh) @ wo

    def _B_ref(x, k_cache, v_cache, wq, wo):
        B, Sq, D = x.shape
        Hq, Dh = 32, 128
        Hkv = k_cache.shape[1]
        rep = Hq // Hkv
        q = (lambda x, *d: x.permute(*d))((x @ wq).reshape(B, Sq, Hq, Dh), 0, 2, 1, 3)
        k = (lambda x, n, axis: torch.repeat_interleave(x, n, dim=axis))(k_cache, rep, axis=1)
        v = (lambda x, n, axis: torch.repeat_interleave(x, n, dim=axis))(v_cache, rep, axis=1)
        scores = torch.einsum("bhqd,bhkd->bhqk", q, k) * (Dh ** -0.5)
        p = _softmax(scores, axis=-1)
        o = torch.einsum("bhqk,bhkd->bhqd", p, v)
        return (lambda x, *d: x.permute(*d))(o, 0, 2, 1, 3).reshape(B, Sq, Hq * Dh) @ wo

    a = _A_ref(A_x, A_k_cache, A_v_cache, A_wq, A_wo)
    b = _B_ref(B_x, B_k_cache, B_v_cache, B_wq, B_wo)
    return torch.concatenate([a, b], axis=-1)
```

```cpp
#include <hip/hip_runtime.h>
#include <hip/hip_bf16.h>
#include <cstdio>

typedef float    f32x16 __attribute__((ext_vector_type(16)));
typedef float    f32x2  __attribute__((ext_vector_type(2)));
typedef float    f32x4  __attribute__((ext_vector_type(4)));
typedef __bf16   bf16x8 __attribute__((ext_vector_type(8)));
typedef __bf16   bf16x2 __attribute__((ext_vector_type(2)));
typedef short    s16x4  __attribute__((ext_vector_type(4)));
typedef short    s16x8  __attribute__((ext_vector_type(8)));
typedef unsigned u32x4  __attribute__((ext_vector_type(4)));
#define DI __device__ __forceinline__
#define LDSP(T, p) ((__attribute__((address_space(3))) T*)(p))
#define GLBP(p) ((const __attribute__((address_space(1))) void*)(p))

DI unsigned cvtpk(float lo, float hi) { f32x2 v = {lo, hi}; bf16x2 b = __builtin_convertvector(v, bf16x2); return __builtin_bit_cast(unsigned, b); }
DI void dma16(const void* g, char* l) { __builtin_amdgcn_global_load_lds(GLBP(g), LDSP(void, l), 16, 0, 0); }
DI bf16x8 trfrag(const char* p0, const char* p1) {
  s16x4 a = __builtin_amdgcn_ds_read_tr16_b64_v4i16(LDSP(s16x4, p0));
  s16x4 b = __builtin_amdgcn_ds_read_tr16_b64_v4i16(LDSP(s16x4, p1));
  s16x8 r = __builtin_shufflevector(a, b, 0, 1, 2, 3, 4, 5, 6, 7);
  return __builtin_bit_cast(bf16x8, r);
}
DI bf16x8 pack8(const f32x16& a, int s, float sc) {
  u32x4 w;
  w[0] = cvtpk(a[8 * s + 0] * sc, a[8 * s + 1] * sc); w[1] = cvtpk(a[8 * s + 2] * sc, a[8 * s + 3] * sc);
  w[2] = cvtpk(a[8 * s + 4] * sc, a[8 * s + 5] * sc); w[3] = cvtpk(a[8 * s + 6] * sc, a[8 * s + 7] * sc);
  return __builtin_bit_cast(bf16x8, w);
}
DI float xhalf_max(float v) { auto pr = __builtin_amdgcn_permlane32_swap(__float_as_uint(v), __float_as_uint(v), false, false); return fmaxf(__uint_as_float(pr[0]), __uint_as_float(pr[1])); }
DI float xhalf_add(float v) { auto pr = __builtin_amdgcn_permlane32_swap(__float_as_uint(v), __float_as_uint(v), false, false); return __uint_as_float(pr[0]) + __uint_as_float(pr[1]); }

DI void attn_item_safe(const char* __restrict__ x, const char* __restrict__ kc, const char* __restrict__ vc, const char* __restrict__ wq,
                       char* __restrict__ ofrag, int b, int h, char* lds) {
  const int tid = threadIdx.x, lane = tid & 63, w = tid >> 6, r = lane & 31, hh = lane >> 5;
  char* Wl = lds;
  char* Kl = lds + 16384;
  char* Xl = lds + 32768 + 8192 * w;
  const int g = lane >> 4, i16 = lane & 15, q4 = i16 >> 2, p4 = i16 & 3, c2 = g & 1;
  const int sw = ((p4 & 1) << 1) | (p4 >> 1);
  const int wbase = 2048 * hh + 64 * q4 + 32 * c2 + 8 * sw;
  const int vbase = 256 * hh + 64 * q4 + 32 * c2 + 8 * p4;
  f32x16 acc[4][2];
#pragma unroll
  for (int dt = 0; dt < 4; ++dt)
#pragma unroll
    for (int qb = 0; qb < 2; ++qb)
#pragma unroll
      for (int e = 0; e < 16; ++e) acc[dt][qb][e] = 0.f;
  for (int kt = 0; kt < 8; ++kt) {
    __syncthreads();
#pragma unroll
    for (int jj = 0; jj < 4; ++jj) {
      const int j = 4 * w + jj;
      const int row = 64 * kt + 8 * (j >> 1) + ((lane >> 2) & 7), col = h * 128 + 64 * (j & 1) + 32 * (lane >> 5) + 8 * (lane & 3);
      dma16(wq + ((size_t)row * 4096 + col) * 2, Wl + 1024 * j);
    }
#pragma unroll
    for (int j = 0; j < 8; ++j) {
      int row = 64 * w + 8 * j + (lane >> 3); row = row > 239 ? 239 : row;
      const int c = (lane & 7) ^ (4 * (j & 1) + (lane >> 4));
      dma16(x + ((size_t)(b * 240 + row) * 512 + 64 * kt + 8 * c) * 2, Xl + 1024 * j);
    }
    __syncthreads();
#pragma unroll
    for (int kk = 0; kk < 4; ++kk) {
      bf16x8 bx[2];
#pragma unroll
      for (int qb = 0; qb < 2; ++qb) {
        const int row = 32 * qb + r;
        bx[qb] = *(const bf16x8*)(Xl + 128 * row + 16 * ((2 * kk + hh) ^ ((row >> 1) & 7)));
      }
#pragma unroll
      for (int dt = 0; dt < 4; ++dt) {
        bf16x8 a = trfrag(Wl + wbase + 4096 * kk + 512 * dt, Wl + wbase + 4096 * kk + 512 * dt + 256);
#pragma unroll
        for (int qb = 0; qb < 2; ++qb) acc[dt][qb] = __builtin_amdgcn_mfma_f32_32x32x16_bf16(a, bx[qb], acc[dt][qb], 0, 0, 0);
      }
    }
  }
  bf16x8 Q[2][8];
#pragma unroll
  for (int qb = 0; qb < 2; ++qb)
#pragma unroll
    for (int dt = 0; dt < 4; ++dt)
#pragma unroll
      for (int s = 0; s < 2; ++s) Q[qb][2 * dt + s] = pack8(acc[dt][qb], s, 0.12751743082f);
#pragma unroll
  for (int dt = 0; dt < 4; ++dt)
#pragma unroll
    for (int qb = 0; qb < 2; ++qb)
#pragma unroll
      for (int e = 0; e < 16; ++e) acc[dt][qb][e] = 0.f;
  float m[2] = {-3.0e38f, -3.0e38f}, l[2] = {0.f, 0.f};
  const char* kbase = kc + (size_t)(b * 32 + h) * 512 * 256;
  const char* vbase_g = vc + (size_t)(b * 32 + h) * 512 * 256;
  for (int t = 0; t < 8; ++t) {
    __syncthreads();
#pragma unroll
    for (int jj = 0; jj < 4; ++jj) {
      const int j = 4 * jj + w;
      const int row = 4 * j + (lane >> 4), c16 = (lane & 15) ^ (row & 15);
      dma16(kbase + (size_t)(64 * t + row) * 256 + 16 * c16, Kl + 1024 * j);
    }
#pragma unroll
    for (int jj = 0; jj < 4; ++jj) {
      const int j = 4 * w + jj;
      const int row = 64 * t + 8 * (j >> 1) + ((lane >> 2) & 7), col = 64 * (j & 1) + 32 * (lane >> 5) + 8 * (lane & 3);
      dma16(vbase_g + (size_t)row * 256 + col * 2, Wl + 1024 * j);
    }
    __syncthreads();
    f32x16 S[2][2];
#pragma unroll
    for (int kb = 0; kb < 2; ++kb)
#pragma unroll
      for (int qb = 0; qb < 2; ++qb)
#pragma unroll
        for (int e = 0; e < 16; ++e) S[kb][qb][e] = 0.f;
#pragma unroll
    for (int ks = 0; ks < 8; ++ks)
#pragma unroll
      for (int kb = 0; kb < 2; ++kb) {
        bf16x8 a = *(const bf16x8*)(Kl + 256 * (32 * kb + r) + 16 * ((2 * ks + hh) ^ (r & 15)));
#pragma unroll
        for (int qb = 0; qb < 2; ++qb) S[kb][qb] = __builtin_amdgcn_mfma_f32_32x32x16_bf16(a, Q[qb][ks], S[kb][qb], 0, 0, 0);
      }
    bf16x8 P[2][4];
#pragma unroll
    for (int qb = 0; qb < 2; ++qb) {
      float mx = S[0][qb][0];
#pragma unroll
      for (int kb = 0; kb < 2; ++kb)
#pragma unroll
        for (int e = 0; e < 16; ++e) mx = fmaxf(mx, S[kb][qb][e]);
      mx = xhalf_max(mx);
      const float mn = fmaxf(m[qb], mx);
      const float f = __builtin_amdgcn_exp2f(m[qb] - mn);
      m[qb] = mn;
      float rs = 0.f;
#pragma unroll
      for (int kb = 0; kb < 2; ++kb)
#pragma unroll
        for (int e = 0; e < 16; ++e) { const float p = __builtin_amdgcn_exp2f(S[kb][qb][e] - mn); S[kb][qb][e] = p; rs += p; }
      l[qb] = l[qb] * f + rs;
#pragma unroll
      for (int dt = 0; dt < 4; ++dt)
#pragma unroll
        for (int e = 0; e < 16; ++e) acc[dt][qb][e] *= f;
#pragma unroll
      for (int kb = 0; kb < 2; ++kb)
#pragma unroll
        for (int s = 0; s < 2; ++s) P[qb][2 * kb + s] = pack8(S[kb][qb], s, 1.0f);
    }
#pragma unroll
    for (int ks = 0; ks < 4; ++ks)
#pragma unroll
      for (int dt = 0; dt < 4; ++dt) {
        bf16x8 a = trfrag(Wl + vbase + 4096 * ks + 512 * dt, Wl + vbase + 4096 * ks + 2048 + 512 * dt);
#pragma unroll
        for (int qb = 0; qb < 2; ++qb) acc[dt][qb] = __builtin_amdgcn_mfma_f32_32x32x16_bf16(a, P[qb][ks], acc[dt][qb], 0, 0, 0);
      }
  }
#pragma unroll
  for (int qb = 0; qb < 2; ++qb) {
    const float il = 1.0f / xhalf_add(l[qb]);
    char* dst = ofrag + ((size_t)((b * 8 + 2 * w + qb) * 256 + h * 8) * 64 + lane) * 16;
#pragma unroll
    for (int dt = 0; dt < 4; ++dt)
#pragma unroll
      for (int s = 0; s < 2; ++s) *(bf16x8*)(dst + (2 * dt + s) * 1024) = pack8(acc[dt][qb], s, il);
  }
}

__global__ __launch_bounds__(256, 1) void k1_safe(const char* xA, const char* kA, const char* vA, const char* wqA,
                                                  const char* xB, const char* kB, const char* vB, const char* wqB, char* ofrag) {
  __shared__ __attribute__((aligned(16))) char lds[65536];
  const int item = blockIdx.x, blk = item >> 8, b = (item >> 5) & 7, h = item & 31;
  attn_item_safe(blk ? xB : xA, blk ? kB : kA, blk ? vB : vA, blk ? wqB : wqA, ofrag + (size_t)blk * 16777216, b, h, lds);
}

__global__ __launch_bounds__(256, 1) void k2_simple(const char* __restrict__ ofrag, const char* __restrict__ woA, const char* __restrict__ woB, char* __restrict__ out) {
  __shared__ __attribute__((aligned(16))) char lds[65536];
  const int tid = threadIdx.x, lane = tid & 63, w = tid >> 6, r = lane & 31, hh = lane >> 5;
  const int id = blockIdx.x, nt = id & 3, qt = (id >> 2) & 3, b = (id >> 4) & 7, blk = id >> 7;
  const char* wo = blk ? woB : woA;
  char* Wl = lds + 16384 * w;
  const int g = lane >> 4, i16 = lane & 15, q4 = i16 >> 2, p4 = i16 & 3, c2 = g & 1;
  const int sw = ((p4 & 1) << 1) | (p4 >> 1);
  const int abase = 256 * hh + 64 * q4 + 32 * c2 + 8 * sw;
  f32x16 acc[4][2];
#pragma unroll
  for (int dt = 0; dt < 4; ++dt)
#pragma unroll
    for (int qb = 0; qb < 2; ++qb)
#pragma unroll
      for (int e = 0; e < 16; ++e) acc[dt][qb][e] = 0.f;
  const char* of = ofrag + (size_t)blk * 16777216 + ((size_t)((b * 8 + 2 * qt) * 256 + 64 * w) * 64 + lane) * 16;
  for (int kt = 0; kt < 16; ++kt) {
    __builtin_amdgcn_s_waitcnt(0);
    asm volatile("" ::: "memory");
#pragma unroll
    for (int j = 0; j < 16; ++j) {
      const int row = 1024 * w + 64 * kt + 8 * (j >> 1) + ((lane >> 2) & 7), col = nt * 128 + 64 * (j & 1) + 32 * (lane >> 5) + 8 * (lane & 3);
      dma16(wo + ((size_t)row * 512 + col) * 2, Wl + 1024 * j);
    }
    bf16x8 bo[2][4];
#pragma unroll
    for (int qb = 0; qb < 2; ++qb)
#pragma unroll
      for (int ks = 0; ks < 4; ++ks) bo[qb][ks] = *(const bf16x8*)(of + (size_t)qb * 262144 + (4 * kt + ks) * 1024);
    __builtin_amdgcn_s_waitcnt(0);
    asm volatile("" ::: "memory");
#pragma unroll
    for (int ks = 0; ks < 4; ++ks)
#pragma unroll
      for (int dt = 0; dt < 4; ++dt) {
        bf16x8 a = trfrag(Wl + abase + 4096 * ks + 512 * dt, Wl + abase + 4096 * ks + 2048 + 512 * dt);
#pragma unroll
        for (int qb = 0; qb < 2; ++qb) acc[dt][qb] = __builtin_amdgcn_mfma_f32_32x32x16_bf16(a, bo[qb][ks], acc[dt][qb], 0, 0, 0);
      }
  }
  __syncthreads();
  float* red = (float*)lds;
  if (w >= 2) {
#pragma unroll
    for (int dt = 0; dt < 4; ++dt)
#pragma unroll
      for (int qb = 0; qb < 2; ++qb)
#pragma unroll
        for (int e = 0; e < 16; ++e) red[(w - 2) * 8192 + ((dt * 2 + qb) * 16 + e) * 64 + lane] = acc[dt][qb][e];
  }
  __syncthreads();
  if (w < 2) {
#pragma unroll
    for (int dt = 0; dt < 4; ++dt)
#pragma unroll
      for (int qb = 0; qb < 2; ++qb)
#pragma unroll
        for (int e = 0; e < 16; ++e) acc[dt][qb][e] += red[w * 8192 + ((dt * 2 + qb) * 16 + e) * 64 + lane];
  }
  __syncthreads();
  if (w == 1) {
#pragma unroll
    for (int dt = 0; dt < 4; ++dt)
#pragma unroll
      for (int qb = 0; qb < 2; ++qb)
#pragma unroll
        for (int e = 0; e < 16; ++e) red[((dt * 2 + qb) * 16 + e) * 64 + lane] = acc[dt][qb][e];
  }
  __syncthreads();
  if (w == 0) {
#pragma unroll
    for (int qb = 0; qb < 2; ++qb) {
      const int q = 64 * qt + 32 * qb + r;
#pragma unroll
      for (int dt = 0; dt < 4; ++dt) {
#pragma unroll
        for (int e = 0; e < 16; ++e) acc[dt][qb][e] += red[((dt * 2 + qb) * 16 + e) * 64 + lane];
        if (q < 240) {
#pragma unroll
          for (int s = 0; s < 2; ++s)
            *(bf16x8*)(out + ((size_t)(b * 240 + q) * 1024 + blk * 512 + nt * 128 + 32 * dt + 16 * s + 8 * hh) * 2) = pack8(acc[dt][qb], s, 1.0f);
        }
      }
    }
  }
}

extern "C" void kernel_launch(void* const* d_in, const int* in_sizes, int n_in, void* d_out, int out_size, void* d_ws, size_t ws_size, hipStream_t stream) {
  char* ofrag = (char*)d_ws;
  hipLaunchKernelGGL(k1_safe, dim3(512), dim3(256), 0, stream, (const char*)d_in[0], (const char*)d_in[1], (const char*)d_in[2], (const char*)d_in[3],
                     (const char*)d_in[5], (const char*)d_in[6], (const char*)d_in[7], (const char*)d_in[8], ofrag);
  hipLaunchKernelGGL(k2_simple, dim3(256), dim3(256), 0, stream, (const char*)ofrag, (const char*)d_in[4], (const char*)d_in[9], (char*)d_out);
}
```

```cpp
#include <hip/hip_runtime.h>
#include <hip/hip_bf16.h>
typedef float    f32x2  __attribute__((ext_vector_type(2)));
typedef float    f32x4  __attribute__((ext_vector_type(4)));
typedef __bf16   bf16x8 __attribute__((ext_vector_type(8)));
typedef __bf16   bf16x2 __attribute__((ext_vector_type(2)));
typedef short    s16x4  __attribute__((ext_vector_type(4)));
typedef short    s16x8  __attribute__((ext_vector_type(8)));
typedef unsigned u32x4  __attribute__((ext_vector_type(4)));
typedef unsigned u32x2  __attribute__((ext_vector_type(2)));
#define DI __device__ __forceinline__
#define LDSP(T, p) ((__attribute__((address_space(3))) T*)(p))
#define GLBP(p) ((const __attribute__((address_space(1))) void*)(p))

DI unsigned cvtpk(float lo, float hi) { f32x2 v = {lo, hi}; bf16x2 b = __builtin_convertvector(v, bf16x2); return __builtin_bit_cast(unsigned, b); }
DI void dma16(const void* g, char* l) { __builtin_amdgcn_global_load_lds(GLBP(g), LDSP(void, l), 16, 0, 0); }
DI bf16x8 trfrag(const char* p0, const char* p1) {
  s16x4 a = __builtin_amdgcn_ds_read_tr16_b64_v4i16(LDSP(s16x4, p0));
  s16x4 b = __builtin_amdgcn_ds_read_tr16_b64_v4i16(LDSP(s16x4, p1));
  s16x8 r = __builtin_shufflevector(a, b, 0, 1, 2, 3, 4, 5, 6, 7);
  return __builtin_bit_cast(bf16x8, r);
}
DI bf16x8 pack2(const f32x4& x, const f32x4& y, float sc) {
  u32x4 w;
  w[0] = cvtpk(x[0] * sc, x[1] * sc); w[1] = cvtpk(x[2] * sc, x[3] * sc);
  w[2] = cvtpk(y[0] * sc, y[1] * sc); w[3] = cvtpk(y[2] * sc, y[3] * sc);
  return __builtin_bit_cast(bf16x8, w);
}
DI float grp_max(float v) {
  auto p = __builtin_amdgcn_permlane32_swap(__float_as_uint(v), __float_as_uint(v), false, false);
  v = fmaxf(__uint_as_float(p[0]), __uint_as_float(p[1]));
  auto r = __builtin_amdgcn_permlane16_swap(__float_as_uint(v), __float_as_uint(v), false, false);
  return fmaxf(__uint_as_float(r[0]), __uint_as_float(r[1]));
}
DI float grp_add(float v) {
  auto p = __builtin_amdgcn_permlane32_swap(__float_as_uint(v), __float_as_uint(v), false, false);
  v = __uint_as_float(p[0]) + __uint_as_float(p[1]);
  auto r = __builtin_amdgcn_permlane16_swap(__float_as_uint(v), __float_as_uint(v), false, false);
  return __uint_as_float(r[0]) + __uint_as_float(r[1]);
}
#define MFMA16(a, b, c) __builtin_amdgcn_mfma_f32_16x16x32_bf16(a, b, c, 0, 0, 0)

DI void attn_head_safe(const char* __restrict__ x, const char* __restrict__ kc, const char* __restrict__ vc, const char* __restrict__ wq,
                       char* __restrict__ ofrag, int b, int h, char* lds, int w, int lane, bool dost = true) {
  char* Wl = lds;
  char* Kl = lds + 16384;
  char* Xl = lds + 32768 + 8192 * w;
  const int g = lane >> 4, i16 = lane & 15, q4 = i16 >> 2, p4 = i16 & 3;
  const int line = lane >> 4, slot = (lane >> 1) & 7, half = lane & 1, rr = slot & 3;
  f32x4 acc[8][4];
#pragma unroll
  for (int dt = 0; dt < 8; ++dt)
#pragma unroll
    for (int qt = 0; qt < 4; ++qt) acc[dt][qt] = (f32x4){0.f, 0.f, 0.f, 0.f};
  for (int s = 0; s < 8; ++s) {
    __syncthreads();
#pragma unroll
    for (int jj = 0; jj < 4; ++jj) {
      const int j = 4 * w + jj;
      const int dt = 2 * line + ((slot >> 2) ^ ((j >> 1) & 1));
      dma16(wq + (size_t)(64 * s + 4 * j + rr) * 8192 + h * 256 + 32 * dt + 16 * half, Wl + 1024 * j);
    }
#pragma unroll
    for (int j = 0; j < 8; ++j) {
      int row = 64 * w + 8 * j + (lane >> 3); row = row > 239 ? 239 : row;
      const int c = (lane & 7) ^ (4 * (j & 1) + (lane >> 4));
      dma16(x + (size_t)(b * 240 + row) * 1024 + 128 * s + 16 * c, Xl + 1024 * j);
    }
    __syncthreads();
#pragma unroll
    for (int kk = 0; kk < 2; ++kk) {
      bf16x8 bx[4];
#pragma unroll
      for (int qt = 0; qt < 4; ++qt) bx[qt] = *(const bf16x8*)(Xl + 2048 * qt + 128 * i16 + 16 * ((4 * kk + g) ^ (i16 >> 1)));
#pragma unroll
      for (int dt = 0; dt < 8; ++dt) {
        const char* pa = Wl + 2048 * g + 32 * (q4 + 4 * ((g & 1) ^ (dt & 1))) + 8 * p4 + 8192 * kk + 256 * (dt >> 1);
        bf16x8 a = trfrag(pa, pa + 1024);
#pragma unroll
        for (int qt = 0; qt < 4; ++qt) acc[dt][qt] = MFMA16(a, bx[qt], acc[dt][qt]);
      }
    }
  }
  bf16x8 Q[4][4];
#pragma unroll
  for (int qt = 0; qt < 4; ++qt)
#pragma unroll
    for (int ks = 0; ks < 4; ++ks) {
      u32x4 v = __builtin_bit_cast(u32x4, pack2(acc[2 * ks][qt], acc[2 * ks + 1][qt], 0.12751743082f));
      auto a0 = __builtin_amdgcn_permlane32_swap(v[0], v[2], false, false);
      auto a1 = __builtin_amdgcn_permlane32_swap(v[1], v[3], false, false);
      auto b0 = __builtin_amdgcn_permlane16_swap(a0[0], a0[1], false, false);
      auto b1 = __builtin_amdgcn_permlane16_swap(a1[0], a1[1], false, false);
      u32x4 o = {b0[0], b1[0], b0[1], b1[1]};
      Q[qt][ks] = __builtin_bit_cast(bf16x8, o);
    }
#pragma unroll
  for (int dt = 0; dt < 8; ++dt)
#pragma unroll
    for (int qt = 0; qt < 4; ++qt) acc[dt][qt] = (f32x4){0.f, 0.f, 0.f, 0.f};
  float m[4] = {-3.0e38f, -3.0e38f, -3.0e38f, -3.0e38f}, l[4] = {0.f, 0.f, 0.f, 0.f};
  const char* kbase = kc + (size_t)(b * 32 + h) * 131072;
  const char* vbase = vc + (size_t)(b * 32 + h) * 131072;
  for (int t = 0; t < 8; ++t) {
    __syncthreads();
#pragma unroll
    for (int jj = 0; jj < 4; ++jj) {
      const int j = 4 * w + jj;
      dma16(kbase + 16384 * t + 1024 * j + 256 * (lane >> 4) + 16 * ((lane & 15) ^ (4 * jj + (lane >> 4))), Kl + 1024 * j);
      dma16(vbase + 16384 * t + 1024 * j + 256 * rr + 32 * (2 * line + ((slot >> 2) ^ (jj & 1))) + 16 * half, Wl + 1024 * j);
    }
    __syncthreads();
    f32x4 S[4][4];
#pragma unroll
    for (int kp = 0; kp < 4; ++kp) {
#pragma unroll
      for (int qt = 0; qt < 4; ++qt) S[kp][qt] = (f32x4){0.f, 0.f, 0.f, 0.f};
#pragma unroll
      for (int ks = 0; ks < 4; ++ks) {
        bf16x8 a = *(const bf16x8*)(Kl + 4096 * kp + 256 * i16 + 16 * ((4 * ks + g) ^ i16));
#pragma unroll
        for (int qt = 0; qt < 4; ++qt) S[kp][qt] = MFMA16(a, Q[qt][ks], S[kp][qt]);
      }
    }
    bf16x8 P[4][2];
#pragma unroll
    for (int qt = 0; qt < 4; ++qt) {
      float mx = S[0][qt][0];
#pragma unroll
      for (int kp = 0; kp < 4; ++kp)
#pragma unroll
        for (int e = 0; e < 4; ++e) mx = fmaxf(mx, S[kp][qt][e]);
      mx = grp_max(mx);
      const float mn = fmaxf(m[qt], mx);
      const float f = __builtin_amdgcn_exp2f(m[qt] - mn);
      m[qt] = mn;
      float rs = 0.f;
#pragma unroll
      for (int kp = 0; kp < 4; ++kp)
#pragma unroll
        for (int e = 0; e < 4; ++e) { const float p = __builtin_amdgcn_exp2f(S[kp][qt][e] - mn); S[kp][qt][e] = p; rs += p; }
      l[qt] = l[qt] * f + rs;
#pragma unroll
      for (int dt = 0; dt < 8; ++dt) acc[dt][qt] *= f;
      P[qt][0] = pack2(S[0][qt], S[1][qt], 1.0f);
      P[qt][1] = pack2(S[2][qt], S[3][qt], 1.0f);
    }
#pragma unroll
    for (int u = 0; u < 2; ++u)
#pragma unroll
      for (int dt = 0; dt < 8; ++dt) {
        const char* pa = Wl + 1024 * g + 32 * (q4 + 4 * ((g & 1) ^ (dt & 1))) + 8 * p4 + 8192 * u + 256 * (dt >> 1);
        bf16x8 a = trfrag(pa, pa + 4096);
#pragma unroll
        for (int qt = 0; qt < 4; ++qt) acc[dt][qt] = MFMA16(a, P[qt][u], acc[dt][qt]);
      }
  }
#pragma unroll
  for (int qt = 0; qt < 4; ++qt) {
    const float il = 1.0f / grp_add(l[qt]);
    if (dost && 4 * w + qt < 15) {
      char* dst = ofrag + ((size_t)((b * 15 + 4 * w + qt) * 128 + h * 4) * 64 + lane) * 16;
#pragma unroll
      for (int ks = 0; ks < 4; ++ks) *(bf16x8*)(dst + ks * 1024) = pack2(acc[2 * ks][qt], acc[2 * ks + 1][qt], il);
    }
  }
}

__global__ __launch_bounds__(256, 1) void kf(const char* xA, const char* kA, const char* vA, const char* wqA, const char* woA,
                                             const char* xB, const char* kB, const char* vB, const char* wqB, const char* woB, char* ws, char* out) {
  extern __shared__ __attribute__((aligned(16))) char lds[];
  unsigned id = blockIdx.x;
  unsigned wv = __builtin_amdgcn_readfirstlane(threadIdx.x >> 6);
#define xcd (id & 7)
#define idx (id >> 3)
#define blk (xcd >> 2)
#define ks (xcd & 3)
#define nt (idx & 3)
#define b (idx >> 2)
#define pair (4 * ks + nt)
#define grp ((blk * 8 + b) * 4 + nt)
#define oA ((14u << 20) + (xcd * 8 + b) * 1024 + wv * 256)
#define oX ((14u << 20) + 65536 + grp * 512 + wv * 128)
  unsigned bad, any;
  asm volatile("s_getpc_b64 s[60:61]\n.Lkf_pc0:\ns_and_b32 s61, s61, 0xffff\ns_mov_b32 s62, .Lkf_end-.Lkf_pc0\ns_mov_b32 s63, 0x20000\n"
               "buffer_load_dword a192, %0, s[60:63], %1 offen\n"
               :: "v"((threadIdx.x & 63) * 128), "s"(((idx * 4 + wv) & 15) * 8192) : "s60", "s61", "s62", "s63", "a192", "scc", "memory");
  typedef const char* const __attribute__((address_space(4)))* kargp;
  kargp ka = (kargp)__builtin_amdgcn_kernarg_segment_ptr();
  {
    const char* x = blk ? xB : xA; const char* kc = blk ? kB : kA; const char* vc = blk ? vB : vA; const char* wq = blk ? wqB : wqA;
    const unsigned long long px = (unsigned long long)(x + (size_t)b * 245760), pw = (unsigned long long)(wq + pair * 512);
    const unsigned long long pk = (unsigned long long)(kc + (size_t)(b * 32 + 2 * pair) * 131072), pv = (unsigned long long)(vc + (size_t)(b * 32 + 2 * pair) * 131072);
    const unsigned long long pwo = (unsigned long long)((blk ? woB : woA) + (size_t)ks * 1048576 + nt * 256);
    const unsigned long long py = (unsigned long long)(out + (size_t)b * (240 * 2048) + blk * 1024 + nt * 256 + ks * 64);
    asm volatile(
".Lkf_start:\n"
"s_cmp_eq_u32 %[wv], 3\n"
"s_cbranch_scc1 .Lw3_%=\n"
"s_mov_b32 s36, %[xl]\n"
"s_and_b32 s37, %[xh], 0xffff\n"
"s_mov_b32 s38, 245760\n"
"s_mov_b32 s39, 0x20000\n"
"s_mov_b32 s40, %[wl]\n"
"s_and_b32 s41, %[wh], 0xffff\n"
"s_mov_b32 s42, 4186624\n"
"s_mov_b32 s43, 0x20000\n"
"s_mov_b32 s44, %[kl]\n"
"s_and_b32 s45, %[kh], 0xffff\n"
"s_mov_b32 s46, 262144\n"
"s_mov_b32 s47, 0x20000\n"
"s_mov_b32 s48, %[vl]\n"
"s_and_b32 s49, %[vh], 0xffff\n"
"s_mov_b32 s50, 262144\n"
"s_mov_b32 s51, 0x20000\n"
"s_add_u32 s52, %[wsl], %[oof]\n"
"s_addc_u32 s53, %[wsh], 0\n"
"s_and_b32 s53, s53, 0xffff\n"
"s_mov_b32 s54, 1843200\n"
"s_mov_b32 s55, 0x20000\n"
"v_mbcnt_lo_u32_b32 v20, -1, 0\n"
"v_mbcnt_hi_u32_b32 v20, -1, v20\n"
"s_lshl_b32 s77, %[wv], 12\n"
"v_and_b32 v21, 15, v20\n"
"v_lshrrev_b32 v22, 4, v20\n"
"v_bfe_u32 v23, v20, 1, 2\n"
"v_bfe_u32 v24, v20, 3, 1\n"
"v_and_b32 v25, 1, v20\n"
"v_lshl_add_u32 v26, v22, 1, v24\n"
"v_lshlrev_b32 v26, 5, v26\n"
"v_lshl_add_u32 v26, v25, 4, v26\n"
"s_lshl_b32 s74, %[wv], 17\n"
"v_lshl_add_u32 v12, v23, 13, v26\n"
"v_add_u32 v12, s74, v12\n"
"v_xor_b32 v13, 32, v12\n"
"v_lshrrev_b32 v27, 3, v20\n"
"v_and_b32 v28, 7, v20\n"
"v_xor_b32 v28, v28, v22\n"
"v_lshlrev_b32 v28, 4, v28\n"
"s_lshl_b32 s74, %[wv], 16\n"
"v_lshl_add_u32 v10, v27, 10, v28\n"
"v_add_u32 v10, s74, v10\n"
"v_xor_b32 v11, 64, v10\n"
"s_mov_b32 s56, 54144\n"
"s_mov_b32 s57, 0xfff97400\n"
"s_add_u32 s58, s77, 0x8000\n"
"s_lshl_b32 s59, %[wv], 13\n"
"s_add_u32 s59, s59, 131072\n"
"s_add_u32 m0, s58, 0\n"
"s_add_u32 s57, s57, 0x68c00\n"
"buffer_load_dwordx4 v12, s[40:43], s57 offen offset:0 nt lds\n"
"s_add_u32 m0, s58, 0\n"
"s_add_u32 s57, s57, 0x7c00\n"
"buffer_load_dwordx4 v12, s[40:43], s57 offen offset:1024 nt lds\n"
"s_add_u32 m0, s58, 0\n"
"s_add_u32 s57, s57, 0x7c00\n"
"buffer_load_dwordx4 v13, s[40:43], s57 offen offset:2048 nt lds\n"
"s_add_u32 m0, s58, 0\n"
"s_add_u32 s57, s57, 0x7c00\n"
"buffer_load_dwordx4 v13, s[40:43], s57 offen offset:3072 nt lds\n"
"s_add_u32 m0, s58, 16128\n"
"s_add_u32 s57, s57, 0xfffe8c00\n"
"buffer_load_dwordx4 v12, s[40:43], s57 offen offset:256 nt lds\n"
"s_add_u32 m0, s58, 16128\n"
"s_add_u32 s57, s57, 0x7c00\n"
"buffer_load_dwordx4 v12, s[40:43], s57 offen offset:1280 nt lds\n"
"s_add_u32 m0, s58, 16128\n"
"s_add_u32 s57, s57, 0x7c00\n"
"buffer_load_dwordx4 v13, s[40:43], s57 offen offset:2304 nt lds\n"
"s_add_u32 m0, s58, 16128\n"
"s_add_u32 s57, s57, 0x7c00\n"
"buffer_load_dwordx4 v13, s[40:43], s57 offen offset:3328 nt lds\n"
"s_xor_b32 s58, s58, 0x10000\n"
"s_add_u32 m0, s59, 0\n"
"s_add_u32 s56, s56, 0xffff2c80\n"
"buffer_load_dwordx4 v10, s[36:39], s56 offen offset:0 lds\n"
"s_add_u32 m0, s59, 0\n"
"s_add_u32 s56, s56, 0x1c00\n"
"buffer_load_dwordx4 v11, s[36:39], s56 offen offset:1024 lds\n"
"s_add_u32 m0, s59, 0\n"
"s_add_u32 s56, s56, 0x1c00\n"
"buffer_load_dwordx4 v10, s[36:39], s56 offen offset:2048 lds\n"
"s_add_u32 m0, s59, 0\n"
"s_add_u32 s56, s56, 0x1c00\n"
"buffer_load_dwordx4 v11, s[36:39], s56 offen offset:3072 lds\n"
"s_add_u32 m0, s59, 4096\n"
"s_add_u32 s56, s56, 0x2c00\n"
"buffer_load_dwordx4 v10, s[36:39], s56 offen offset:0 lds\n"
"s_add_u32 m0, s59, 4096\n"
"s_add_u32 s56, s56, 0x1c00\n"
"buffer_load_dwordx4 v11, s[36:39], s56 offen offset:1024 lds\n"
"s_add_u32 m0, s59, 4096\n"
"s_add_u32 s56, s56, 0x1c00\n"
"buffer_load_dwordx4 v10, s[36:39], s56 offen offset:2048 lds\n"
"s_add_u32 m0, s59, 4096\n"
"s_add_u32 s56, s56, 0x1c00\n"
"buffer_load_dwordx4 v11, s[36:39], s56 offen offset:3072 lds\n"
"s_add_u32 m0, s58, 0\n"
"s_add_u32 s57, s57, 0x68c00\n"
"buffer_load_dwordx4 v12, s[40:43], s57 offen offset:0 nt lds\n"
"s_add_u32 m0, s58, 0\n"
"s_add_u32 s57, s57, 0x7c00\n"
"buffer_load_dwordx4 v12, s[40:43], s57 offen offset:1024 nt lds\n"
"s_add_u32 m0, s58, 0\n"
"s_add_u32 s57, s57, 0x7c00\n"
"buffer_load_dwordx4 v13, s[40:43], s57 offen offset:2048 nt lds\n"
"s_add_u32 m0, s58, 0\n"
"s_add_u32 s57, s57, 0x7c00\n"
"buffer_load_dwordx4 v13, s[40:43], s57 offen offset:3072 nt lds\n"
"s_add_u32 m0, s58, 16128\n"
"s_add_u32 s57, s57, 0xfffe8c00\n"
"buffer_load_dwordx4 v12, s[40:43], s57 offen offset:256 nt lds\n"
"s_add_u32 m0, s58, 16128\n"
"s_add_u32 s57, s57, 0x7c00\n"
"buffer_load_dwordx4 v12, s[40:43], s57 offen offset:1280 nt lds\n"
"s_add_u32 m0, s58, 16128\n"
"s_add_u32 s57, s57, 0x7c00\n"
"buffer_load_dwordx4 v13, s[40:43], s57 offen offset:2304 nt lds\n"
"s_add_u32 m0, s58, 16128\n"
"s_add_u32 s57, s57, 0x7c00\n"
"buffer_load_dwordx4 v13, s[40:43], s57 offen offset:3328 nt lds\n"
"s_xor_b32 s58, s58, 0x10000\n"
"v_lshl_add_u32 v4, v23, 8, v26\n"
"v_add_u32 v4, s77, v4\n"
"v_xor_b32 v5, 32, v4\n"
"v_xor_b32 v29, v21, v22\n"
"v_lshlrev_b32 v29, 4, v29\n"
"v_lshl_add_u32 v0, v22, 8, v29\n"
"v_add_u32 v0, s77, v0\n"
"v_xor_b32 v1, 64, v0\n"
"v_xor_b32 v2, 128, v0\n"
"v_xor_b32 v3, 192, v0\n"
"v_lshl_add_u32 v15, v21, 8, v29\n"
"v_lshlrev_b32 v14, 10, v22\n"
"v_lshrrev_b32 v30, 1, v21\n"
"v_xor_b32 v30, v30, v22\n"
"v_lshlrev_b32 v30, 4, v30\n"
"v_lshl_add_u32 v6, v21, 7, v30\n"
"v_add_u32 v6, s59, v6\n"
"v_xor_b32 v7, 64, v6\n"
"v_lshrrev_b32 v30, 2, v21\n"
"v_and_b32 v31, 1, v22\n"
"v_lshl_add_u32 v30, v31, 2, v30\n"
"v_lshlrev_b32 v30, 5, v30\n"
"v_and_b32 v31, 3, v20\n"
"v_lshl_add_u32 v30, v31, 3, v30\n"
"v_lshl_add_u32 v8, v22, 11, v30\n"
"v_add_u32 v8, 0x8000, v8\n"
"v_xor_b32 v9, 0x80, v8\n"
"s_mov_b32 s60, 0xc000\n"
"s_mov_b32 s61, 0x8000\n"
"s_add_u32 s62, s77, 32768\n"
"s_add_u32 s63, s77, 114688\n"
"s_mov_b32 s64, 0x4000\n"
"s_mov_b32 s65, 0x8000\n"
"s_mov_b32 s67, 0x53800000\n"
"s_mov_b32 s68, 0x3e0293ee\n"
"s_mov_b32 s69, 0x3e0293ee\n"
"s_mov_b64 s[70:71], 0\n"
"s_mov_b32 s75, 0\n"
"s_mov_b32 s76, s77\n"
"s_mov_b32 s66, 3\n"
"v_mov_b32 v16, 0\n"
"v_mov_b32 v17, 0\n"
"v_mov_b32 v18, 0\n"
"v_mov_b32 v19, 0\n"
"v_mov_b32 v20, 0\n"
"v_mov_b32 v21, 0\n"
"v_mov_b32 v22, 0\n"
"v_mov_b32 v23, 0\n"
"v_mov_b32 v24, 0\n"
"v_mov_b32 v25, 0\n"
"v_mov_b32 v26, 0\n"
"v_mov_b32 v27, 0\n"
"v_mov_b32 v28, 0\n"
"v_mov_b32 v29, 0\n"
"v_mov_b32 v30, 0\n"
"v_mov_b32 v31, 0\n"
"v_mov_b32 v32, 0\n"
"v_mov_b32 v33, 0\n"
"v_mov_b32 v34, 0\n"
"v_mov_b32 v35, 0\n"
"v_mov_b32 v36, 0\n"
"v_mov_b32 v37, 0\n"
"v_mov_b32 v38, 0\n"
"v_mov_b32 v39, 0\n"
"v_mov_b32 v40, 0\n"
"v_mov_b32 v41, 0\n"
"v_mov_b32 v42, 0\n"
"v_mov_b32 v43, 0\n"
"v_mov_b32 v44, 0\n"
"v_mov_b32 v45, 0\n"
"v_mov_b32 v46, 0\n"
"v_mov_b32 v47, 0\n"
"v_mov_b32 v48, 0\n"
"v_mov_b32 v49, 0\n"
"v_mov_b32 v50, 0\n"
"v_mov_b32 v51, 0\n"
"v_mov_b32 v52, 0\n"
"v_mov_b32 v53, 0\n"
"v_mov_b32 v54, 0\n"
"v_mov_b32 v55, 0\n"
"v_mov_b32 v56, 0\n"
"v_mov_b32 v57, 0\n"
"v_mov_b32 v58, 0\n"
"v_mov_b32 v59, 0\n"
"v_mov_b32 v60, 0\n"
"v_mov_b32 v61, 0\n"
"v_mov_b32 v62, 0\n"
"v_mov_b32 v63, 0\n"
"v_mov_b32 v64, 0\n"
"v_mov_b32 v65, 0\n"
"v_mov_b32 v66, 0\n"
"v_mov_b32 v67, 0\n"
"v_mov_b32 v68, 0\n"
"v_mov_b32 v69, 0\n"
"v_mov_b32 v70, 0\n"
"v_mov_b32 v71, 0\n"
"v_mov_b32 v72, 0\n"
"v_mov_b32 v73, 0\n"
"v_mov_b32 v74, 0\n"
"v_mov_b32 v75, 0\n"
"v_mov_b32 v76, 0\n"
"v_mov_b32 v77, 0\n"
"v_mov_b32 v78, 0\n"
"v_mov_b32 v79, 0\n"
"v_mov_b32 v80, 0\n"
"v_mov_b32 v81, 0\n"
"v_mov_b32 v82, 0\n"
"v_mov_b32 v83, 0\n"
"v_mov_b32 v84, 0\n"
"v_mov_b32 v85, 0\n"
"v_mov_b32 v86, 0\n"
"v_mov_b32 v87, 0\n"
"v_mov_b32 v88, 0\n"
"v_mov_b32 v89, 0\n"
"v_mov_b32 v90, 0\n"
"v_mov_b32 v91, 0\n"
"v_mov_b32 v92, 0\n"
"v_mov_b32 v93, 0\n"
"v_mov_b32 v94, 0\n"
"v_mov_b32 v95, 0\n"
"v_mov_b32 v96, 0\n"
"v_mov_b32 v97, 0\n"
"v_mov_b32 v98, 0\n"
"v_mov_b32 v99, 0\n"
"v_mov_b32 v100, 0\n"
"v_mov_b32 v101, 0\n"
"v_mov_b32 v102, 0\n"
"v_mov_b32 v103, 0\n"
"v_mov_b32 v104, 0\n"
"v_mov_b32 v105, 0\n"
"v_mov_b32 v106, 0\n"
"v_mov_b32 v107, 0\n"
"v_mov_b32 v108, 0\n"
"v_mov_b32 v109, 0\n"
"v_mov_b32 v110, 0\n"
"v_mov_b32 v111, 0\n"
"v_mov_b32 v112, 0\n"
"v_mov_b32 v113, 0\n"
"v_mov_b32 v114, 0\n"
"v_mov_b32 v115, 0\n"
"v_mov_b32 v116, 0\n"
"v_mov_b32 v117, 0\n"
"v_mov_b32 v118, 0\n"
"v_mov_b32 v119, 0\n"
"v_mov_b32 v120, 0\n"
"v_mov_b32 v121, 0\n"
"v_mov_b32 v122, 0\n"
"v_mov_b32 v123, 0\n"
"v_mov_b32 v124, 0\n"
"v_mov_b32 v125, 0\n"
"v_mov_b32 v126, 0\n"
"v_mov_b32 v127, 0\n"
"v_mov_b32 v128, 0\n"
"v_mov_b32 v129, 0\n"
"v_mov_b32 v130, 0\n"
"v_mov_b32 v131, 0\n"
"v_mov_b32 v132, 0\n"
"v_mov_b32 v133, 0\n"
"v_mov_b32 v134, 0\n"
"v_mov_b32 v135, 0\n"
"v_mov_b32 v136, 0\n"
"v_mov_b32 v137, 0\n"
"v_mov_b32 v138, 0\n"
"v_mov_b32 v139, 0\n"
"v_mov_b32 v140, 0\n"
"v_mov_b32 v141, 0\n"
"v_mov_b32 v142, 0\n"
"v_mov_b32 v143, 0\n"
"v_mov_b32 v144, 0\n"
"v_mov_b32 v145, 0\n"
"v_mov_b32 v146, 0\n"
"v_mov_b32 v147, 0\n"
"v_mov_b32 v148, 0\n"
"v_mov_b32 v149, 0\n"
"v_mov_b32 v150, 0\n"
"v_mov_b32 v151, 0\n"
"v_mov_b32 v152, 0\n"
"v_mov_b32 v153, 0\n"
"v_mov_b32 v154, 0\n"
"v_mov_b32 v155, 0\n"
"v_mov_b32 v156, 0\n"
"v_mov_b32 v157, 0\n"
"v_mov_b32 v158, 0\n"
"v_mov_b32 v159, 0\n"
"v_mov_b32 v160, 0\n"
"v_mov_b32 v161, 0\n"
"v_mov_b32 v162, 0\n"
"v_mov_b32 v163, 0\n"
"v_mov_b32 v164, 0\n"
"v_mov_b32 v165, 0\n"
"v_mov_b32 v166, 0\n"
"v_mov_b32 v167, 0\n"
"v_mov_b32 v168, 0\n"
"v_mov_b32 v169, 0\n"
"v_mov_b32 v170, 0\n"
"v_mov_b32 v171, 0\n"
"v_mov_b32 v172, 0\n"
"v_mov_b32 v173, 0\n"
"v_mov_b32 v174, 0\n"
"v_mov_b32 v175, 0\n"
"v_mov_b32 v176, 0\n"
"v_mov_b32 v177, 0\n"
"v_mov_b32 v178, 0\n"
"v_mov_b32 v179, 0\n"
"v_mov_b32 v180, 0\n"
"v_mov_b32 v181, 0\n"
"v_mov_b32 v182, 0\n"
"v_mov_b32 v183, 0\n"
"v_mov_b32 v184, 0\n"
"v_mov_b32 v185, 0\n"
"v_mov_b32 v186, 0\n"
"v_mov_b32 v187, 0\n"
"v_mov_b32 v188, 0\n"
"v_mov_b32 v189, 0\n"
"v_mov_b32 v190, 0\n"
"v_mov_b32 v191, 0\n"
"v_mov_b32 v192, 0\n"
"v_mov_b32 v193, 0\n"
"v_mov_b32 v194, 0\n"
"v_mov_b32 v195, 0\n"
"v_mov_b32 v196, 0\n"
"v_mov_b32 v197, 0\n"
"v_mov_b32 v198, 0\n"
"v_mov_b32 v199, 0\n"
"v_mov_b32 v200, 0\n"
"v_mov_b32 v201, 0\n"
"v_mov_b32 v202, 0\n"
"v_mov_b32 v203, 0\n"
"v_mov_b32 v204, 0\n"
"v_mov_b32 v205, 0\n"
"v_mov_b32 v206, 0\n"
"v_mov_b32 v207, 0\n"
"v_mov_b32 v208, 0\n"
"v_mov_b32 v209, 0\n"
"v_mov_b32 v210, 0\n"
"v_mov_b32 v211, 0\n"
"v_mov_b32 v212, 0\n"
"v_mov_b32 v213, 0\n"
"v_mov_b32 v214, 0\n"
"v_mov_b32 v215, 0\n"
"v_mov_b32 v216, 0\n"
"v_mov_b32 v217, 0\n"
"v_mov_b32 v218, 0\n"
"v_mov_b32 v219, 0\n"
"v_mov_b32 v220, 0\n"
"v_mov_b32 v221, 0\n"
"v_mov_b32 v222, 0\n"
"v_mov_b32 v223, 0\n"
"v_mov_b32 v224, 0\n"
"v_mov_b32 v225, 0\n"
"v_mov_b32 v226, 0\n"
"v_mov_b32 v227, 0\n"
"v_mov_b32 v228, 0\n"
"v_mov_b32 v229, 0\n"
"v_mov_b32 v230, 0\n"
"v_mov_b32 v231, 0\n"
"v_mov_b32 v232, 0\n"
"v_mov_b32 v233, 0\n"
"v_mov_b32 v234, 0\n"
"v_mov_b32 v235, 0\n"
"v_mov_b32 v236, 0\n"
"v_mov_b32 v237, 0\n"
"v_mov_b32 v238, 0\n"
"v_mov_b32 v239, 0\n"
"v_mov_b32 v240, 0\n"
"v_mov_b32 v241, 0\n"
"v_mov_b32 v242, 0\n"
"v_mov_b32 v243, 0\n"
"v_mov_b32 v244, 0\n"
"v_mov_b32 v245, 0\n"
"v_mov_b32 v246, 0\n"
"v_mov_b32 v247, 0\n"
"v_mov_b32 v248, 0\n"
"v_mov_b32 v249, 0\n"
"v_mov_b32 v250, 0\n"
"v_mov_b32 v251, 0\n"
"v_mov_b32 v252, 0\n"
"v_mov_b32 v253, 0\n"
"v_mov_b32 v254, 0\n"
"v_mov_b32 v255, 0\n"
"v_accvgpr_write_b32 a204, 0\n"
"v_accvgpr_write_b32 a205, 0\n"
"v_accvgpr_write_b32 a206, 0\n"
"v_accvgpr_write_b32 a207, 0\n"
"v_accvgpr_write_b32 a220, 0\n"
"v_accvgpr_write_b32 a221, 0\n"
"v_accvgpr_write_b32 a222, 0\n"
"v_accvgpr_write_b32 a223, 0\n"
"v_accvgpr_write_b32 a236, 0\n"
"v_accvgpr_write_b32 a237, 0\n"
"v_accvgpr_write_b32 a238, 0\n"
"v_accvgpr_write_b32 a239, 0\n"
"v_accvgpr_write_b32 a252, 0\n"
"v_accvgpr_write_b32 a253, 0\n"
"v_accvgpr_write_b32 a254, 0\n"
"v_accvgpr_write_b32 a255, 0\n"
"s_waitcnt lgkmcnt(0)\n"
"s_waitcnt vmcnt(16)\n"
"s_barrier\n"
"ds_read_b64_tr_b16 a[64:65], v8 offset:16384\n"
"ds_read_b64_tr_b16 a[66:67], v8 offset:17408\n"
"ds_read_b64_tr_b16 a[68:69], v8 offset:24576\n"
"ds_read_b64_tr_b16 a[70:71], v8 offset:25600\n"
"ds_read_b64_tr_b16 a[72:73], v9 offset:16384\n"
"ds_read_b64_tr_b16 a[74:75], v9 offset:17408\n"
"ds_read_b64_tr_b16 a[76:77], v9 offset:24576\n"
"ds_read_b64_tr_b16 a[78:79], v9 offset:25600\n"
"ds_read_b64_tr_b16 a[80:81], v8 offset:16640\n"
"ds_read_b64_tr_b16 a[82:83], v8 offset:17664\n"
"ds_read_b64_tr_b16 a[84:85], v8 offset:24832\n"
"ds_read_b64_tr_b16 a[86:87], v8 offset:25856\n"
"ds_read_b64_tr_b16 a[88:89], v9 offset:16640\n"
"ds_read_b64_tr_b16 a[90:91], v9 offset:17664\n"
"ds_read_b64_tr_b16 a[92:93], v9 offset:24832\n"
"ds_read_b64_tr_b16 a[94:95], v9 offset:25856\n"
"ds_read_b64_tr_b16 a[96:97], v8 offset:16896\n"
"ds_read_b64_tr_b16 a[98:99], v8 offset:17920\n"
"ds_read_b64_tr_b16 a[100:101], v8 offset:25088\n"
"ds_read_b64_tr_b16 a[102:103], v8 offset:26112\n"
"ds_read_b64_tr_b16 a[104:105], v9 offset:16896\n"
"ds_read_b64_tr_b16 a[106:107], v9 offset:17920\n"
"s_waitcnt vmcnt(8)\n"
"ds_read_b128 a[0:3], v6 offset:0\n"
"ds_read_b128 a[4:7], v7 offset:0\n"
"ds_read_b128 a[8:11], v6 offset:2048\n"
"ds_read_b128 a[12:15], v7 offset:2048\n"
"ds_read_b128 a[16:19], v6 offset:4096\n"
"ds_read_b128 a[20:23], v7 offset:4096\n"
"ds_read_b128 a[24:27], v6 offset:6144\n"
"ds_read_b128 a[28:31], v7 offset:6144\n"
"ds_read_b64_tr_b16 a[108:109], v9 offset:25088\n"
"ds_read_b64_tr_b16 a[110:111], v9 offset:26112\n"
"ds_read_b64_tr_b16 a[112:113], v8 offset:17152\n"
"ds_read_b64_tr_b16 a[114:115], v8 offset:18176\n"
"ds_read_b64_tr_b16 a[116:117], v8 offset:25344\n"
"ds_read_b64_tr_b16 a[118:119], v8 offset:26368\n"
"ds_read_b64_tr_b16 a[120:121], v9 offset:17152\n"
"ds_read_b64_tr_b16 a[122:123], v9 offset:18176\n"
".LQa_%=:\n"
"s_waitcnt lgkmcnt(15)\n"
"v_mfma_f32_16x16x32_bf16 v[16:19], a[64:67], a[0:3], v[16:19]\n"
"ds_read_b64_tr_b16 a[124:125], v9 offset:25344\n"
"ds_read_b64_tr_b16 a[126:127], v9 offset:26368\n"
"s_waitcnt lgkmcnt(15)\n"
"v_mfma_f32_16x16x32_bf16 v[16:19], a[68:71], a[4:7], v[16:19]\n"
"s_add_u32 m0, s59, 0\n"
"s_add_u32 s56, s56, 0xffff2c80\n"
"s_waitcnt lgkmcnt(10)\n"
"buffer_load_dwordx4 v10, s[36:39], s56 offen offset:0 lds\n"
"v_mfma_f32_16x16x32_bf16 v[44:47], a[64:67], a[8:11], v[44:47]\n"
"v_mfma_f32_16x16x32_bf16 v[44:47], a[68:71], a[12:15], v[44:47]\n"
"v_mfma_f32_16x16x32_bf16 v[72:75], a[64:67], a[16:19], v[72:75]\n"
"v_mfma_f32_16x16x32_bf16 v[72:75], a[68:71], a[20:23], v[72:75]\n"
"s_add_u32 m0, s76, 0\n"
"s_nop 0\n"
"buffer_load_dwordx4 v0, s[44:47], s75 offen offset:0 nt lds\n"
"v_mfma_f32_16x16x32_bf16 v[100:103], a[64:67], a[24:27], v[100:103]\n"
"ds_read_b64_tr_b16 a[64:65], v8 offset:0\n"
"ds_read_b64_tr_b16 a[66:67], v8 offset:1024\n"
"v_mfma_f32_16x16x32_bf16 v[100:103], a[68:71], a[28:31], v[100:103]\n"
"ds_read_b64_tr_b16 a[68:69], v8 offset:8192\n"
"ds_read_b64_tr_b16 a[70:71], v8 offset:9216\n"
"v_mfma_f32_16x16x32_bf16 v[20:23], a[72:75], a[0:3], v[20:23]\n"
"v_mfma_f32_16x16x32_bf16 v[20:23], a[76:79], a[4:7], v[20:23]\n"
"s_add_u32 m0, s59, 0\n"
"s_add_u32 s56, s56, 0x1c00\n"
"buffer_load_dwordx4 v11, s[36:39], s56 offen offset:1024 lds\n"
"v_mfma_f32_16x16x32_bf16 v[48:51], a[72:75], a[8:11], v[48:51]\n"
"v_mfma_f32_16x16x32_bf16 v[48:51], a[76:79], a[12:15], v[48:51]\n"
"v_mfma_f32_16x16x32_bf16 v[76:79], a[72:75], a[16:19], v[76:79]\n"
"v_mfma_f32_16x16x32_bf16 v[76:79], a[76:79], a[20:23], v[76:79]\n"
"v_mfma_f32_16x16x32_bf16 v[104:107], a[72:75], a[24:27], v[104:107]\n"
"ds_read_b64_tr_b16 a[72:73], v9 offset:0\n"
"ds_read_b64_tr_b16 a[74:75], v9 offset:1024\n"
"v_mfma_f32_16x16x32_bf16 v[104:107], a[76:79], a[28:31], v[104:107]\n"
"ds_read_b64_tr_b16 a[76:77], v9 offset:8192\n"
"ds_read_b64_tr_b16 a[78:79], v9 offset:9216\n"
"v_mfma_f32_16x16x32_bf16 v[24:27], a[80:83], a[0:3], v[24:27]\n"
"v_mfma_f32_16x16x32_bf16 v[24:27], a[84:87], a[4:7], v[24:27]\n"
"s_add_u32 m0, s59, 0\n"
"s_add_u32 s56, s56, 0x1c00\n"
"buffer_load_dwordx4 v10, s[36:39], s56 offen offset:2048 lds\n"
"v_mfma_f32_16x16x32_bf16 v[52:55], a[80:83], a[8:11], v[52:55]\n"
"v_mfma_f32_16x16x32_bf16 v[52:55], a[84:87], a[12:15], v[52:55]\n"
"v_mfma_f32_16x16x32_bf16 v[80:83], a[80:83], a[16:19], v[80:83]\n"
"v_mfma_f32_16x16x32_bf16 v[80:83], a[84:87], a[20:23], v[80:83]\n"
"s_add_u32 m0, s76, 0\n"
"s_nop 0\n"
"buffer_load_dwordx4 v1, s[44:47], s75 offen offset:1024 nt lds\n"
"v_mfma_f32_16x16x32_bf16 v[108:111], a[80:83], a[24:27], v[108:111]\n"
"ds_read_b64_tr_b16 a[80:81], v8 offset:256\n"
"ds_read_b64_tr_b16 a[82:83], v8 offset:1280\n"
"v_mfma_f32_16x16x32_bf16 v[108:111], a[84:87], a[28:31], v[108:111]\n"
"ds_read_b64_tr_b16 a[84:85], v8 offset:8448\n"
"ds_read_b64_tr_b16 a[86:87], v8 offset:9472\n"
"v_mfma_f32_16x16x32_bf16 v[28:31], a[88:91], a[0:3], v[28:31]\n"
"v_mfma_f32_16x16x32_bf16 v[28:31], a[92:95], a[4:7], v[28:31]\n"
"s_add_u32 m0, s59, 0\n"
"s_add_u32 s56, s56, 0x1c00\n"
"buffer_load_dwordx4 v11, s[36:39], s56 offen offset:3072 lds\n"
"v_mfma_f32_16x16x32_bf16 v[56:59], a[88:91], a[8:11], v[56:59]\n"
"v_mfma_f32_16x16x32_bf16 v[56:59], a[92:95], a[12:15], v[56:59]\n"
"v_mfma_f32_16x16x32_bf16 v[84:87], a[88:91], a[16:19], v[84:87]\n"
"v_mfma_f32_16x16x32_bf16 v[84:87], a[92:95], a[20:23], v[84:87]\n"
"v_mfma_f32_16x16x32_bf16 v[112:115], a[88:91], a[24:27], v[112:115]\n"
"ds_read_b64_tr_b16 a[88:89], v9 offset:256\n"
"ds_read_b64_tr_b16 a[90:91], v9 offset:1280\n"
"v_mfma_f32_16x16x32_bf16 v[112:115], a[92:95], a[28:31], v[112:115]\n"
"ds_read_b64_tr_b16 a[92:93], v9 offset:8448\n"
"ds_read_b64_tr_b16 a[94:95], v9 offset:9472\n"
"v_mfma_f32_16x16x32_bf16 v[32:35], a[96:99], a[0:3], v[32:35]\n"
"v_mfma_f32_16x16x32_bf16 v[32:35], a[100:103], a[4:7], v[32:35]\n"
"s_add_u32 m0, s59, 4096\n"
"s_add_u32 s56, s56, 0x2c00\n"
"buffer_load_dwordx4 v10, s[36:39], s56 offen offset:0 lds\n"
"v_mfma_f32_16x16x32_bf16 v[60:63], a[96:99], a[8:11], v[60:63]\n"
"v_mfma_f32_16x16x32_bf16 v[60:63], a[100:103], a[12:15], v[60:63]\n"
"v_mfma_f32_16x16x32_bf16 v[88:91], a[96:99], a[16:19], v[88:91]\n"
"v_mfma_f32_16x16x32_bf16 v[88:91], a[100:103], a[20:23], v[88:91]\n"
"s_add_u32 m0, s76, 0\n"
"s_nop 0\n"
"buffer_load_dwordx4 v2, s[44:47], s75 offen offset:2048 nt lds\n"
"v_mfma_f32_16x16x32_bf16 v[116:119], a[96:99], a[24:27], v[116:119]\n"
"ds_read_b64_tr_b16 a[96:97], v8 offset:512\n"
"ds_read_b64_tr_b16 a[98:99], v8 offset:1536\n"
"v_mfma_f32_16x16x32_bf16 v[116:119], a[100:103], a[28:31], v[116:119]\n"
"ds_read_b64_tr_b16 a[100:101], v8 offset:8704\n"
"ds_read_b64_tr_b16 a[102:103], v8 offset:9728\n"
"v_mfma_f32_16x16x32_bf16 v[36:39], a[104:107], a[0:3], v[36:39]\n"
"s_waitcnt lgkmcnt(15)\n"
"v_mfma_f32_16x16x32_bf16 v[36:39], a[108:111], a[4:7], v[36:39]\n"
"s_add_u32 m0, s59, 4096\n"
"s_add_u32 s56, s56, 0x1c00\n"
"buffer_load_dwordx4 v11, s[36:39], s56 offen offset:1024 lds\n"
"v_mfma_f32_16x16x32_bf16 v[64:67], a[104:107], a[8:11], v[64:67]\n"
"v_mfma_f32_16x16x32_bf16 v[64:67], a[108:111], a[12:15], v[64:67]\n"
"v_mfma_f32_16x16x32_bf16 v[92:95], a[104:107], a[16:19], v[92:95]\n"
"v_mfma_f32_16x16x32_bf16 v[92:95], a[108:111], a[20:23], v[92:95]\n"
"v_mfma_f32_16x16x32_bf16 v[120:123], a[104:107], a[24:27], v[120:123]\n"
"ds_read_b64_tr_b16 a[104:105], v9 offset:512\n"
"ds_read_b64_tr_b16 a[106:107], v9 offset:1536\n"
"v_mfma_f32_16x16x32_bf16 v[120:123], a[108:111], a[28:31], v[120:123]\n"
"ds_read_b64_tr_b16 a[108:109], v9 offset:8704\n"
"ds_read_b64_tr_b16 a[110:111], v9 offset:9728\n"
"v_mfma_f32_16x16x32_bf16 v[40:43], a[112:115], a[0:3], v[40:43]\n"
"v_mfma_f32_16x16x32_bf16 v[40:43], a[116:119], a[4:7], v[40:43]\n"
"s_add_u32 m0, s59, 4096\n"
"s_add_u32 s56, s56, 0x1c00\n"
"buffer_load_dwordx4 v10, s[36:39], s56 offen offset:2048 lds\n"
"v_mfma_f32_16x16x32_bf16 v[68:71], a[112:115], a[8:11], v[68:71]\n"
"v_mfma_f32_16x16x32_bf16 v[68:71], a[116:119], a[12:15], v[68:71]\n"
"v_mfma_f32_16x16x32_bf16 v[96:99], a[112:115], a[16:19], v[96:99]\n"
"v_mfma_f32_16x16x32_bf16 v[96:99], a[116:119], a[20:23], v[96:99]\n"
"s_add_u32 m0, s76, 0\n"
"s_nop 0\n"
"buffer_load_dwordx4 v3, s[44:47], s75 offen offset:3072 nt lds\n"
"v_mfma_f32_16x16x32_bf16 v[124:127], a[112:115], a[24:27], v[124:127]\n"
"ds_read_b64_tr_b16 a[112:113], v8 offset:768\n"
"ds_read_b64_tr_b16 a[114:115], v8 offset:1792\n"
"v_mfma_f32_16x16x32_bf16 v[124:127], a[116:119], a[28:31], v[124:127]\n"
"ds_read_b64_tr_b16 a[116:117], v8 offset:8960\n"
"ds_read_b64_tr_b16 a[118:119], v8 offset:9984\n"
"v_mfma_f32_16x16x32_bf16 a[204:207], a[120:123], a[0:3], a[204:207]\n"
"v_mfma_f32_16x16x32_bf16 a[204:207], a[124:127], a[4:7], a[204:207]\n"
"s_add_u32 m0, s59, 4096\n"
"s_add_u32 s56, s56, 0x1c00\n"
"buffer_load_dwordx4 v11, s[36:39], s56 offen offset:3072 lds\n"
"v_mfma_f32_16x16x32_bf16 a[220:223], a[120:123], a[8:11], a[220:223]\n"
"v_mfma_f32_16x16x32_bf16 a[220:223], a[124:127], a[12:15], a[220:223]\n"
"v_mfma_f32_16x16x32_bf16 a[236:239], a[120:123], a[16:19], a[236:239]\n"
"v_mfma_f32_16x16x32_bf16 a[236:239], a[124:127], a[20:23], a[236:239]\n"
"v_mfma_f32_16x16x32_bf16 a[252:255], a[120:123], a[24:27], a[252:255]\n"
"ds_read_b64_tr_b16 a[120:121], v9 offset:768\n"
"ds_read_b64_tr_b16 a[122:123], v9 offset:1792\n"
"v_mfma_f32_16x16x32_bf16 a[252:255], a[124:127], a[28:31], a[252:255]\n"
"ds_read_b64_tr_b16 a[124:125], v9 offset:8960\n"
"ds_read_b64_tr_b16 a[126:127], v9 offset:9984\n"
"v_mfma_f32_16x16x32_bf16 v[128:131], a[64:67], a[0:3], v[128:131]\n"
"v_mfma_f32_16x16x32_bf16 v[128:131], a[68:71], a[4:7], v[128:131]\n"
"v_mfma_f32_16x16x32_bf16 v[160:163], a[64:67], a[8:11], v[160:163]\n"
"v_mfma_f32_16x16x32_bf16 v[160:163], a[68:71], a[12:15], v[160:163]\n"
"v_mfma_f32_16x16x32_bf16 v[192:195], a[64:67], a[16:19], v[192:195]\n"
"v_mfma_f32_16x16x32_bf16 v[192:195], a[68:71], a[20:23], v[192:195]\n"
"v_mfma_f32_16x16x32_bf16 v[224:227], a[64:67], a[24:27], v[224:227]\n"
"v_mfma_f32_16x16x32_bf16 v[224:227], a[68:71], a[28:31], v[224:227]\n"
"s_waitcnt lgkmcnt(15)\n"
"v_mfma_f32_16x16x32_bf16 v[132:135], a[72:75], a[0:3], v[132:135]\n"
"s_waitcnt lgkmcnt(0)\n"
"s_waitcnt vmcnt(12)\n"
"s_barrier\n"
"v_xor_b32 v8, 0x10000, v8\n"
"v_xor_b32 v9, 0x10000, v9\n"
"v_mfma_f32_16x16x32_bf16 v[132:135], a[76:79], a[4:7], v[132:135]\n"
"s_add_u32 m0, s58, 0\n"
"s_add_u32 s57, s57, 0x68c00\n"
"buffer_load_dwordx4 v12, s[40:43], s57 offen offset:0 nt lds\n"
"v_mfma_f32_16x16x32_bf16 v[164:167], a[72:75], a[8:11], v[164:167]\n"
"ds_read_b64_tr_b16 a[64:65], v8 offset:16384\n"
"ds_read_b64_tr_b16 a[66:67], v8 offset:17408\n"
"v_mfma_f32_16x16x32_bf16 v[164:167], a[76:79], a[12:15], v[164:167]\n"
"ds_read_b64_tr_b16 a[68:69], v8 offset:24576\n"
"ds_read_b64_tr_b16 a[70:71], v8 offset:25600\n"
"v_mfma_f32_16x16x32_bf16 v[196:199], a[72:75], a[16:19], v[196:199]\n"
"v_mfma_f32_16x16x32_bf16 v[196:199], a[76:79], a[20:23], v[196:199]\n"
"v_mfma_f32_16x16x32_bf16 v[228:231], a[72:75], a[24:27], v[228:231]\n"
"ds_read_b64_tr_b16 a[72:73], v9 offset:16384\n"
"ds_read_b64_tr_b16 a[74:75], v9 offset:17408\n"
"v_mfma_f32_16x16x32_bf16 v[228:231], a[76:79], a[28:31], v[228:231]\n"
"s_add_u32 m0, s58, 0\n"
"s_add_u32 s57, s57, 0x7c00\n"
"buffer_load_dwordx4 v12, s[40:43], s57 offen offset:1024 nt lds\n"
"v_mfma_f32_16x16x32_bf16 v[136:139], a[80:83], a[0:3], v[136:139]\n"
"ds_read_b64_tr_b16 a[76:77], v9 offset:24576\n"
"ds_read_b64_tr_b16 a[78:79], v9 offset:25600\n"
"v_mfma_f32_16x16x32_bf16 v[136:139], a[84:87], a[4:7], v[136:139]\n"
"v_mfma_f32_16x16x32_bf16 v[168:171], a[80:83], a[8:11], v[168:171]\n"
"v_mfma_f32_16x16x32_bf16 v[168:171], a[84:87], a[12:15], v[168:171]\n"
"v_mfma_f32_16x16x32_bf16 v[200:203], a[80:83], a[16:19], v[200:203]\n"
"v_mfma_f32_16x16x32_bf16 v[200:203], a[84:87], a[20:23], v[200:203]\n"
"s_add_u32 m0, s58, 0\n"
"s_add_u32 s57, s57, 0x7c00\n"
"buffer_load_dwordx4 v13, s[40:43], s57 offen offset:2048 nt lds\n"
"v_mfma_f32_16x16x32_bf16 v[232:235], a[80:83], a[24:27], v[232:235]\n"
"ds_read_b64_tr_b16 a[80:81], v8 offset:16640\n"
"ds_read_b64_tr_b16 a[82:83], v8 offset:17664\n"
"v_mfma_f32_16x16x32_bf16 v[232:235], a[84:87], a[28:31], v[232:235]\n"
"ds_read_b64_tr_b16 a[84:85], v8 offset:24832\n"
"ds_read_b64_tr_b16 a[86:87], v8 offset:25856\n"
"v_mfma_f32_16x16x32_bf16 v[140:143], a[88:91], a[0:3], v[140:143]\n"
"v_mfma_f32_16x16x32_bf16 v[140:143], a[92:95], a[4:7], v[140:143]\n"
"v_mfma_f32_16x16x32_bf16 v[172:175], a[88:91], a[8:11], v[172:175]\n"
"v_mfma_f32_16x16x32_bf16 v[172:175], a[92:95], a[12:15], v[172:175]\n"
"s_add_u32 m0, s58, 0\n"
"s_add_u32 s57, s57, 0x7c00\n"
"buffer_load_dwordx4 v13, s[40:43], s57 offen offset:3072 nt lds\n"
"v_mfma_f32_16x16x32_bf16 v[204:207], a[88:91], a[16:19], v[204:207]\n"
"v_mfma_f32_16x16x32_bf16 v[204:207], a[92:95], a[20:23], v[204:207]\n"
"v_mfma_f32_16x16x32_bf16 v[236:239], a[88:91], a[24:27], v[236:239]\n"
"ds_read_b64_tr_b16 a[88:89], v9 offset:16640\n"
"ds_read_b64_tr_b16 a[90:91], v9 offset:17664\n"
"v_mfma_f32_16x16x32_bf16 v[236:239], a[92:95], a[28:31], v[236:239]\n"
"ds_read_b64_tr_b16 a[92:93], v9 offset:24832\n"
"ds_read_b64_tr_b16 a[94:95], v9 offset:25856\n"
"v_mfma_f32_16x16x32_bf16 v[144:147], a[96:99], a[0:3], v[144:147]\n"
"v_mfma_f32_16x16x32_bf16 v[144:147], a[100:103], a[4:7], v[144:147]\n"
"s_add_u32 m0, s58, 16128\n"
"s_add_u32 s57, s57, 0xfffe8c00\n"
"buffer_load_dwordx4 v12, s[40:43], s57 offen offset:256 nt lds\n"
"v_mfma_f32_16x16x32_bf16 v[176:179], a[96:99], a[8:11], v[176:179]\n"
"v_mfma_f32_16x16x32_bf16 v[176:179], a[100:103], a[12:15], v[176:179]\n"
"v_mfma_f32_16x16x32_bf16 v[208:211], a[96:99], a[16:19], v[208:211]\n"
"v_mfma_f32_16x16x32_bf16 v[208:211], a[100:103], a[20:23], v[208:211]\n"
"v_mfma_f32_16x16x32_bf16 v[240:243], a[96:99], a[24:27], v[240:243]\n"
"ds_read_b64_tr_b16 a[96:97], v8 offset:16896\n"
"ds_read_b64_tr_b16 a[98:99], v8 offset:17920\n"
"v_mfma_f32_16x16x32_bf16 v[240:243], a[100:103], a[28:31], v[240:243]\n"
"s_add_u32 m0, s58, 16128\n"
"s_add_u32 s57, s57, 0x7c00\n"
"buffer_load_dwordx4 v12, s[40:43], s57 offen offset:1280 nt lds\n"
"v_mfma_f32_16x16x32_bf16 v[148:151], a[104:107], a[0:3], v[148:151]\n"
"ds_read_b64_tr_b16 a[100:101], v8 offset:25088\n"
"ds_read_b64_tr_b16 a[102:103], v8 offset:26112\n"
"v_mfma_f32_16x16x32_bf16 v[148:151], a[108:111], a[4:7], v[148:151]\n"
"v_mfma_f32_16x16x32_bf16 v[180:183], a[104:107], a[8:11], v[180:183]\n"
"v_mfma_f32_16x16x32_bf16 v[180:183], a[108:111], a[12:15], v[180:183]\n"
"v_mfma_f32_16x16x32_bf16 v[212:215], a[104:107], a[16:19], v[212:215]\n"
"v_mfma_f32_16x16x32_bf16 v[212:215], a[108:111], a[20:23], v[212:215]\n"
"s_add_u32 m0, s58, 16128\n"
"s_add_u32 s57, s57, 0x7c00\n"
"buffer_load_dwordx4 v13, s[40:43], s57 offen offset:2304 nt lds\n"
"v_mfma_f32_16x16x32_bf16 v[244:247], a[104:107], a[24:27], v[244:247]\n"
"ds_read_b64_tr_b16 a[104:105], v9 offset:16896\n"
"ds_read_b64_tr_b16 a[106:107], v9 offset:17920\n"
"v_mfma_f32_16x16x32_bf16 v[244:247], a[108:111], a[28:31], v[244:247]\n"
"s_waitcnt vmcnt(7)\n"
"ds_read_b128 a[32:35], v6 offset:0\n"
"v_mfma_f32_16x16x32_bf16 v[152:155], a[112:115], a[0:3], v[152:155]\n"
"ds_read_b128 a[36:39], v7 offset:0\n"
"ds_read_b128 a[40:43], v6 offset:2048\n"
"v_mfma_f32_16x16x32_bf16 v[152:155], a[116:119], a[4:7], v[152:155]\n"
"ds_read_b128 a[44:47], v7 offset:2048\n"
"ds_read_b128 a[48:51], v6 offset:4096\n"
"v_mfma_f32_16x16x32_bf16 v[184:187], a[112:115], a[8:11], v[184:187]\n"
"ds_read_b128 a[52:55], v7 offset:4096\n"
"ds_read_b128 a[56:59], v6 offset:6144\n"
"v_mfma_f32_16x16x32_bf16 v[184:187], a[116:119], a[12:15], v[184:187]\n"
"s_add_u32 m0, s58, 16128\n"
"s_add_u32 s57, s57, 0x7c00\n"
"buffer_load_dwordx4 v13, s[40:43], s57 offen offset:3328 nt lds\n"
"s_xor_b32 s58, s58, 0x10000\n"
"v_mfma_f32_16x16x32_bf16 v[216:219], a[112:115], a[16:19], v[216:219]\n"
"ds_read_b128 a[60:63], v7 offset:6144\n"
"ds_read_b64_tr_b16 a[108:109], v9 offset:25088\n"
"v_mfma_f32_16x16x32_bf16 v[216:219], a[116:119], a[20:23], v[216:219]\n"
"ds_read_b64_tr_b16 a[110:111], v9 offset:26112\n"
"v_mfma_f32_16x16x32_bf16 v[248:251], a[112:115], a[24:27], v[248:251]\n"
"ds_read_b64_tr_b16 a[112:113], v8 offset:17152\n"
"ds_read_b64_tr_b16 a[114:115], v8 offset:18176\n"
"v_mfma_f32_16x16x32_bf16 v[248:251], a[116:119], a[28:31], v[248:251]\n"
"ds_read_b64_tr_b16 a[116:117], v8 offset:25344\n"
"ds_read_b64_tr_b16 a[118:119], v8 offset:26368\n"
"v_mfma_f32_16x16x32_bf16 v[156:159], a[120:123], a[0:3], v[156:159]\n"
"v_mfma_f32_16x16x32_bf16 v[156:159], a[124:127], a[4:7], v[156:159]\n"
"v_mfma_f32_16x16x32_bf16 v[188:191], a[120:123], a[8:11], v[188:191]\n"
"v_mfma_f32_16x16x32_bf16 v[188:191], a[124:127], a[12:15], v[188:191]\n"
"v_mfma_f32_16x16x32_bf16 v[220:223], a[120:123], a[16:19], v[220:223]\n"
"v_mfma_f32_16x16x32_bf16 v[220:223], a[124:127], a[20:23], v[220:223]\n"
"v_mfma_f32_16x16x32_bf16 v[252:255], a[120:123], a[24:27], v[252:255]\n"
"ds_read_b64_tr_b16 a[120:121], v9 offset:17152\n"
"ds_read_b64_tr_b16 a[122:123], v9 offset:18176\n"
"v_mfma_f32_16x16x32_bf16 v[252:255], a[124:127], a[28:31], v[252:255]\n"
"s_waitcnt lgkmcnt(15)\n"
"v_mfma_f32_16x16x32_bf16 v[16:19], a[64:67], a[32:35], v[16:19]\n"
"ds_read_b64_tr_b16 a[124:125], v9 offset:25344\n"
"ds_read_b64_tr_b16 a[126:127], v9 offset:26368\n"
"s_waitcnt lgkmcnt(15)\n"
"v_mfma_f32_16x16x32_bf16 v[16:19], a[68:71], a[36:39], v[16:19]\n"
"s_add_u32 m0, s59, 0\n"
"s_add_u32 s56, s56, 0xffff2c80\n"
"s_waitcnt lgkmcnt(10)\n"
"buffer_load_dwordx4 v10, s[36:39], s56 offen offset:0 lds\n"
"v_mfma_f32_16x16x32_bf16 v[44:47], a[64:67], a[40:43], v[44:47]\n"
"v_mfma_f32_16x16x32_bf16 v[44:47], a[68:71], a[44:47], v[44:47]\n"
"v_mfma_f32_16x16x32_bf16 v[72:75], a[64:67], a[48:51], v[72:75]\n"
"v_mfma_f32_16x16x32_bf16 v[72:75], a[68:71], a[52:55], v[72:75]\n"
"s_add_u32 m0, s76, 65536\n"
"s_nop 0\n"
"buffer_load_dwordx4 v4, s[48:51], s75 offen offset:0 nt lds\n"
"v_mfma_f32_16x16x32_bf16 v[100:103], a[64:67], a[56:59], v[100:103]\n"
"ds_read_b64_tr_b16 a[64:65], v8 offset:0\n"
"ds_read_b64_tr_b16 a[66:67], v8 offset:1024\n"
"v_mfma_f32_16x16x32_bf16 v[100:103], a[68:71], a[60:63], v[100:103]\n"
"ds_read_b64_tr_b16 a[68:69], v8 offset:8192\n"
"ds_read_b64_tr_b16 a[70:71], v8 offset:9216\n"
"v_mfma_f32_16x16x32_bf16 v[20:23], a[72:75], a[32:35], v[20:23]\n"
"v_mfma_f32_16x16x32_bf16 v[20:23], a[76:79], a[36:39], v[20:23]\n"
"s_add_u32 m0, s59, 0\n"
"s_add_u32 s56, s56, 0x1c00\n"
"buffer_load_dwordx4 v11, s[36:39], s56 offen offset:1024 lds\n"
"v_mfma_f32_16x16x32_bf16 v[48:51], a[72:75], a[40:43], v[48:51]\n"
"v_mfma_f32_16x16x32_bf16 v[48:51], a[76:79], a[44:47], v[48:51]\n"
"v_mfma_f32_16x16x32_bf16 v[76:79], a[72:75], a[48:51], v[76:79]\n"
"v_mfma_f32_16x16x32_bf16 v[76:79], a[76:79], a[52:55], v[76:79]\n"
"v_mfma_f32_16x16x32_bf16 v[104:107], a[72:75], a[56:59], v[104:107]\n"
"ds_read_b64_tr_b16 a[72:73], v9 offset:0\n"
"ds_read_b64_tr_b16 a[74:75], v9 offset:1024\n"
"v_mfma_f32_16x16x32_bf16 v[104:107], a[76:79], a[60:63], v[104:107]\n"
"ds_read_b64_tr_b16 a[76:77], v9 offset:8192\n"
"ds_read_b64_tr_b16 a[78:79], v9 offset:9216\n"
"v_mfma_f32_16x16x32_bf16 v[24:27], a[80:83], a[32:35], v[24:27]\n"
"v_mfma_f32_16x16x32_bf16 v[24:27], a[84:87], a[36:39], v[24:27]\n"
"s_add_u32 m0, s59, 0\n"
"s_add_u32 s56, s56, 0x1c00\n"
"buffer_load_dwordx4 v10, s[36:39], s56 offen offset:2048 lds\n"
"v_mfma_f32_16x16x32_bf16 v[52:55], a[80:83], a[40:43], v[52:55]\n"
"v_mfma_f32_16x16x32_bf16 v[52:55], a[84:87], a[44:47], v[52:55]\n"
"v_mfma_f32_16x16x32_bf16 v[80:83], a[80:83], a[48:51], v[80:83]\n"
"v_mfma_f32_16x16x32_bf16 v[80:83], a[84:87], a[52:55], v[80:83]\n"
"s_add_u32 m0, s76, 65536\n"
"s_nop 0\n"
"buffer_load_dwordx4 v5, s[48:51], s75 offen offset:1024 nt lds\n"
"v_mfma_f32_16x16x32_bf16 v[108:111], a[80:83], a[56:59], v[108:111]\n"
"ds_read_b64_tr_b16 a[80:81], v8 offset:256\n"
"ds_read_b64_tr_b16 a[82:83], v8 offset:1280\n"
"v_mfma_f32_16x16x32_bf16 v[108:111], a[84:87], a[60:63], v[108:111]\n"
"ds_read_b64_tr_b16 a[84:85], v8 offset:8448\n"
"ds_read_b64_tr_b16 a[86:87], v8 offset:9472\n"
"v_mfma_f32_16x16x32_bf16 v[28:31], a[88:91], a[32:35], v[28:31]\n"
"v_mfma_f32_16x16x32_bf16 v[28:31], a[92:95], a[36:39], v[28:31]\n"
"s_add_u32 m0, s59, 0\n"
"s_add_u32 s56, s56, 0x1c00\n"
"buffer_load_dwordx4 v11, s[36:39], s56 offen offset:3072 lds\n"
"v_mfma_f32_16x16x32_bf16 v[56:59], a[88:91], a[40:43], v[56:59]\n"
"v_mfma_f32_16x16x32_bf16 v[56:59], a[92:95], a[44:47], v[56:59]\n"
"v_mfma_f32_16x16x32_bf16 v[84:87], a[88:91], a[48:51], v[84:87]\n"
"v_mfma_f32_16x16x32_bf16 v[84:87], a[92:95], a[52:55], v[84:87]\n"
"v_mfma_f32_16x16x32_bf16 v[112:115], a[88:91], a[56:59], v[112:115]\n"
"ds_read_b64_tr_b16 a[88:89], v9 offset:256\n"
"ds_read_b64_tr_b16 a[90:91], v9 offset:1280\n"
"v_mfma_f32_16x16x32_bf16 v[112:115], a[92:95], a[60:63], v[112:115]\n"
"ds_read_b64_tr_b16 a[92:93], v9 offset:8448\n"
"ds_read_b64_tr_b16 a[94:95], v9 offset:9472\n"
"v_mfma_f32_16x16x32_bf16 v[32:35], a[96:99], a[32:35], v[32:35]\n"
"v_mfma_f32_16x16x32_bf16 v[32:35], a[100:103], a[36:39], v[32:35]\n"
"s_add_u32 m0, s59, 4096\n"
"s_add_u32 s56, s56, 0x2c00\n"
"buffer_load_dwordx4 v10, s[36:39], s56 offen offset:0 lds\n"
"v_mfma_f32_16x16x32_bf16 v[60:63], a[96:99], a[40:43], v[60:63]\n"
"v_mfma_f32_16x16x32_bf16 v[60:63], a[100:103], a[44:47], v[60:63]\n"
"v_mfma_f32_16x16x32_bf16 v[88:91], a[96:99], a[48:51], v[88:91]\n"
"v_mfma_f32_16x16x32_bf16 v[88:91], a[100:103], a[52:55], v[88:91]\n"
"s_add_u32 m0, s76, 65536\n"
"s_nop 0\n"
"buffer_load_dwordx4 v4, s[48:51], s75 offen offset:2048 nt lds\n"
"v_mfma_f32_16x16x32_bf16 v[116:119], a[96:99], a[56:59], v[116:119]\n"
"ds_read_b64_tr_b16 a[96:97], v8 offset:512\n"
"ds_read_b64_tr_b16 a[98:99], v8 offset:1536\n"
"v_mfma_f32_16x16x32_bf16 v[116:119], a[100:103], a[60:63], v[116:119]\n"
"ds_read_b64_tr_b16 a[100:101], v8 offset:8704\n"
"ds_read_b64_tr_b16 a[102:103], v8 offset:9728\n"
"v_mfma_f32_16x16x32_bf16 v[36:39], a[104:107], a[32:35], v[36:39]\n"
"s_waitcnt lgkmcnt(15)\n"
"v_mfma_f32_16x16x32_bf16 v[36:39], a[108:111], a[36:39], v[36:39]\n"
"s_add_u32 m0, s59, 4096\n"
"s_add_u32 s56, s56, 0x1c00\n"
"buffer_load_dwordx4 v11, s[36:39], s56 offen offset:1024 lds\n"
"v_mfma_f32_16x16x32_bf16 v[64:67], a[104:107], a[40:43], v[64:67]\n"
"v_mfma_f32_16x16x32_bf16 v[64:67], a[108:111], a[44:47], v[64:67]\n"
"v_mfma_f32_16x16x32_bf16 v[92:95], a[104:107], a[48:51], v[92:95]\n"
"v_mfma_f32_16x16x32_bf16 v[92:95], a[108:111], a[52:55], v[92:95]\n"
"v_mfma_f32_16x16x32_bf16 v[120:123], a[104:107], a[56:59], v[120:123]\n"
"ds_read_b64_tr_b16 a[104:105], v9 offset:512\n"
"ds_read_b64_tr_b16 a[106:107], v9 offset:1536\n"
"v_mfma_f32_16x16x32_bf16 v[120:123], a[108:111], a[60:63], v[120:123]\n"
"ds_read_b64_tr_b16 a[108:109], v9 offset:8704\n"
"ds_read_b64_tr_b16 a[110:111], v9 offset:9728\n"
"v_mfma_f32_16x16x32_bf16 v[40:43], a[112:115], a[32:35], v[40:43]\n"
"v_mfma_f32_16x16x32_bf16 v[40:43], a[116:119], a[36:39], v[40:43]\n"
"s_add_u32 m0, s59, 4096\n"
"s_add_u32 s56, s56, 0x1c00\n"
"buffer_load_dwordx4 v10, s[36:39], s56 offen offset:2048 lds\n"
"v_mfma_f32_16x16x32_bf16 v[68:71], a[112:115], a[40:43], v[68:71]\n"
"v_mfma_f32_16x16x32_bf16 v[68:71], a[116:119], a[44:47], v[68:71]\n"
"v_mfma_f32_16x16x32_bf16 v[96:99], a[112:115], a[48:51], v[96:99]\n"
"v_mfma_f32_16x16x32_bf16 v[96:99], a[116:119], a[52:55], v[96:99]\n"
"s_add_u32 m0, s76, 65536\n"
"s_nop 0\n"
"buffer_load_dwordx4 v5, s[48:51], s75 offen offset:3072 nt lds\n"
"s_add_u32 s75, s75, 0x4000\n"
"s_lshr_b32 s74, s75, 15\n"
"s_mul_i32 s74, s74, 0x24000\n"
"s_add_u32 s76, s76, 0x4000\n"
"s_add_u32 s76, s76, s74\n"
"v_mfma_f32_16x16x32_bf16 v[124:127], a[112:115], a[56:59], v[124:127]\n"
"ds_read_b64_tr_b16 a[112:113], v8 offset:768\n"
"ds_read_b64_tr_b16 a[114:115], v8 offset:1792\n"
"v_mfma_f32_16x16x32_bf16 v[124:127], a[116:119], a[60:63], v[124:127]\n"
"ds_read_b64_tr_b16 a[116:117], v8 offset:8960\n"
"ds_read_b64_tr_b16 a[118:119], v8 offset:9984\n"
"v_mfma_f32_16x16x32_bf16 a[204:207], a[120:123], a[32:35], a[204:207]\n"
"v_mfma_f32_16x16x32_bf16 a[204:207], a[124:127], a[36:39], a[204:207]\n"
"s_add_u32 m0, s59, 4096\n"
"s_add_u32 s56, s56, 0x1c00\n"
"buffer_load_dwordx4 v11, s[36:39], s56 offen offset:3072 lds\n"
"v_mfma_f32_16x16x32_bf16 a[220:223], a[120:123], a[40:43], a[220:223]\n"
"v_mfma_f32_16x16x32_bf16 a[220:223], a[124:127], a[44:47], a[220:223]\n"
"v_mfma_f32_16x16x32_bf16 a[236:239], a[120:123], a[48:51], a[236:239]\n"
"v_mfma_f32_16x16x32_bf16 a[236:239], a[124:127], a[52:55], a[236:239]\n"
"v_mfma_f32_16x16x32_bf16 a[252:255], a[120:123], a[56:59], a[252:255]\n"
"ds_read_b64_tr_b16 a[120:121], v9 offset:768\n"
"ds_read_b64_tr_b16 a[122:123], v9 offset:1792\n"
"v_mfma_f32_16x16x32_bf16 a[252:255], a[124:127], a[60:63], a[252:255]\n"
"ds_read_b64_tr_b16 a[124:125], v9 offset:8960\n"
"ds_read_b64_tr_b16 a[126:127], v9 offset:9984\n"
"v_mfma_f32_16x16x32_bf16 v[128:131], a[64:67], a[32:35], v[128:131]\n"
"v_mfma_f32_16x16x32_bf16 v[128:131], a[68:71], a[36:39], v[128:131]\n"
"v_mfma_f32_16x16x32_bf16 v[160:163], a[64:67], a[40:43], v[160:163]\n"
"v_mfma_f32_16x16x32_bf16 v[160:163], a[68:71], a[44:47], v[160:163]\n"
"v_mfma_f32_16x16x32_bf16 v[192:195], a[64:67], a[48:51], v[192:195]\n"
"v_mfma_f32_16x16x32_bf16 v[192:195], a[68:71], a[52:55], v[192:195]\n"
"v_mfma_f32_16x16x32_bf16 v[224:227], a[64:67], a[56:59], v[224:227]\n"
"v_mfma_f32_16x16x32_bf16 v[224:227], a[68:71], a[60:63], v[224:227]\n"
"s_waitcnt lgkmcnt(15)\n"
"v_mfma_f32_16x16x32_bf16 v[132:135], a[72:75], a[32:35], v[132:135]\n"
"s_waitcnt lgkmcnt(0)\n"
"s_waitcnt vmcnt(12)\n"
"s_barrier\n"
"v_xor_b32 v8, 0x10000, v8\n"
"v_xor_b32 v9, 0x10000, v9\n"
"v_mfma_f32_16x16x32_bf16 v[132:135], a[76:79], a[36:39], v[132:135]\n"
"s_add_u32 m0, s58, 0\n"
"s_add_u32 s57, s57, 0x68c00\n"
"buffer_load_dwordx4 v12, s[40:43], s57 offen offset:0 nt lds\n"
"v_mfma_f32_16x16x32_bf16 v[164:167], a[72:75], a[40:43], v[164:167]\n"
"ds_read_b64_tr_b16 a[64:65], v8 offset:16384\n"
"ds_read_b64_tr_b16 a[66:67], v8 offset:17408\n"
"v_mfma_f32_16x16x32_bf16 v[164:167], a[76:79], a[44:47], v[164:167]\n"
"ds_read_b64_tr_b16 a[68:69], v8 offset:24576\n"
"ds_read_b64_tr_b16 a[70:71], v8 offset:25600\n"
"v_mfma_f32_16x16x32_bf16 v[196:199], a[72:75], a[48:51], v[196:199]\n"
"v_mfma_f32_16x16x32_bf16 v[196:199], a[76:79], a[52:55], v[196:199]\n"
"v_mfma_f32_16x16x32_bf16 v[228:231], a[72:75], a[56:59], v[228:231]\n"
"ds_read_b64_tr_b16 a[72:73], v9 offset:16384\n"
"ds_read_b64_tr_b16 a[74:75], v9 offset:17408\n"
"v_mfma_f32_16x16x32_bf16 v[228:231], a[76:79], a[60:63], v[228:231]\n"
"s_add_u32 m0, s58, 0\n"
"s_add_u32 s57, s57, 0x7c00\n"
"buffer_load_dwordx4 v12, s[40:43], s57 offen offset:1024 nt lds\n"
"v_mfma_f32_16x16x32_bf16 v[136:139], a[80:83], a[32:35], v[136:139]\n"
"ds_read_b64_tr_b16 a[76:77], v9 offset:24576\n"
"ds_read_b64_tr_b16 a[78:79], v9 offset:25600\n"
"v_mfma_f32_16x16x32_bf16 v[136:139], a[84:87], a[36:39], v[136:139]\n"
"v_mfma_f32_16x16x32_bf16 v[168:171], a[80:83], a[40:43], v[168:171]\n"
"v_mfma_f32_16x16x32_bf16 v[168:171], a[84:87], a[44:47], v[168:171]\n"
"v_mfma_f32_16x16x32_bf16 v[200:203], a[80:83], a[48:51], v[200:203]\n"
"v_mfma_f32_16x16x32_bf16 v[200:203], a[84:87], a[52:55], v[200:203]\n"
"s_add_u32 m0, s58, 0\n"
"s_add_u32 s57, s57, 0x7c00\n"
"buffer_load_dwordx4 v13, s[40:43], s57 offen offset:2048 nt lds\n"
"v_mfma_f32_16x16x32_bf16 v[232:235], a[80:83], a[56:59], v[232:235]\n"
"ds_read_b64_tr_b16 a[80:81], v8 offset:16640\n"
"ds_read_b64_tr_b16 a[82:83], v8 offset:17664\n"
"v_mfma_f32_16x16x32_bf16 v[232:235], a[84:87], a[60:63], v[232:235]\n"
"ds_read_b64_tr_b16 a[84:85], v8 offset:24832\n"
"ds_read_b64_tr_b16 a[86:87], v8 offset:25856\n"
"v_mfma_f32_16x16x32_bf16 v[140:143], a[88:91], a[32:35], v[140:143]\n"
"v_mfma_f32_16x16x32_bf16 v[140:143], a[92:95], a[36:39], v[140:143]\n"
"v_mfma_f32_16x16x32_bf16 v[172:175], a[88:91], a[40:43], v[172:175]\n"
"v_mfma_f32_16x16x32_bf16 v[172:175], a[92:95], a[44:47], v[172:175]\n"
"s_add_u32 m0, s58, 0\n"
"s_add_u32 s57, s57, 0x7c00\n"
"buffer_load_dwordx4 v13, s[40:43], s57 offen offset:3072 nt lds\n"
"v_mfma_f32_16x16x32_bf16 v[204:207], a[88:91], a[48:51], v[204:207]\n"
"v_mfma_f32_16x16x32_bf16 v[204:207], a[92:95], a[52:55], v[204:207]\n"
"v_mfma_f32_16x16x32_bf16 v[236:239], a[88:91], a[56:59], v[236:239]\n"
"ds_read_b64_tr_b16 a[88:89], v9 offset:16640\n"
"ds_read_b64_tr_b16 a[90:91], v9 offset:17664\n"
"v_mfma_f32_16x16x32_bf16 v[236:239], a[92:95], a[60:63], v[236:239]\n"
"ds_read_b64_tr_b16 a[92:93], v9 offset:24832\n"
"ds_read_b64_tr_b16 a[94:95], v9 offset:25856\n"
"v_mfma_f32_16x16x32_bf16 v[144:147], a[96:99], a[32:35], v[144:147]\n"
"v_mfma_f32_16x16x32_bf16 v[144:147], a[100:103], a[36:39], v[144:147]\n"
"s_add_u32 m0, s58, 16128\n"
"s_add_u32 s57, s57, 0xfffe8c00\n"
"buffer_load_dwordx4 v12, s[40:43], s57 offen offset:256 nt lds\n"
"v_mfma_f32_16x16x32_bf16 v[176:179], a[96:99], a[40:43], v[176:179]\n"
"v_mfma_f32_16x16x32_bf16 v[176:179], a[100:103], a[44:47], v[176:179]\n"
"v_mfma_f32_16x16x32_bf16 v[208:211], a[96:99], a[48:51], v[208:211]\n"
"v_mfma_f32_16x16x32_bf16 v[208:211], a[100:103], a[52:55], v[208:211]\n"
"v_mfma_f32_16x16x32_bf16 v[240:243], a[96:99], a[56:59], v[240:243]\n"
"ds_read_b64_tr_b16 a[96:97], v8 offset:16896\n"
"ds_read_b64_tr_b16 a[98:99], v8 offset:17920\n"
"v_mfma_f32_16x16x32_bf16 v[240:243], a[100:103], a[60:63], v[240:243]\n"
"s_add_u32 m0, s58, 16128\n"
"s_add_u32 s57, s57, 0x7c00\n"
"buffer_load_dwordx4 v12, s[40:43], s57 offen offset:1280 nt lds\n"
"v_mfma_f32_16x16x32_bf16 v[148:151], a[104:107], a[32:35], v[148:151]\n"
"ds_read_b64_tr_b16 a[100:101], v8 offset:25088\n"
"ds_read_b64_tr_b16 a[102:103], v8 offset:26112\n"
"v_mfma_f32_16x16x32_bf16 v[148:151], a[108:111], a[36:39], v[148:151]\n"
"v_mfma_f32_16x16x32_bf16 v[180:183], a[104:107], a[40:43], v[180:183]\n"
"v_mfma_f32_16x16x32_bf16 v[180:183], a[108:111], a[44:47], v[180:183]\n"
"v_mfma_f32_16x16x32_bf16 v[212:215], a[104:107], a[48:51], v[212:215]\n"
"v_mfma_f32_16x16x32_bf16 v[212:215], a[108:111], a[52:55], v[212:215]\n"
"s_add_u32 m0, s58, 16128\n"
"s_add_u32 s57, s57, 0x7c00\n"
"buffer_load_dwordx4 v13, s[40:43], s57 offen offset:2304 nt lds\n"
"v_mfma_f32_16x16x32_bf16 v[244:247], a[104:107], a[56:59], v[244:247]\n"
"ds_read_b64_tr_b16 a[104:105], v9 offset:16896\n"
"ds_read_b64_tr_b16 a[106:107], v9 offset:17920\n"
"v_mfma_f32_16x16x32_bf16 v[244:247], a[108:111], a[60:63], v[244:247]\n"
"s_waitcnt vmcnt(7)\n"
"ds_read_b128 a[0:3], v6 offset:0\n"
"v_mfma_f32_16x16x32_bf16 v[152:155], a[112:115], a[32:35], v[152:155]\n"
"ds_read_b128 a[4:7], v7 offset:0\n"
"ds_read_b128 a[8:11], v6 offset:2048\n"
"v_mfma_f32_16x16x32_bf16 v[152:155], a[116:119], a[36:39], v[152:155]\n"
"ds_read_b128 a[12:15], v7 offset:2048\n"
"ds_read_b128 a[16:19], v6 offset:4096\n"
"v_mfma_f32_16x16x32_bf16 v[184:187], a[112:115], a[40:43], v[184:187]\n"
"ds_read_b128 a[20:23], v7 offset:4096\n"
"ds_read_b128 a[24:27], v6 offset:6144\n"
"v_mfma_f32_16x16x32_bf16 v[184:187], a[116:119], a[44:47], v[184:187]\n"
"s_add_u32 m0, s58, 16128\n"
"s_add_u32 s57, s57, 0x7c00\n"
"buffer_load_dwordx4 v13, s[40:43], s57 offen offset:3328 nt lds\n"
"s_xor_b32 s58, s58, 0x10000\n"
"v_mfma_f32_16x16x32_bf16 v[216:219], a[112:115], a[48:51], v[216:219]\n"
"ds_read_b128 a[28:31], v7 offset:6144\n"
"ds_read_b64_tr_b16 a[108:109], v9 offset:25088\n"
"v_mfma_f32_16x16x32_bf16 v[216:219], a[116:119], a[52:55], v[216:219]\n"
"ds_read_b64_tr_b16 a[110:111], v9 offset:26112\n"
"v_mfma_f32_16x16x32_bf16 v[248:251], a[112:115], a[56:59], v[248:251]\n"
"ds_read_b64_tr_b16 a[112:113], v8 offset:17152\n"
"ds_read_b64_tr_b16 a[114:115], v8 offset:18176\n"
"v_mfma_f32_16x16x32_bf16 v[248:251], a[116:119], a[60:63], v[248:251]\n"
"ds_read_b64_tr_b16 a[116:117], v8 offset:25344\n"
"ds_read_b64_tr_b16 a[118:119], v8 offset:26368\n"
"v_mfma_f32_16x16x32_bf16 v[156:159], a[120:123], a[32:35], v[156:159]\n"
"v_mfma_f32_16x16x32_bf16 v[156:159], a[124:127], a[36:39], v[156:159]\n"
"v_mfma_f32_16x16x32_bf16 v[188:191], a[120:123], a[40:43], v[188:191]\n"
"v_mfma_f32_16x16x32_bf16 v[188:191], a[124:127], a[44:47], v[188:191]\n"
"v_mfma_f32_16x16x32_bf16 v[220:223], a[120:123], a[48:51], v[220:223]\n"
"v_mfma_f32_16x16x32_bf16 v[220:223], a[124:127], a[52:55], v[220:223]\n"
"v_mfma_f32_16x16x32_bf16 v[252:255], a[120:123], a[56:59], v[252:255]\n"
"ds_read_b64_tr_b16 a[120:121], v9 offset:17152\n"
"ds_read_b64_tr_b16 a[122:123], v9 offset:18176\n"
"v_mfma_f32_16x16x32_bf16 v[252:255], a[124:127], a[60:63], v[252:255]\n"
"s_sub_u32 s66, s66, 1\n"
"s_cmp_lg_u32 s66, 0\n"
"s_cbranch_scc1 .LQa_%=\n"
"s_waitcnt lgkmcnt(15)\n"
"v_mfma_f32_16x16x32_bf16 v[16:19], a[64:67], a[0:3], v[16:19]\n"
"ds_read_b64_tr_b16 a[124:125], v9 offset:25344\n"
"ds_read_b64_tr_b16 a[126:127], v9 offset:26368\n"
"s_waitcnt lgkmcnt(15)\n"
"v_mfma_f32_16x16x32_bf16 v[16:19], a[68:71], a[4:7], v[16:19]\n"
"s_add_u32 m0, s59, 0\n"
"s_add_u32 s56, s56, 0xffff2c80\n"
"s_waitcnt lgkmcnt(10)\n"
"buffer_load_dwordx4 v10, s[36:39], s56 offen offset:0 lds\n"
"v_mfma_f32_16x16x32_bf16 v[44:47], a[64:67], a[8:11], v[44:47]\n"
"v_mfma_f32_16x16x32_bf16 v[44:47], a[68:71], a[12:15], v[44:47]\n"
"v_mfma_f32_16x16x32_bf16 v[72:75], a[64:67], a[16:19], v[72:75]\n"
"v_mfma_f32_16x16x32_bf16 v[72:75], a[68:71], a[20:23], v[72:75]\n"
"v_mfma_f32_16x16x32_bf16 v[100:103], a[64:67], a[24:27], v[100:103]\n"
"ds_read_b64_tr_b16 a[64:65], v8 offset:0\n"
"ds_read_b64_tr_b16 a[66:67], v8 offset:1024\n"
"v_mfma_f32_16x16x32_bf16 v[100:103], a[68:71], a[28:31], v[100:103]\n"
"ds_read_b64_tr_b16 a[68:69], v8 offset:8192\n"
"ds_read_b64_tr_b16 a[70:71], v8 offset:9216\n"
"v_mfma_f32_16x16x32_bf16 v[20:23], a[72:75], a[0:3], v[20:23]\n"
"v_mfma_f32_16x16x32_bf16 v[20:23], a[76:79], a[4:7], v[20:23]\n"
"s_add_u32 m0, s59, 0\n"
"s_add_u32 s56, s56, 0x1c00\n"
"buffer_load_dwordx4 v11, s[36:39], s56 offen offset:1024 lds\n"
"v_mfma_f32_16x16x32_bf16 v[48:51], a[72:75], a[8:11], v[48:51]\n"
"v_mfma_f32_16x16x32_bf16 v[48:51], a[76:79], a[12:15], v[48:51]\n"
"v_mfma_f32_16x16x32_bf16 v[76:79], a[72:75], a[16:19], v[76:79]\n"
"v_mfma_f32_16x16x32_bf16 v[76:79], a[76:79], a[20:23], v[76:79]\n"
"v_mfma_f32_16x16x32_bf16 v[104:107], a[72:75], a[24:27], v[104:107]\n"
"ds_read_b64_tr_b16 a[72:73], v9 offset:0\n"
"ds_read_b64_tr_b16 a[74:75], v9 offset:1024\n"
"v_mfma_f32_16x16x32_bf16 v[104:107], a[76:79], a[28:31], v[104:107]\n"
"ds_read_b64_tr_b16 a[76:77], v9 offset:8192\n"
"ds_read_b64_tr_b16 a[78:79], v9 offset:9216\n"
"v_mfma_f32_16x16x32_bf16 v[24:27], a[80:83], a[0:3], v[24:27]\n"
"v_mfma_f32_16x16x32_bf16 v[24:27], a[84:87], a[4:7], v[24:27]\n"
"s_add_u32 m0, s59, 0\n"
"s_add_u32 s56, s56, 0x1c00\n"
"buffer_load_dwordx4 v10, s[36:39], s56 offen offset:2048 lds\n"
"v_mfma_f32_16x16x32_bf16 v[52:55], a[80:83], a[8:11], v[52:55]\n"
"v_mfma_f32_16x16x32_bf16 v[52:55], a[84:87], a[12:15], v[52:55]\n"
"v_mfma_f32_16x16x32_bf16 v[80:83], a[80:83], a[16:19], v[80:83]\n"
"v_mfma_f32_16x16x32_bf16 v[80:83], a[84:87], a[20:23], v[80:83]\n"
"v_mfma_f32_16x16x32_bf16 v[108:111], a[80:83], a[24:27], v[108:111]\n"
"ds_read_b64_tr_b16 a[80:81], v8 offset:256\n"
"ds_read_b64_tr_b16 a[82:83], v8 offset:1280\n"
"v_mfma_f32_16x16x32_bf16 v[108:111], a[84:87], a[28:31], v[108:111]\n"
"ds_read_b64_tr_b16 a[84:85], v8 offset:8448\n"
"ds_read_b64_tr_b16 a[86:87], v8 offset:9472\n"
"v_mfma_f32_16x16x32_bf16 v[28:31], a[88:91], a[0:3], v[28:31]\n"
"v_mfma_f32_16x16x32_bf16 v[28:31], a[92:95], a[4:7], v[28:31]\n"
"s_add_u32 m0, s59, 0\n"
"s_add_u32 s56, s56, 0x1c00\n"
"buffer_load_dwordx4 v11, s[36:39], s56 offen offset:3072 lds\n"
"v_mfma_f32_16x16x32_bf16 v[56:59], a[88:91], a[8:11], v[56:59]\n"
"v_mfma_f32_16x16x32_bf16 v[56:59], a[92:95], a[12:15], v[56:59]\n"
"v_mfma_f32_16x16x32_bf16 v[84:87], a[88:91], a[16:19], v[84:87]\n"
"v_mfma_f32_16x16x32_bf16 v[84:87], a[92:95], a[20:23], v[84:87]\n"
"v_mfma_f32_16x16x32_bf16 v[112:115], a[88:91], a[24:27], v[112:115]\n"
"ds_read_b64_tr_b16 a[88:89], v9 offset:256\n"
"ds_read_b64_tr_b16 a[90:91], v9 offset:1280\n"
"v_mfma_f32_16x16x32_bf16 v[112:115], a[92:95], a[28:31], v[112:115]\n"
"ds_read_b64_tr_b16 a[92:93], v9 offset:8448\n"
"ds_read_b64_tr_b16 a[94:95], v9 offset:9472\n"
"v_mfma_f32_16x16x32_bf16 v[32:35], a[96:99], a[0:3], v[32:35]\n"
"v_mfma_f32_16x16x32_bf16 v[32:35], a[100:103], a[4:7], v[32:35]\n"
"s_add_u32 m0, s59, 4096\n"
"s_add_u32 s56, s56, 0x2c00\n"
"buffer_load_dwordx4 v10, s[36:39], s56 offen offset:0 lds\n"
"v_mfma_f32_16x16x32_bf16 v[60:63], a[96:99], a[8:11], v[60:63]\n"
"v_mfma_f32_16x16x32_bf16 v[60:63], a[100:103], a[12:15], v[60:63]\n"
"v_mfma_f32_16x16x32_bf16 v[88:91], a[96:99], a[16:19], v[88:91]\n"
"v_mfma_f32_16x16x32_bf16 v[88:91], a[100:103], a[20:23], v[88:91]\n"
"v_mfma_f32_16x16x32_bf16 v[116:119], a[96:99], a[24:27], v[116:119]\n"
"ds_read_b64_tr_b16 a[96:97], v8 offset:512\n"
"ds_read_b64_tr_b16 a[98:99], v8 offset:1536\n"
"v_mfma_f32_16x16x32_bf16 v[116:119], a[100:103], a[28:31], v[116:119]\n"
"ds_read_b64_tr_b16 a[100:101], v8 offset:8704\n"
"ds_read_b64_tr_b16 a[102:103], v8 offset:9728\n"
"v_mfma_f32_16x16x32_bf16 v[36:39], a[104:107], a[0:3], v[36:39]\n"
"s_waitcnt lgkmcnt(15)\n"
"v_mfma_f32_16x16x32_bf16 v[36:39], a[108:111], a[4:7], v[36:39]\n"
"s_add_u32 m0, s59, 4096\n"
"s_add_u32 s56, s56, 0x1c00\n"
"buffer_load_dwordx4 v11, s[36:39], s56 offen offset:1024 lds\n"
"v_mfma_f32_16x16x32_bf16 v[64:67], a[104:107], a[8:11], v[64:67]\n"
"v_mfma_f32_16x16x32_bf16 v[64:67], a[108:111], a[12:15], v[64:67]\n"
"v_mfma_f32_16x16x32_bf16 v[92:95], a[104:107], a[16:19], v[92:95]\n"
"v_mfma_f32_16x16x32_bf16 v[92:95], a[108:111], a[20:23], v[92:95]\n"
"v_mfma_f32_16x16x32_bf16 v[120:123], a[104:107], a[24:27], v[120:123]\n"
"ds_read_b64_tr_b16 a[104:105], v9 offset:512\n"
"ds_read_b64_tr_b16 a[106:107], v9 offset:1536\n"
"v_mfma_f32_16x16x32_bf16 v[120:123], a[108:111], a[28:31], v[120:123]\n"
"ds_read_b64_tr_b16 a[108:109], v9 offset:8704\n"
"ds_read_b64_tr_b16 a[110:111], v9 offset:9728\n"
"v_mfma_f32_16x16x32_bf16 v[40:43], a[112:115], a[0:3], v[40:43]\n"
"v_mfma_f32_16x16x32_bf16 v[40:43], a[116:119], a[4:7], v[40:43]\n"
"s_add_u32 m0, s59, 4096\n"
"s_add_u32 s56, s56, 0x1c00\n"
"buffer_load_dwordx4 v10, s[36:39], s56 offen offset:2048 lds\n"
"v_mfma_f32_16x16x32_bf16 v[68:71], a[112:115], a[8:11], v[68:71]\n"
"v_mfma_f32_16x16x32_bf16 v[68:71], a[116:119], a[12:15], v[68:71]\n"
"v_mfma_f32_16x16x32_bf16 v[96:99], a[112:115], a[16:19], v[96:99]\n"
"v_mfma_f32_16x16x32_bf16 v[96:99], a[116:119], a[20:23], v[96:99]\n"
"v_mfma_f32_16x16x32_bf16 v[124:127], a[112:115], a[24:27], v[124:127]\n"
"ds_read_b64_tr_b16 a[112:113], v8 offset:768\n"
"ds_read_b64_tr_b16 a[114:115], v8 offset:1792\n"
"v_mfma_f32_16x16x32_bf16 v[124:127], a[116:119], a[28:31], v[124:127]\n"
"ds_read_b64_tr_b16 a[116:117], v8 offset:8960\n"
"ds_read_b64_tr_b16 a[118:119], v8 offset:9984\n"
"v_mfma_f32_16x16x32_bf16 a[204:207], a[120:123], a[0:3], a[204:207]\n"
"v_mfma_f32_16x16x32_bf16 a[204:207], a[124:127], a[4:7], a[204:207]\n"
"s_add_u32 m0, s59, 4096\n"
"s_add_u32 s56, s56, 0x1c00\n"
"buffer_load_dwordx4 v11, s[36:39], s56 offen offset:3072 lds\n"
"v_mfma_f32_16x16x32_bf16 a[220:223], a[120:123], a[8:11], a[220:223]\n"
"v_mfma_f32_16x16x32_bf16 a[220:223], a[124:127], a[12:15], a[220:223]\n"
"v_mfma_f32_16x16x32_bf16 a[236:239], a[120:123], a[16:19], a[236:239]\n"
"v_mfma_f32_16x16x32_bf16 a[236:239], a[124:127], a[20:23], a[236:239]\n"
"v_mfma_f32_16x16x32_bf16 a[252:255], a[120:123], a[24:27], a[252:255]\n"
"ds_read_b64_tr_b16 a[120:121], v9 offset:768\n"
"ds_read_b64_tr_b16 a[122:123], v9 offset:1792\n"
"v_mfma_f32_16x16x32_bf16 a[252:255], a[124:127], a[28:31], a[252:255]\n"
"ds_read_b64_tr_b16 a[124:125], v9 offset:8960\n"
"ds_read_b64_tr_b16 a[126:127], v9 offset:9984\n"
"v_mfma_f32_16x16x32_bf16 v[128:131], a[64:67], a[0:3], v[128:131]\n"
"v_mfma_f32_16x16x32_bf16 v[128:131], a[68:71], a[4:7], v[128:131]\n"
"v_mfma_f32_16x16x32_bf16 v[160:163], a[64:67], a[8:11], v[160:163]\n"
"v_mfma_f32_16x16x32_bf16 v[160:163], a[68:71], a[12:15], v[160:163]\n"
"v_mfma_f32_16x16x32_bf16 v[192:195], a[64:67], a[16:19], v[192:195]\n"
"v_mfma_f32_16x16x32_bf16 v[192:195], a[68:71], a[20:23], v[192:195]\n"
"v_mfma_f32_16x16x32_bf16 v[224:227], a[64:67], a[24:27], v[224:227]\n"
"v_mfma_f32_16x16x32_bf16 v[224:227], a[68:71], a[28:31], v[224:227]\n"
"s_waitcnt lgkmcnt(15)\n"
"v_mfma_f32_16x16x32_bf16 v[132:135], a[72:75], a[0:3], v[132:135]\n"
"s_waitcnt lgkmcnt(0)\n"
"s_waitcnt vmcnt(8)\n"
"s_barrier\n"
"v_xor_b32 v8, 0x10000, v8\n"
"v_xor_b32 v9, 0x10000, v9\n"
"v_mfma_f32_16x16x32_bf16 v[132:135], a[76:79], a[4:7], v[132:135]\n"
"s_add_u32 m0, s77, 49152\n"
"s_mov_b32 s75, 0x8000\n"
"buffer_load_dwordx4 v0, s[44:47], s75 offen offset:0 nt lds\n"
"buffer_load_dwordx4 v1, s[44:47], s75 offen offset:1024 nt lds\n"
"buffer_load_dwordx4 v2, s[44:47], s75 offen offset:2048 nt lds\n"
"buffer_load_dwordx4 v3, s[44:47], s75 offen offset:3072 nt lds\n"
"v_mfma_f32_16x16x32_bf16 v[164:167], a[72:75], a[8:11], v[164:167]\n"
"ds_read_b64_tr_b16 a[64:65], v8 offset:16384\n"
"ds_read_b64_tr_b16 a[66:67], v8 offset:17408\n"
"v_mfma_f32_16x16x32_bf16 v[164:167], a[76:79], a[12:15], v[164:167]\n"
"ds_read_b64_tr_b16 a[68:69], v8 offset:24576\n"
"ds_read_b64_tr_b16 a[70:71], v8 offset:25600\n"
"v_mfma_f32_16x16x32_bf16 v[196:199], a[72:75], a[16:19], v[196:199]\n"
"v_mfma_f32_16x16x32_bf16 v[196:199], a[76:79], a[20:23], v[196:199]\n"
"v_mfma_f32_16x16x32_bf16 v[228:231], a[72:75], a[24:27], v[228:231]\n"
"ds_read_b64_tr_b16 a[72:73], v9 offset:16384\n"
"ds_read_b64_tr_b16 a[74:75], v9 offset:17408\n"
"v_mfma_f32_16x16x32_bf16 v[228:231], a[76:79], a[28:31], v[228:231]\n"
"v_mfma_f32_16x16x32_bf16 v[136:139], a[80:83], a[0:3], v[136:139]\n"
"ds_read_b64_tr_b16 a[76:77], v9 offset:24576\n"
"ds_read_b64_tr_b16 a[78:79], v9 offset:25600\n"
"v_mfma_f32_16x16x32_bf16 v[136:139], a[84:87], a[4:7], v[136:139]\n"
"v_mfma_f32_16x16x32_bf16 v[168:171], a[80:83], a[8:11], v[168:171]\n"
"v_mfma_f32_16x16x32_bf16 v[168:171], a[84:87], a[12:15], v[168:171]\n"
"v_mfma_f32_16x16x32_bf16 v[200:203], a[80:83], a[16:19], v[200:203]\n"
"v_mfma_f32_16x16x32_bf16 v[200:203], a[84:87], a[20:23], v[200:203]\n"
"v_mfma_f32_16x16x32_bf16 v[232:235], a[80:83], a[24:27], v[232:235]\n"
"ds_read_b64_tr_b16 a[80:81], v8 offset:16640\n"
"ds_read_b64_tr_b16 a[82:83], v8 offset:17664\n"
"v_mfma_f32_16x16x32_bf16 v[232:235], a[84:87], a[28:31], v[232:235]\n"
"ds_read_b64_tr_b16 a[84:85], v8 offset:24832\n"
"ds_read_b64_tr_b16 a[86:87], v8 offset:25856\n"
"v_mfma_f32_16x16x32_bf16 v[140:143], a[88:91], a[0:3], v[140:143]\n"
"v_mfma_f32_16x16x32_bf16 v[140:143], a[92:95], a[4:7], v[140:143]\n"
"v_mfma_f32_16x16x32_bf16 v[172:175], a[88:91], a[8:11], v[172:175]\n"
"v_mfma_f32_16x16x32_bf16 v[172:175], a[92:95], a[12:15], v[172:175]\n"
"v_mfma_f32_16x16x32_bf16 v[204:207], a[88:91], a[16:19], v[204:207]\n"
"v_mfma_f32_16x16x32_bf16 v[204:207], a[92:95], a[20:23], v[204:207]\n"
"v_mfma_f32_16x16x32_bf16 v[236:239], a[88:91], a[24:27], v[236:239]\n"
"ds_read_b64_tr_b16 a[88:89], v9 offset:16640\n"
"ds_read_b64_tr_b16 a[90:91], v9 offset:17664\n"
"v_mfma_f32_16x16x32_bf16 v[236:239], a[92:95], a[28:31], v[236:239]\n"
"ds_read_b64_tr_b16 a[92:93], v9 offset:24832\n"
"ds_read_b64_tr_b16 a[94:95], v9 offset:25856\n"
"v_mfma_f32_16x16x32_bf16 v[144:147], a[96:99], a[0:3], v[144:147]\n"
"v_mfma_f32_16x16x32_bf16 v[144:147], a[100:103], a[4:7], v[144:147]\n"
"v_mfma_f32_16x16x32_bf16 v[176:179], a[96:99], a[8:11], v[176:179]\n"
"v_mfma_f32_16x16x32_bf16 v[176:179], a[100:103], a[12:15], v[176:179]\n"
"v_mfma_f32_16x16x32_bf16 v[208:211], a[96:99], a[16:19], v[208:211]\n"
"v_mfma_f32_16x16x32_bf16 v[208:211], a[100:103], a[20:23], v[208:211]\n"
"v_mfma_f32_16x16x32_bf16 v[240:243], a[96:99], a[24:27], v[240:243]\n"
"ds_read_b64_tr_b16 a[96:97], v8 offset:16896\n"
"ds_read_b64_tr_b16 a[98:99], v8 offset:17920\n"
"v_mfma_f32_16x16x32_bf16 v[240:243], a[100:103], a[28:31], v[240:243]\n"
"v_mfma_f32_16x16x32_bf16 v[148:151], a[104:107], a[0:3], v[148:151]\n"
"ds_read_b64_tr_b16 a[100:101], v8 offset:25088\n"
"ds_read_b64_tr_b16 a[102:103], v8 offset:26112\n"
"v_mfma_f32_16x16x32_bf16 v[148:151], a[108:111], a[4:7], v[148:151]\n"
"v_mfma_f32_16x16x32_bf16 v[180:183], a[104:107], a[8:11], v[180:183]\n"
"v_mfma_f32_16x16x32_bf16 v[180:183], a[108:111], a[12:15], v[180:183]\n"
"v_mfma_f32_16x16x32_bf16 v[212:215], a[104:107], a[16:19], v[212:215]\n"
"v_mfma_f32_16x16x32_bf16 v[212:215], a[108:111], a[20:23], v[212:215]\n"
"v_mfma_f32_16x16x32_bf16 v[244:247], a[104:107], a[24:27], v[244:247]\n"
"ds_read_b64_tr_b16 a[104:105], v9 offset:16896\n"
"ds_read_b64_tr_b16 a[106:107], v9 offset:17920\n"
"v_mfma_f32_16x16x32_bf16 v[244:247], a[108:111], a[28:31], v[244:247]\n"
"s_waitcnt vmcnt(4)\n"
"ds_read_b128 a[32:35], v6 offset:0\n"
"v_mfma_f32_16x16x32_bf16 v[152:155], a[112:115], a[0:3], v[152:155]\n"
"ds_read_b128 a[36:39], v7 offset:0\n"
"ds_read_b128 a[40:43], v6 offset:2048\n"
"v_mfma_f32_16x16x32_bf16 v[152:155], a[116:119], a[4:7], v[152:155]\n"
"ds_read_b128 a[44:47], v7 offset:2048\n"
"ds_read_b128 a[48:51], v6 offset:4096\n"
"v_mfma_f32_16x16x32_bf16 v[184:187], a[112:115], a[8:11], v[184:187]\n"
"ds_read_b128 a[52:55], v7 offset:4096\n"
"ds_read_b128 a[56:59], v6 offset:6144\n"
"v_mfma_f32_16x16x32_bf16 v[184:187], a[116:119], a[12:15], v[184:187]\n"
"v_mfma_f32_16x16x32_bf16 v[216:219], a[112:115], a[16:19], v[216:219]\n"
"ds_read_b128 a[60:63], v7 offset:6144\n"
"ds_read_b64_tr_b16 a[108:109], v9 offset:25088\n"
"v_mfma_f32_16x16x32_bf16 v[216:219], a[116:119], a[20:23], v[216:219]\n"
"ds_read_b64_tr_b16 a[110:111], v9 offset:26112\n"
"v_mfma_f32_16x16x32_bf16 v[248:251], a[112:115], a[24:27], v[248:251]\n"
"ds_read_b64_tr_b16 a[112:113], v8 offset:17152\n"
"ds_read_b64_tr_b16 a[114:115], v8 offset:18176\n"
"v_mfma_f32_16x16x32_bf16 v[248:251], a[116:119], a[28:31], v[248:251]\n"
"ds_read_b64_tr_b16 a[116:117], v8 offset:25344\n"
"ds_read_b64_tr_b16 a[118:119], v8 offset:26368\n"
"v_mfma_f32_16x16x32_bf16 v[156:159], a[120:123], a[0:3], v[156:159]\n"
"v_mfma_f32_16x16x32_bf16 v[156:159], a[124:127], a[4:7], v[156:159]\n"
"v_mfma_f32_16x16x32_bf16 v[188:191], a[120:123], a[8:11], v[188:191]\n"
"v_mfma_f32_16x16x32_bf16 v[188:191], a[124:127], a[12:15], v[188:191]\n"
"v_mfma_f32_16x16x32_bf16 v[220:223], a[120:123], a[16:19], v[220:223]\n"
"v_mfma_f32_16x16x32_bf16 v[220:223], a[124:127], a[20:23], v[220:223]\n"
"v_mfma_f32_16x16x32_bf16 v[252:255], a[120:123], a[24:27], v[252:255]\n"
"ds_read_b64_tr_b16 a[120:121], v9 offset:17152\n"
"ds_read_b64_tr_b16 a[122:123], v9 offset:18176\n"
"v_mfma_f32_16x16x32_bf16 v[252:255], a[124:127], a[28:31], v[252:255]\n"
"s_waitcnt lgkmcnt(15)\n"
"v_mfma_f32_16x16x32_bf16 v[16:19], a[64:67], a[32:35], v[16:19]\n"
"ds_read_b64_tr_b16 a[124:125], v9 offset:25344\n"
"ds_read_b64_tr_b16 a[126:127], v9 offset:26368\n"
"s_waitcnt lgkmcnt(15)\n"
"v_mfma_f32_16x16x32_bf16 v[16:19], a[68:71], a[36:39], v[16:19]\n"
"s_mov_b32 s40, %[wol]\n"
"s_and_b32 s41, %[woh], 0xffff\n"
"s_mov_b32 s42, 1047808\n"
"v_mbcnt_lo_u32_b32 v13, -1, 0\n"
"v_mbcnt_hi_u32_b32 v13, -1, v13\n"
"v_and_b32 v12, 1, v13\n"
"v_lshrrev_b32 v13, 1, v13\n"
"v_lshlrev_b32 v12, 7, v12\n"
"v_lshl_add_u32 v12, v13, 10, v12\n"
"s_mov_b32 m0, 161792\n"
"s_nop 0\n"
"buffer_load_dword v12, s[40:43], %[wto] offen lds\n"
"v_mfma_f32_16x16x32_bf16 v[44:47], a[64:67], a[40:43], v[44:47]\n"
"s_waitcnt lgkmcnt(14)\n"
"v_mfma_f32_16x16x32_bf16 v[44:47], a[68:71], a[44:47], v[44:47]\n"
"s_waitcnt lgkmcnt(13)\n"
"v_mfma_f32_16x16x32_bf16 v[72:75], a[64:67], a[48:51], v[72:75]\n"
"s_waitcnt lgkmcnt(12)\n"
"v_mfma_f32_16x16x32_bf16 v[72:75], a[68:71], a[52:55], v[72:75]\n"
"s_waitcnt lgkmcnt(11)\n"
"v_mfma_f32_16x16x32_bf16 v[100:103], a[64:67], a[56:59], v[100:103]\n"
"ds_read_b64_tr_b16 a[64:65], v8 offset:0\n"
"ds_read_b64_tr_b16 a[66:67], v8 offset:1024\n"
"s_waitcnt lgkmcnt(12)\n"
"v_mfma_f32_16x16x32_bf16 v[100:103], a[68:71], a[60:63], v[100:103]\n"
"ds_read_b64_tr_b16 a[68:69], v8 offset:8192\n"
"ds_read_b64_tr_b16 a[70:71], v8 offset:9216\n"
"v_mfma_f32_16x16x32_bf16 v[20:23], a[72:75], a[32:35], v[20:23]\n"
"v_mfma_f32_16x16x32_bf16 v[20:23], a[76:79], a[36:39], v[20:23]\n"
"v_mfma_f32_16x16x32_bf16 v[48:51], a[72:75], a[40:43], v[48:51]\n"
"v_mfma_f32_16x16x32_bf16 v[48:51], a[76:79], a[44:47], v[48:51]\n"
"v_mfma_f32_16x16x32_bf16 v[76:79], a[72:75], a[48:51], v[76:79]\n"
"v_pk_mul_f32 v[16:17], v[16:17], s[68:69]\n"
"v_mfma_f32_16x16x32_bf16 v[76:79], a[76:79], a[52:55], v[76:79]\n"
"v_pk_mul_f32 v[18:19], v[18:19], s[68:69]\n"
"v_mfma_f32_16x16x32_bf16 v[104:107], a[72:75], a[56:59], v[104:107]\n"
"ds_read_b64_tr_b16 a[72:73], v9 offset:0\n"
"ds_read_b64_tr_b16 a[74:75], v9 offset:1024\n"
"v_mfma_f32_16x16x32_bf16 v[104:107], a[76:79], a[60:63], v[104:107]\n"
"ds_read_b64_tr_b16 a[76:77], v9 offset:8192\n"
"ds_read_b64_tr_b16 a[78:79], v9 offset:9216\n"
"v_mfma_f32_16x16x32_bf16 v[24:27], a[80:83], a[32:35], v[24:27]\n"
"v_pk_mul_f32 v[20:21], v[20:21], s[68:69]\n"
"v_mfma_f32_16x16x32_bf16 v[24:27], a[84:87], a[36:39], v[24:27]\n"
"v_pk_mul_f32 v[22:23], v[22:23], s[68:69]\n"
"v_mfma_f32_16x16x32_bf16 v[52:55], a[80:83], a[40:43], v[52:55]\n"
"v_cvt_pk_bf16_f32 v16, v16, v17\n"
"v_cvt_pk_bf16_f32 v17, v18, v19\n"
"v_mfma_f32_16x16x32_bf16 v[52:55], a[84:87], a[44:47], v[52:55]\n"
"v_cvt_pk_bf16_f32 v18, v20, v21\n"
"v_cvt_pk_bf16_f32 v19, v22, v23\n"
"v_mfma_f32_16x16x32_bf16 v[80:83], a[80:83], a[48:51], v[80:83]\n"
"v_permlane32_swap_b32 v16, v18\n"
"v_permlane32_swap_b32 v17, v19\n"
"v_mfma_f32_16x16x32_bf16 v[80:83], a[84:87], a[52:55], v[80:83]\n"
"v_permlane16_swap_b32 v16, v18\n"
"v_permlane16_swap_b32 v17, v19\n"
"v_mfma_f32_16x16x32_bf16 v[108:111], a[80:83], a[56:59], v[108:111]\n"
"ds_read_b64_tr_b16 a[80:81], v8 offset:256\n"
"ds_read_b64_tr_b16 a[82:83], v8 offset:1280\n"
"v_mfma_f32_16x16x32_bf16 v[108:111], a[84:87], a[60:63], v[108:111]\n"
"ds_read_b64_tr_b16 a[84:85], v8 offset:8448\n"
"ds_read_b64_tr_b16 a[86:87], v8 offset:9472\n"
"v_mfma_f32_16x16x32_bf16 v[28:31], a[88:91], a[32:35], v[28:31]\n"
"v_accvgpr_write_b32 a192, v16\n"
"v_accvgpr_write_b32 a193, v17\n"
"v_mfma_f32_16x16x32_bf16 v[28:31], a[92:95], a[36:39], v[28:31]\n"
"v_accvgpr_write_b32 a194, v18\n"
"v_accvgpr_write_b32 a195, v19\n"
"v_mfma_f32_16x16x32_bf16 v[56:59], a[88:91], a[40:43], v[56:59]\n"
"v_pk_mul_f32 v[44:45], v[44:45], s[68:69]\n"
"v_mfma_f32_16x16x32_bf16 v[56:59], a[92:95], a[44:47], v[56:59]\n"
"v_pk_mul_f32 v[46:47], v[46:47], s[68:69]\n"
"v_mfma_f32_16x16x32_bf16 v[84:87], a[88:91], a[48:51], v[84:87]\n"
"v_pk_mul_f32 v[48:49], v[48:49], s[68:69]\n"
"v_mfma_f32_16x16x32_bf16 v[84:87], a[92:95], a[52:55], v[84:87]\n"
"v_pk_mul_f32 v[50:51], v[50:51], s[68:69]\n"
"v_mfma_f32_16x16x32_bf16 v[112:115], a[88:91], a[56:59], v[112:115]\n"
"ds_read_b64_tr_b16 a[88:89], v9 offset:256\n"
"ds_read_b64_tr_b16 a[90:91], v9 offset:1280\n"
"v_mfma_f32_16x16x32_bf16 v[112:115], a[92:95], a[60:63], v[112:115]\n"
"ds_read_b64_tr_b16 a[92:93], v9 offset:8448\n"
"ds_read_b64_tr_b16 a[94:95], v9 offset:9472\n"
"v_mfma_f32_16x16x32_bf16 v[32:35], a[96:99], a[32:35], v[32:35]\n"
"v_cvt_pk_bf16_f32 v44, v44, v45\n"
"v_cvt_pk_bf16_f32 v45, v46, v47\n"
"v_mfma_f32_16x16x32_bf16 v[32:35], a[100:103], a[36:39], v[32:35]\n"
"v_cvt_pk_bf16_f32 v46, v48, v49\n"
"v_cvt_pk_bf16_f32 v47, v50, v51\n"
"v_mfma_f32_16x16x32_bf16 v[60:63], a[96:99], a[40:43], v[60:63]\n"
"v_permlane32_swap_b32 v44, v46\n"
"v_permlane32_swap_b32 v45, v47\n"
"v_mfma_f32_16x16x32_bf16 v[60:63], a[100:103], a[44:47], v[60:63]\n"
"v_permlane16_swap_b32 v44, v46\n"
"v_permlane16_swap_b32 v45, v47\n"
"v_mfma_f32_16x16x32_bf16 v[88:91], a[96:99], a[48:51], v[88:91]\n"
"v_accvgpr_write_b32 a208, v44\n"
"v_accvgpr_write_b32 a209, v45\n"
"v_mfma_f32_16x16x32_bf16 v[88:91], a[100:103], a[52:55], v[88:91]\n"
"v_accvgpr_write_b32 a210, v46\n"
"v_accvgpr_write_b32 a211, v47\n"
"v_mfma_f32_16x16x32_bf16 v[116:119], a[96:99], a[56:59], v[116:119]\n"
"ds_read_b64_tr_b16 a[96:97], v8 offset:512\n"
"ds_read_b64_tr_b16 a[98:99], v8 offset:1536\n"
"v_mfma_f32_16x16x32_bf16 v[116:119], a[100:103], a[60:63], v[116:119]\n"
"ds_read_b64_tr_b16 a[100:101], v8 offset:8704\n"
"ds_read_b64_tr_b16 a[102:103], v8 offset:9728\n"
"v_mfma_f32_16x16x32_bf16 v[36:39], a[104:107], a[32:35], v[36:39]\n"
"v_pk_mul_f32 v[72:73], v[72:73], s[68:69]\n"
"s_waitcnt lgkmcnt(15)\n"
"v_mfma_f32_16x16x32_bf16 v[36:39], a[108:111], a[36:39], v[36:39]\n"
"v_pk_mul_f32 v[74:75], v[74:75], s[68:69]\n"
"v_mfma_f32_16x16x32_bf16 v[64:67], a[104:107], a[40:43], v[64:67]\n"
"v_pk_mul_f32 v[76:77], v[76:77], s[68:69]\n"
"v_mfma_f32_16x16x32_bf16 v[64:67], a[108:111], a[44:47], v[64:67]\n"
"v_pk_mul_f32 v[78:79], v[78:79], s[68:69]\n"
"v_mfma_f32_16x16x32_bf16 v[92:95], a[104:107], a[48:51], v[92:95]\n"
"v_cvt_pk_bf16_f32 v72, v72, v73\n"
"v_cvt_pk_bf16_f32 v73, v74, v75\n"
"v_mfma_f32_16x16x32_bf16 v[92:95], a[108:111], a[52:55], v[92:95]\n"
"v_cvt_pk_bf16_f32 v74, v76, v77\n"
"v_cvt_pk_bf16_f32 v75, v78, v79\n"
"v_mfma_f32_16x16x32_bf16 v[120:123], a[104:107], a[56:59], v[120:123]\n"
"ds_read_b64_tr_b16 a[104:105], v9 offset:512\n"
"ds_read_b64_tr_b16 a[106:107], v9 offset:1536\n"
"v_mfma_f32_16x16x32_bf16 v[120:123], a[108:111], a[60:63], v[120:123]\n"
"ds_read_b64_tr_b16 a[108:109], v9 offset:8704\n"
"ds_read_b64_tr_b16 a[110:111], v9 offset:9728\n"
"v_mfma_f32_16x16x32_bf16 v[40:43], a[112:115], a[32:35], v[40:43]\n"
"v_permlane32_swap_b32 v72, v74\n"
"v_permlane32_swap_b32 v73, v75\n"
"v_mfma_f32_16x16x32_bf16 v[40:43], a[116:119], a[36:39], v[40:43]\n"
"v_permlane16_swap_b32 v72, v74\n"
"v_permlane16_swap_b32 v73, v75\n"
"v_mfma_f32_16x16x32_bf16 v[68:71], a[112:115], a[40:43], v[68:71]\n"
"v_accvgpr_write_b32 a224, v72\n"
"v_accvgpr_write_b32 a225, v73\n"
"v_mfma_f32_16x16x32_bf16 v[68:71], a[116:119], a[44:47], v[68:71]\n"
"v_accvgpr_write_b32 a226, v74\n"
"v_accvgpr_write_b32 a227, v75\n"
"v_mfma_f32_16x16x32_bf16 v[96:99], a[112:115], a[48:51], v[96:99]\n"
"v_pk_mul_f32 v[100:101], v[100:101], s[68:69]\n"
"v_mfma_f32_16x16x32_bf16 v[96:99], a[116:119], a[52:55], v[96:99]\n"
"v_pk_mul_f32 v[102:103], v[102:103], s[68:69]\n"
"v_mfma_f32_16x16x32_bf16 v[124:127], a[112:115], a[56:59], v[124:127]\n"
"ds_read_b64_tr_b16 a[112:113], v8 offset:768\n"
"ds_read_b64_tr_b16 a[114:115], v8 offset:1792\n"
"v_mfma_f32_16x16x32_bf16 v[124:127], a[116:119], a[60:63], v[124:127]\n"
"ds_read_b64_tr_b16 a[116:117], v8 offset:8960\n"
"ds_read_b64_tr_b16 a[118:119], v8 offset:9984\n"
"v_mfma_f32_16x16x32_bf16 a[204:207], a[120:123], a[32:35], a[204:207]\n"
"v_pk_mul_f32 v[104:105], v[104:105], s[68:69]\n"
"v_mfma_f32_16x16x32_bf16 a[204:207], a[124:127], a[36:39], a[204:207]\n"
"v_pk_mul_f32 v[106:107], v[106:107], s[68:69]\n"
"v_mfma_f32_16x16x32_bf16 a[220:223], a[120:123], a[40:43], a[220:223]\n"
"v_cvt_pk_bf16_f32 v100, v100, v101\n"
"v_cvt_pk_bf16_f32 v101, v102, v103\n"
"v_mfma_f32_16x16x32_bf16 a[220:223], a[124:127], a[44:47], a[220:223]\n"
"v_cvt_pk_bf16_f32 v102, v104, v105\n"
"v_cvt_pk_bf16_f32 v103, v106, v107\n"
"v_mfma_f32_16x16x32_bf16 a[236:239], a[120:123], a[48:51], a[236:239]\n"
"v_permlane32_swap_b32 v100, v102\n"
"v_permlane32_swap_b32 v101, v103\n"
"v_mfma_f32_16x16x32_bf16 a[236:239], a[124:127], a[52:55], a[236:239]\n"
"v_permlane16_swap_b32 v100, v102\n"
"v_permlane16_swap_b32 v101, v103\n"
"v_mfma_f32_16x16x32_bf16 a[252:255], a[120:123], a[56:59], a[252:255]\n"
"ds_read_b64_tr_b16 a[120:121], v9 offset:768\n"
"ds_read_b64_tr_b16 a[122:123], v9 offset:1792\n"
"v_mfma_f32_16x16x32_bf16 a[252:255], a[124:127], a[60:63], a[252:255]\n"
"ds_read_b64_tr_b16 a[124:125], v9 offset:8960\n"
"ds_read_b64_tr_b16 a[126:127], v9 offset:9984\n"
"v_mfma_f32_16x16x32_bf16 v[128:131], a[64:67], a[32:35], v[128:131]\n"
"v_accvgpr_write_b32 a240, v100\n"
"v_accvgpr_write_b32 a241, v101\n"
"v_mfma_f32_16x16x32_bf16 v[128:131], a[68:71], a[36:39], v[128:131]\n"
"v_accvgpr_write_b32 a242, v102\n"
"v_accvgpr_write_b32 a243, v103\n"
"v_mfma_f32_16x16x32_bf16 v[160:163], a[64:67], a[40:43], v[160:163]\n"
"v_pk_mul_f32 v[24:25], v[24:25], s[68:69]\n"
"v_mfma_f32_16x16x32_bf16 v[160:163], a[68:71], a[44:47], v[160:163]\n"
"v_pk_mul_f32 v[26:27], v[26:27], s[68:69]\n"
"v_mfma_f32_16x16x32_bf16 v[192:195], a[64:67], a[48:51], v[192:195]\n"
"v_pk_mul_f32 v[28:29], v[28:29], s[68:69]\n"
"v_mfma_f32_16x16x32_bf16 v[192:195], a[68:71], a[52:55], v[192:195]\n"
"v_pk_mul_f32 v[30:31], v[30:31], s[68:69]\n"
"v_mfma_f32_16x16x32_bf16 v[224:227], a[64:67], a[56:59], v[224:227]\n"
"v_cvt_pk_bf16_f32 v24, v24, v25\n"
"v_cvt_pk_bf16_f32 v25, v26, v27\n"
"v_mfma_f32_16x16x32_bf16 v[224:227], a[68:71], a[60:63], v[224:227]\n"
"v_cvt_pk_bf16_f32 v26, v28, v29\n"
"v_cvt_pk_bf16_f32 v27, v30, v31\n"
"s_waitcnt lgkmcnt(15)\n"
"v_mfma_f32_16x16x32_bf16 v[132:135], a[72:75], a[32:35], v[132:135]\n"
"s_waitcnt lgkmcnt(0)\n"
"s_waitcnt vmcnt(63)\n"
"s_barrier\n"
"v_mfma_f32_16x16x32_bf16 v[132:135], a[76:79], a[36:39], v[132:135]\n"
"v_sub_u32 v10, v8, v14\n"
"v_sub_u32 v11, v9, v14\n"
"v_mfma_f32_16x16x32_bf16 v[164:167], a[72:75], a[40:43], v[164:167]\n"
"v_xor_b32 v6, 0, v15\n"
"v_xor_b32 v7, 64, v15\n"
"v_mfma_f32_16x16x32_bf16 v[164:167], a[76:79], a[44:47], v[164:167]\n"
"v_xor_b32 v8, 128, v15\n"
"v_xor_b32 v9, 192, v15\n"
"v_mfma_f32_16x16x32_bf16 v[196:199], a[72:75], a[48:51], v[196:199]\n"
"v_mbcnt_lo_u32_b32 v12, -1, 0\n"
"v_mbcnt_hi_u32_b32 v12, -1, v12\n"
"v_mfma_f32_16x16x32_bf16 v[196:199], a[76:79], a[52:55], v[196:199]\n"
"v_and_b32 v13, 3, v12\n"
"v_lshlrev_b32 v13, 5, v13\n"
"v_mfma_f32_16x16x32_bf16 v[228:231], a[72:75], a[56:59], v[228:231]\n"
"s_lshl_b32 s74, s77, 7\n"
"v_lshlrev_b32 v12, 4, v12\n"
"v_mfma_f32_16x16x32_bf16 v[228:231], a[76:79], a[60:63], v[228:231]\n"
"v_add_u32 v12, s74, v12\n"
"s_mov_b32 s66, 6\n"
"v_mfma_f32_16x16x32_bf16 v[136:139], a[80:83], a[32:35], v[136:139]\n"
"s_add_u32 s36, %[wsl], 0x2000000\n"
"s_addc_u32 s37, %[wsh], 0\n"
"s_and_b32 s37, s37, 0xffff\n"
"s_mov_b32 s38, 0x2000000\n"
"v_mfma_f32_16x16x32_bf16 v[136:139], a[84:87], a[36:39], v[136:139]\n"
"v_pk_mul_f32 v[128:129], v[128:129], s[68:69]\n"
"v_mfma_f32_16x16x32_bf16 v[168:171], a[80:83], a[40:43], v[168:171]\n"
"v_pk_mul_f32 v[130:131], v[130:131], s[68:69]\n"
"v_mfma_f32_16x16x32_bf16 v[168:171], a[84:87], a[44:47], v[168:171]\n"
"v_pk_mul_f32 v[132:133], v[132:133], s[68:69]\n"
"v_mfma_f32_16x16x32_bf16 v[200:203], a[80:83], a[48:51], v[200:203]\n"
"v_pk_mul_f32 v[134:135], v[134:135], s[68:69]\n"
"v_mfma_f32_16x16x32_bf16 v[200:203], a[84:87], a[52:55], v[200:203]\n"
"v_cvt_pk_bf16_f32 v128, v128, v129\n"
"v_cvt_pk_bf16_f32 v129, v130, v131\n"
"v_mfma_f32_16x16x32_bf16 v[232:235], a[80:83], a[56:59], v[232:235]\n"
"v_cvt_pk_bf16_f32 v130, v132, v133\n"
"v_cvt_pk_bf16_f32 v131, v134, v135\n"
"v_mfma_f32_16x16x32_bf16 v[232:235], a[84:87], a[60:63], v[232:235]\n"
"v_permlane32_swap_b32 v128, v130\n"
"v_permlane32_swap_b32 v129, v131\n"
"v_mfma_f32_16x16x32_bf16 v[140:143], a[88:91], a[32:35], v[140:143]\n"
"v_permlane16_swap_b32 v128, v130\n"
"v_permlane16_swap_b32 v129, v131\n"
"v_mfma_f32_16x16x32_bf16 v[140:143], a[92:95], a[36:39], v[140:143]\n"
"v_accvgpr_write_b32 a128, v128\n"
"v_accvgpr_write_b32 a129, v129\n"
"v_mfma_f32_16x16x32_bf16 v[172:175], a[88:91], a[40:43], v[172:175]\n"
"v_accvgpr_write_b32 a130, v130\n"
"v_accvgpr_write_b32 a131, v131\n"
"v_mfma_f32_16x16x32_bf16 v[172:175], a[92:95], a[44:47], v[172:175]\n"
"v_permlane32_swap_b32 v24, v26\n"
"v_permlane32_swap_b32 v25, v27\n"
"v_mfma_f32_16x16x32_bf16 v[204:207], a[88:91], a[48:51], v[204:207]\n"
"v_permlane16_swap_b32 v24, v26\n"
"v_permlane16_swap_b32 v25, v27\n"
"v_mfma_f32_16x16x32_bf16 v[204:207], a[92:95], a[52:55], v[204:207]\n"
"v_accvgpr_write_b32 a196, v24\n"
"v_accvgpr_write_b32 a197, v25\n"
"v_mfma_f32_16x16x32_bf16 v[236:239], a[88:91], a[56:59], v[236:239]\n"
"v_accvgpr_write_b32 a198, v26\n"
"v_accvgpr_write_b32 a199, v27\n"
"v_mfma_f32_16x16x32_bf16 v[236:239], a[92:95], a[60:63], v[236:239]\n"
"v_pk_mul_f32 v[52:53], v[52:53], s[68:69]\n"
"v_mfma_f32_16x16x32_bf16 v[144:147], a[96:99], a[32:35], v[144:147]\n"
"v_pk_mul_f32 v[54:55], v[54:55], s[68:69]\n"
"v_mfma_f32_16x16x32_bf16 v[144:147], a[100:103], a[36:39], v[144:147]\n"
"v_pk_mul_f32 v[56:57], v[56:57], s[68:69]\n"
"v_mfma_f32_16x16x32_bf16 v[176:179], a[96:99], a[40:43], v[176:179]\n"
"v_pk_mul_f32 v[58:59], v[58:59], s[68:69]\n"
"v_mfma_f32_16x16x32_bf16 v[176:179], a[100:103], a[44:47], v[176:179]\n"
"v_cvt_pk_bf16_f32 v52, v52, v53\n"
"v_cvt_pk_bf16_f32 v53, v54, v55\n"
"v_mfma_f32_16x16x32_bf16 v[208:211], a[96:99], a[48:51], v[208:211]\n"
"v_cvt_pk_bf16_f32 v54, v56, v57\n"
"v_cvt_pk_bf16_f32 v55, v58, v59\n"
"v_mfma_f32_16x16x32_bf16 v[208:211], a[100:103], a[52:55], v[208:211]\n"
"v_permlane32_swap_b32 v52, v54\n"
"v_permlane32_swap_b32 v53, v55\n"
"v_mfma_f32_16x16x32_bf16 v[240:243], a[96:99], a[56:59], v[240:243]\n"
"v_permlane16_swap_b32 v52, v54\n"
"v_permlane16_swap_b32 v53, v55\n"
"v_mfma_f32_16x16x32_bf16 v[240:243], a[100:103], a[60:63], v[240:243]\n"
"buffer_load_dword a96, v13, s[36:39], %[ma] offen offset:4 sc1\n"
"buffer_load_dword a97, v13, s[36:39], %[ma] offen offset:132 sc1\n"
"v_mfma_f32_16x16x32_bf16 v[148:151], a[104:107], a[32:35], v[148:151]\n"
"v_accvgpr_write_b32 a212, v52\n"
"v_accvgpr_write_b32 a213, v53\n"
"v_mfma_f32_16x16x32_bf16 v[148:151], a[108:111], a[36:39], v[148:151]\n"
"v_accvgpr_write_b32 a214, v54\n"
"v_accvgpr_write_b32 a215, v55\n"
"v_mfma_f32_16x16x32_bf16 v[180:183], a[104:107], a[40:43], v[180:183]\n"
"v_pk_mul_f32 v[80:81], v[80:81], s[68:69]\n"
"v_mfma_f32_16x16x32_bf16 v[180:183], a[108:111], a[44:47], v[180:183]\n"
"v_pk_mul_f32 v[82:83], v[82:83], s[68:69]\n"
"v_mfma_f32_16x16x32_bf16 v[212:215], a[104:107], a[48:51], v[212:215]\n"
"v_pk_mul_f32 v[84:85], v[84:85], s[68:69]\n"
"v_mfma_f32_16x16x32_bf16 v[212:215], a[108:111], a[52:55], v[212:215]\n"
"ds_read_b128 a[0:3], v6 offset:0\n"
"ds_read_b128 a[4:7], v7 offset:0\n"
"v_mfma_f32_16x16x32_bf16 v[244:247], a[104:107], a[56:59], v[244:247]\n"
"ds_read_b128 a[8:11], v8 offset:0\n"
"ds_read_b128 a[12:15], v9 offset:0\n"
"v_mfma_f32_16x16x32_bf16 v[244:247], a[108:111], a[60:63], v[244:247]\n"
"s_mov_b32 m0, s62\n"
"v_accvgpr_read_b32 v16, a204\n"
"v_mfma_f32_16x16x32_bf16 v[152:155], a[112:115], a[32:35], v[152:155]\n"
"buffer_load_dwordx4 v0, s[44:47], s60 offen offset:0 nt lds\n"
"v_mfma_f32_16x16x32_bf16 v[152:155], a[116:119], a[36:39], v[152:155]\n"
"ds_read_b128 a[16:19], v6 offset:4096\n"
"v_accvgpr_read_b32 v17, a205\n"
"v_mfma_f32_16x16x32_bf16 v[184:187], a[112:115], a[40:43], v[184:187]\n"
"ds_read_b128 a[20:23], v7 offset:4096\n"
"ds_read_b128 a[24:27], v8 offset:4096\n"
"v_mfma_f32_16x16x32_bf16 v[184:187], a[116:119], a[44:47], v[184:187]\n"
"ds_read_b128 a[28:31], v9 offset:4096\n"
"v_accvgpr_read_b32 v18, a206\n"
"v_mfma_f32_16x16x32_bf16 v[216:219], a[112:115], a[48:51], v[216:219]\n"
"v_pk_mul_f32 v[86:87], v[86:87], s[68:69]\n"
"v_mfma_f32_16x16x32_bf16 v[216:219], a[116:119], a[52:55], v[216:219]\n"
"v_cvt_pk_bf16_f32 v80, v80, v81\n"
"v_cvt_pk_bf16_f32 v81, v82, v83\n"
"v_mfma_f32_16x16x32_bf16 v[248:251], a[112:115], a[56:59], v[248:251]\n"
"v_cvt_pk_bf16_f32 v82, v84, v85\n"
"v_cvt_pk_bf16_f32 v83, v86, v87\n"
"v_mfma_f32_16x16x32_bf16 v[248:251], a[116:119], a[60:63], v[248:251]\n"
"v_permlane32_swap_b32 v80, v82\n"
"v_permlane32_swap_b32 v81, v83\n"
"v_mfma_f32_16x16x32_bf16 v[156:159], a[120:123], a[32:35], v[156:159]\n"
"v_permlane16_swap_b32 v80, v82\n"
"v_permlane16_swap_b32 v81, v83\n"
"v_mfma_f32_16x16x32_bf16 v[156:159], a[124:127], a[36:39], v[156:159]\n"
"v_accvgpr_write_b32 a228, v80\n"
"v_accvgpr_write_b32 a229, v81\n"
"v_mfma_f32_16x16x32_bf16 v[188:191], a[120:123], a[40:43], v[188:191]\n"
"v_accvgpr_write_b32 a230, v82\n"
"v_accvgpr_write_b32 a231, v83\n"
"v_mfma_f32_16x16x32_bf16 v[188:191], a[124:127], a[44:47], v[188:191]\n"
"v_pk_mul_f32 v[108:109], v[108:109], s[68:69]\n"
"v_mfma_f32_16x16x32_bf16 v[220:223], a[120:123], a[48:51], v[220:223]\n"
"buffer_load_dwordx4 v1, s[44:47], s60 offen offset:1024 nt lds\n"
"v_mfma_f32_16x16x32_bf16 v[220:223], a[124:127], a[52:55], v[220:223]\n"
"v_pk_mul_f32 v[110:111], v[110:111], s[68:69]\n"
"v_mfma_f32_16x16x32_bf16 v[252:255], a[120:123], a[56:59], v[252:255]\n"
"v_pk_mul_f32 v[112:113], v[112:113], s[68:69]\n"
"v_mfma_f32_16x16x32_bf16 v[252:255], a[124:127], a[60:63], v[252:255]\n"
"v_pk_mul_f32 v[114:115], v[114:115], s[68:69]\n"
"v_cvt_pk_bf16_f32 v108, v108, v109\n"
"v_cvt_pk_bf16_f32 v109, v110, v111\n"
"v_cvt_pk_bf16_f32 v110, v112, v113\n"
"v_cvt_pk_bf16_f32 v111, v114, v115\n"
"s_nop 0\n"
"v_permlane32_swap_b32 v108, v110\n"
"v_permlane32_swap_b32 v109, v111\n"
"s_nop 0\n"
"v_permlane16_swap_b32 v108, v110\n"
"v_permlane16_swap_b32 v109, v111\n"
"v_accvgpr_write_b32 a244, v108\n"
"v_accvgpr_write_b32 a245, v109\n"
"v_accvgpr_write_b32 a246, v110\n"
"v_accvgpr_write_b32 a247, v111\n"
"v_pk_mul_f32 v[32:33], v[32:33], s[68:69]\n"
"v_pk_mul_f32 v[34:35], v[34:35], s[68:69]\n"
"v_pk_mul_f32 v[36:37], v[36:37], s[68:69]\n"
"v_pk_mul_f32 v[38:39], v[38:39], s[68:69]\n"
"v_cvt_pk_bf16_f32 v32, v32, v33\n"
"v_cvt_pk_bf16_f32 v33, v34, v35\n"
"v_cvt_pk_bf16_f32 v34, v36, v37\n"
"v_cvt_pk_bf16_f32 v35, v38, v39\n"
"s_nop 0\n"
"v_permlane32_swap_b32 v32, v34\n"
"v_permlane32_swap_b32 v33, v35\n"
"s_nop 0\n"
"v_permlane16_swap_b32 v32, v34\n"
"v_permlane16_swap_b32 v33, v35\n"
"v_accvgpr_write_b32 a200, v32\n"
"v_accvgpr_write_b32 a201, v33\n"
"v_accvgpr_write_b32 a202, v34\n"
"v_accvgpr_write_b32 a203, v35\n"
"v_pk_mul_f32 v[60:61], v[60:61], s[68:69]\n"
"v_pk_mul_f32 v[62:63], v[62:63], s[68:69]\n"
"v_pk_mul_f32 v[64:65], v[64:65], s[68:69]\n"
"v_pk_mul_f32 v[66:67], v[66:67], s[68:69]\n"
"v_cvt_pk_bf16_f32 v60, v60, v61\n"
"v_cvt_pk_bf16_f32 v61, v62, v63\n"
"v_cvt_pk_bf16_f32 v62, v64, v65\n"
"v_cvt_pk_bf16_f32 v63, v66, v67\n"
"s_nop 0\n"
"v_permlane32_swap_b32 v60, v62\n"
"v_permlane32_swap_b32 v61, v63\n"
"s_nop 0\n"
"v_permlane16_swap_b32 v60, v62\n"
"v_permlane16_swap_b32 v61, v63\n"
"v_accvgpr_write_b32 a216, v60\n"
"v_accvgpr_write_b32 a217, v61\n"
"v_accvgpr_write_b32 a218, v62\n"
"v_accvgpr_write_b32 a219, v63\n"
"v_pk_mul_f32 v[88:89], v[88:89], s[68:69]\n"
"v_pk_mul_f32 v[90:91], v[90:91], s[68:69]\n"
"v_pk_mul_f32 v[92:93], v[92:93], s[68:69]\n"
"v_pk_mul_f32 v[94:95], v[94:95], s[68:69]\n"
"v_cvt_pk_bf16_f32 v88, v88, v89\n"
"v_cvt_pk_bf16_f32 v89, v90, v91\n"
"v_cvt_pk_bf16_f32 v90, v92, v93\n"
"v_cvt_pk_bf16_f32 v91, v94, v95\n"
"s_nop 0\n"
"v_permlane32_swap_b32 v88, v90\n"
"v_permlane32_swap_b32 v89, v91\n"
"s_nop 0\n"
"v_permlane16_swap_b32 v88, v90\n"
"v_permlane16_swap_b32 v89, v91\n"
"v_accvgpr_write_b32 a232, v88\n"
"v_accvgpr_write_b32 a233, v89\n"
"v_accvgpr_write_b32 a234, v90\n"
"v_accvgpr_write_b32 a235, v91\n"
"v_pk_mul_f32 v[116:117], v[116:117], s[68:69]\n"
"v_pk_mul_f32 v[118:119], v[118:119], s[68:69]\n"
"v_pk_mul_f32 v[120:121], v[120:121], s[68:69]\n"
"v_pk_mul_f32 v[122:123], v[122:123], s[68:69]\n"
"v_cvt_pk_bf16_f32 v116, v116, v117\n"
"v_cvt_pk_bf16_f32 v117, v118, v119\n"
"v_cvt_pk_bf16_f32 v118, v120, v121\n"
"v_cvt_pk_bf16_f32 v119, v122, v123\n"
"s_nop 0\n"
"v_permlane32_swap_b32 v116, v118\n"
"v_permlane32_swap_b32 v117, v119\n"
"s_nop 0\n"
"v_permlane16_swap_b32 v116, v118\n"
"v_permlane16_swap_b32 v117, v119\n"
"v_accvgpr_write_b32 a248, v116\n"
"v_accvgpr_write_b32 a249, v117\n"
"v_accvgpr_write_b32 a250, v118\n"
"v_accvgpr_write_b32 a251, v119\n"
"v_accvgpr_read_b32 v19, a207\n"
"v_pk_mul_f32 v[40:41], v[40:41], s[68:69]\n"
"v_pk_mul_f32 v[42:43], v[42:43], s[68:69]\n"
"v_pk_mul_f32 v[16:17], v[16:17], s[68:69]\n"
"v_pk_mul_f32 v[18:19], v[18:19], s[68:69]\n"
"v_cvt_pk_bf16_f32 v40, v40, v41\n"
"v_cvt_pk_bf16_f32 v41, v42, v43\n"
"v_cvt_pk_bf16_f32 v42, v16, v17\n"
"v_cvt_pk_bf16_f32 v43, v18, v19\n"
"s_nop 0\n"
"v_permlane32_swap_b32 v40, v42\n"
"v_permlane32_swap_b32 v41, v43\n"
"s_nop 0\n"
"v_permlane16_swap_b32 v40, v42\n"
"v_permlane16_swap_b32 v41, v43\n"
"v_accvgpr_write_b32 a204, v40\n"
"v_accvgpr_write_b32 a205, v41\n"
"v_accvgpr_write_b32 a206, v42\n"
"v_accvgpr_write_b32 a207, v43\n"
"v_accvgpr_read_b32 v44, a220\n"
"v_accvgpr_read_b32 v45, a221\n"
"v_accvgpr_read_b32 v46, a222\n"
"v_accvgpr_read_b32 v47, a223\n"
"v_pk_mul_f32 v[68:69], v[68:69], s[68:69]\n"
"v_pk_mul_f32 v[70:71], v[70:71], s[68:69]\n"
"v_pk_mul_f32 v[44:45], v[44:45], s[68:69]\n"
"v_pk_mul_f32 v[46:47], v[46:47], s[68:69]\n"
"v_cvt_pk_bf16_f32 v68, v68, v69\n"
"v_cvt_pk_bf16_f32 v69, v70, v71\n"
"v_cvt_pk_bf16_f32 v70, v44, v45\n"
"v_cvt_pk_bf16_f32 v71, v46, v47\n"
"s_nop 0\n"
"v_permlane32_swap_b32 v68, v70\n"
"v_permlane32_swap_b32 v69, v71\n"
"s_nop 0\n"
"v_permlane16_swap_b32 v68, v70\n"
"v_permlane16_swap_b32 v69, v71\n"
"v_accvgpr_write_b32 a220, v68\n"
"v_accvgpr_write_b32 a221, v69\n"
"v_accvgpr_write_b32 a222, v70\n"
"v_accvgpr_write_b32 a223, v71\n"
"v_accvgpr_read_b32 v72, a236\n"
"v_accvgpr_read_b32 v73, a237\n"
"v_accvgpr_read_b32 v74, a238\n"
"v_accvgpr_read_b32 v75, a239\n"
"v_pk_mul_f32 v[96:97], v[96:97], s[68:69]\n"
"v_pk_mul_f32 v[98:99], v[98:99], s[68:69]\n"
"v_pk_mul_f32 v[72:73], v[72:73], s[68:69]\n"
"v_pk_mul_f32 v[74:75], v[74:75], s[68:69]\n"
"v_cvt_pk_bf16_f32 v96, v96, v97\n"
"v_cvt_pk_bf16_f32 v97, v98, v99\n"
"v_cvt_pk_bf16_f32 v98, v72, v73\n"
"v_cvt_pk_bf16_f32 v99, v74, v75\n"
"s_nop 0\n"
"v_permlane32_swap_b32 v96, v98\n"
"v_permlane32_swap_b32 v97, v99\n"
"s_nop 0\n"
"v_permlane16_swap_b32 v96, v98\n"
"v_permlane16_swap_b32 v97, v99\n"
"v_accvgpr_write_b32 a236, v96\n"
"v_accvgpr_write_b32 a237, v97\n"
"v_accvgpr_write_b32 a238, v98\n"
"v_accvgpr_write_b32 a239, v99\n"
"v_accvgpr_read_b32 v100, a252\n"
"v_accvgpr_read_b32 v101, a253\n"
"v_accvgpr_read_b32 v102, a254\n"
"v_accvgpr_read_b32 v103, a255\n"
"v_pk_mul_f32 v[124:125], v[124:125], s[68:69]\n"
"v_pk_mul_f32 v[126:127], v[126:127], s[68:69]\n"
"v_pk_mul_f32 v[100:101], v[100:101], s[68:69]\n"
"v_pk_mul_f32 v[102:103], v[102:103], s[68:69]\n"
"v_cvt_pk_bf16_f32 v124, v124, v125\n"
"v_cvt_pk_bf16_f32 v125, v126, v127\n"
"v_cvt_pk_bf16_f32 v126, v100, v101\n"
"v_cvt_pk_bf16_f32 v127, v102, v103\n"
"s_nop 0\n"
"v_permlane32_swap_b32 v124, v126\n"
"v_permlane32_swap_b32 v125, v127\n"
"s_nop 0\n"
"v_permlane16_swap_b32 v124, v126\n"
"v_permlane16_swap_b32 v125, v127\n"
"v_accvgpr_write_b32 a252, v124\n"
"v_accvgpr_write_b32 a253, v125\n"
"v_accvgpr_write_b32 a254, v126\n"
"v_accvgpr_write_b32 a255, v127\n"
"v_pk_mul_f32 v[136:137], v[136:137], s[68:69]\n"
"v_pk_mul_f32 v[138:139], v[138:139], s[68:69]\n"
"v_pk_mul_f32 v[140:141], v[140:141], s[68:69]\n"
"v_pk_mul_f32 v[142:143], v[142:143], s[68:69]\n"
"v_cvt_pk_bf16_f32 v136, v136, v137\n"
"v_cvt_pk_bf16_f32 v137, v138, v139\n"
"v_cvt_pk_bf16_f32 v138, v140, v141\n"
"v_cvt_pk_bf16_f32 v139, v142, v143\n"
"s_nop 0\n"
"v_permlane32_swap_b32 v136, v138\n"
"v_permlane32_swap_b32 v137, v139\n"
"s_nop 0\n"
"v_permlane16_swap_b32 v136, v138\n"
"v_permlane16_swap_b32 v137, v139\n"
"v_accvgpr_write_b32 a132, v136\n"
"v_accvgpr_write_b32 a133, v137\n"
"v_accvgpr_write_b32 a134, v138\n"
"v_accvgpr_write_b32 a135, v139\n"
"s_waitcnt lgkmcnt(7)\n"
"v_mfma_f32_16x16x32_bf16 v[64:67], a[0:3], a[128:131], 0\n"
"v_pk_mul_f32 v[144:145], v[144:145], s[68:69]\n"
"v_pk_mul_f32 v[146:147], v[146:147], s[68:69]\n"
"v_pk_mul_f32 v[148:149], v[148:149], s[68:69]\n"
"v_pk_mul_f32 v[150:151], v[150:151], s[68:69]\n"
"v_cvt_pk_bf16_f32 v144, v144, v145\n"
"v_cvt_pk_bf16_f32 v145, v146, v147\n"
"v_cvt_pk_bf16_f32 v146, v148, v149\n"
"v_cvt_pk_bf16_f32 v147, v150, v151\n"
"s_nop 0\n"
"v_permlane32_swap_b32 v144, v146\n"
"v_permlane32_swap_b32 v145, v147\n"
"s_nop 0\n"
"v_permlane16_swap_b32 v144, v146\n"
"v_permlane16_swap_b32 v145, v147\n"
"v_accvgpr_write_b32 a136, v144\n"
"v_accvgpr_write_b32 a137, v145\n"
"v_accvgpr_write_b32 a138, v146\n"
"v_accvgpr_write_b32 a139, v147\n"
"s_waitcnt lgkmcnt(6)\n"
"v_mfma_f32_16x16x32_bf16 v[64:67], a[4:7], a[132:135], v[64:67]\n"
"v_pk_mul_f32 v[152:153], v[152:153], s[68:69]\n"
"v_pk_mul_f32 v[154:155], v[154:155], s[68:69]\n"
"v_pk_mul_f32 v[156:157], v[156:157], s[68:69]\n"
"v_pk_mul_f32 v[158:159], v[158:159], s[68:69]\n"
"v_cvt_pk_bf16_f32 v152, v152, v153\n"
"v_cvt_pk_bf16_f32 v153, v154, v155\n"
"v_cvt_pk_bf16_f32 v154, v156, v157\n"
"v_cvt_pk_bf16_f32 v155, v158, v159\n"
"s_nop 0\n"
"v_permlane32_swap_b32 v152, v154\n"
"v_permlane32_swap_b32 v153, v155\n"
"s_nop 0\n"
"v_permlane16_swap_b32 v152, v154\n"
"v_permlane16_swap_b32 v153, v155\n"
"v_accvgpr_write_b32 a140, v152\n"
"v_accvgpr_write_b32 a141, v153\n"
"v_accvgpr_write_b32 a142, v154\n"
"v_accvgpr_write_b32 a143, v155\n"
"s_waitcnt lgkmcnt(5)\n"
"v_mfma_f32_16x16x32_bf16 v[64:67], a[8:11], a[136:139], v[64:67]\n"
"v_pk_mul_f32 v[160:161], v[160:161], s[68:69]\n"
"v_pk_mul_f32 v[162:163], v[162:163], s[68:69]\n"
"v_pk_mul_f32 v[164:165], v[164:165], s[68:69]\n"
"v_pk_mul_f32 v[166:167], v[166:167], s[68:69]\n"
"v_cvt_pk_bf16_f32 v160, v160, v161\n"
"v_cvt_pk_bf16_f32 v161, v162, v163\n"
"v_cvt_pk_bf16_f32 v162, v164, v165\n"
"v_cvt_pk_bf16_f32 v163, v166, v167\n"
"s_nop 0\n"
"v_permlane32_swap_b32 v160, v162\n"
"v_permlane32_swap_b32 v161, v163\n"
"s_nop 0\n"
"v_permlane16_swap_b32 v160, v162\n"
"v_permlane16_swap_b32 v161, v163\n"
"v_accvgpr_write_b32 a144, v160\n"
"v_accvgpr_write_b32 a145, v161\n"
"v_accvgpr_write_b32 a146, v162\n"
"v_accvgpr_write_b32 a147, v163\n"
"s_waitcnt lgkmcnt(4)\n"
"v_mfma_f32_16x16x32_bf16 v[64:67], a[12:15], a[140:143], v[64:67]\n"
"v_pk_mul_f32 v[168:169], v[168:169], s[68:69]\n"
"v_pk_mul_f32 v[170:171], v[170:171], s[68:69]\n"
"v_pk_mul_f32 v[172:173], v[172:173], s[68:69]\n"
"v_pk_mul_f32 v[174:175], v[174:175], s[68:69]\n"
"v_cvt_pk_bf16_f32 v168, v168, v169\n"
"v_cvt_pk_bf16_f32 v169, v170, v171\n"
"v_cvt_pk_bf16_f32 v170, v172, v173\n"
"v_cvt_pk_bf16_f32 v171, v174, v175\n"
"s_nop 0\n"
"v_permlane32_swap_b32 v168, v170\n"
"v_permlane32_swap_b32 v169, v171\n"
"s_nop 0\n"
"v_permlane16_swap_b32 v168, v170\n"
"v_permlane16_swap_b32 v169, v171\n"
"v_accvgpr_write_b32 a148, v168\n"
"v_accvgpr_write_b32 a149, v169\n"
"v_accvgpr_write_b32 a150, v170\n"
"v_accvgpr_write_b32 a151, v171\n"
"v_mfma_f32_16x16x32_bf16 v[80:83], a[0:3], a[144:147], 0\n"
"v_pk_mul_f32 v[176:177], v[176:177], s[68:69]\n"
"v_pk_mul_f32 v[178:179], v[178:179], s[68:69]\n"
"v_pk_mul_f32 v[180:181], v[180:181], s[68:69]\n"
"v_pk_mul_f32 v[182:183], v[182:183], s[68:69]\n"
"v_cvt_pk_bf16_f32 v176, v176, v177\n"
"v_cvt_pk_bf16_f32 v177, v178, v179\n"
"v_cvt_pk_bf16_f32 v178, v180, v181\n"
"v_cvt_pk_bf16_f32 v179, v182, v183\n"
"s_nop 0\n"
"v_permlane32_swap_b32 v176, v178\n"
"v_permlane32_swap_b32 v177, v179\n"
"s_nop 0\n"
"v_permlane16_swap_b32 v176, v178\n"
"v_permlane16_swap_b32 v177, v179\n"
"v_accvgpr_write_b32 a152, v176\n"
"v_accvgpr_write_b32 a153, v177\n"
"v_accvgpr_write_b32 a154, v178\n"
"v_accvgpr_write_b32 a155, v179\n"
"v_mfma_f32_16x16x32_bf16 v[80:83], a[4:7], a[148:151], v[80:83]\n"
"v_pk_mul_f32 v[184:185], v[184:185], s[68:69]\n"
"v_pk_mul_f32 v[186:187], v[186:187], s[68:69]\n"
"v_pk_mul_f32 v[188:189], v[188:189], s[68:69]\n"
"v_pk_mul_f32 v[190:191], v[190:191], s[68:69]\n"
"v_cvt_pk_bf16_f32 v184, v184, v185\n"
"v_cvt_pk_bf16_f32 v185, v186, v187\n"
"v_cvt_pk_bf16_f32 v186, v188, v189\n"
"v_cvt_pk_bf16_f32 v187, v190, v191\n"
"s_nop 0\n"
"v_permlane32_swap_b32 v184, v186\n"
"v_permlane32_swap_b32 v185, v187\n"
"s_nop 0\n"
"v_permlane16_swap_b32 v184, v186\n"
"v_permlane16_swap_b32 v185, v187\n"
"v_accvgpr_write_b32 a156, v184\n"
"v_accvgpr_write_b32 a157, v185\n"
"v_accvgpr_write_b32 a158, v186\n"
"v_accvgpr_write_b32 a159, v187\n"
"v_mfma_f32_16x16x32_bf16 v[80:83], a[8:11], a[152:155], v[80:83]\n"
"v_pk_mul_f32 v[192:193], v[192:193], s[68:69]\n"
"v_pk_mul_f32 v[194:195], v[194:195], s[68:69]\n"
"v_pk_mul_f32 v[196:197], v[196:197], s[68:69]\n"
"v_pk_mul_f32 v[198:199], v[198:199], s[68:69]\n"
"v_cvt_pk_bf16_f32 v192, v192, v193\n"
"v_cvt_pk_bf16_f32 v193, v194, v195\n"
"v_cvt_pk_bf16_f32 v194, v196, v197\n"
"v_cvt_pk_bf16_f32 v195, v198, v199\n"
"s_nop 0\n"
"v_permlane32_swap_b32 v192, v194\n"
"v_permlane32_swap_b32 v193, v195\n"
"s_nop 0\n"
"v_permlane16_swap_b32 v192, v194\n"
"v_permlane16_swap_b32 v193, v195\n"
"v_accvgpr_write_b32 a160, v192\n"
"v_accvgpr_write_b32 a161, v193\n"
"v_accvgpr_write_b32 a162, v194\n"
"v_accvgpr_write_b32 a163, v195\n"
"v_mfma_f32_16x16x32_bf16 v[80:83], a[12:15], a[156:159], v[80:83]\n"
"v_pk_mul_f32 v[200:201], v[200:201], s[68:69]\n"
"v_pk_mul_f32 v[202:203], v[202:203], s[68:69]\n"
"v_pk_mul_f32 v[204:205], v[204:205], s[68:69]\n"
"v_pk_mul_f32 v[206:207], v[206:207], s[68:69]\n"
"v_cvt_pk_bf16_f32 v200, v200, v201\n"
"v_cvt_pk_bf16_f32 v201, v202, v203\n"
"v_cvt_pk_bf16_f32 v202, v204, v205\n"
"v_cvt_pk_bf16_f32 v203, v206, v207\n"
"s_nop 0\n"
"v_permlane32_swap_b32 v200, v202\n"
"v_permlane32_swap_b32 v201, v203\n"
"s_nop 0\n"
"v_permlane16_swap_b32 v200, v202\n"
"v_permlane16_swap_b32 v201, v203\n"
"v_accvgpr_write_b32 a164, v200\n"
"v_accvgpr_write_b32 a165, v201\n"
"v_accvgpr_write_b32 a166, v202\n"
"v_accvgpr_write_b32 a167, v203\n"
"v_mfma_f32_16x16x32_bf16 v[96:99], a[0:3], a[160:163], 0\n"
"buffer_load_dwordx4 v2, s[44:47], s60 offen offset:2048 nt lds\n"
"v_pk_mul_f32 v[208:209], v[208:209], s[68:69]\n"
"v_pk_mul_f32 v[210:211], v[210:211], s[68:69]\n"
"v_pk_mul_f32 v[212:213], v[212:213], s[68:69]\n"
"v_pk_mul_f32 v[214:215], v[214:215], s[68:69]\n"
"v_cvt_pk_bf16_f32 v208, v208, v209\n"
"v_cvt_pk_bf16_f32 v209, v210, v211\n"
"v_cvt_pk_bf16_f32 v210, v212, v213\n"
"v_cvt_pk_bf16_f32 v211, v214, v215\n"
"s_nop 0\n"
"v_permlane32_swap_b32 v208, v210\n"
"v_permlane32_swap_b32 v209, v211\n"
"s_nop 0\n"
"v_permlane16_swap_b32 v208, v210\n"
"v_permlane16_swap_b32 v209, v211\n"
"v_accvgpr_write_b32 a168, v208\n"
"v_accvgpr_write_b32 a169, v209\n"
"v_accvgpr_write_b32 a170, v210\n"
"v_accvgpr_write_b32 a171, v211\n"
"v_mfma_f32_16x16x32_bf16 v[96:99], a[4:7], a[164:167], v[96:99]\n"
"v_pk_mul_f32 v[216:217], v[216:217], s[68:69]\n"
"v_pk_mul_f32 v[218:219], v[218:219], s[68:69]\n"
"v_pk_mul_f32 v[220:221], v[220:221], s[68:69]\n"
"v_pk_mul_f32 v[222:223], v[222:223], s[68:69]\n"
"v_cvt_pk_bf16_f32 v216, v216, v217\n"
"v_cvt_pk_bf16_f32 v217, v218, v219\n"
"v_cvt_pk_bf16_f32 v218, v220, v221\n"
"v_cvt_pk_bf16_f32 v219, v222, v223\n"
"s_nop 0\n"
"v_permlane32_swap_b32 v216, v218\n"
"v_permlane32_swap_b32 v217, v219\n"
"s_nop 0\n"
"v_permlane16_swap_b32 v216, v218\n"
"v_permlane16_swap_b32 v217, v219\n"
"v_accvgpr_write_b32 a172, v216\n"
"v_accvgpr_write_b32 a173, v217\n"
"v_accvgpr_write_b32 a174, v218\n"
"v_accvgpr_write_b32 a175, v219\n"
"v_mfma_f32_16x16x32_bf16 v[96:99], a[8:11], a[168:171], v[96:99]\n"
"v_pk_mul_f32 v[224:225], v[224:225], s[68:69]\n"
"v_pk_mul_f32 v[226:227], v[226:227], s[68:69]\n"
"v_pk_mul_f32 v[228:229], v[228:229], s[68:69]\n"
"v_pk_mul_f32 v[230:231], v[230:231], s[68:69]\n"
"v_cvt_pk_bf16_f32 v224, v224, v225\n"
"v_cvt_pk_bf16_f32 v225, v226, v227\n"
"v_cvt_pk_bf16_f32 v226, v228, v229\n"
"v_cvt_pk_bf16_f32 v227, v230, v231\n"
"s_nop 0\n"
"v_permlane32_swap_b32 v224, v226\n"
"v_permlane32_swap_b32 v225, v227\n"
"s_nop 0\n"
"v_permlane16_swap_b32 v224, v226\n"
"v_permlane16_swap_b32 v225, v227\n"
"v_accvgpr_write_b32 a176, v224\n"
"v_accvgpr_write_b32 a177, v225\n"
"v_accvgpr_write_b32 a178, v226\n"
"v_accvgpr_write_b32 a179, v227\n"
"v_mfma_f32_16x16x32_bf16 v[96:99], a[12:15], a[172:175], v[96:99]\n"
"v_pk_mul_f32 v[232:233], v[232:233], s[68:69]\n"
"v_pk_mul_f32 v[234:235], v[234:235], s[68:69]\n"
"v_pk_mul_f32 v[236:237], v[236:237], s[68:69]\n"
"v_pk_mul_f32 v[238:239], v[238:239], s[68:69]\n"
"v_cvt_pk_bf16_f32 v232, v232, v233\n"
"v_cvt_pk_bf16_f32 v233, v234, v235\n"
"v_cvt_pk_bf16_f32 v234, v236, v237\n"
"v_cvt_pk_bf16_f32 v235, v238, v239\n"
"s_nop 0\n"
"v_permlane32_swap_b32 v232, v234\n"
"v_permlane32_swap_b32 v233, v235\n"
"s_nop 0\n"
"v_permlane16_swap_b32 v232, v234\n"
"v_permlane16_swap_b32 v233, v235\n"
"v_accvgpr_write_b32 a180, v232\n"
"v_accvgpr_write_b32 a181, v233\n"
"v_accvgpr_write_b32 a182, v234\n"
"v_accvgpr_write_b32 a183, v235\n"
"v_mfma_f32_16x16x32_bf16 v[112:115], a[0:3], a[176:179], 0\n"
"ds_read_b128 a[0:3], v6 offset:8192\n"
"v_pk_mul_f32 v[240:241], v[240:241], s[68:69]\n"
"v_pk_mul_f32 v[242:243], v[242:243], s[68:69]\n"
"v_pk_mul_f32 v[244:245], v[244:245], s[68:69]\n"
"v_pk_mul_f32 v[246:247], v[246:247], s[68:69]\n"
"v_cvt_pk_bf16_f32 v240, v240, v241\n"
"v_cvt_pk_bf16_f32 v241, v242, v243\n"
"v_cvt_pk_bf16_f32 v242, v244, v245\n"
"v_cvt_pk_bf16_f32 v243, v246, v247\n"
"s_nop 0\n"
"v_permlane32_swap_b32 v240, v242\n"
"v_permlane32_swap_b32 v241, v243\n"
"s_nop 0\n"
"v_permlane16_swap_b32 v240, v242\n"
"v_permlane16_swap_b32 v241, v243\n"
"v_accvgpr_write_b32 a184, v240\n"
"v_accvgpr_write_b32 a185, v241\n"
"v_accvgpr_write_b32 a186, v242\n"
"v_accvgpr_write_b32 a187, v243\n"
"v_mfma_f32_16x16x32_bf16 v[112:115], a[4:7], a[180:183], v[112:115]\n"
"ds_read_b128 a[4:7], v7 offset:8192\n"
"v_pk_mul_f32 v[248:249], v[248:249], s[68:69]\n"
"v_pk_mul_f32 v[250:251], v[250:251], s[68:69]\n"
"v_pk_mul_f32 v[252:253], v[252:253], s[68:69]\n"
"v_pk_mul_f32 v[254:255], v[254:255], s[68:69]\n"
"v_cvt_pk_bf16_f32 v248, v248, v249\n"
"v_cvt_pk_bf16_f32 v249, v250, v251\n"
"v_cvt_pk_bf16_f32 v250, v252, v253\n"
"v_cvt_pk_bf16_f32 v251, v254, v255\n"
"s_nop 0\n"
"v_permlane32_swap_b32 v248, v250\n"
"v_permlane32_swap_b32 v249, v251\n"
"s_nop 0\n"
"v_permlane16_swap_b32 v248, v250\n"
"v_permlane16_swap_b32 v249, v251\n"
"v_accvgpr_write_b32 a188, v248\n"
"v_accvgpr_write_b32 a189, v249\n"
"v_accvgpr_write_b32 a190, v250\n"
"v_accvgpr_write_b32 a191, v251\n"
"v_mfma_f32_16x16x32_bf16 v[112:115], a[8:11], a[184:187], v[112:115]\n"
"ds_read_b128 a[8:11], v8 offset:8192\n"
"v_max3_f32 v16, v64, v65, v66\n"
"v_mfma_f32_16x16x32_bf16 v[112:115], a[12:15], a[188:191], v[112:115]\n"
"ds_read_b128 a[12:15], v9 offset:8192\n"
"v_max_f32 v16, v16, v67\n"
"v_mov_b32 v17, v16\n"
"s_nop 1\n"
"v_permlane32_swap_b32 v16, v17\n"
"v_max_f32 v16, v16, v17\n"
"v_mov_b32 v17, v16\n"
"s_nop 1\n"
"v_permlane16_swap_b32 v16, v17\n"
"v_min_f32 v18, -v16, -v17\n"
"v_mov_b32 v19, v18\n"
"v_mov_b32 v16, v18\n"
"v_mov_b32 v17, v18\n"
"s_waitcnt lgkmcnt(7)\n"
"s_nop 0\n"
"v_mfma_f32_16x16x32_bf16 v[68:71], a[16:19], a[128:131], v[16:19]\n"
"v_max3_f32 v20, v80, v81, v82\n"
"v_max_f32 v20, v20, v83\n"
"s_waitcnt lgkmcnt(6)\n"
"v_mfma_f32_16x16x32_bf16 v[68:71], a[20:23], a[132:135], v[68:71]\n"
"v_mov_b32 v21, v20\n"
"s_nop 1\n"
"v_permlane32_swap_b32 v20, v21\n"
"s_waitcnt lgkmcnt(5)\n"
"v_mfma_f32_16x16x32_bf16 v[68:71], a[24:27], a[136:139], v[68:71]\n"
"v_max_f32 v20, v20, v21\n"
"v_mov_b32 v21, v20\n"
"s_waitcnt lgkmcnt(4)\n"
"v_mfma_f32_16x16x32_bf16 v[68:71], a[28:31], a[140:143], v[68:71]\n"
"v_permlane16_swap_b32 v20, v21\n"
"v_min_f32 v22, -v20, -v21\n"
"v_mov_b32 v23, v22\n"
"v_mov_b32 v20, v22\n"
"v_mov_b32 v21, v22\n"
"s_nop 1\n"
"v_mfma_f32_16x16x32_bf16 v[84:87], a[16:19], a[144:147], v[20:23]\n"
"buffer_load_dwordx4 v3, s[44:47], s60 offen offset:3072 nt lds\n"
"v_mfma_f32_16x16x32_bf16 v[84:87], a[20:23], a[148:151], v[84:87]\n"
"v_max3_f32 v24, v96, v97, v98\n"
"v_max_f32 v24, v24, v99\n"
"v_mfma_f32_16x16x32_bf16 v[84:87], a[24:27], a[152:155], v[84:87]\n"
"v_mov_b32 v25, v24\n"
"s_nop 1\n"
"v_permlane32_swap_b32 v24, v25\n"
"v_mfma_f32_16x16x32_bf16 v[84:87], a[28:31], a[156:159], v[84:87]\n"
"v_max_f32 v24, v24, v25\n"
"v_mov_b32 v25, v24\n"
"s_nop 1\n"
"v_permlane16_swap_b32 v24, v25\n"
"v_min_f32 v26, -v24, -v25\n"
"v_mov_b32 v27, v26\n"
"v_mov_b32 v24, v26\n"
"v_mov_b32 v25, v26\n"
"s_nop 1\n"
"v_mfma_f32_16x16x32_bf16 v[100:103], a[16:19], a[160:163], v[24:27]\n"
"v_max3_f32 v28, v112, v113, v114\n"
"v_max_f32 v28, v28, v115\n"
"v_mfma_f32_16x16x32_bf16 v[100:103], a[20:23], a[164:167], v[100:103]\n"
"v_mov_b32 v29, v28\n"
"s_nop 1\n"
"v_permlane32_swap_b32 v28, v29\n"
"v_mfma_f32_16x16x32_bf16 v[100:103], a[24:27], a[168:171], v[100:103]\n"
"v_max_f32 v28, v28, v29\n"
"v_mov_b32 v29, v28\n"
"v_mfma_f32_16x16x32_bf16 v[100:103], a[28:31], a[172:175], v[100:103]\n"
"s_nop 0\n"
"v_permlane16_swap_b32 v28, v29\n"
"v_min_f32 v30, -v28, -v29\n"
"v_mov_b32 v31, v30\n"
"v_mov_b32 v28, v30\n"
"v_mov_b32 v29, v30\n"
"s_nop 1\n"
"v_mfma_f32_16x16x32_bf16 v[116:119], a[16:19], a[176:179], v[28:31]\n"
"ds_read_b128 a[16:19], v6 offset:12288\n"
"v_add_f32 v64, v64, v18\n"
"v_mfma_f32_16x16x32_bf16 v[116:119], a[20:23], a[180:183], v[116:119]\n"
"ds_read_b128 a[20:23], v7 offset:12288\n"
"v_add_f32 v65, v65, v18\n"
"v_mfma_f32_16x16x32_bf16 v[116:119], a[24:27], a[184:187], v[116:119]\n"
"ds_read_b128 a[24:27], v8 offset:12288\n"
"v_add_f32 v66, v66, v18\n"
"v_mfma_f32_16x16x32_bf16 v[116:119], a[28:31], a[188:191], v[116:119]\n"
"s_mov_b32 m0, s63\n"
"ds_read_b128 a[28:31], v9 offset:12288\n"
"s_waitcnt lgkmcnt(7)\n"
"v_mfma_f32_16x16x32_bf16 v[72:75], a[0:3], a[128:131], v[16:19]\n"
"buffer_load_dwordx4 v4, s[48:51], s61 offen offset:0 nt lds\n"
"s_waitcnt lgkmcnt(6)\n"
"v_mfma_f32_16x16x32_bf16 v[72:75], a[4:7], a[132:135], v[72:75]\n"
"v_exp_f32 v64, v64\n"
"s_waitcnt lgkmcnt(5)\n"
"v_mfma_f32_16x16x32_bf16 v[72:75], a[8:11], a[136:139], v[72:75]\n"
"v_exp_f32 v65, v65\n"
"s_waitcnt lgkmcnt(4)\n"
"v_mfma_f32_16x16x32_bf16 v[72:75], a[12:15], a[140:143], v[72:75]\n"
"v_exp_f32 v66, v66\n"
"v_mfma_f32_16x16x32_bf16 v[88:91], a[0:3], a[144:147], v[20:23]\n"
"v_add_f32 v67, v67, v18\n"
"v_add_f32 v80, v80, v22\n"
"v_mfma_f32_16x16x32_bf16 v[88:91], a[4:7], a[148:151], v[88:91]\n"
"v_exp_f32 v67, v67\n"
"v_mfma_f32_16x16x32_bf16 v[88:91], a[8:11], a[152:155], v[88:91]\n"
"v_exp_f32 v68, v68\n"
"v_mfma_f32_16x16x32_bf16 v[88:91], a[12:15], a[156:159], v[88:91]\n"
"v_exp_f32 v69, v69\n"
"v_mfma_f32_16x16x32_bf16 v[104:107], a[0:3], a[160:163], v[24:27]\n"
"v_exp_f32 v70, v70\n"
"v_mfma_f32_16x16x32_bf16 v[104:107], a[4:7], a[164:167], v[104:107]\n"
"v_exp_f32 v71, v71\n"
"v_mfma_f32_16x16x32_bf16 v[104:107], a[8:11], a[168:171], v[104:107]\n"
"v_exp_f32 v84, v84\n"
"v_mfma_f32_16x16x32_bf16 v[104:107], a[12:15], a[172:175], v[104:107]\n"
"v_exp_f32 v85, v85\n"
"v_mfma_f32_16x16x32_bf16 v[120:123], a[0:3], a[176:179], v[28:31]\n"
"buffer_load_dwordx4 v5, s[48:51], s61 offen offset:1024 nt lds\n"
"v_mfma_f32_16x16x32_bf16 v[120:123], a[4:7], a[180:183], v[120:123]\n"
"v_exp_f32 v86, v86\n"
"v_mfma_f32_16x16x32_bf16 v[120:123], a[8:11], a[184:187], v[120:123]\n"
"v_exp_f32 v87, v87\n"
"v_mfma_f32_16x16x32_bf16 v[120:123], a[12:15], a[188:191], v[120:123]\n"
"v_exp_f32 v100, v100\n"
"s_waitcnt lgkmcnt(3)\n"
"v_mfma_f32_16x16x32_bf16 v[76:79], a[16:19], a[128:131], v[16:19]\n"
"v_exp_f32 v101, v101\n"
"s_waitcnt lgkmcnt(2)\n"
"v_mfma_f32_16x16x32_bf16 v[76:79], a[20:23], a[132:135], v[76:79]\n"
"v_exp_f32 v102, v102\n"
"s_waitcnt lgkmcnt(1)\n"
"v_mfma_f32_16x16x32_bf16 v[76:79], a[24:27], a[136:139], v[76:79]\n"
"v_exp_f32 v103, v103\n"
"s_waitcnt lgkmcnt(0)\n"
"v_mfma_f32_16x16x32_bf16 v[76:79], a[28:31], a[140:143], v[76:79]\n"
"v_exp_f32 v116, v116\n"
"v_mfma_f32_16x16x32_bf16 v[92:95], a[16:19], a[144:147], v[20:23]\n"
"v_exp_f32 v117, v117\n"
"v_mfma_f32_16x16x32_bf16 v[92:95], a[20:23], a[148:151], v[92:95]\n"
"v_exp_f32 v118, v118\n"
"v_mfma_f32_16x16x32_bf16 v[92:95], a[24:27], a[152:155], v[92:95]\n"
"v_exp_f32 v119, v119\n"
"v_mfma_f32_16x16x32_bf16 v[92:95], a[28:31], a[156:159], v[92:95]\n"
"v_exp_f32 v72, v72\n"
"v_mfma_f32_16x16x32_bf16 v[108:111], a[16:19], a[160:163], v[24:27]\n"
"buffer_load_dwordx4 v4, s[48:51], s61 offen offset:2048 nt lds\n"
"v_mfma_f32_16x16x32_bf16 v[108:111], a[20:23], a[164:167], v[108:111]\n"
"v_exp_f32 v73, v73\n"
"v_mfma_f32_16x16x32_bf16 v[108:111], a[24:27], a[168:171], v[108:111]\n"
"v_exp_f32 v74, v74\n"
"v_mfma_f32_16x16x32_bf16 v[108:111], a[28:31], a[172:175], v[108:111]\n"
"v_exp_f32 v75, v75\n"
"v_mfma_f32_16x16x32_bf16 v[124:127], a[16:19], a[176:179], v[28:31]\n"
"v_exp_f32 v88, v88\n"
"v_mfma_f32_16x16x32_bf16 v[124:127], a[20:23], a[180:183], v[124:127]\n"
"v_exp_f32 v89, v89\n"
"v_mfma_f32_16x16x32_bf16 v[124:127], a[24:27], a[184:187], v[124:127]\n"
"v_exp_f32 v90, v90\n"
"v_mfma_f32_16x16x32_bf16 v[124:127], a[28:31], a[188:191], v[124:127]\n"
"v_exp_f32 v91, v91\n"
"v_exp_f32 v104, v104\n"
"v_exp_f32 v105, v105\n"
"v_exp_f32 v106, v106\n"
"v_exp_f32 v107, v107\n"
"buffer_load_dwordx4 v5, s[48:51], s61 offen offset:3072 nt lds\n"
"v_xor_b32 v6, s64, v6\n"
"v_xor_b32 v7, s64, v7\n"
"v_xor_b32 v8, s64, v8\n"
"v_xor_b32 v9, s64, v9\n"
"v_xor_b32 v10, s65, v10\n"
"v_xor_b32 v11, s65, v11\n"
"s_xor_b32 s62, s62, s65\n"
"s_xor_b32 s63, s63, s64\n"
"s_add_u32 s60, s60, 0x4000\n"
"s_add_u32 s61, s61, 0x4000\n"
"s_xor_b32 s64, s64, 0xc000\n"
"s_xor_b32 s65, s65, 0xc000\n"
"v_exp_f32 v120, v120\n"
"v_exp_f32 v121, v121\n"
"v_exp_f32 v122, v122\n"
"v_exp_f32 v123, v123\n"
"v_exp_f32 v76, v76\n"
"v_cvt_pk_bf16_f32 v32, v64, v65\n"
"v_cvt_pk_bf16_f32 v33, v66, v67\n"
"v_cvt_pk_bf16_f32 v34, v68, v69\n"
"v_cvt_pk_bf16_f32 v35, v70, v71\n"
"v_cvt_pk_bf16_f32 v36, v72, v73\n"
"v_cvt_pk_bf16_f32 v37, v74, v75\n"
"v_exp_f32 v77, v77\n"
"v_exp_f32 v78, v78\n"
"v_exp_f32 v79, v79\n"
"v_cvt_pk_bf16_f32 v38, v76, v77\n"
"v_cvt_pk_bf16_f32 v39, v78, v79\n"
"v_exp_f32 v92, v92\n"
"v_exp_f32 v93, v93\n"
"v_exp_f32 v94, v94\n"
"v_exp_f32 v95, v95\n"
"v_exp_f32 v108, v108\n"
"v_exp_f32 v109, v109\n"
"v_exp_f32 v110, v110\n"
"v_exp_f32 v111, v111\n"
"v_exp_f32 v124, v124\n"
"v_exp_f32 v125, v125\n"
"v_exp_f32 v126, v126\n"
"v_exp_f32 v80, v80\n"
"v_add_f32 v81, v81, v22\n"
"v_exp_f32 v81, v81\n"
"s_nop 0\n"
"v_cvt_pk_bf16_f32 v40, v80, v81\n"
"v_add_f32 v82, v82, v22\n"
"v_exp_f32 v82, v82\n"
"v_add_f32 v83, v83, v22\n"
"v_exp_f32 v83, v83\n"
"s_nop 0\n"
"v_cvt_pk_bf16_f32 v41, v82, v83\n"
"v_cvt_pk_bf16_f32 v42, v84, v85\n"
"v_cvt_pk_bf16_f32 v43, v86, v87\n"
"v_cvt_pk_bf16_f32 v44, v88, v89\n"
"v_cvt_pk_bf16_f32 v45, v90, v91\n"
"v_cvt_pk_bf16_f32 v46, v92, v93\n"
"v_cvt_pk_bf16_f32 v47, v94, v95\n"
"v_exp_f32 v127, v127\n"
"v_add_f32 v96, v96, v26\n"
"v_add_f32 v97, v97, v26\n"
"v_exp_f32 v96, v96\n"
"v_exp_f32 v97, v97\n"
"v_add_f32 v98, v98, v26\n"
"v_add_f32 v99, v99, v26\n"
"v_exp_f32 v98, v98\n"
"v_exp_f32 v99, v99\n"
"v_add_f32 v112, v112, v30\n"
"v_add_f32 v113, v113, v30\n"
"v_exp_f32 v112, v112\n"
"v_exp_f32 v113, v113\n"
"v_add_f32 v114, v114, v30\n"
"v_add_f32 v115, v115, v30\n"
"v_exp_f32 v114, v114\n"
"v_exp_f32 v115, v115\n"
"v_mov_b32 v14, 0x3f803f80\n"
"v_accvgpr_write_b32 a112, v14\n"
"s_waitcnt vmcnt(63)\n"
"s_barrier\n"
"ds_read_b128 a[0:3], v6 offset:0\n"
"ds_read_b128 a[4:7], v7 offset:0\n"
"ds_read_b128 a[8:11], v8 offset:0\n"
"ds_read_b128 a[12:15], v9 offset:0\n"
"s_mov_b32 m0, s62\n"
"v_cvt_pk_bf16_f32 v48, v96, v97\n"
"buffer_load_dwordx4 v0, s[44:47], s60 offen offset:0 nt lds\n"
"v_cvt_pk_bf16_f32 v49, v98, v99\n"
"ds_read_b128 a[16:19], v6 offset:4096\n"
"ds_read_b128 a[20:23], v7 offset:4096\n"
"ds_read_b128 a[24:27], v8 offset:4096\n"
"ds_read_b128 a[28:31], v9 offset:4096\n"
"v_cvt_pk_bf16_f32 v50, v100, v101\n"
"v_cvt_pk_bf16_f32 v51, v102, v103\n"
"v_cvt_pk_bf16_f32 v52, v104, v105\n"
"v_cvt_pk_bf16_f32 v53, v106, v107\n"
"v_cvt_pk_bf16_f32 v54, v108, v109\n"
"v_cvt_pk_bf16_f32 v55, v110, v111\n"
"ds_read_b64_tr_b16 a[32:33], v10 offset:0\n"
"ds_read_b64_tr_b16 a[34:35], v10 offset:4096\n"
"ds_read_b64_tr_b16 a[36:37], v10 offset:8192\n"
"ds_read_b64_tr_b16 a[38:39], v10 offset:12288\n"
"ds_read_b64_tr_b16 a[40:41], v11 offset:0\n"
"ds_read_b64_tr_b16 a[42:43], v11 offset:4096\n"
"ds_read_b64_tr_b16 a[44:45], v11 offset:8192\n"
"ds_read_b64_tr_b16 a[46:47], v11 offset:12288\n"
"ds_read_b64_tr_b16 a[48:49], v10 offset:256\n"
"ds_read_b64_tr_b16 a[50:51], v10 offset:4352\n"
"ds_read_b64_tr_b16 a[52:53], v10 offset:8448\n"
"buffer_load_dwordx4 v1, s[44:47], s60 offen offset:1024 nt lds\n"
"ds_read_b64_tr_b16 a[54:55], v10 offset:12544\n"
"ds_read_b64_tr_b16 a[56:57], v11 offset:256\n"
"v_cvt_pk_bf16_f32 v56, v112, v113\n"
"v_cvt_pk_bf16_f32 v57, v114, v115\n"
"v_accvgpr_write_b32 a113, v14\n"
"v_accvgpr_write_b32 a114, v14\n"
"s_waitcnt lgkmcnt(15)\n"
"v_mfma_f32_16x16x32_bf16 v[64:67], a[0:3], a[128:131], v[16:19]\n"
"v_cvt_pk_bf16_f32 v58, v116, v117\n"
"v_cvt_pk_bf16_f32 v59, v118, v119\n"
"v_accvgpr_write_b32 a115, v14\n"
"v_mfma_f32_16x16x32_bf16 v[64:67], a[4:7], a[132:135], v[64:67]\n"
"v_cvt_pk_bf16_f32 v60, v120, v121\n"
"v_cvt_pk_bf16_f32 v61, v122, v123\n"
"v_mfma_f32_16x16x32_bf16 v[64:67], a[8:11], a[136:139], v[64:67]\n"
"v_cvt_pk_bf16_f32 v62, v124, v125\n"
"v_cvt_pk_bf16_f32 v63, v126, v127\n"
"v_mfma_f32_16x16x32_bf16 v[64:67], a[12:15], a[140:143], v[64:67]\n"
"ds_read_b64_tr_b16 a[58:59], v11 offset:4352\n"
"ds_read_b64_tr_b16 a[60:61], v11 offset:8448\n"
"v_mfma_f32_16x16x32_bf16 v[80:83], a[0:3], a[144:147], v[20:23]\n"
"ds_read_b64_tr_b16 a[62:63], v11 offset:12544\n"
"ds_read_b64_tr_b16 a[64:65], v10 offset:512\n"
"v_mfma_f32_16x16x32_bf16 v[80:83], a[4:7], a[148:151], v[80:83]\n"
"ds_read_b64_tr_b16 a[66:67], v10 offset:4608\n"
"ds_read_b64_tr_b16 a[68:69], v10 offset:8704\n"
"v_mfma_f32_16x16x32_bf16 v[80:83], a[8:11], a[152:155], v[80:83]\n"
"ds_read_b64_tr_b16 a[70:71], v10 offset:12800\n"
"ds_read_b64_tr_b16 a[72:73], v11 offset:512\n"
"v_mfma_f32_16x16x32_bf16 v[80:83], a[12:15], a[156:159], v[80:83]\n"
"ds_read_b64_tr_b16 a[74:75], v11 offset:4608\n"
"ds_read_b64_tr_b16 a[76:77], v11 offset:8704\n"
"v_mfma_f32_16x16x32_bf16 v[96:99], a[0:3], a[160:163], v[24:27]\n"
"buffer_load_dwordx4 v2, s[44:47], s60 offen offset:2048 nt lds\n"
"v_mfma_f32_16x16x32_bf16 v[96:99], a[4:7], a[164:167], v[96:99]\n"
"ds_read_b64_tr_b16 a[78:79], v11 offset:12800\n"
"ds_read_b64_tr_b16 a[80:81], v10 offset:768\n"
"v_mfma_f32_16x16x32_bf16 v[96:99], a[8:11], a[168:171], v[96:99]\n"
"ds_read_b64_tr_b16 a[82:83], v10 offset:4864\n"
"ds_read_b64_tr_b16 a[84:85], v10 offset:8960\n"
"v_mfma_f32_16x16x32_bf16 v[96:99], a[12:15], a[172:175], v[96:99]\n"
"ds_read_b64_tr_b16 a[86:87], v10 offset:13056\n"
"ds_read_b64_tr_b16 a[88:89], v11 offset:768\n"
"v_mfma_f32_16x16x32_bf16 v[112:115], a[0:3], a[176:179], v[28:31]\n"
"ds_read_b128 a[0:3], v6 offset:8192\n"
"ds_read_b64_tr_b16 a[90:91], v11 offset:4864\n"
"v_mfma_f32_16x16x32_bf16 v[112:115], a[4:7], a[180:183], v[112:115]\n"
"ds_read_b128 a[4:7], v7 offset:8192\n"
"ds_read_b64_tr_b16 a[92:93], v11 offset:8960\n"
"v_mfma_f32_16x16x32_bf16 v[112:115], a[8:11], a[184:187], v[112:115]\n"
"ds_read_b128 a[8:11], v8 offset:8192\n"
"ds_read_b64_tr_b16 a[94:95], v11 offset:13056\n"
"v_mfma_f32_16x16x32_bf16 v[112:115], a[12:15], a[188:191], v[112:115]\n"
"ds_read_b128 a[12:15], v9 offset:8192\n"
"v_mfma_f32_16x16x32_bf16 v[68:71], a[16:19], a[128:131], v[16:19]\n"
"v_exp_f32 v64, v64\n"
"v_mfma_f32_16x16x32_bf16 v[68:71], a[20:23], a[132:135], v[68:71]\n"
"v_exp_f32 v65, v65\n"
"s_waitcnt lgkmcnt(15)\n"
"v_mfma_f32_16x16x32_bf16 v[68:71], a[24:27], a[136:139], v[68:71]\n"
"v_exp_f32 v66, v66\n"
"v_mfma_f32_16x16x32_bf16 v[68:71], a[28:31], a[140:143], v[68:71]\n"
"v_exp_f32 v67, v67\n"
"v_mfma_f32_16x16x32_bf16 v[84:87], a[16:19], a[144:147], v[20:23]\n"
"buffer_load_dwordx4 v3, s[44:47], s60 offen offset:3072 nt lds\n"
"v_mfma_f32_16x16x32_bf16 v[84:87], a[20:23], a[148:151], v[84:87]\n"
"v_exp_f32 v80, v80\n"
"v_mfma_f32_16x16x32_bf16 v[84:87], a[24:27], a[152:155], v[84:87]\n"
"v_exp_f32 v81, v81\n"
"v_mfma_f32_16x16x32_bf16 v[84:87], a[28:31], a[156:159], v[84:87]\n"
"v_exp_f32 v82, v82\n"
"v_mfma_f32_16x16x32_bf16 v[100:103], a[16:19], a[160:163], v[24:27]\n"
"v_exp_f32 v83, v83\n"
"v_mfma_f32_16x16x32_bf16 v[100:103], a[20:23], a[164:167], v[100:103]\n"
"v_exp_f32 v96, v96\n"
"v_mfma_f32_16x16x32_bf16 v[100:103], a[24:27], a[168:171], v[100:103]\n"
"v_exp_f32 v97, v97\n"
"v_mfma_f32_16x16x32_bf16 v[100:103], a[28:31], a[172:175], v[100:103]\n"
"v_exp_f32 v98, v98\n"
"v_mfma_f32_16x16x32_bf16 v[116:119], a[16:19], a[176:179], v[28:31]\n"
"ds_read_b128 a[16:19], v6 offset:12288\n"
"v_mfma_f32_16x16x32_bf16 v[116:119], a[20:23], a[180:183], v[116:119]\n"
"ds_read_b128 a[20:23], v7 offset:12288\n"
"v_mfma_f32_16x16x32_bf16 v[116:119], a[24:27], a[184:187], v[116:119]\n"
"ds_read_b128 a[24:27], v8 offset:12288\n"
"v_mfma_f32_16x16x32_bf16 v[116:119], a[28:31], a[188:191], v[116:119]\n"
"s_mov_b32 m0, s63\n"
"ds_read_b128 a[28:31], v9 offset:12288\n"
"s_waitcnt lgkmcnt(10)\n"
"v_mfma_f32_16x16x32_bf16 v[72:75], a[0:3], a[128:131], v[16:19]\n"
"buffer_load_dwordx4 v4, s[48:51], s61 offen offset:0 nt lds\n"
"s_waitcnt lgkmcnt(8)\n"
"v_mfma_f32_16x16x32_bf16 v[72:75], a[4:7], a[132:135], v[72:75]\n"
"v_exp_f32 v99, v99\n"
"s_waitcnt lgkmcnt(6)\n"
"v_mfma_f32_16x16x32_bf16 v[72:75], a[8:11], a[136:139], v[72:75]\n"
"v_exp_f32 v112, v112\n"
"s_waitcnt lgkmcnt(4)\n"
"v_mfma_f32_16x16x32_bf16 v[72:75], a[12:15], a[140:143], v[72:75]\n"
"v_exp_f32 v113, v113\n"
"v_mfma_f32_16x16x32_bf16 v[88:91], a[0:3], a[144:147], v[20:23]\n"
"v_exp_f32 v114, v114\n"
"v_mfma_f32_16x16x32_bf16 v[88:91], a[4:7], a[148:151], v[88:91]\n"
"v_exp_f32 v115, v115\n"
"v_mfma_f32_16x16x32_bf16 v[88:91], a[8:11], a[152:155], v[88:91]\n"
"v_exp_f32 v68, v68\n"
"v_mfma_f32_16x16x32_bf16 v[88:91], a[12:15], a[156:159], v[88:91]\n"
"v_exp_f32 v69, v69\n"
"v_mfma_f32_16x16x32_bf16 v[104:107], a[0:3], a[160:163], v[24:27]\n"
"v_exp_f32 v70, v70\n"
"v_mfma_f32_16x16x32_bf16 v[104:107], a[4:7], a[164:167], v[104:107]\n"
"v_exp_f32 v71, v71\n"
"v_mfma_f32_16x16x32_bf16 v[104:107], a[8:11], a[168:171], v[104:107]\n"
"v_exp_f32 v84, v84\n"
"v_mfma_f32_16x16x32_bf16 v[104:107], a[12:15], a[172:175], v[104:107]\n"
"v_exp_f32 v85, v85\n"
"v_mfma_f32_16x16x32_bf16 v[120:123], a[0:3], a[176:179], v[28:31]\n"
"buffer_load_dwordx4 v5, s[48:51], s61 offen offset:1024 nt lds\n"
"v_mfma_f32_16x16x32_bf16 v[120:123], a[4:7], a[180:183], v[120:123]\n"
"v_exp_f32 v86, v86\n"
"v_mfma_f32_16x16x32_bf16 v[120:123], a[8:11], a[184:187], v[120:123]\n"
"v_exp_f32 v87, v87\n"
"v_mfma_f32_16x16x32_bf16 v[120:123], a[12:15], a[188:191], v[120:123]\n"
"v_exp_f32 v100, v100\n"
"s_waitcnt lgkmcnt(3)\n"
"v_mfma_f32_16x16x32_bf16 v[76:79], a[16:19], a[128:131], v[16:19]\n"
"v_exp_f32 v101, v101\n"
"s_waitcnt lgkmcnt(2)\n"
"v_mfma_f32_16x16x32_bf16 v[76:79], a[20:23], a[132:135], v[76:79]\n"
"v_exp_f32 v102, v102\n"
"s_waitcnt lgkmcnt(1)\n"
"v_mfma_f32_16x16x32_bf16 v[76:79], a[24:27], a[136:139], v[76:79]\n"
"v_exp_f32 v103, v103\n"
"s_waitcnt lgkmcnt(0)\n"
"v_mfma_f32_16x16x32_bf16 v[76:79], a[28:31], a[140:143], v[76:79]\n"
"v_exp_f32 v116, v116\n"
"v_mfma_f32_16x16x32_bf16 v[92:95], a[16:19], a[144:147], v[20:23]\n"
"v_exp_f32 v117, v117\n"
"v_mfma_f32_16x16x32_bf16 v[92:95], a[20:23], a[148:151], v[92:95]\n"
"v_exp_f32 v118, v118\n"
"v_mfma_f32_16x16x32_bf16 v[92:95], a[24:27], a[152:155], v[92:95]\n"
"v_exp_f32 v119, v119\n"
"v_mfma_f32_16x16x32_bf16 v[92:95], a[28:31], a[156:159], v[92:95]\n"
"v_exp_f32 v72, v72\n"
"v_mfma_f32_16x16x32_bf16 v[108:111], a[16:19], a[160:163], v[24:27]\n"
"buffer_load_dwordx4 v4, s[48:51], s61 offen offset:2048 nt lds\n"
"v_mfma_f32_16x16x32_bf16 v[108:111], a[20:23], a[164:167], v[108:111]\n"
"v_exp_f32 v73, v73\n"
"v_mfma_f32_16x16x32_bf16 v[108:111], a[24:27], a[168:171], v[108:111]\n"
"v_exp_f32 v74, v74\n"
"v_mfma_f32_16x16x32_bf16 v[108:111], a[28:31], a[172:175], v[108:111]\n"
"v_exp_f32 v75, v75\n"
"v_mfma_f32_16x16x32_bf16 v[124:127], a[16:19], a[176:179], v[28:31]\n"
"v_exp_f32 v88, v88\n"
"v_mfma_f32_16x16x32_bf16 v[124:127], a[20:23], a[180:183], v[124:127]\n"
"v_exp_f32 v89, v89\n"
"s_waitcnt vmcnt(16)\n"
"v_accvgpr_read_b32 v14, a96\n"
"v_add_u32 v14, 1, v14\n"
"v_accvgpr_write_b32 a116, v14\n"
"s_waitcnt vmcnt(15)\n"
"v_accvgpr_read_b32 v14, a97\n"
"v_add_u32 v14, 1, v14\n"
"v_accvgpr_write_b32 a118, v14\n"
"v_lshrrev_b32 v15, 2, v13\n"
"s_mov_b64 exec, 15\n"
"buffer_store_dword a116, v15, s[36:39], %[mw] offen offset:0 sc1\n"
"buffer_store_dword a118, v15, s[36:39], %[mw] offen offset:128 sc1\n"
"s_mov_b64 exec, -1\n"
"v_mfma_f32_16x16x32_bf16 v[124:127], a[24:27], a[184:187], v[124:127]\n"
"v_exp_f32 v90, v90\n"
"v_mfma_f32_16x16x32_bf16 v[124:127], a[28:31], a[188:191], v[124:127]\n"
"v_exp_f32 v91, v91\n"
"v_mfma_f32_16x16x32_bf16 a[96:99], a[112:115], v[32:35], 0\n"
"v_exp_f32 v104, v104\n"
"v_mfma_f32_16x16x32_bf16 a[96:99], a[112:115], v[36:39], a[96:99]\n"
"v_exp_f32 v105, v105\n"
"v_mfma_f32_16x16x32_bf16 v[128:131], a[32:35], v[32:35], 0\n"
"v_exp_f32 v106, v106\n"
"v_mfma_f32_16x16x32_bf16 v[128:131], a[36:39], v[36:39], v[128:131]\n"
"v_exp_f32 v107, v107\n"
"v_mfma_f32_16x16x32_bf16 v[132:135], a[40:43], v[32:35], 0\n"
"buffer_load_dwordx4 v5, s[48:51], s61 offen offset:3072 nt lds\n"
"v_mfma_f32_16x16x32_bf16 v[132:135], a[44:47], v[36:39], v[132:135]\n"
"v_xor_b32 v6, s64, v6\n"
"v_xor_b32 v7, s64, v7\n"
"v_mfma_f32_16x16x32_bf16 v[136:139], a[48:51], v[32:35], 0\n"
"v_xor_b32 v8, s64, v8\n"
"v_xor_b32 v9, s64, v9\n"
"v_mfma_f32_16x16x32_bf16 v[136:139], a[52:55], v[36:39], v[136:139]\n"
"v_xor_b32 v10, s65, v10\n"
"v_xor_b32 v11, s65, v11\n"
"v_mfma_f32_16x16x32_bf16 v[140:143], a[56:59], v[32:35], 0\n"
"s_xor_b32 s62, s62, s65\n"
"s_xor_b32 s63, s63, s64\n"
"v_mfma_f32_16x16x32_bf16 v[140:143], a[60:63], v[36:39], v[140:143]\n"
"s_add_u32 s60, s60, 0x4000\n"
"s_add_u32 s61, s61, 0x4000\n"
"v_mfma_f32_16x16x32_bf16 v[144:147], a[64:67], v[32:35], 0\n"
"s_xor_b32 s64, s64, 0xc000\n"
"s_xor_b32 s65, s65, 0xc000\n"
"v_mfma_f32_16x16x32_bf16 v[144:147], a[68:71], v[36:39], v[144:147]\n"
"v_exp_f32 v120, v120\n"
"v_mfma_f32_16x16x32_bf16 v[148:151], a[72:75], v[32:35], 0\n"
"v_exp_f32 v121, v121\n"
"v_mfma_f32_16x16x32_bf16 v[148:151], a[76:79], v[36:39], v[148:151]\n"
"v_exp_f32 v122, v122\n"
"v_mfma_f32_16x16x32_bf16 v[152:155], a[80:83], v[32:35], 0\n"
"v_exp_f32 v123, v123\n"
"v_mfma_f32_16x16x32_bf16 v[152:155], a[84:87], v[36:39], v[152:155]\n"
"v_exp_f32 v76, v76\n"
"v_mfma_f32_16x16x32_bf16 v[156:159], a[88:91], v[32:35], 0\n"
"v_cvt_pk_bf16_f32 v32, v64, v65\n"
"v_cvt_pk_bf16_f32 v33, v66, v67\n"
"v_mfma_f32_16x16x32_bf16 v[156:159], a[92:95], v[36:39], v[156:159]\n"
"v_cvt_pk_bf16_f32 v34, v68, v69\n"
"v_cvt_pk_bf16_f32 v35, v70, v71\n"
"v_mfma_f32_16x16x32_bf16 a[100:103], a[112:115], v[40:43], 0\n"
"v_cvt_pk_bf16_f32 v36, v72, v73\n"
"v_cvt_pk_bf16_f32 v37, v74, v75\n"
"v_mfma_f32_16x16x32_bf16 a[100:103], a[112:115], v[44:47], a[100:103]\n"
"v_exp_f32 v77, v77\n"
"v_mfma_f32_16x16x32_bf16 v[160:163], a[32:35], v[40:43], 0\n"
"v_exp_f32 v78, v78\n"
"v_mfma_f32_16x16x32_bf16 v[160:163], a[36:39], v[44:47], v[160:163]\n"
"v_exp_f32 v79, v79\n"
"v_mfma_f32_16x16x32_bf16 v[164:167], a[40:43], v[40:43], 0\n"
"v_cvt_pk_bf16_f32 v38, v76, v77\n"
"v_cvt_pk_bf16_f32 v39, v78, v79\n"
"v_mfma_f32_16x16x32_bf16 v[164:167], a[44:47], v[44:47], v[164:167]\n"
"v_exp_f32 v92, v92\n"
"v_mfma_f32_16x16x32_bf16 v[168:171], a[48:51], v[40:43], 0\n"
"v_exp_f32 v93, v93\n"
"v_mfma_f32_16x16x32_bf16 v[168:171], a[52:55], v[44:47], v[168:171]\n"
"v_exp_f32 v94, v94\n"
"v_mfma_f32_16x16x32_bf16 v[172:175], a[56:59], v[40:43], 0\n"
"v_exp_f32 v95, v95\n"
"v_mfma_f32_16x16x32_bf16 v[172:175], a[60:63], v[44:47], v[172:175]\n"
"v_exp_f32 v108, v108\n"
"v_mfma_f32_16x16x32_bf16 v[176:179], a[64:67], v[40:43], 0\n"
"v_exp_f32 v109, v109\n"
"v_mfma_f32_16x16x32_bf16 v[176:179], a[68:71], v[44:47], v[176:179]\n"
"v_exp_f32 v110, v110\n"
"v_mfma_f32_16x16x32_bf16 v[180:183], a[72:75], v[40:43], 0\n"
"v_exp_f32 v111, v111\n"
"v_mfma_f32_16x16x32_bf16 v[180:183], a[76:79], v[44:47], v[180:183]\n"
"v_exp_f32 v124, v124\n"
"v_mfma_f32_16x16x32_bf16 v[184:187], a[80:83], v[40:43], 0\n"
"v_exp_f32 v125, v125\n"
"v_mfma_f32_16x16x32_bf16 v[184:187], a[84:87], v[44:47], v[184:187]\n"
"v_exp_f32 v126, v126\n"
"v_mfma_f32_16x16x32_bf16 v[188:191], a[88:91], v[40:43], 0\n"
"v_cvt_pk_bf16_f32 v40, v80, v81\n"
"v_cvt_pk_bf16_f32 v41, v82, v83\n"
"v_mfma_f32_16x16x32_bf16 v[188:191], a[92:95], v[44:47], v[188:191]\n"
"v_cvt_pk_bf16_f32 v42, v84, v85\n"
"v_cvt_pk_bf16_f32 v43, v86, v87\n"
"v_mfma_f32_16x16x32_bf16 a[104:107], a[112:115], v[48:51], 0\n"
"v_cvt_pk_bf16_f32 v44, v88, v89\n"
"v_cvt_pk_bf16_f32 v45, v90, v91\n"
"v_mfma_f32_16x16x32_bf16 a[104:107], a[112:115], v[52:55], a[104:107]\n"
"v_cvt_pk_bf16_f32 v46, v92, v93\n"
"v_cvt_pk_bf16_f32 v47, v94, v95\n"
"v_mfma_f32_16x16x32_bf16 v[192:195], a[32:35], v[48:51], 0\n"
"v_exp_f32 v127, v127\n"
"v_mfma_f32_16x16x32_bf16 v[192:195], a[36:39], v[52:55], v[192:195]\n"
"v_mfma_f32_16x16x32_bf16 v[196:199], a[40:43], v[48:51], 0\n"
"v_mfma_f32_16x16x32_bf16 v[196:199], a[44:47], v[52:55], v[196:199]\n"
"v_mfma_f32_16x16x32_bf16 v[200:203], a[48:51], v[48:51], 0\n"
"v_mfma_f32_16x16x32_bf16 v[200:203], a[52:55], v[52:55], v[200:203]\n"
"v_mfma_f32_16x16x32_bf16 v[204:207], a[56:59], v[48:51], 0\n"
"v_mfma_f32_16x16x32_bf16 v[204:207], a[60:63], v[52:55], v[204:207]\n"
"v_mfma_f32_16x16x32_bf16 v[208:211], a[64:67], v[48:51], 0\n"
"v_mfma_f32_16x16x32_bf16 v[208:211], a[68:71], v[52:55], v[208:211]\n"
"v_mfma_f32_16x16x32_bf16 v[212:215], a[72:75], v[48:51], 0\n"
"v_mfma_f32_16x16x32_bf16 v[212:215], a[76:79], v[52:55], v[212:215]\n"
"v_mfma_f32_16x16x32_bf16 v[216:219], a[80:83], v[48:51], 0\n"
"v_mfma_f32_16x16x32_bf16 v[216:219], a[84:87], v[52:55], v[216:219]\n"
"v_mfma_f32_16x16x32_bf16 v[220:223], a[88:91], v[48:51], 0\n"
"s_waitcnt vmcnt(63)\n"
"s_barrier\n"
"v_mfma_f32_16x16x32_bf16 v[220:223], a[92:95], v[52:55], v[220:223]\n"
"ds_read_b128 a[0:3], v6 offset:0\n"
"ds_read_b128 a[4:7], v7 offset:0\n"
"v_mfma_f32_16x16x32_bf16 a[108:111], a[112:115], v[56:59], 0\n"
"ds_read_b128 a[8:11], v8 offset:0\n"
"ds_read_b128 a[12:15], v9 offset:0\n"
"v_mfma_f32_16x16x32_bf16 a[108:111], a[112:115], v[60:63], a[108:111]\n"
"s_mov_b32 m0, s62\n"
"v_cvt_pk_bf16_f32 v48, v96, v97\n"
"v_mfma_f32_16x16x32_bf16 v[224:227], a[32:35], v[56:59], 0\n"
"buffer_load_dwordx4 v0, s[44:47], s60 offen offset:0 nt lds\n"
"v_mfma_f32_16x16x32_bf16 v[224:227], a[36:39], v[60:63], v[224:227]\n"
"v_cvt_pk_bf16_f32 v49, v98, v99\n"
"ds_read_b128 a[16:19], v6 offset:4096\n"
"v_mfma_f32_16x16x32_bf16 v[228:231], a[40:43], v[56:59], 0\n"
"ds_read_b128 a[20:23], v7 offset:4096\n"
"ds_read_b128 a[24:27], v8 offset:4096\n"
"v_mfma_f32_16x16x32_bf16 v[228:231], a[44:47], v[60:63], v[228:231]\n"
"ds_read_b128 a[28:31], v9 offset:4096\n"
"v_cvt_pk_bf16_f32 v50, v100, v101\n"
"v_mfma_f32_16x16x32_bf16 v[232:235], a[48:51], v[56:59], 0\n"
"v_cvt_pk_bf16_f32 v51, v102, v103\n"
"v_cvt_pk_bf16_f32 v52, v104, v105\n"
"v_mfma_f32_16x16x32_bf16 v[232:235], a[52:55], v[60:63], v[232:235]\n"
"v_cvt_pk_bf16_f32 v53, v106, v107\n"
"v_cvt_pk_bf16_f32 v54, v108, v109\n"
"v_mfma_f32_16x16x32_bf16 v[236:239], a[56:59], v[56:59], 0\n"
"v_cvt_pk_bf16_f32 v55, v110, v111\n"
"ds_read_b64_tr_b16 a[32:33], v10 offset:0\n"
"v_mfma_f32_16x16x32_bf16 v[236:239], a[60:63], v[60:63], v[236:239]\n"
"ds_read_b64_tr_b16 a[34:35], v10 offset:4096\n"
"ds_read_b64_tr_b16 a[36:37], v10 offset:8192\n"
"v_mfma_f32_16x16x32_bf16 v[240:243], a[64:67], v[56:59], 0\n"
"ds_read_b64_tr_b16 a[38:39], v10 offset:12288\n"
"ds_read_b64_tr_b16 a[40:41], v11 offset:0\n"
"v_mfma_f32_16x16x32_bf16 v[240:243], a[68:71], v[60:63], v[240:243]\n"
"ds_read_b64_tr_b16 a[42:43], v11 offset:4096\n"
"ds_read_b64_tr_b16 a[44:45], v11 offset:8192\n"
"v_mfma_f32_16x16x32_bf16 v[244:247], a[72:75], v[56:59], 0\n"
"ds_read_b64_tr_b16 a[46:47], v11 offset:12288\n"
"ds_read_b64_tr_b16 a[48:49], v10 offset:256\n"
"v_mfma_f32_16x16x32_bf16 v[244:247], a[76:79], v[60:63], v[244:247]\n"
"ds_read_b64_tr_b16 a[50:51], v10 offset:4352\n"
"ds_read_b64_tr_b16 a[52:53], v10 offset:8448\n"
"v_mfma_f32_16x16x32_bf16 v[248:251], a[80:83], v[56:59], 0\n"
"buffer_load_dwordx4 v1, s[44:47], s60 offen offset:1024 nt lds\n"
"v_mfma_f32_16x16x32_bf16 v[248:251], a[84:87], v[60:63], v[248:251]\n"
"ds_read_b64_tr_b16 a[54:55], v10 offset:12544\n"
"ds_read_b64_tr_b16 a[56:57], v11 offset:256\n"
"v_mfma_f32_16x16x32_bf16 v[252:255], a[88:91], v[56:59], 0\n"
"v_cvt_pk_bf16_f32 v56, v112, v113\n"
"v_cvt_pk_bf16_f32 v57, v114, v115\n"
"v_mfma_f32_16x16x32_bf16 v[252:255], a[92:95], v[60:63], v[252:255]\n"
".LG0a_%=:\n"
"s_waitcnt lgkmcnt(15)\n"
"v_mfma_f32_16x16x32_bf16 v[64:67], a[0:3], a[128:131], v[16:19]\n"
"v_cvt_pk_bf16_f32 v58, v116, v117\n"
"v_cvt_pk_bf16_f32 v59, v118, v119\n"
"v_mfma_f32_16x16x32_bf16 v[64:67], a[4:7], a[132:135], v[64:67]\n"
"v_cvt_pk_bf16_f32 v60, v120, v121\n"
"v_cvt_pk_bf16_f32 v61, v122, v123\n"
"v_mfma_f32_16x16x32_bf16 v[64:67], a[8:11], a[136:139], v[64:67]\n"
"v_cvt_pk_bf16_f32 v62, v124, v125\n"
"v_cvt_pk_bf16_f32 v63, v126, v127\n"
"v_mfma_f32_16x16x32_bf16 v[64:67], a[12:15], a[140:143], v[64:67]\n"
"ds_read_b64_tr_b16 a[58:59], v11 offset:4352\n"
"ds_read_b64_tr_b16 a[60:61], v11 offset:8448\n"
"v_mfma_f32_16x16x32_bf16 v[80:83], a[0:3], a[144:147], v[20:23]\n"
"ds_read_b64_tr_b16 a[62:63], v11 offset:12544\n"
"ds_read_b64_tr_b16 a[64:65], v10 offset:512\n"
"v_mfma_f32_16x16x32_bf16 v[80:83], a[4:7], a[148:151], v[80:83]\n"
"ds_read_b64_tr_b16 a[66:67], v10 offset:4608\n"
"ds_read_b64_tr_b16 a[68:69], v10 offset:8704\n"
"v_mfma_f32_16x16x32_bf16 v[80:83], a[8:11], a[152:155], v[80:83]\n"
"ds_read_b64_tr_b16 a[70:71], v10 offset:12800\n"
"ds_read_b64_tr_b16 a[72:73], v11 offset:512\n"
"v_mfma_f32_16x16x32_bf16 v[80:83], a[12:15], a[156:159], v[80:83]\n"
"ds_read_b64_tr_b16 a[74:75], v11 offset:4608\n"
"ds_read_b64_tr_b16 a[76:77], v11 offset:8704\n"
"v_mfma_f32_16x16x32_bf16 v[96:99], a[0:3], a[160:163], v[24:27]\n"
"buffer_load_dwordx4 v2, s[44:47], s60 offen offset:2048 nt lds\n"
"v_mfma_f32_16x16x32_bf16 v[96:99], a[4:7], a[164:167], v[96:99]\n"
"ds_read_b64_tr_b16 a[78:79], v11 offset:12800\n"
"ds_read_b64_tr_b16 a[80:81], v10 offset:768\n"
"v_mfma_f32_16x16x32_bf16 v[96:99], a[8:11], a[168:171], v[96:99]\n"
"ds_read_b64_tr_b16 a[82:83], v10 offset:4864\n"
"ds_read_b64_tr_b16 a[84:85], v10 offset:8960\n"
"v_mfma_f32_16x16x32_bf16 v[96:99], a[12:15], a[172:175], v[96:99]\n"
"ds_read_b64_tr_b16 a[86:87], v10 offset:13056\n"
"ds_read_b64_tr_b16 a[88:89], v11 offset:768\n"
"v_mfma_f32_16x16x32_bf16 v[112:115], a[0:3], a[176:179], v[28:31]\n"
"ds_read_b128 a[0:3], v6 offset:8192\n"
"ds_read_b64_tr_b16 a[90:91], v11 offset:4864\n"
"v_mfma_f32_16x16x32_bf16 v[112:115], a[4:7], a[180:183], v[112:115]\n"
"ds_read_b128 a[4:7], v7 offset:8192\n"
"ds_read_b64_tr_b16 a[92:93], v11 offset:8960\n"
"v_mfma_f32_16x16x32_bf16 v[112:115], a[8:11], a[184:187], v[112:115]\n"
"ds_read_b128 a[8:11], v8 offset:8192\n"
"ds_read_b64_tr_b16 a[94:95], v11 offset:13056\n"
"v_mfma_f32_16x16x32_bf16 v[112:115], a[12:15], a[188:191], v[112:115]\n"
"ds_read_b128 a[12:15], v9 offset:8192\n"
"v_mfma_f32_16x16x32_bf16 v[68:71], a[16:19], a[128:131], v[16:19]\n"
"v_exp_f32 v64, v64\n"
"v_mfma_f32_16x16x32_bf16 v[68:71], a[20:23], a[132:135], v[68:71]\n"
"v_exp_f32 v65, v65\n"
"s_waitcnt lgkmcnt(15)\n"
"v_mfma_f32_16x16x32_bf16 v[68:71], a[24:27], a[136:139], v[68:71]\n"
"v_exp_f32 v66, v66\n"
"v_mfma_f32_16x16x32_bf16 v[68:71], a[28:31], a[140:143], v[68:71]\n"
"v_exp_f32 v67, v67\n"
"v_mfma_f32_16x16x32_bf16 v[84:87], a[16:19], a[144:147], v[20:23]\n"
"buffer_load_dwordx4 v3, s[44:47], s60 offen offset:3072 nt lds\n"
"v_mfma_f32_16x16x32_bf16 v[84:87], a[20:23], a[148:151], v[84:87]\n"
"v_exp_f32 v80, v80\n"
"v_mfma_f32_16x16x32_bf16 v[84:87], a[24:27], a[152:155], v[84:87]\n"
"v_exp_f32 v81, v81\n"
"v_mfma_f32_16x16x32_bf16 v[84:87], a[28:31], a[156:159], v[84:87]\n"
"v_exp_f32 v82, v82\n"
"v_mfma_f32_16x16x32_bf16 v[100:103], a[16:19], a[160:163], v[24:27]\n"
"v_exp_f32 v83, v83\n"
"v_mfma_f32_16x16x32_bf16 v[100:103], a[20:23], a[164:167], v[100:103]\n"
"v_exp_f32 v96, v96\n"
"v_mfma_f32_16x16x32_bf16 v[100:103], a[24:27], a[168:171], v[100:103]\n"
"v_exp_f32 v97, v97\n"
"v_mfma_f32_16x16x32_bf16 v[100:103], a[28:31], a[172:175], v[100:103]\n"
"v_exp_f32 v98, v98\n"
"v_mfma_f32_16x16x32_bf16 v[116:119], a[16:19], a[176:179], v[28:31]\n"
"ds_read_b128 a[16:19], v6 offset:12288\n"
"v_mfma_f32_16x16x32_bf16 v[116:119], a[20:23], a[180:183], v[116:119]\n"
"ds_read_b128 a[20:23], v7 offset:12288\n"
"v_mfma_f32_16x16x32_bf16 v[116:119], a[24:27], a[184:187], v[116:119]\n"
"ds_read_b128 a[24:27], v8 offset:12288\n"
"v_mfma_f32_16x16x32_bf16 v[116:119], a[28:31], a[188:191], v[116:119]\n"
"s_mov_b32 m0, s63\n"
"ds_read_b128 a[28:31], v9 offset:12288\n"
"s_waitcnt lgkmcnt(10)\n"
"v_mfma_f32_16x16x32_bf16 v[72:75], a[0:3], a[128:131], v[16:19]\n"
"buffer_load_dwordx4 v4, s[48:51], s61 offen offset:0 nt lds\n"
"s_waitcnt lgkmcnt(8)\n"
"v_mfma_f32_16x16x32_bf16 v[72:75], a[4:7], a[132:135], v[72:75]\n"
"v_exp_f32 v99, v99\n"
"s_waitcnt lgkmcnt(6)\n"
"v_mfma_f32_16x16x32_bf16 v[72:75], a[8:11], a[136:139], v[72:75]\n"
"v_exp_f32 v112, v112\n"
"s_waitcnt lgkmcnt(4)\n"
"v_mfma_f32_16x16x32_bf16 v[72:75], a[12:15], a[140:143], v[72:75]\n"
"v_exp_f32 v113, v113\n"
"v_mfma_f32_16x16x32_bf16 v[88:91], a[0:3], a[144:147], v[20:23]\n"
"v_exp_f32 v114, v114\n"
"v_mfma_f32_16x16x32_bf16 v[88:91], a[4:7], a[148:151], v[88:91]\n"
"v_exp_f32 v115, v115\n"
"v_mfma_f32_16x16x32_bf16 v[88:91], a[8:11], a[152:155], v[88:91]\n"
"v_exp_f32 v68, v68\n"
"v_mfma_f32_16x16x32_bf16 v[88:91], a[12:15], a[156:159], v[88:91]\n"
"v_exp_f32 v69, v69\n"
"v_mfma_f32_16x16x32_bf16 v[104:107], a[0:3], a[160:163], v[24:27]\n"
"v_exp_f32 v70, v70\n"
"v_mfma_f32_16x16x32_bf16 v[104:107], a[4:7], a[164:167], v[104:107]\n"
"v_exp_f32 v71, v71\n"
"v_mfma_f32_16x16x32_bf16 v[104:107], a[8:11], a[168:171], v[104:107]\n"
"v_exp_f32 v84, v84\n"
"v_mfma_f32_16x16x32_bf16 v[104:107], a[12:15], a[172:175], v[104:107]\n"
"v_exp_f32 v85, v85\n"
"v_mfma_f32_16x16x32_bf16 v[120:123], a[0:3], a[176:179], v[28:31]\n"
"buffer_load_dwordx4 v5, s[48:51], s61 offen offset:1024 nt lds\n"
"v_mfma_f32_16x16x32_bf16 v[120:123], a[4:7], a[180:183], v[120:123]\n"
"v_exp_f32 v86, v86\n"
"v_mfma_f32_16x16x32_bf16 v[120:123], a[8:11], a[184:187], v[120:123]\n"
"v_exp_f32 v87, v87\n"
"v_mfma_f32_16x16x32_bf16 v[120:123], a[12:15], a[188:191], v[120:123]\n"
"v_exp_f32 v100, v100\n"
"s_waitcnt lgkmcnt(3)\n"
"v_mfma_f32_16x16x32_bf16 v[76:79], a[16:19], a[128:131], v[16:19]\n"
"v_exp_f32 v101, v101\n"
"s_waitcnt lgkmcnt(2)\n"
"v_mfma_f32_16x16x32_bf16 v[76:79], a[20:23], a[132:135], v[76:79]\n"
"v_exp_f32 v102, v102\n"
"s_waitcnt lgkmcnt(1)\n"
"v_mfma_f32_16x16x32_bf16 v[76:79], a[24:27], a[136:139], v[76:79]\n"
"v_exp_f32 v103, v103\n"
"s_waitcnt lgkmcnt(0)\n"
"v_mfma_f32_16x16x32_bf16 v[76:79], a[28:31], a[140:143], v[76:79]\n"
"v_exp_f32 v116, v116\n"
"v_mfma_f32_16x16x32_bf16 v[92:95], a[16:19], a[144:147], v[20:23]\n"
"v_exp_f32 v117, v117\n"
"v_mfma_f32_16x16x32_bf16 v[92:95], a[20:23], a[148:151], v[92:95]\n"
"v_exp_f32 v118, v118\n"
"v_mfma_f32_16x16x32_bf16 v[92:95], a[24:27], a[152:155], v[92:95]\n"
"v_exp_f32 v119, v119\n"
"v_mfma_f32_16x16x32_bf16 v[92:95], a[28:31], a[156:159], v[92:95]\n"
"v_exp_f32 v72, v72\n"
"v_mfma_f32_16x16x32_bf16 v[108:111], a[16:19], a[160:163], v[24:27]\n"
"buffer_load_dwordx4 v4, s[48:51], s61 offen offset:2048 nt lds\n"
"v_mfma_f32_16x16x32_bf16 v[108:111], a[20:23], a[164:167], v[108:111]\n"
"v_exp_f32 v73, v73\n"
"v_mfma_f32_16x16x32_bf16 v[108:111], a[24:27], a[168:171], v[108:111]\n"
"v_exp_f32 v74, v74\n"
"v_mfma_f32_16x16x32_bf16 v[108:111], a[28:31], a[172:175], v[108:111]\n"
"v_exp_f32 v75, v75\n"
"v_mfma_f32_16x16x32_bf16 v[124:127], a[16:19], a[176:179], v[28:31]\n"
"v_exp_f32 v88, v88\n"
"v_mfma_f32_16x16x32_bf16 v[124:127], a[20:23], a[180:183], v[124:127]\n"
"v_exp_f32 v89, v89\n"
"v_mfma_f32_16x16x32_bf16 v[124:127], a[24:27], a[184:187], v[124:127]\n"
"v_exp_f32 v90, v90\n"
"v_mfma_f32_16x16x32_bf16 v[124:127], a[28:31], a[188:191], v[124:127]\n"
"v_exp_f32 v91, v91\n"
"v_mfma_f32_16x16x32_bf16 a[96:99], a[112:115], v[32:35], a[96:99]\n"
"v_exp_f32 v104, v104\n"
"v_mfma_f32_16x16x32_bf16 a[96:99], a[112:115], v[36:39], a[96:99]\n"
"v_exp_f32 v105, v105\n"
"v_mfma_f32_16x16x32_bf16 v[128:131], a[32:35], v[32:35], v[128:131]\n"
"v_exp_f32 v106, v106\n"
"v_mfma_f32_16x16x32_bf16 v[128:131], a[36:39], v[36:39], v[128:131]\n"
"v_exp_f32 v107, v107\n"
"v_mfma_f32_16x16x32_bf16 v[132:135], a[40:43], v[32:35], v[132:135]\n"
"buffer_load_dwordx4 v5, s[48:51], s61 offen offset:3072 nt lds\n"
"v_mfma_f32_16x16x32_bf16 v[132:135], a[44:47], v[36:39], v[132:135]\n"
"v_xor_b32 v6, s64, v6\n"
"v_xor_b32 v7, s64, v7\n"
"v_mfma_f32_16x16x32_bf16 v[136:139], a[48:51], v[32:35], v[136:139]\n"
"v_xor_b32 v8, s64, v8\n"
"v_xor_b32 v9, s64, v9\n"
"v_mfma_f32_16x16x32_bf16 v[136:139], a[52:55], v[36:39], v[136:139]\n"
"v_xor_b32 v10, s65, v10\n"
"v_xor_b32 v11, s65, v11\n"
"v_mfma_f32_16x16x32_bf16 v[140:143], a[56:59], v[32:35], v[140:143]\n"
"s_xor_b32 s62, s62, s65\n"
"s_xor_b32 s63, s63, s64\n"
"v_mfma_f32_16x16x32_bf16 v[140:143], a[60:63], v[36:39], v[140:143]\n"
"s_add_u32 s60, s60, 0x4000\n"
"s_add_u32 s61, s61, 0x4000\n"
"v_mfma_f32_16x16x32_bf16 v[144:147], a[64:67], v[32:35], v[144:147]\n"
"s_xor_b32 s64, s64, 0xc000\n"
"s_xor_b32 s65, s65, 0xc000\n"
"v_mfma_f32_16x16x32_bf16 v[144:147], a[68:71], v[36:39], v[144:147]\n"
"v_exp_f32 v120, v120\n"
"v_mfma_f32_16x16x32_bf16 v[148:151], a[72:75], v[32:35], v[148:151]\n"
"v_exp_f32 v121, v121\n"
"v_mfma_f32_16x16x32_bf16 v[148:151], a[76:79], v[36:39], v[148:151]\n"
"v_exp_f32 v122, v122\n"
"v_mfma_f32_16x16x32_bf16 v[152:155], a[80:83], v[32:35], v[152:155]\n"
"v_exp_f32 v123, v123\n"
"v_mfma_f32_16x16x32_bf16 v[152:155], a[84:87], v[36:39], v[152:155]\n"
"v_exp_f32 v76, v76\n"
"v_mfma_f32_16x16x32_bf16 v[156:159], a[88:91], v[32:35], v[156:159]\n"
"v_cvt_pk_bf16_f32 v32, v64, v65\n"
"v_cvt_pk_bf16_f32 v33, v66, v67\n"
"v_mfma_f32_16x16x32_bf16 v[156:159], a[92:95], v[36:39], v[156:159]\n"
"v_cvt_pk_bf16_f32 v34, v68, v69\n"
"v_cvt_pk_bf16_f32 v35, v70, v71\n"
"v_mfma_f32_16x16x32_bf16 a[100:103], a[112:115], v[40:43], a[100:103]\n"
"v_cvt_pk_bf16_f32 v36, v72, v73\n"
"v_cvt_pk_bf16_f32 v37, v74, v75\n"
"v_mfma_f32_16x16x32_bf16 a[100:103], a[112:115], v[44:47], a[100:103]\n"
"v_exp_f32 v77, v77\n"
"v_mfma_f32_16x16x32_bf16 v[160:163], a[32:35], v[40:43], v[160:163]\n"
"v_exp_f32 v78, v78\n"
"v_mfma_f32_16x16x32_bf16 v[160:163], a[36:39], v[44:47], v[160:163]\n"
"v_exp_f32 v79, v79\n"
"v_mfma_f32_16x16x32_bf16 v[164:167], a[40:43], v[40:43], v[164:167]\n"
"v_cvt_pk_bf16_f32 v38, v76, v77\n"
"v_cvt_pk_bf16_f32 v39, v78, v79\n"
"v_mfma_f32_16x16x32_bf16 v[164:167], a[44:47], v[44:47], v[164:167]\n"
"v_exp_f32 v92, v92\n"
"v_mfma_f32_16x16x32_bf16 v[168:171], a[48:51], v[40:43], v[168:171]\n"
"v_exp_f32 v93, v93\n"
"v_mfma_f32_16x16x32_bf16 v[168:171], a[52:55], v[44:47], v[168:171]\n"
"v_exp_f32 v94, v94\n"
"v_mfma_f32_16x16x32_bf16 v[172:175], a[56:59], v[40:43], v[172:175]\n"
"v_exp_f32 v95, v95\n"
"v_mfma_f32_16x16x32_bf16 v[172:175], a[60:63], v[44:47], v[172:175]\n"
"v_exp_f32 v108, v108\n"
"v_mfma_f32_16x16x32_bf16 v[176:179], a[64:67], v[40:43], v[176:179]\n"
"v_exp_f32 v109, v109\n"
"v_mfma_f32_16x16x32_bf16 v[176:179], a[68:71], v[44:47], v[176:179]\n"
"v_exp_f32 v110, v110\n"
"v_mfma_f32_16x16x32_bf16 v[180:183], a[72:75], v[40:43], v[180:183]\n"
"v_exp_f32 v111, v111\n"
"v_mfma_f32_16x16x32_bf16 v[180:183], a[76:79], v[44:47], v[180:183]\n"
"v_exp_f32 v124, v124\n"
"v_mfma_f32_16x16x32_bf16 v[184:187], a[80:83], v[40:43], v[184:187]\n"
"v_exp_f32 v125, v125\n"
"v_mfma_f32_16x16x32_bf16 v[184:187], a[84:87], v[44:47], v[184:187]\n"
"v_exp_f32 v126, v126\n"
"v_mfma_f32_16x16x32_bf16 v[188:191], a[88:91], v[40:43], v[188:191]\n"
"v_cvt_pk_bf16_f32 v40, v80, v81\n"
"v_cvt_pk_bf16_f32 v41, v82, v83\n"
"v_mfma_f32_16x16x32_bf16 v[188:191], a[92:95], v[44:47], v[188:191]\n"
"v_cvt_pk_bf16_f32 v42, v84, v85\n"
"v_cvt_pk_bf16_f32 v43, v86, v87\n"
"v_mfma_f32_16x16x32_bf16 a[104:107], a[112:115], v[48:51], a[104:107]\n"
"v_cvt_pk_bf16_f32 v44, v88, v89\n"
"v_cvt_pk_bf16_f32 v45, v90, v91\n"
"v_mfma_f32_16x16x32_bf16 a[104:107], a[112:115], v[52:55], a[104:107]\n"
"v_cvt_pk_bf16_f32 v46, v92, v93\n"
"v_cvt_pk_bf16_f32 v47, v94, v95\n"
"v_mfma_f32_16x16x32_bf16 v[192:195], a[32:35], v[48:51], v[192:195]\n"
"v_exp_f32 v127, v127\n"
"v_mfma_f32_16x16x32_bf16 v[192:195], a[36:39], v[52:55], v[192:195]\n"
"v_mfma_f32_16x16x32_bf16 v[196:199], a[40:43], v[48:51], v[196:199]\n"
"v_mfma_f32_16x16x32_bf16 v[196:199], a[44:47], v[52:55], v[196:199]\n"
"v_mfma_f32_16x16x32_bf16 v[200:203], a[48:51], v[48:51], v[200:203]\n"
"v_mfma_f32_16x16x32_bf16 v[200:203], a[52:55], v[52:55], v[200:203]\n"
"v_mfma_f32_16x16x32_bf16 v[204:207], a[56:59], v[48:51], v[204:207]\n"
"v_mfma_f32_16x16x32_bf16 v[204:207], a[60:63], v[52:55], v[204:207]\n"
"v_mfma_f32_16x16x32_bf16 v[208:211], a[64:67], v[48:51], v[208:211]\n"
"v_mfma_f32_16x16x32_bf16 v[208:211], a[68:71], v[52:55], v[208:211]\n"
"v_mfma_f32_16x16x32_bf16 v[212:215], a[72:75], v[48:51], v[212:215]\n"
"v_mfma_f32_16x16x32_bf16 v[212:215], a[76:79], v[52:55], v[212:215]\n"
"v_mfma_f32_16x16x32_bf16 v[216:219], a[80:83], v[48:51], v[216:219]\n"
"v_mfma_f32_16x16x32_bf16 v[216:219], a[84:87], v[52:55], v[216:219]\n"
"v_mfma_f32_16x16x32_bf16 v[220:223], a[88:91], v[48:51], v[220:223]\n"
"s_waitcnt vmcnt(16)\n"
"s_barrier\n"
"v_mfma_f32_16x16x32_bf16 v[220:223], a[92:95], v[52:55], v[220:223]\n"
"ds_read_b128 a[0:3], v6 offset:0\n"
"ds_read_b128 a[4:7], v7 offset:0\n"
"v_mfma_f32_16x16x32_bf16 a[108:111], a[112:115], v[56:59], a[108:111]\n"
"ds_read_b128 a[8:11], v8 offset:0\n"
"ds_read_b128 a[12:15], v9 offset:0\n"
"v_mfma_f32_16x16x32_bf16 a[108:111], a[112:115], v[60:63], a[108:111]\n"
"s_mov_b32 m0, s62\n"
"v_cvt_pk_bf16_f32 v48, v96, v97\n"
"v_mfma_f32_16x16x32_bf16 v[224:227], a[32:35], v[56:59], v[224:227]\n"
"buffer_load_dwordx4 v0, s[44:47], s60 offen offset:0 nt lds\n"
"v_mfma_f32_16x16x32_bf16 v[224:227], a[36:39], v[60:63], v[224:227]\n"
"v_cvt_pk_bf16_f32 v49, v98, v99\n"
"ds_read_b128 a[16:19], v6 offset:4096\n"
"v_mfma_f32_16x16x32_bf16 v[228:231], a[40:43], v[56:59], v[228:231]\n"
"ds_read_b128 a[20:23], v7 offset:4096\n"
"ds_read_b128 a[24:27], v8 offset:4096\n"
"v_mfma_f32_16x16x32_bf16 v[228:231], a[44:47], v[60:63], v[228:231]\n"
"ds_read_b128 a[28:31], v9 offset:4096\n"
"v_cvt_pk_bf16_f32 v50, v100, v101\n"
"v_mfma_f32_16x16x32_bf16 v[232:235], a[48:51], v[56:59], v[232:235]\n"
"v_cvt_pk_bf16_f32 v51, v102, v103\n"
"v_cvt_pk_bf16_f32 v52, v104, v105\n"
"v_mfma_f32_16x16x32_bf16 v[232:235], a[52:55], v[60:63], v[232:235]\n"
"v_cvt_pk_bf16_f32 v53, v106, v107\n"
"v_cvt_pk_bf16_f32 v54, v108, v109\n"
"v_mfma_f32_16x16x32_bf16 v[236:239], a[56:59], v[56:59], v[236:239]\n"
"v_cvt_pk_bf16_f32 v55, v110, v111\n"
"ds_read_b64_tr_b16 a[32:33], v10 offset:0\n"
"v_mfma_f32_16x16x32_bf16 v[236:239], a[60:63], v[60:63], v[236:239]\n"
"ds_read_b64_tr_b16 a[34:35], v10 offset:4096\n"
"ds_read_b64_tr_b16 a[36:37], v10 offset:8192\n"
"v_mfma_f32_16x16x32_bf16 v[240:243], a[64:67], v[56:59], v[240:243]\n"
"ds_read_b64_tr_b16 a[38:39], v10 offset:12288\n"
"ds_read_b64_tr_b16 a[40:41], v11 offset:0\n"
"v_mfma_f32_16x16x32_bf16 v[240:243], a[68:71], v[60:63], v[240:243]\n"
"ds_read_b64_tr_b16 a[42:43], v11 offset:4096\n"
"ds_read_b64_tr_b16 a[44:45], v11 offset:8192\n"
"v_mfma_f32_16x16x32_bf16 v[244:247], a[72:75], v[56:59], v[244:247]\n"
"ds_read_b64_tr_b16 a[46:47], v11 offset:12288\n"
"ds_read_b64_tr_b16 a[48:49], v10 offset:256\n"
"v_mfma_f32_16x16x32_bf16 v[244:247], a[76:79], v[60:63], v[244:247]\n"
"ds_read_b64_tr_b16 a[50:51], v10 offset:4352\n"
"ds_read_b64_tr_b16 a[52:53], v10 offset:8448\n"
"v_mfma_f32_16x16x32_bf16 v[248:251], a[80:83], v[56:59], v[248:251]\n"
"buffer_load_dwordx4 v1, s[44:47], s60 offen offset:1024 nt lds\n"
"v_mfma_f32_16x16x32_bf16 v[248:251], a[84:87], v[60:63], v[248:251]\n"
"ds_read_b64_tr_b16 a[54:55], v10 offset:12544\n"
"ds_read_b64_tr_b16 a[56:57], v11 offset:256\n"
"v_mfma_f32_16x16x32_bf16 v[252:255], a[88:91], v[56:59], v[252:255]\n"
"v_cvt_pk_bf16_f32 v56, v112, v113\n"
"v_cvt_pk_bf16_f32 v57, v114, v115\n"
"v_mfma_f32_16x16x32_bf16 v[252:255], a[92:95], v[60:63], v[252:255]\n"
"s_sub_u32 s66, s66, 1\n"
"s_cmp_lg_u32 s66, 0\n"
"s_cbranch_scc1 .LG0a_%=\n"
"s_waitcnt lgkmcnt(15)\n"
"v_mfma_f32_16x16x32_bf16 v[64:67], a[0:3], a[192:195], 0\n"
"v_cvt_pk_bf16_f32 v58, v116, v117\n"
"v_cvt_pk_bf16_f32 v59, v118, v119\n"
"v_mfma_f32_16x16x32_bf16 v[64:67], a[4:7], a[196:199], v[64:67]\n"
"v_cvt_pk_bf16_f32 v60, v120, v121\n"
"v_cvt_pk_bf16_f32 v61, v122, v123\n"
"v_mfma_f32_16x16x32_bf16 v[64:67], a[8:11], a[200:203], v[64:67]\n"
"v_cvt_pk_bf16_f32 v62, v124, v125\n"
"v_cvt_pk_bf16_f32 v63, v126, v127\n"
"v_mfma_f32_16x16x32_bf16 v[64:67], a[12:15], a[204:207], v[64:67]\n"
"ds_read_b64_tr_b16 a[58:59], v11 offset:4352\n"
"ds_read_b64_tr_b16 a[60:61], v11 offset:8448\n"
"v_mfma_f32_16x16x32_bf16 v[80:83], a[0:3], a[208:211], 0\n"
"ds_read_b64_tr_b16 a[62:63], v11 offset:12544\n"
"ds_read_b64_tr_b16 a[64:65], v10 offset:512\n"
"v_mfma_f32_16x16x32_bf16 v[80:83], a[4:7], a[212:215], v[80:83]\n"
"ds_read_b64_tr_b16 a[66:67], v10 offset:4608\n"
"ds_read_b64_tr_b16 a[68:69], v10 offset:8704\n"
"v_mfma_f32_16x16x32_bf16 v[80:83], a[8:11], a[216:219], v[80:83]\n"
"ds_read_b64_tr_b16 a[70:71], v10 offset:12800\n"
"ds_read_b64_tr_b16 a[72:73], v11 offset:512\n"
"v_mfma_f32_16x16x32_bf16 v[80:83], a[12:15], a[220:223], v[80:83]\n"
"ds_read_b64_tr_b16 a[74:75], v11 offset:4608\n"
"ds_read_b64_tr_b16 a[76:77], v11 offset:8704\n"
"v_mfma_f32_16x16x32_bf16 v[96:99], a[0:3], a[224:227], 0\n"
"buffer_load_dwordx4 v2, s[44:47], s60 offen offset:2048 nt lds\n"
"v_mfma_f32_16x16x32_bf16 v[96:99], a[4:7], a[228:231], v[96:99]\n"
"ds_read_b64_tr_b16 a[78:79], v11 offset:12800\n"
"ds_read_b64_tr_b16 a[80:81], v10 offset:768\n"
"v_mfma_f32_16x16x32_bf16 v[96:99], a[8:11], a[232:235], v[96:99]\n"
"ds_read_b64_tr_b16 a[82:83], v10 offset:4864\n"
"ds_read_b64_tr_b16 a[84:85], v10 offset:8960\n"
"buffer_load_dword a117, v13, s[36:39], %[ma] offen sc1\n"
"v_mfma_f32_16x16x32_bf16 v[96:99], a[12:15], a[236:239], v[96:99]\n"
"ds_read_b64_tr_b16 a[86:87], v10 offset:13056\n"
"ds_read_b64_tr_b16 a[88:89], v11 offset:768\n"
"v_mfma_f32_16x16x32_bf16 v[112:115], a[0:3], a[240:243], 0\n"
"ds_read_b128 a[0:3], v6 offset:8192\n"
"ds_read_b64_tr_b16 a[90:91], v11 offset:4864\n"
"v_mfma_f32_16x16x32_bf16 v[112:115], a[4:7], a[244:247], v[112:115]\n"
"ds_read_b128 a[4:7], v7 offset:8192\n"
"ds_read_b64_tr_b16 a[92:93], v11 offset:8960\n"
"v_mfma_f32_16x16x32_bf16 v[112:115], a[8:11], a[248:251], v[112:115]\n"
"ds_read_b128 a[8:11], v8 offset:8192\n"
"ds_read_b64_tr_b16 a[94:95], v11 offset:13056\n"
"v_mfma_f32_16x16x32_bf16 v[112:115], a[12:15], a[252:255], v[112:115]\n"
"ds_read_b128 a[12:15], v9 offset:8192\n"
"v_max3_f32 v16, v64, v65, v66\n"
"v_max_f32 v16, v16, v67\n"
"v_mov_b32 v17, v16\n"
"s_nop 1\n"
"v_permlane32_swap_b32 v16, v17\n"
"v_max_f32 v16, v16, v17\n"
"v_mov_b32 v17, v16\n"
"s_nop 1\n"
"v_permlane16_swap_b32 v16, v17\n"
"v_min_f32 v18, -v16, -v17\n"
"v_mov_b32 v19, v18\n"
"v_mov_b32 v16, v18\n"
"v_mov_b32 v17, v18\n"
"s_nop 1\n"
"v_mfma_f32_16x16x32_bf16 v[68:71], a[16:19], a[192:195], v[16:19]\n"
"v_max3_f32 v20, v80, v81, v82\n"
"v_max_f32 v20, v20, v83\n"
"v_mfma_f32_16x16x32_bf16 v[68:71], a[20:23], a[196:199], v[68:71]\n"
"v_mov_b32 v21, v20\n"
"s_nop 1\n"
"v_permlane32_swap_b32 v20, v21\n"
"s_waitcnt lgkmcnt(15)\n"
"v_mfma_f32_16x16x32_bf16 v[68:71], a[24:27], a[200:203], v[68:71]\n"
"v_max_f32 v20, v20, v21\n"
"v_mov_b32 v21, v20\n"
"v_mfma_f32_16x16x32_bf16 v[68:71], a[28:31], a[204:207], v[68:71]\n"
"s_nop 0\n"
"v_permlane16_swap_b32 v20, v21\n"
"v_min_f32 v22, -v20, -v21\n"
"v_mov_b32 v23, v22\n"
"v_mov_b32 v20, v22\n"
"v_mov_b32 v21, v22\n"
"s_nop 1\n"
"v_mfma_f32_16x16x32_bf16 v[84:87], a[16:19], a[208:211], v[20:23]\n"
"buffer_load_dwordx4 v3, s[44:47], s60 offen offset:3072 nt lds\n"
"v_mfma_f32_16x16x32_bf16 v[84:87], a[20:23], a[212:215], v[84:87]\n"
"v_max3_f32 v24, v96, v97, v98\n"
"v_max_f32 v24, v24, v99\n"
"v_mfma_f32_16x16x32_bf16 v[84:87], a[24:27], a[216:219], v[84:87]\n"
"v_mov_b32 v25, v24\n"
"s_nop 1\n"
"v_permlane32_swap_b32 v24, v25\n"
"v_mfma_f32_16x16x32_bf16 v[84:87], a[28:31], a[220:223], v[84:87]\n"
"v_max_f32 v24, v24, v25\n"
"v_mov_b32 v25, v24\n"
"s_nop 1\n"
"v_permlane16_swap_b32 v24, v25\n"
"v_min_f32 v26, -v24, -v25\n"
"v_mov_b32 v27, v26\n"
"v_mov_b32 v24, v26\n"
"v_mov_b32 v25, v26\n"
"s_nop 1\n"
"v_mfma_f32_16x16x32_bf16 v[100:103], a[16:19], a[224:227], v[24:27]\n"
"v_max3_f32 v28, v112, v113, v114\n"
"v_max_f32 v28, v28, v115\n"
"v_mfma_f32_16x16x32_bf16 v[100:103], a[20:23], a[228:231], v[100:103]\n"
"v_mov_b32 v29, v28\n"
"s_nop 1\n"
"v_permlane32_swap_b32 v28, v29\n"
"v_mfma_f32_16x16x32_bf16 v[100:103], a[24:27], a[232:235], v[100:103]\n"
"v_max_f32 v28, v28, v29\n"
"v_mov_b32 v29, v28\n"
"v_mfma_f32_16x16x32_bf16 v[100:103], a[28:31], a[236:239], v[100:103]\n"
"s_nop 0\n"
"v_permlane16_swap_b32 v28, v29\n"
"v_min_f32 v30, -v28, -v29\n"
"v_mov_b32 v31, v30\n"
"v_mov_b32 v28, v30\n"
"v_mov_b32 v29, v30\n"
"s_nop 1\n"
"v_mfma_f32_16x16x32_bf16 v[116:119], a[16:19], a[240:243], v[28:31]\n"
"ds_read_b128 a[16:19], v6 offset:12288\n"
"v_add_f32 v64, v64, v18\n"
"v_mfma_f32_16x16x32_bf16 v[116:119], a[20:23], a[244:247], v[116:119]\n"
"ds_read_b128 a[20:23], v7 offset:12288\n"
"v_add_f32 v65, v65, v18\n"
"v_mfma_f32_16x16x32_bf16 v[116:119], a[24:27], a[248:251], v[116:119]\n"
"ds_read_b128 a[24:27], v8 offset:12288\n"
"v_add_f32 v66, v66, v18\n"
"v_mfma_f32_16x16x32_bf16 v[116:119], a[28:31], a[252:255], v[116:119]\n"
"s_mov_b32 m0, s63\n"
"ds_read_b128 a[28:31], v9 offset:12288\n"
"s_waitcnt lgkmcnt(10)\n"
"v_mfma_f32_16x16x32_bf16 v[72:75], a[0:3], a[192:195], v[16:19]\n"
"buffer_load_dwordx4 v4, s[48:51], s61 offen offset:0 nt lds\n"
"s_waitcnt lgkmcnt(8)\n"
"v_mfma_f32_16x16x32_bf16 v[72:75], a[4:7], a[196:199], v[72:75]\n"
"v_exp_f32 v64, v64\n"
"s_waitcnt lgkmcnt(6)\n"
"v_mfma_f32_16x16x32_bf16 v[72:75], a[8:11], a[200:203], v[72:75]\n"
"v_exp_f32 v65, v65\n"
"s_waitcnt lgkmcnt(4)\n"
"v_mfma_f32_16x16x32_bf16 v[72:75], a[12:15], a[204:207], v[72:75]\n"
"v_exp_f32 v66, v66\n"
"v_mfma_f32_16x16x32_bf16 v[88:91], a[0:3], a[208:211], v[20:23]\n"
"v_add_f32 v67, v67, v18\n"
"v_add_f32 v80, v80, v22\n"
"v_mfma_f32_16x16x32_bf16 v[88:91], a[4:7], a[212:215], v[88:91]\n"
"v_exp_f32 v67, v67\n"
"v_mfma_f32_16x16x32_bf16 v[88:91], a[8:11], a[216:219], v[88:91]\n"
"v_exp_f32 v68, v68\n"
"v_mfma_f32_16x16x32_bf16 v[88:91], a[12:15], a[220:223], v[88:91]\n"
"v_exp_f32 v69, v69\n"
"v_mfma_f32_16x16x32_bf16 v[104:107], a[0:3], a[224:227], v[24:27]\n"
"v_exp_f32 v70, v70\n"
"v_mfma_f32_16x16x32_bf16 v[104:107], a[4:7], a[228:231], v[104:107]\n"
"v_exp_f32 v71, v71\n"
"v_mfma_f32_16x16x32_bf16 v[104:107], a[8:11], a[232:235], v[104:107]\n"
"v_exp_f32 v84, v84\n"
"v_mfma_f32_16x16x32_bf16 v[104:107], a[12:15], a[236:239], v[104:107]\n"
"v_exp_f32 v85, v85\n"
"v_mfma_f32_16x16x32_bf16 v[120:123], a[0:3], a[240:243], v[28:31]\n"
"buffer_load_dwordx4 v5, s[48:51], s61 offen offset:1024 nt lds\n"
"v_mfma_f32_16x16x32_bf16 v[120:123], a[4:7], a[244:247], v[120:123]\n"
"v_exp_f32 v86, v86\n"
"v_mfma_f32_16x16x32_bf16 v[120:123], a[8:11], a[248:251], v[120:123]\n"
"v_exp_f32 v87, v87\n"
"v_mfma_f32_16x16x32_bf16 v[120:123], a[12:15], a[252:255], v[120:123]\n"
"v_exp_f32 v100, v100\n"
"s_waitcnt lgkmcnt(3)\n"
"v_mfma_f32_16x16x32_bf16 v[76:79], a[16:19], a[192:195], v[16:19]\n"
"v_exp_f32 v101, v101\n"
"s_waitcnt lgkmcnt(2)\n"
"v_mfma_f32_16x16x32_bf16 v[76:79], a[20:23], a[196:199], v[76:79]\n"
"v_exp_f32 v102, v102\n"
"s_waitcnt lgkmcnt(1)\n"
"v_mfma_f32_16x16x32_bf16 v[76:79], a[24:27], a[200:203], v[76:79]\n"
"v_exp_f32 v103, v103\n"
"s_waitcnt lgkmcnt(0)\n"
"v_mfma_f32_16x16x32_bf16 v[76:79], a[28:31], a[204:207], v[76:79]\n"
"v_exp_f32 v116, v116\n"
"v_mfma_f32_16x16x32_bf16 v[92:95], a[16:19], a[208:211], v[20:23]\n"
"v_exp_f32 v117, v117\n"
"v_mfma_f32_16x16x32_bf16 v[92:95], a[20:23], a[212:215], v[92:95]\n"
"v_exp_f32 v118, v118\n"
"v_mfma_f32_16x16x32_bf16 v[92:95], a[24:27], a[216:219], v[92:95]\n"
"v_exp_f32 v119, v119\n"
"v_mfma_f32_16x16x32_bf16 v[92:95], a[28:31], a[220:223], v[92:95]\n"
"v_exp_f32 v72, v72\n"
"v_mfma_f32_16x16x32_bf16 v[108:111], a[16:19], a[224:227], v[24:27]\n"
"buffer_load_dwordx4 v4, s[48:51], s61 offen offset:2048 nt lds\n"
"v_mfma_f32_16x16x32_bf16 v[108:111], a[20:23], a[228:231], v[108:111]\n"
"v_exp_f32 v73, v73\n"
"v_mfma_f32_16x16x32_bf16 v[108:111], a[24:27], a[232:235], v[108:111]\n"
"v_exp_f32 v74, v74\n"
"v_mfma_f32_16x16x32_bf16 v[108:111], a[28:31], a[236:239], v[108:111]\n"
"v_exp_f32 v75, v75\n"
"v_mfma_f32_16x16x32_bf16 v[124:127], a[16:19], a[240:243], v[28:31]\n"
"v_exp_f32 v88, v88\n"
"v_mfma_f32_16x16x32_bf16 v[124:127], a[20:23], a[244:247], v[124:127]\n"
"v_exp_f32 v89, v89\n"
"v_mfma_f32_16x16x32_bf16 v[124:127], a[24:27], a[248:251], v[124:127]\n"
"v_exp_f32 v90, v90\n"
"v_mfma_f32_16x16x32_bf16 v[124:127], a[28:31], a[252:255], v[124:127]\n"
"v_exp_f32 v91, v91\n"
"v_mfma_f32_16x16x32_bf16 a[96:99], a[112:115], v[32:35], a[96:99]\n"
"v_exp_f32 v104, v104\n"
"v_mfma_f32_16x16x32_bf16 a[96:99], a[112:115], v[36:39], a[96:99]\n"
"v_exp_f32 v105, v105\n"
"v_mfma_f32_16x16x32_bf16 v[128:131], a[32:35], v[32:35], v[128:131]\n"
"v_exp_f32 v106, v106\n"
"v_mfma_f32_16x16x32_bf16 v[128:131], a[36:39], v[36:39], v[128:131]\n"
"v_exp_f32 v107, v107\n"
"v_mfma_f32_16x16x32_bf16 v[132:135], a[40:43], v[32:35], v[132:135]\n"
"buffer_load_dwordx4 v5, s[48:51], s61 offen offset:3072 nt lds\n"
"v_mfma_f32_16x16x32_bf16 v[132:135], a[44:47], v[36:39], v[132:135]\n"
"v_xor_b32 v6, s64, v6\n"
"v_xor_b32 v7, s64, v7\n"
"v_mfma_f32_16x16x32_bf16 v[136:139], a[48:51], v[32:35], v[136:139]\n"
"v_xor_b32 v8, s64, v8\n"
"v_xor_b32 v9, s64, v9\n"
"v_mfma_f32_16x16x32_bf16 v[136:139], a[52:55], v[36:39], v[136:139]\n"
"v_xor_b32 v10, s65, v10\n"
"v_xor_b32 v11, s65, v11\n"
"v_mfma_f32_16x16x32_bf16 v[140:143], a[56:59], v[32:35], v[140:143]\n"
"s_xor_b32 s62, s62, s65\n"
"s_xor_b32 s63, s63, s64\n"
"v_mfma_f32_16x16x32_bf16 v[140:143], a[60:63], v[36:39], v[140:143]\n"
"s_add_u32 s60, s60, 0x4000\n"
"s_add_u32 s61, s61, 0x4000\n"
"v_mfma_f32_16x16x32_bf16 v[144:147], a[64:67], v[32:35], v[144:147]\n"
"s_xor_b32 s64, s64, 0xc000\n"
"s_xor_b32 s65, s65, 0xc000\n"
"v_mfma_f32_16x16x32_bf16 v[144:147], a[68:71], v[36:39], v[144:147]\n"
"v_exp_f32 v120, v120\n"
"v_mfma_f32_16x16x32_bf16 v[148:151], a[72:75], v[32:35], v[148:151]\n"
"v_exp_f32 v121, v121\n"
"v_mfma_f32_16x16x32_bf16 v[148:151], a[76:79], v[36:39], v[148:151]\n"
"v_exp_f32 v122, v122\n"
"v_mfma_f32_16x16x32_bf16 v[152:155], a[80:83], v[32:35], v[152:155]\n"
"v_exp_f32 v123, v123\n"
"v_mfma_f32_16x16x32_bf16 v[152:155], a[84:87], v[36:39], v[152:155]\n"
"v_exp_f32 v76, v76\n"
"v_mfma_f32_16x16x32_bf16 v[156:159], a[88:91], v[32:35], v[156:159]\n"
"v_cvt_pk_bf16_f32 v32, v64, v65\n"
"v_cvt_pk_bf16_f32 v33, v66, v67\n"
"v_mfma_f32_16x16x32_bf16 v[156:159], a[92:95], v[36:39], v[156:159]\n"
"v_cvt_pk_bf16_f32 v34, v68, v69\n"
"v_cvt_pk_bf16_f32 v35, v70, v71\n"
"v_mfma_f32_16x16x32_bf16 a[100:103], a[112:115], v[40:43], a[100:103]\n"
"v_cvt_pk_bf16_f32 v36, v72, v73\n"
"v_cvt_pk_bf16_f32 v37, v74, v75\n"
"v_mfma_f32_16x16x32_bf16 a[100:103], a[112:115], v[44:47], a[100:103]\n"
"v_exp_f32 v77, v77\n"
"v_mfma_f32_16x16x32_bf16 v[160:163], a[32:35], v[40:43], v[160:163]\n"
"v_exp_f32 v78, v78\n"
"v_mfma_f32_16x16x32_bf16 v[160:163], a[36:39], v[44:47], v[160:163]\n"
"v_exp_f32 v79, v79\n"
"v_mfma_f32_16x16x32_bf16 v[164:167], a[40:43], v[40:43], v[164:167]\n"
"v_cvt_pk_bf16_f32 v38, v76, v77\n"
"v_cvt_pk_bf16_f32 v39, v78, v79\n"
"v_mfma_f32_16x16x32_bf16 v[164:167], a[44:47], v[44:47], v[164:167]\n"
"v_exp_f32 v92, v92\n"
"v_mfma_f32_16x16x32_bf16 v[168:171], a[48:51], v[40:43], v[168:171]\n"
"v_exp_f32 v93, v93\n"
"v_mfma_f32_16x16x32_bf16 v[168:171], a[52:55], v[44:47], v[168:171]\n"
"v_exp_f32 v94, v94\n"
"v_mfma_f32_16x16x32_bf16 v[172:175], a[56:59], v[40:43], v[172:175]\n"
"v_exp_f32 v95, v95\n"
"v_mfma_f32_16x16x32_bf16 v[172:175], a[60:63], v[44:47], v[172:175]\n"
"v_exp_f32 v108, v108\n"
"v_mfma_f32_16x16x32_bf16 v[176:179], a[64:67], v[40:43], v[176:179]\n"
"v_exp_f32 v109, v109\n"
"v_mfma_f32_16x16x32_bf16 v[176:179], a[68:71], v[44:47], v[176:179]\n"
"v_exp_f32 v110, v110\n"
"v_mfma_f32_16x16x32_bf16 v[180:183], a[72:75], v[40:43], v[180:183]\n"
"v_exp_f32 v111, v111\n"
"v_mfma_f32_16x16x32_bf16 v[180:183], a[76:79], v[44:47], v[180:183]\n"
"v_exp_f32 v124, v124\n"
"v_mfma_f32_16x16x32_bf16 v[184:187], a[80:83], v[40:43], v[184:187]\n"
"v_exp_f32 v125, v125\n"
"v_mfma_f32_16x16x32_bf16 v[184:187], a[84:87], v[44:47], v[184:187]\n"
"v_exp_f32 v126, v126\n"
"v_mfma_f32_16x16x32_bf16 v[188:191], a[88:91], v[40:43], v[188:191]\n"
"v_exp_f32 v80, v80\n"
"v_add_f32 v81, v81, v22\n"
"v_exp_f32 v81, v81\n"
"s_nop 0\n"
"v_cvt_pk_bf16_f32 v40, v80, v81\n"
"v_add_f32 v82, v82, v22\n"
"v_exp_f32 v82, v82\n"
"v_add_f32 v83, v83, v22\n"
"v_exp_f32 v83, v83\n"
"s_nop 0\n"
"v_cvt_pk_bf16_f32 v41, v82, v83\n"
"v_mfma_f32_16x16x32_bf16 v[188:191], a[92:95], v[44:47], v[188:191]\n"
"v_cvt_pk_bf16_f32 v42, v84, v85\n"
"v_cvt_pk_bf16_f32 v43, v86, v87\n"
"v_mfma_f32_16x16x32_bf16 a[104:107], a[112:115], v[48:51], a[104:107]\n"
"v_cvt_pk_bf16_f32 v44, v88, v89\n"
"v_cvt_pk_bf16_f32 v45, v90, v91\n"
"v_mfma_f32_16x16x32_bf16 a[104:107], a[112:115], v[52:55], a[104:107]\n"
"v_cvt_pk_bf16_f32 v46, v92, v93\n"
"v_cvt_pk_bf16_f32 v47, v94, v95\n"
"v_mfma_f32_16x16x32_bf16 v[192:195], a[32:35], v[48:51], v[192:195]\n"
"v_exp_f32 v127, v127\n"
"v_mfma_f32_16x16x32_bf16 v[192:195], a[36:39], v[52:55], v[192:195]\n"
"v_add_f32 v96, v96, v26\n"
"v_add_f32 v97, v97, v26\n"
"v_mfma_f32_16x16x32_bf16 v[196:199], a[40:43], v[48:51], v[196:199]\n"
"v_exp_f32 v96, v96\n"
"v_mfma_f32_16x16x32_bf16 v[196:199], a[44:47], v[52:55], v[196:199]\n"
"v_exp_f32 v97, v97\n"
"v_mfma_f32_16x16x32_bf16 v[200:203], a[48:51], v[48:51], v[200:203]\n"
"v_add_f32 v98, v98, v26\n"
"v_add_f32 v99, v99, v26\n"
"v_mfma_f32_16x16x32_bf16 v[200:203], a[52:55], v[52:55], v[200:203]\n"
"v_exp_f32 v98, v98\n"
"v_mfma_f32_16x16x32_bf16 v[204:207], a[56:59], v[48:51], v[204:207]\n"
"v_exp_f32 v99, v99\n"
"v_mfma_f32_16x16x32_bf16 v[204:207], a[60:63], v[52:55], v[204:207]\n"
"v_add_f32 v112, v112, v30\n"
"v_add_f32 v113, v113, v30\n"
"v_mfma_f32_16x16x32_bf16 v[208:211], a[64:67], v[48:51], v[208:211]\n"
"v_exp_f32 v112, v112\n"
"v_mfma_f32_16x16x32_bf16 v[208:211], a[68:71], v[52:55], v[208:211]\n"
"v_exp_f32 v113, v113\n"
"v_mfma_f32_16x16x32_bf16 v[212:215], a[72:75], v[48:51], v[212:215]\n"
"v_add_f32 v114, v114, v30\n"
"v_add_f32 v115, v115, v30\n"
"v_mfma_f32_16x16x32_bf16 v[212:215], a[76:79], v[52:55], v[212:215]\n"
"v_exp_f32 v114, v114\n"
"v_mfma_f32_16x16x32_bf16 v[216:219], a[80:83], v[48:51], v[216:219]\n"
"v_exp_f32 v115, v115\n"
"v_mfma_f32_16x16x32_bf16 v[216:219], a[84:87], v[52:55], v[216:219]\n"
"s_mov_b32 s66, 4\n"
"s_mov_b32 s56, 0\n"
"s_mov_b32 s57, 131072\n"
"s_mov_b32 s58, 262144\n"
"s_mov_b32 s59, 393216\n"
"v_accvgpr_read_b32 v14, a96\n"
"v_mfma_f32_16x16x32_bf16 v[220:223], a[88:91], v[48:51], v[220:223]\n"
"s_waitcnt vmcnt(17)\n"
"s_barrier\n"
"v_mfma_f32_16x16x32_bf16 v[220:223], a[92:95], v[52:55], v[220:223]\n"
"ds_read_b128 a[0:3], v6 offset:0\n"
"ds_read_b128 a[4:7], v7 offset:0\n"
"v_mfma_f32_16x16x32_bf16 a[108:111], a[112:115], v[56:59], a[108:111]\n"
"ds_read_b128 a[8:11], v8 offset:0\n"
"ds_read_b128 a[12:15], v9 offset:0\n"
"v_mfma_f32_16x16x32_bf16 a[108:111], a[112:115], v[60:63], a[108:111]\n"
"s_mov_b32 m0, s62\n"
"v_cvt_pk_bf16_f32 v48, v96, v97\n"
"v_mfma_f32_16x16x32_bf16 v[224:227], a[32:35], v[56:59], v[224:227]\n"
"buffer_load_dwordx4 v0, s[44:47], s60 offen offset:0 nt lds\n"
"v_mfma_f32_16x16x32_bf16 v[224:227], a[36:39], v[60:63], v[224:227]\n"
"v_cvt_pk_bf16_f32 v49, v98, v99\n"
"ds_read_b128 a[16:19], v6 offset:4096\n"
"v_mfma_f32_16x16x32_bf16 v[228:231], a[40:43], v[56:59], v[228:231]\n"
"ds_read_b128 a[20:23], v7 offset:4096\n"
"ds_read_b128 a[24:27], v8 offset:4096\n"
"v_mfma_f32_16x16x32_bf16 v[228:231], a[44:47], v[60:63], v[228:231]\n"
"ds_read_b128 a[28:31], v9 offset:4096\n"
"v_cvt_pk_bf16_f32 v50, v100, v101\n"
"v_mfma_f32_16x16x32_bf16 v[232:235], a[48:51], v[56:59], v[232:235]\n"
"v_cvt_pk_bf16_f32 v51, v102, v103\n"
"v_cvt_pk_bf16_f32 v52, v104, v105\n"
"v_mfma_f32_16x16x32_bf16 v[232:235], a[52:55], v[60:63], v[232:235]\n"
"v_cvt_pk_bf16_f32 v53, v106, v107\n"
"v_cvt_pk_bf16_f32 v54, v108, v109\n"
"v_mfma_f32_16x16x32_bf16 v[236:239], a[56:59], v[56:59], v[236:239]\n"
"v_cvt_pk_bf16_f32 v55, v110, v111\n"
"ds_read_b64_tr_b16 a[32:33], v10 offset:0\n"
"v_mfma_f32_16x16x32_bf16 v[236:239], a[60:63], v[60:63], v[236:239]\n"
"ds_read_b64_tr_b16 a[34:35], v10 offset:4096\n"
"ds_read_b64_tr_b16 a[36:37], v10 offset:8192\n"
"v_mfma_f32_16x16x32_bf16 v[240:243], a[64:67], v[56:59], v[240:243]\n"
"ds_read_b64_tr_b16 a[38:39], v10 offset:12288\n"
"ds_read_b64_tr_b16 a[40:41], v11 offset:0\n"
"v_mfma_f32_16x16x32_bf16 v[240:243], a[68:71], v[60:63], v[240:243]\n"
"ds_read_b64_tr_b16 a[42:43], v11 offset:4096\n"
"ds_read_b64_tr_b16 a[44:45], v11 offset:8192\n"
"v_mfma_f32_16x16x32_bf16 v[244:247], a[72:75], v[56:59], v[244:247]\n"
"ds_read_b64_tr_b16 a[46:47], v11 offset:12288\n"
"ds_read_b64_tr_b16 a[48:49], v10 offset:256\n"
"v_mfma_f32_16x16x32_bf16 v[244:247], a[76:79], v[60:63], v[244:247]\n"
"ds_read_b64_tr_b16 a[50:51], v10 offset:4352\n"
"ds_read_b64_tr_b16 a[52:53], v10 offset:8448\n"
"v_mfma_f32_16x16x32_bf16 v[248:251], a[80:83], v[56:59], v[248:251]\n"
"buffer_load_dwordx4 v1, s[44:47], s60 offen offset:1024 nt lds\n"
"v_mfma_f32_16x16x32_bf16 v[248:251], a[84:87], v[60:63], v[248:251]\n"
"ds_read_b64_tr_b16 a[54:55], v10 offset:12544\n"
"ds_read_b64_tr_b16 a[56:57], v11 offset:256\n"
"v_mfma_f32_16x16x32_bf16 v[252:255], a[88:91], v[56:59], v[252:255]\n"
"v_cvt_pk_bf16_f32 v56, v112, v113\n"
"v_cvt_pk_bf16_f32 v57, v114, v115\n"
"v_mfma_f32_16x16x32_bf16 v[252:255], a[92:95], v[60:63], v[252:255]\n"
"v_cmp_ngt_f32 vcc, s67, v14\n"
"s_mov_b32 s72, 0\n"
"s_or_b64 s[70:71], s[70:71], vcc\n"
"s_waitcnt lgkmcnt(15)\n"
"v_mfma_f32_16x16x32_bf16 v[64:67], a[0:3], a[192:195], v[16:19]\n"
"v_cvt_pk_bf16_f32 v58, v116, v117\n"
"v_cvt_pk_bf16_f32 v59, v118, v119\n"
"v_mfma_f32_16x16x32_bf16 v[64:67], a[4:7], a[196:199], v[64:67]\n"
"v_cvt_pk_bf16_f32 v60, v120, v121\n"
"v_cvt_pk_bf16_f32 v61, v122, v123\n"
"v_mfma_f32_16x16x32_bf16 v[64:67], a[8:11], a[200:203], v[64:67]\n"
"v_cvt_pk_bf16_f32 v62, v124, v125\n"
"v_cvt_pk_bf16_f32 v63, v126, v127\n"
"v_mfma_f32_16x16x32_bf16 v[64:67], a[12:15], a[204:207], v[64:67]\n"
"ds_read_b64_tr_b16 a[58:59], v11 offset:4352\n"
"ds_read_b64_tr_b16 a[60:61], v11 offset:8448\n"
"v_mfma_f32_16x16x32_bf16 v[80:83], a[0:3], a[208:211], v[20:23]\n"
"ds_read_b64_tr_b16 a[62:63], v11 offset:12544\n"
"ds_read_b64_tr_b16 a[64:65], v10 offset:512\n"
"v_mfma_f32_16x16x32_bf16 v[80:83], a[4:7], a[212:215], v[80:83]\n"
"ds_read_b64_tr_b16 a[66:67], v10 offset:4608\n"
"ds_read_b64_tr_b16 a[68:69], v10 offset:8704\n"
"v_mfma_f32_16x16x32_bf16 v[80:83], a[8:11], a[216:219], v[80:83]\n"
"ds_read_b64_tr_b16 a[70:71], v10 offset:12800\n"
"ds_read_b64_tr_b16 a[72:73], v11 offset:512\n"
"v_mfma_f32_16x16x32_bf16 v[80:83], a[12:15], a[220:223], v[80:83]\n"
"ds_read_b64_tr_b16 a[74:75], v11 offset:4608\n"
"ds_read_b64_tr_b16 a[76:77], v11 offset:8704\n"
"v_mfma_f32_16x16x32_bf16 v[96:99], a[0:3], a[224:227], v[24:27]\n"
"buffer_load_dwordx4 v2, s[44:47], s60 offen offset:2048 nt lds\n"
"v_mfma_f32_16x16x32_bf16 v[96:99], a[4:7], a[228:231], v[96:99]\n"
"ds_read_b64_tr_b16 a[78:79], v11 offset:12800\n"
"ds_read_b64_tr_b16 a[80:81], v10 offset:768\n"
"v_mfma_f32_16x16x32_bf16 v[96:99], a[8:11], a[232:235], v[96:99]\n"
"ds_read_b64_tr_b16 a[82:83], v10 offset:4864\n"
"ds_read_b64_tr_b16 a[84:85], v10 offset:8960\n"
"v_mfma_f32_16x16x32_bf16 v[96:99], a[12:15], a[236:239], v[96:99]\n"
"ds_read_b64_tr_b16 a[86:87], v10 offset:13056\n"
"ds_read_b64_tr_b16 a[88:89], v11 offset:768\n"
"v_mfma_f32_16x16x32_bf16 v[112:115], a[0:3], a[240:243], v[28:31]\n"
"ds_read_b128 a[0:3], v6 offset:8192\n"
"ds_read_b64_tr_b16 a[90:91], v11 offset:4864\n"
"v_mfma_f32_16x16x32_bf16 v[112:115], a[4:7], a[244:247], v[112:115]\n"
"ds_read_b128 a[4:7], v7 offset:8192\n"
"ds_read_b64_tr_b16 a[92:93], v11 offset:8960\n"
"v_mfma_f32_16x16x32_bf16 v[112:115], a[8:11], a[248:251], v[112:115]\n"
"ds_read_b128 a[8:11], v8 offset:8192\n"
"ds_read_b64_tr_b16 a[94:95], v11 offset:13056\n"
"v_mfma_f32_16x16x32_bf16 v[112:115], a[12:15], a[252:255], v[112:115]\n"
"ds_read_b128 a[12:15], v9 offset:8192\n"
"v_accvgpr_read_b32 v15, a100\n"
"v_mfma_f32_16x16x32_bf16 v[68:71], a[16:19], a[192:195], v[16:19]\n"
"v_exp_f32 v64, v64\n"
"v_mfma_f32_16x16x32_bf16 v[68:71], a[20:23], a[196:199], v[68:71]\n"
"v_exp_f32 v65, v65\n"
"s_waitcnt lgkmcnt(15)\n"
"v_mfma_f32_16x16x32_bf16 v[68:71], a[24:27], a[200:203], v[68:71]\n"
"v_exp_f32 v66, v66\n"
"v_mfma_f32_16x16x32_bf16 v[68:71], a[28:31], a[204:207], v[68:71]\n"
"v_exp_f32 v67, v67\n"
"v_mfma_f32_16x16x32_bf16 v[84:87], a[16:19], a[208:211], v[20:23]\n"
"buffer_load_dwordx4 v3, s[44:47], s60 offen offset:3072 nt lds\n"
"v_mfma_f32_16x16x32_bf16 v[84:87], a[20:23], a[212:215], v[84:87]\n"
"v_exp_f32 v80, v80\n"
"v_mfma_f32_16x16x32_bf16 v[84:87], a[24:27], a[216:219], v[84:87]\n"
"v_exp_f32 v81, v81\n"
"v_mfma_f32_16x16x32_bf16 v[84:87], a[28:31], a[220:223], v[84:87]\n"
"v_exp_f32 v82, v82\n"
"v_mfma_f32_16x16x32_bf16 v[100:103], a[16:19], a[224:227], v[24:27]\n"
"v_exp_f32 v83, v83\n"
"v_mfma_f32_16x16x32_bf16 v[100:103], a[20:23], a[228:231], v[100:103]\n"
"v_exp_f32 v96, v96\n"
"v_mfma_f32_16x16x32_bf16 v[100:103], a[24:27], a[232:235], v[100:103]\n"
"v_exp_f32 v97, v97\n"
"v_mfma_f32_16x16x32_bf16 v[100:103], a[28:31], a[236:239], v[100:103]\n"
"v_exp_f32 v98, v98\n"
"v_mfma_f32_16x16x32_bf16 v[116:119], a[16:19], a[240:243], v[28:31]\n"
"ds_read_b128 a[16:19], v6 offset:12288\n"
"v_mfma_f32_16x16x32_bf16 v[116:119], a[20:23], a[244:247], v[116:119]\n"
"ds_read_b128 a[20:23], v7 offset:12288\n"
"v_mfma_f32_16x16x32_bf16 v[116:119], a[24:27], a[248:251], v[116:119]\n"
"ds_read_b128 a[24:27], v8 offset:12288\n"
"v_mfma_f32_16x16x32_bf16 v[116:119], a[28:31], a[252:255], v[116:119]\n"
"s_mov_b32 m0, s63\n"
"ds_read_b128 a[28:31], v9 offset:12288\n"
"s_waitcnt lgkmcnt(10)\n"
"v_mfma_f32_16x16x32_bf16 v[72:75], a[0:3], a[192:195], v[16:19]\n"
"buffer_load_dwordx4 v4, s[48:51], s61 offen offset:0 nt lds\n"
"s_waitcnt lgkmcnt(8)\n"
"v_mfma_f32_16x16x32_bf16 v[72:75], a[4:7], a[196:199], v[72:75]\n"
"v_exp_f32 v99, v99\n"
"s_waitcnt lgkmcnt(6)\n"
"v_mfma_f32_16x16x32_bf16 v[72:75], a[8:11], a[200:203], v[72:75]\n"
"v_exp_f32 v112, v112\n"
"s_waitcnt lgkmcnt(4)\n"
"v_mfma_f32_16x16x32_bf16 v[72:75], a[12:15], a[204:207], v[72:75]\n"
"v_exp_f32 v113, v113\n"
"v_mfma_f32_16x16x32_bf16 v[88:91], a[0:3], a[208:211], v[20:23]\n"
"v_exp_f32 v114, v114\n"
"v_mfma_f32_16x16x32_bf16 v[88:91], a[4:7], a[212:215], v[88:91]\n"
"v_exp_f32 v115, v115\n"
"v_mfma_f32_16x16x32_bf16 v[88:91], a[8:11], a[216:219], v[88:91]\n"
"v_exp_f32 v68, v68\n"
"v_mfma_f32_16x16x32_bf16 v[88:91], a[12:15], a[220:223], v[88:91]\n"
"v_exp_f32 v69, v69\n"
"v_mfma_f32_16x16x32_bf16 v[104:107], a[0:3], a[224:227], v[24:27]\n"
"v_exp_f32 v70, v70\n"
"v_mfma_f32_16x16x32_bf16 v[104:107], a[4:7], a[228:231], v[104:107]\n"
"v_exp_f32 v71, v71\n"
"v_mfma_f32_16x16x32_bf16 v[104:107], a[8:11], a[232:235], v[104:107]\n"
"v_exp_f32 v84, v84\n"
"v_mfma_f32_16x16x32_bf16 v[104:107], a[12:15], a[236:239], v[104:107]\n"
"v_exp_f32 v85, v85\n"
"v_mfma_f32_16x16x32_bf16 v[120:123], a[0:3], a[240:243], v[28:31]\n"
"buffer_load_dwordx4 v5, s[48:51], s61 offen offset:1024 nt lds\n"
"v_mfma_f32_16x16x32_bf16 v[120:123], a[4:7], a[244:247], v[120:123]\n"
"v_exp_f32 v86, v86\n"
"v_mfma_f32_16x16x32_bf16 v[120:123], a[8:11], a[248:251], v[120:123]\n"
"v_exp_f32 v87, v87\n"
"v_mfma_f32_16x16x32_bf16 v[120:123], a[12:15], a[252:255], v[120:123]\n"
"v_exp_f32 v100, v100\n"
"s_waitcnt lgkmcnt(3)\n"
"v_mfma_f32_16x16x32_bf16 v[76:79], a[16:19], a[192:195], v[16:19]\n"
"v_exp_f32 v101, v101\n"
"s_waitcnt lgkmcnt(2)\n"
"v_mfma_f32_16x16x32_bf16 v[76:79], a[20:23], a[196:199], v[76:79]\n"
"v_exp_f32 v102, v102\n"
"s_waitcnt lgkmcnt(1)\n"
"v_mfma_f32_16x16x32_bf16 v[76:79], a[24:27], a[200:203], v[76:79]\n"
"v_exp_f32 v103, v103\n"
"s_waitcnt lgkmcnt(0)\n"
"v_mfma_f32_16x16x32_bf16 v[76:79], a[28:31], a[204:207], v[76:79]\n"
"v_exp_f32 v116, v116\n"
"v_mfma_f32_16x16x32_bf16 v[92:95], a[16:19], a[208:211], v[20:23]\n"
"v_exp_f32 v117, v117\n"
"v_mfma_f32_16x16x32_bf16 v[92:95], a[20:23], a[212:215], v[92:95]\n"
"v_exp_f32 v118, v118\n"
"v_mfma_f32_16x16x32_bf16 v[92:95], a[24:27], a[216:219], v[92:95]\n"
"v_exp_f32 v119, v119\n"
"v_mfma_f32_16x16x32_bf16 v[92:95], a[28:31], a[220:223], v[92:95]\n"
"v_exp_f32 v72, v72\n"
"v_mfma_f32_16x16x32_bf16 v[108:111], a[16:19], a[224:227], v[24:27]\n"
"buffer_load_dwordx4 v4, s[48:51], s61 offen offset:2048 nt lds\n"
"v_mfma_f32_16x16x32_bf16 v[108:111], a[20:23], a[228:231], v[108:111]\n"
"v_exp_f32 v73, v73\n"
"v_mfma_f32_16x16x32_bf16 v[108:111], a[24:27], a[232:235], v[108:111]\n"
"v_exp_f32 v74, v74\n"
"v_mfma_f32_16x16x32_bf16 v[108:111], a[28:31], a[236:239], v[108:111]\n"
"v_exp_f32 v75, v75\n"
"v_mfma_f32_16x16x32_bf16 v[124:127], a[16:19], a[240:243], v[28:31]\n"
"v_exp_f32 v88, v88\n"
"v_mfma_f32_16x16x32_bf16 v[124:127], a[20:23], a[244:247], v[124:127]\n"
"v_exp_f32 v89, v89\n"
"v_mfma_f32_16x16x32_bf16 v[124:127], a[24:27], a[248:251], v[124:127]\n"
"v_exp_f32 v90, v90\n"
"v_mfma_f32_16x16x32_bf16 v[124:127], a[28:31], a[252:255], v[124:127]\n"
"v_exp_f32 v91, v91\n"
"v_mfma_f32_16x16x32_bf16 a[96:99], a[112:115], v[32:35], 0\n"
"v_exp_f32 v104, v104\n"
"v_rcp_f32 v14, v14\n"
"s_nop 0\n"
"v_mul_f32 v128, v128, v14\n"
"v_mul_f32 v129, v129, v14\n"
"v_mul_f32 v130, v130, v14\n"
"v_mul_f32 v131, v131, v14\n"
"v_mul_f32 v132, v132, v14\n"
"v_mul_f32 v133, v133, v14\n"
"v_mul_f32 v134, v134, v14\n"
"v_mul_f32 v135, v135, v14\n"
"v_cvt_pk_bf16_f32 v128, v128, v129\n"
"v_cvt_pk_bf16_f32 v129, v130, v131\n"
"v_cvt_pk_bf16_f32 v130, v132, v133\n"
"v_cvt_pk_bf16_f32 v131, v134, v135\n"
"v_accvgpr_write_b32 a128, v128\n"
"v_accvgpr_write_b32 a129, v129\n"
"v_accvgpr_write_b32 a130, v130\n"
"v_accvgpr_write_b32 a131, v131\n"
"v_mfma_f32_16x16x32_bf16 a[96:99], a[112:115], v[36:39], a[96:99]\n"
"v_exp_f32 v105, v105\n"
"v_mfma_f32_16x16x32_bf16 v[128:131], a[32:35], v[32:35], 0\n"
"v_exp_f32 v106, v106\n"
"v_mfma_f32_16x16x32_bf16 v[128:131], a[36:39], v[36:39], v[128:131]\n"
"v_exp_f32 v107, v107\n"
"v_mfma_f32_16x16x32_bf16 v[132:135], a[40:43], v[32:35], 0\n"
"buffer_load_dwordx4 v5, s[48:51], s61 offen offset:3072 nt lds\n"
"v_mul_f32 v136, v136, v14\n"
"v_mul_f32 v137, v137, v14\n"
"v_mul_f32 v138, v138, v14\n"
"v_mul_f32 v139, v139, v14\n"
"v_mul_f32 v140, v140, v14\n"
"v_mul_f32 v141, v141, v14\n"
"v_mul_f32 v142, v142, v14\n"
"v_mul_f32 v143, v143, v14\n"
"v_cvt_pk_bf16_f32 v136, v136, v137\n"
"v_cvt_pk_bf16_f32 v137, v138, v139\n"
"v_cvt_pk_bf16_f32 v138, v140, v141\n"
"v_cvt_pk_bf16_f32 v139, v142, v143\n"
"v_accvgpr_write_b32 a144, v136\n"
"v_accvgpr_write_b32 a145, v137\n"
"v_accvgpr_write_b32 a146, v138\n"
"v_accvgpr_write_b32 a147, v139\n"
"v_mfma_f32_16x16x32_bf16 v[132:135], a[44:47], v[36:39], v[132:135]\n"
"v_xor_b32 v6, s64, v6\n"
"v_xor_b32 v7, s64, v7\n"
"v_mfma_f32_16x16x32_bf16 v[136:139], a[48:51], v[32:35], 0\n"
"v_xor_b32 v8, s64, v8\n"
"v_xor_b32 v9, s64, v9\n"
"v_mfma_f32_16x16x32_bf16 v[136:139], a[52:55], v[36:39], v[136:139]\n"
"v_xor_b32 v10, s65, v10\n"
"v_xor_b32 v11, s65, v11\n"
"v_mfma_f32_16x16x32_bf16 v[140:143], a[56:59], v[32:35], 0\n"
"s_xor_b32 s62, s62, s65\n"
"s_xor_b32 s63, s63, s64\n"
"v_mul_f32 v144, v144, v14\n"
"v_mul_f32 v145, v145, v14\n"
"v_mul_f32 v146, v146, v14\n"
"v_mul_f32 v147, v147, v14\n"
"v_mul_f32 v148, v148, v14\n"
"v_mul_f32 v149, v149, v14\n"
"v_mul_f32 v150, v150, v14\n"
"v_mul_f32 v151, v151, v14\n"
"v_cvt_pk_bf16_f32 v144, v144, v145\n"
"v_cvt_pk_bf16_f32 v145, v146, v147\n"
"v_cvt_pk_bf16_f32 v146, v148, v149\n"
"v_cvt_pk_bf16_f32 v147, v150, v151\n"
"v_accvgpr_write_b32 a160, v144\n"
"v_accvgpr_write_b32 a161, v145\n"
"v_accvgpr_write_b32 a162, v146\n"
"v_accvgpr_write_b32 a163, v147\n"
"v_mfma_f32_16x16x32_bf16 v[140:143], a[60:63], v[36:39], v[140:143]\n"
"s_add_u32 s60, s60, 0x4000\n"
"s_add_u32 s61, s61, 0x4000\n"
"v_mfma_f32_16x16x32_bf16 v[144:147], a[64:67], v[32:35], 0\n"
"s_xor_b32 s64, s64, 0xc000\n"
"s_xor_b32 s65, s65, 0xc000\n"
"v_mfma_f32_16x16x32_bf16 v[144:147], a[68:71], v[36:39], v[144:147]\n"
"v_exp_f32 v120, v120\n"
"v_mfma_f32_16x16x32_bf16 v[148:151], a[72:75], v[32:35], 0\n"
"v_exp_f32 v121, v121\n"
"v_mul_f32 v152, v152, v14\n"
"v_mul_f32 v153, v153, v14\n"
"v_mul_f32 v154, v154, v14\n"
"v_mul_f32 v155, v155, v14\n"
"v_mul_f32 v156, v156, v14\n"
"v_mul_f32 v157, v157, v14\n"
"v_mul_f32 v158, v158, v14\n"
"v_mul_f32 v159, v159, v14\n"
"v_cvt_pk_bf16_f32 v152, v152, v153\n"
"v_cvt_pk_bf16_f32 v153, v154, v155\n"
"v_cvt_pk_bf16_f32 v154, v156, v157\n"
"v_cvt_pk_bf16_f32 v155, v158, v159\n"
"v_accvgpr_write_b32 a176, v152\n"
"v_accvgpr_write_b32 a177, v153\n"
"v_accvgpr_write_b32 a178, v154\n"
"v_accvgpr_write_b32 a179, v155\n"
"v_mfma_f32_16x16x32_bf16 v[148:151], a[76:79], v[36:39], v[148:151]\n"
"v_exp_f32 v122, v122\n"
"v_mfma_f32_16x16x32_bf16 v[152:155], a[80:83], v[32:35], 0\n"
"v_exp_f32 v123, v123\n"
"v_mfma_f32_16x16x32_bf16 v[152:155], a[84:87], v[36:39], v[152:155]\n"
"v_exp_f32 v76, v76\n"
"v_mfma_f32_16x16x32_bf16 v[156:159], a[88:91], v[32:35], 0\n"
"v_cvt_pk_bf16_f32 v32, v64, v65\n"
"v_cvt_pk_bf16_f32 v33, v66, v67\n"
"v_mfma_f32_16x16x32_bf16 v[156:159], a[92:95], v[36:39], v[156:159]\n"
"v_cvt_pk_bf16_f32 v34, v68, v69\n"
"v_cvt_pk_bf16_f32 v35, v70, v71\n"
"v_mfma_f32_16x16x32_bf16 a[100:103], a[112:115], v[40:43], 0\n"
"v_cvt_pk_bf16_f32 v36, v72, v73\n"
"v_cvt_pk_bf16_f32 v37, v74, v75\n"
"v_cmp_ngt_f32 vcc, s67, v15\n"
"s_mov_b32 s73, 131072\n"
"s_or_b64 s[70:71], s[70:71], vcc\n"
"v_rcp_f32 v15, v15\n"
"s_nop 0\n"
"v_mul_f32 v160, v160, v15\n"
"v_mul_f32 v161, v161, v15\n"
"v_mul_f32 v162, v162, v15\n"
"v_mul_f32 v163, v163, v15\n"
"v_mul_f32 v164, v164, v15\n"
"v_mul_f32 v165, v165, v15\n"
"v_mul_f32 v166, v166, v15\n"
"v_mul_f32 v167, v167, v15\n"
"v_cvt_pk_bf16_f32 v160, v160, v161\n"
"v_cvt_pk_bf16_f32 v161, v162, v163\n"
"v_cvt_pk_bf16_f32 v162, v164, v165\n"
"v_cvt_pk_bf16_f32 v163, v166, v167\n"
"v_accvgpr_write_b32 a132, v160\n"
"v_accvgpr_write_b32 a133, v161\n"
"v_accvgpr_write_b32 a134, v162\n"
"v_accvgpr_write_b32 a135, v163\n"
"v_mfma_f32_16x16x32_bf16 a[100:103], a[112:115], v[44:47], a[100:103]\n"
"v_exp_f32 v77, v77\n"
"v_mfma_f32_16x16x32_bf16 v[160:163], a[32:35], v[40:43], 0\n"
"v_exp_f32 v78, v78\n"
"v_mfma_f32_16x16x32_bf16 v[160:163], a[36:39], v[44:47], v[160:163]\n"
"v_exp_f32 v79, v79\n"
"v_mfma_f32_16x16x32_bf16 v[164:167], a[40:43], v[40:43], 0\n"
"v_cvt_pk_bf16_f32 v38, v76, v77\n"
"v_cvt_pk_bf16_f32 v39, v78, v79\n"
"v_mul_f32 v168, v168, v15\n"
"v_mul_f32 v169, v169, v15\n"
"v_mul_f32 v170, v170, v15\n"
"v_mul_f32 v171, v171, v15\n"
"v_mul_f32 v172, v172, v15\n"
"v_mul_f32 v173, v173, v15\n"
"v_mul_f32 v174, v174, v15\n"
"v_mul_f32 v175, v175, v15\n"
"v_cvt_pk_bf16_f32 v168, v168, v169\n"
"v_cvt_pk_bf16_f32 v169, v170, v171\n"
"v_cvt_pk_bf16_f32 v170, v172, v173\n"
"v_cvt_pk_bf16_f32 v171, v174, v175\n"
"v_accvgpr_write_b32 a148, v168\n"
"v_accvgpr_write_b32 a149, v169\n"
"v_accvgpr_write_b32 a150, v170\n"
"v_accvgpr_write_b32 a151, v171\n"
"v_mfma_f32_16x16x32_bf16 v[164:167], a[44:47], v[44:47], v[164:167]\n"
"v_exp_f32 v92, v92\n"
"v_mfma_f32_16x16x32_bf16 v[168:171], a[48:51], v[40:43], 0\n"
"v_exp_f32 v93, v93\n"
"v_mfma_f32_16x16x32_bf16 v[168:171], a[52:55], v[44:47], v[168:171]\n"
"v_exp_f32 v94, v94\n"
"v_mfma_f32_16x16x32_bf16 v[172:175], a[56:59], v[40:43], 0\n"
"v_exp_f32 v95, v95\n"
"v_mul_f32 v176, v176, v15\n"
"v_mul_f32 v177, v177, v15\n"
"v_mul_f32 v178, v178, v15\n"
"v_mul_f32 v179, v179, v15\n"
"v_mul_f32 v180, v180, v15\n"
"v_mul_f32 v181, v181, v15\n"
"v_mul_f32 v182, v182, v15\n"
"v_mul_f32 v183, v183, v15\n"
"v_cvt_pk_bf16_f32 v176, v176, v177\n"
"v_cvt_pk_bf16_f32 v177, v178, v179\n"
"v_cvt_pk_bf16_f32 v178, v180, v181\n"
"v_cvt_pk_bf16_f32 v179, v182, v183\n"
"v_accvgpr_write_b32 a164, v176\n"
"v_accvgpr_write_b32 a165, v177\n"
"v_accvgpr_write_b32 a166, v178\n"
"v_accvgpr_write_b32 a167, v179\n"
"v_mfma_f32_16x16x32_bf16 v[172:175], a[60:63], v[44:47], v[172:175]\n"
"v_exp_f32 v108, v108\n"
"v_mfma_f32_16x16x32_bf16 v[176:179], a[64:67], v[40:43], 0\n"
"v_exp_f32 v109, v109\n"
"v_mfma_f32_16x16x32_bf16 v[176:179], a[68:71], v[44:47], v[176:179]\n"
"v_exp_f32 v110, v110\n"
"v_mfma_f32_16x16x32_bf16 v[180:183], a[72:75], v[40:43], 0\n"
"v_exp_f32 v111, v111\n"
"v_mul_f32 v184, v184, v15\n"
"v_mul_f32 v185, v185, v15\n"
"v_mul_f32 v186, v186, v15\n"
"v_mul_f32 v187, v187, v15\n"
"v_mul_f32 v188, v188, v15\n"
"v_mul_f32 v189, v189, v15\n"
"v_mul_f32 v190, v190, v15\n"
"v_mul_f32 v191, v191, v15\n"
"v_cvt_pk_bf16_f32 v184, v184, v185\n"
"v_cvt_pk_bf16_f32 v185, v186, v187\n"
"v_cvt_pk_bf16_f32 v186, v188, v189\n"
"v_cvt_pk_bf16_f32 v187, v190, v191\n"
"v_accvgpr_write_b32 a180, v184\n"
"v_accvgpr_write_b32 a181, v185\n"
"v_accvgpr_write_b32 a182, v186\n"
"v_accvgpr_write_b32 a183, v187\n"
"v_mfma_f32_16x16x32_bf16 v[180:183], a[76:79], v[44:47], v[180:183]\n"
"v_exp_f32 v124, v124\n"
"v_mfma_f32_16x16x32_bf16 v[184:187], a[80:83], v[40:43], 0\n"
"v_exp_f32 v125, v125\n"
"v_mfma_f32_16x16x32_bf16 v[184:187], a[84:87], v[44:47], v[184:187]\n"
"v_exp_f32 v126, v126\n"
"v_mfma_f32_16x16x32_bf16 v[188:191], a[88:91], v[40:43], 0\n"
"v_cvt_pk_bf16_f32 v40, v80, v81\n"
"v_cvt_pk_bf16_f32 v41, v82, v83\n"
"v_mfma_f32_16x16x32_bf16 v[188:191], a[92:95], v[44:47], v[188:191]\n"
"v_cvt_pk_bf16_f32 v42, v84, v85\n"
"v_cvt_pk_bf16_f32 v43, v86, v87\n"
"v_accvgpr_read_b32 v14, a104\n"
"v_mfma_f32_16x16x32_bf16 a[104:107], a[112:115], v[48:51], 0\n"
"v_cvt_pk_bf16_f32 v44, v88, v89\n"
"v_cvt_pk_bf16_f32 v45, v90, v91\n"
"v_cmp_ngt_f32 vcc, s67, v14\n"
"s_mov_b32 s72, 262144\n"
"s_or_b64 s[70:71], s[70:71], vcc\n"
"v_rcp_f32 v14, v14\n"
"s_nop 0\n"
"v_mul_f32 v192, v192, v14\n"
"v_mul_f32 v193, v193, v14\n"
"v_mul_f32 v194, v194, v14\n"
"v_mul_f32 v195, v195, v14\n"
"v_mul_f32 v196, v196, v14\n"
"v_mul_f32 v197, v197, v14\n"
"v_mul_f32 v198, v198, v14\n"
"v_mul_f32 v199, v199, v14\n"
"v_cvt_pk_bf16_f32 v192, v192, v193\n"
"v_cvt_pk_bf16_f32 v193, v194, v195\n"
"v_cvt_pk_bf16_f32 v194, v196, v197\n"
"v_cvt_pk_bf16_f32 v195, v198, v199\n"
"v_accvgpr_write_b32 a136, v192\n"
"v_accvgpr_write_b32 a137, v193\n"
"v_accvgpr_write_b32 a138, v194\n"
"v_accvgpr_write_b32 a139, v195\n"
"v_mfma_f32_16x16x32_bf16 a[104:107], a[112:115], v[52:55], a[104:107]\n"
"v_cvt_pk_bf16_f32 v46, v92, v93\n"
"v_cvt_pk_bf16_f32 v47, v94, v95\n"
"v_mfma_f32_16x16x32_bf16 v[192:195], a[32:35], v[48:51], 0\n"
"v_exp_f32 v127, v127\n"
"v_mfma_f32_16x16x32_bf16 v[192:195], a[36:39], v[52:55], v[192:195]\n"
"v_mul_f32 v200, v200, v14\n"
"v_mul_f32 v201, v201, v14\n"
"v_mfma_f32_16x16x32_bf16 v[196:199], a[40:43], v[48:51], 0\n"
"v_mul_f32 v202, v202, v14\n"
"v_mul_f32 v203, v203, v14\n"
"v_mul_f32 v204, v204, v14\n"
"v_mul_f32 v205, v205, v14\n"
"v_mul_f32 v206, v206, v14\n"
"v_mul_f32 v207, v207, v14\n"
"v_cvt_pk_bf16_f32 v200, v200, v201\n"
"v_cvt_pk_bf16_f32 v201, v202, v203\n"
"v_cvt_pk_bf16_f32 v202, v204, v205\n"
"v_cvt_pk_bf16_f32 v203, v206, v207\n"
"v_accvgpr_write_b32 a152, v200\n"
"v_accvgpr_write_b32 a153, v201\n"
"v_accvgpr_write_b32 a154, v202\n"
"v_accvgpr_write_b32 a155, v203\n"
"v_mfma_f32_16x16x32_bf16 v[196:199], a[44:47], v[52:55], v[196:199]\n"
"v_mul_f32 v208, v208, v14\n"
"v_mul_f32 v209, v209, v14\n"
"v_mfma_f32_16x16x32_bf16 v[200:203], a[48:51], v[48:51], 0\n"
"v_mul_f32 v210, v210, v14\n"
"v_mul_f32 v211, v211, v14\n"
"v_mfma_f32_16x16x32_bf16 v[200:203], a[52:55], v[52:55], v[200:203]\n"
"v_mul_f32 v212, v212, v14\n"
"v_mul_f32 v213, v213, v14\n"
"v_mfma_f32_16x16x32_bf16 v[204:207], a[56:59], v[48:51], 0\n"
"v_mul_f32 v214, v214, v14\n"
"v_mul_f32 v215, v215, v14\n"
"v_cvt_pk_bf16_f32 v208, v208, v209\n"
"v_cvt_pk_bf16_f32 v209, v210, v211\n"
"v_cvt_pk_bf16_f32 v210, v212, v213\n"
"v_cvt_pk_bf16_f32 v211, v214, v215\n"
"v_accvgpr_write_b32 a168, v208\n"
"v_accvgpr_write_b32 a169, v209\n"
"v_accvgpr_write_b32 a170, v210\n"
"v_accvgpr_write_b32 a171, v211\n"
"v_mfma_f32_16x16x32_bf16 v[204:207], a[60:63], v[52:55], v[204:207]\n"
"v_mul_f32 v216, v216, v14\n"
"v_mul_f32 v217, v217, v14\n"
"v_mfma_f32_16x16x32_bf16 v[208:211], a[64:67], v[48:51], 0\n"
"v_mul_f32 v218, v218, v14\n"
"v_mul_f32 v219, v219, v14\n"
"v_mfma_f32_16x16x32_bf16 v[208:211], a[68:71], v[52:55], v[208:211]\n"
"v_mul_f32 v220, v220, v14\n"
"v_mul_f32 v221, v221, v14\n"
"v_mfma_f32_16x16x32_bf16 v[212:215], a[72:75], v[48:51], 0\n"
"v_mul_f32 v222, v222, v14\n"
"v_mul_f32 v223, v223, v14\n"
"v_cvt_pk_bf16_f32 v216, v216, v217\n"
"v_cvt_pk_bf16_f32 v217, v218, v219\n"
"v_cvt_pk_bf16_f32 v218, v220, v221\n"
"v_cvt_pk_bf16_f32 v219, v222, v223\n"
"v_accvgpr_write_b32 a184, v216\n"
"v_accvgpr_write_b32 a185, v217\n"
"v_accvgpr_write_b32 a186, v218\n"
"v_accvgpr_write_b32 a187, v219\n"
"v_mfma_f32_16x16x32_bf16 v[212:215], a[76:79], v[52:55], v[212:215]\n"
"v_accvgpr_read_b32 v15, a108\n"
"v_mfma_f32_16x16x32_bf16 v[216:219], a[80:83], v[48:51], 0\n"
"v_cmp_ngt_f32 vcc, s67, v15\n"
"s_mov_b32 s73, 393216\n"
"s_or_b64 s[70:71], s[70:71], vcc\n"
"v_mfma_f32_16x16x32_bf16 v[216:219], a[84:87], v[52:55], v[216:219]\n"
"v_rcp_f32 v15, v15\n"
"v_mfma_f32_16x16x32_bf16 v[220:223], a[88:91], v[48:51], 0\n"
"s_waitcnt vmcnt(17)\n"
"s_barrier\n"
"v_mfma_f32_16x16x32_bf16 v[220:223], a[92:95], v[52:55], v[220:223]\n"
"ds_read_b128 a[0:3], v6 offset:0\n"
"ds_read_b128 a[4:7], v7 offset:0\n"
"v_mfma_f32_16x16x32_bf16 a[108:111], a[112:115], v[56:59], 0\n"
"ds_read_b128 a[8:11], v8 offset:0\n"
"ds_read_b128 a[12:15], v9 offset:0\n"
"v_mul_f32 v224, v224, v15\n"
"v_mul_f32 v225, v225, v15\n"
"v_mul_f32 v226, v226, v15\n"
"v_mul_f32 v227, v227, v15\n"
"v_mul_f32 v228, v228, v15\n"
"v_mul_f32 v229, v229, v15\n"
"v_mul_f32 v230, v230, v15\n"
"v_mul_f32 v231, v231, v15\n"
"v_cvt_pk_bf16_f32 v224, v224, v225\n"
"v_cvt_pk_bf16_f32 v225, v226, v227\n"
"v_cvt_pk_bf16_f32 v226, v228, v229\n"
"v_cvt_pk_bf16_f32 v227, v230, v231\n"
"v_accvgpr_write_b32 a140, v224\n"
"v_accvgpr_write_b32 a141, v225\n"
"v_accvgpr_write_b32 a142, v226\n"
"v_accvgpr_write_b32 a143, v227\n"
"v_mfma_f32_16x16x32_bf16 a[108:111], a[112:115], v[60:63], a[108:111]\n"
"s_mov_b32 m0, s62\n"
"v_cvt_pk_bf16_f32 v48, v96, v97\n"
"v_mfma_f32_16x16x32_bf16 v[224:227], a[32:35], v[56:59], 0\n"
"buffer_load_dwordx4 v0, s[44:47], s60 offen offset:0 nt lds\n"
"v_mfma_f32_16x16x32_bf16 v[224:227], a[36:39], v[60:63], v[224:227]\n"
"v_cvt_pk_bf16_f32 v49, v98, v99\n"
"ds_read_b128 a[16:19], v6 offset:4096\n"
"v_mfma_f32_16x16x32_bf16 v[228:231], a[40:43], v[56:59], 0\n"
"ds_read_b128 a[20:23], v7 offset:4096\n"
"ds_read_b128 a[24:27], v8 offset:4096\n"
"v_mul_f32 v232, v232, v15\n"
"v_mul_f32 v233, v233, v15\n"
"v_mul_f32 v234, v234, v15\n"
"v_mul_f32 v235, v235, v15\n"
"v_mul_f32 v236, v236, v15\n"
"v_mul_f32 v237, v237, v15\n"
"v_mul_f32 v238, v238, v15\n"
"v_mul_f32 v239, v239, v15\n"
"v_cvt_pk_bf16_f32 v232, v232, v233\n"
"v_cvt_pk_bf16_f32 v233, v234, v235\n"
"v_cvt_pk_bf16_f32 v234, v236, v237\n"
"v_cvt_pk_bf16_f32 v235, v238, v239\n"
"v_accvgpr_write_b32 a156, v232\n"
"v_accvgpr_write_b32 a157, v233\n"
"v_accvgpr_write_b32 a158, v234\n"
"v_accvgpr_write_b32 a159, v235\n"
"v_mfma_f32_16x16x32_bf16 v[228:231], a[44:47], v[60:63], v[228:231]\n"
"ds_read_b128 a[28:31], v9 offset:4096\n"
"v_cvt_pk_bf16_f32 v50, v100, v101\n"
"v_mfma_f32_16x16x32_bf16 v[232:235], a[48:51], v[56:59], 0\n"
"v_cvt_pk_bf16_f32 v51, v102, v103\n"
"v_cvt_pk_bf16_f32 v52, v104, v105\n"
"v_mfma_f32_16x16x32_bf16 v[232:235], a[52:55], v[60:63], v[232:235]\n"
"v_cvt_pk_bf16_f32 v53, v106, v107\n"
"v_cvt_pk_bf16_f32 v54, v108, v109\n"
"v_mfma_f32_16x16x32_bf16 v[236:239], a[56:59], v[56:59], 0\n"
"v_cvt_pk_bf16_f32 v55, v110, v111\n"
"ds_read_b64_tr_b16 a[32:33], v10 offset:0\n"
"v_mul_f32 v240, v240, v15\n"
"v_mul_f32 v241, v241, v15\n"
"v_mul_f32 v242, v242, v15\n"
"v_mul_f32 v243, v243, v15\n"
"v_mul_f32 v244, v244, v15\n"
"v_mul_f32 v245, v245, v15\n"
"v_mul_f32 v246, v246, v15\n"
"v_mul_f32 v247, v247, v15\n"
"v_cvt_pk_bf16_f32 v240, v240, v241\n"
"v_cvt_pk_bf16_f32 v241, v242, v243\n"
"v_cvt_pk_bf16_f32 v242, v244, v245\n"
"v_cvt_pk_bf16_f32 v243, v246, v247\n"
"v_accvgpr_write_b32 a172, v240\n"
"v_accvgpr_write_b32 a173, v241\n"
"v_accvgpr_write_b32 a174, v242\n"
"v_accvgpr_write_b32 a175, v243\n"
"v_mfma_f32_16x16x32_bf16 v[236:239], a[60:63], v[60:63], v[236:239]\n"
"ds_read_b64_tr_b16 a[34:35], v10 offset:4096\n"
"ds_read_b64_tr_b16 a[36:37], v10 offset:8192\n"
"v_mfma_f32_16x16x32_bf16 v[240:243], a[64:67], v[56:59], 0\n"
"ds_read_b64_tr_b16 a[38:39], v10 offset:12288\n"
"ds_read_b64_tr_b16 a[40:41], v11 offset:0\n"
"v_mfma_f32_16x16x32_bf16 v[240:243], a[68:71], v[60:63], v[240:243]\n"
"ds_read_b64_tr_b16 a[42:43], v11 offset:4096\n"
"ds_read_b64_tr_b16 a[44:45], v11 offset:8192\n"
"v_mfma_f32_16x16x32_bf16 v[244:247], a[72:75], v[56:59], 0\n"
"ds_read_b64_tr_b16 a[46:47], v11 offset:12288\n"
"ds_read_b64_tr_b16 a[48:49], v10 offset:256\n"
"v_mul_f32 v248, v248, v15\n"
"v_mul_f32 v249, v249, v15\n"
"v_mul_f32 v250, v250, v15\n"
"v_mul_f32 v251, v251, v15\n"
"v_mul_f32 v252, v252, v15\n"
"v_mul_f32 v253, v253, v15\n"
"v_mul_f32 v254, v254, v15\n"
"v_mul_f32 v255, v255, v15\n"
"v_cvt_pk_bf16_f32 v248, v248, v249\n"
"v_cvt_pk_bf16_f32 v249, v250, v251\n"
"v_cvt_pk_bf16_f32 v250, v252, v253\n"
"v_cvt_pk_bf16_f32 v251, v254, v255\n"
"v_accvgpr_write_b32 a188, v248\n"
"v_accvgpr_write_b32 a189, v249\n"
"v_accvgpr_write_b32 a190, v250\n"
"v_accvgpr_write_b32 a191, v251\n"
"v_mfma_f32_16x16x32_bf16 v[244:247], a[76:79], v[60:63], v[244:247]\n"
"ds_read_b64_tr_b16 a[50:51], v10 offset:4352\n"
"ds_read_b64_tr_b16 a[52:53], v10 offset:8448\n"
"v_mfma_f32_16x16x32_bf16 v[248:251], a[80:83], v[56:59], 0\n"
"buffer_load_dwordx4 v1, s[44:47], s60 offen offset:1024 nt lds\n"
"v_mfma_f32_16x16x32_bf16 v[248:251], a[84:87], v[60:63], v[248:251]\n"
"ds_read_b64_tr_b16 a[54:55], v10 offset:12544\n"
"ds_read_b64_tr_b16 a[56:57], v11 offset:256\n"
"v_mfma_f32_16x16x32_bf16 v[252:255], a[88:91], v[56:59], 0\n"
"v_cvt_pk_bf16_f32 v56, v112, v113\n"
"v_cvt_pk_bf16_f32 v57, v114, v115\n"
"v_lshrrev_b32 v14, 2, v13\n"
"v_mfma_f32_16x16x32_bf16 v[252:255], a[92:95], v[60:63], v[252:255]\n"
".LG1a_%=:\n"
"s_waitcnt lgkmcnt(15)\n"
"v_mfma_f32_16x16x32_bf16 v[64:67], a[0:3], a[192:195], v[16:19]\n"
"v_cvt_pk_bf16_f32 v58, v116, v117\n"
"v_cvt_pk_bf16_f32 v59, v118, v119\n"
"v_mfma_f32_16x16x32_bf16 v[64:67], a[4:7], a[196:199], v[64:67]\n"
"v_cvt_pk_bf16_f32 v60, v120, v121\n"
"v_cvt_pk_bf16_f32 v61, v122, v123\n"
"v_mfma_f32_16x16x32_bf16 v[64:67], a[8:11], a[200:203], v[64:67]\n"
"v_cvt_pk_bf16_f32 v62, v124, v125\n"
"v_cvt_pk_bf16_f32 v63, v126, v127\n"
"v_mfma_f32_16x16x32_bf16 v[64:67], a[12:15], a[204:207], v[64:67]\n"
"ds_read_b64_tr_b16 a[58:59], v11 offset:4352\n"
"ds_read_b64_tr_b16 a[60:61], v11 offset:8448\n"
"v_mfma_f32_16x16x32_bf16 v[80:83], a[0:3], a[208:211], v[20:23]\n"
"ds_read_b64_tr_b16 a[62:63], v11 offset:12544\n"
"ds_read_b64_tr_b16 a[64:65], v10 offset:512\n"
"v_mfma_f32_16x16x32_bf16 v[80:83], a[4:7], a[212:215], v[80:83]\n"
"ds_read_b64_tr_b16 a[66:67], v10 offset:4608\n"
"ds_read_b64_tr_b16 a[68:69], v10 offset:8704\n"
"v_mfma_f32_16x16x32_bf16 v[80:83], a[8:11], a[216:219], v[80:83]\n"
"ds_read_b64_tr_b16 a[70:71], v10 offset:12800\n"
"ds_read_b64_tr_b16 a[72:73], v11 offset:512\n"
"v_mfma_f32_16x16x32_bf16 v[80:83], a[12:15], a[220:223], v[80:83]\n"
"ds_read_b64_tr_b16 a[74:75], v11 offset:4608\n"
"ds_read_b64_tr_b16 a[76:77], v11 offset:8704\n"
"v_mfma_f32_16x16x32_bf16 v[96:99], a[0:3], a[224:227], v[24:27]\n"
"buffer_load_dwordx4 v2, s[44:47], s60 offen offset:2048 nt lds\n"
"v_mfma_f32_16x16x32_bf16 v[96:99], a[4:7], a[228:231], v[96:99]\n"
"ds_read_b64_tr_b16 a[78:79], v11 offset:12800\n"
"ds_read_b64_tr_b16 a[80:81], v10 offset:768\n"
"v_mfma_f32_16x16x32_bf16 v[96:99], a[8:11], a[232:235], v[96:99]\n"
"ds_read_b64_tr_b16 a[82:83], v10 offset:4864\n"
"ds_read_b64_tr_b16 a[84:85], v10 offset:8960\n"
"v_mfma_f32_16x16x32_bf16 v[96:99], a[12:15], a[236:239], v[96:99]\n"
"ds_read_b64_tr_b16 a[86:87], v10 offset:13056\n"
"ds_read_b64_tr_b16 a[88:89], v11 offset:768\n"
"v_mfma_f32_16x16x32_bf16 v[112:115], a[0:3], a[240:243], v[28:31]\n"
"ds_read_b128 a[0:3], v6 offset:8192\n"
"ds_read_b64_tr_b16 a[90:91], v11 offset:4864\n"
"v_mfma_f32_16x16x32_bf16 v[112:115], a[4:7], a[244:247], v[112:115]\n"
"ds_read_b128 a[4:7], v7 offset:8192\n"
"ds_read_b64_tr_b16 a[92:93], v11 offset:8960\n"
"v_mfma_f32_16x16x32_bf16 v[112:115], a[8:11], a[248:251], v[112:115]\n"
"ds_read_b128 a[8:11], v8 offset:8192\n"
"ds_read_b64_tr_b16 a[94:95], v11 offset:13056\n"
"v_mfma_f32_16x16x32_bf16 v[112:115], a[12:15], a[252:255], v[112:115]\n"
"ds_read_b128 a[12:15], v9 offset:8192\n"
"v_mfma_f32_16x16x32_bf16 v[68:71], a[16:19], a[192:195], v[16:19]\n"
"v_exp_f32 v64, v64\n"
"v_mfma_f32_16x16x32_bf16 v[68:71], a[20:23], a[196:199], v[68:71]\n"
"v_exp_f32 v65, v65\n"
"s_waitcnt lgkmcnt(15)\n"
"v_mfma_f32_16x16x32_bf16 v[68:71], a[24:27], a[200:203], v[68:71]\n"
"v_exp_f32 v66, v66\n"
"v_mfma_f32_16x16x32_bf16 v[68:71], a[28:31], a[204:207], v[68:71]\n"
"v_exp_f32 v67, v67\n"
"v_mfma_f32_16x16x32_bf16 v[84:87], a[16:19], a[208:211], v[20:23]\n"
"buffer_load_dwordx4 v3, s[44:47], s60 offen offset:3072 nt lds\n"
"v_mfma_f32_16x16x32_bf16 v[84:87], a[20:23], a[212:215], v[84:87]\n"
"v_exp_f32 v80, v80\n"
"v_mfma_f32_16x16x32_bf16 v[84:87], a[24:27], a[216:219], v[84:87]\n"
"v_exp_f32 v81, v81\n"
"v_mfma_f32_16x16x32_bf16 v[84:87], a[28:31], a[220:223], v[84:87]\n"
"v_exp_f32 v82, v82\n"
"v_mfma_f32_16x16x32_bf16 v[100:103], a[16:19], a[224:227], v[24:27]\n"
"v_exp_f32 v83, v83\n"
"v_mfma_f32_16x16x32_bf16 v[100:103], a[20:23], a[228:231], v[100:103]\n"
"v_exp_f32 v96, v96\n"
"v_mfma_f32_16x16x32_bf16 v[100:103], a[24:27], a[232:235], v[100:103]\n"
"v_exp_f32 v97, v97\n"
"v_mfma_f32_16x16x32_bf16 v[100:103], a[28:31], a[236:239], v[100:103]\n"
"v_exp_f32 v98, v98\n"
"v_mfma_f32_16x16x32_bf16 v[116:119], a[16:19], a[240:243], v[28:31]\n"
"ds_read_b128 a[16:19], v6 offset:12288\n"
"v_mfma_f32_16x16x32_bf16 v[116:119], a[20:23], a[244:247], v[116:119]\n"
"ds_read_b128 a[20:23], v7 offset:12288\n"
"v_mfma_f32_16x16x32_bf16 v[116:119], a[24:27], a[248:251], v[116:119]\n"
"ds_read_b128 a[24:27], v8 offset:12288\n"
"v_mfma_f32_16x16x32_bf16 v[116:119], a[28:31], a[252:255], v[116:119]\n"
"s_mov_b32 m0, s63\n"
"ds_read_b128 a[28:31], v9 offset:12288\n"
"s_waitcnt lgkmcnt(10)\n"
"v_mfma_f32_16x16x32_bf16 v[72:75], a[0:3], a[192:195], v[16:19]\n"
"buffer_load_dwordx4 v4, s[48:51], s61 offen offset:0 nt lds\n"
"s_waitcnt lgkmcnt(8)\n"
"v_mfma_f32_16x16x32_bf16 v[72:75], a[4:7], a[196:199], v[72:75]\n"
"v_exp_f32 v99, v99\n"
"s_waitcnt lgkmcnt(6)\n"
"v_mfma_f32_16x16x32_bf16 v[72:75], a[8:11], a[200:203], v[72:75]\n"
"v_exp_f32 v112, v112\n"
"s_waitcnt lgkmcnt(4)\n"
"v_mfma_f32_16x16x32_bf16 v[72:75], a[12:15], a[204:207], v[72:75]\n"
"v_exp_f32 v113, v113\n"
"v_mfma_f32_16x16x32_bf16 v[88:91], a[0:3], a[208:211], v[20:23]\n"
"v_exp_f32 v114, v114\n"
"v_mfma_f32_16x16x32_bf16 v[88:91], a[4:7], a[212:215], v[88:91]\n"
"v_exp_f32 v115, v115\n"
"v_mfma_f32_16x16x32_bf16 v[88:91], a[8:11], a[216:219], v[88:91]\n"
"v_exp_f32 v68, v68\n"
"v_mfma_f32_16x16x32_bf16 v[88:91], a[12:15], a[220:223], v[88:91]\n"
"v_exp_f32 v69, v69\n"
"v_mfma_f32_16x16x32_bf16 v[104:107], a[0:3], a[224:227], v[24:27]\n"
"v_exp_f32 v70, v70\n"
"v_mfma_f32_16x16x32_bf16 v[104:107], a[4:7], a[228:231], v[104:107]\n"
"v_exp_f32 v71, v71\n"
"v_mfma_f32_16x16x32_bf16 v[104:107], a[8:11], a[232:235], v[104:107]\n"
"v_exp_f32 v84, v84\n"
"v_mfma_f32_16x16x32_bf16 v[104:107], a[12:15], a[236:239], v[104:107]\n"
"v_exp_f32 v85, v85\n"
"v_mfma_f32_16x16x32_bf16 v[120:123], a[0:3], a[240:243], v[28:31]\n"
"buffer_load_dwordx4 v5, s[48:51], s61 offen offset:1024 nt lds\n"
"v_mfma_f32_16x16x32_bf16 v[120:123], a[4:7], a[244:247], v[120:123]\n"
"v_exp_f32 v86, v86\n"
"v_mfma_f32_16x16x32_bf16 v[120:123], a[8:11], a[248:251], v[120:123]\n"
"v_exp_f32 v87, v87\n"
"v_mfma_f32_16x16x32_bf16 v[120:123], a[12:15], a[252:255], v[120:123]\n"
"v_exp_f32 v100, v100\n"
"s_waitcnt lgkmcnt(3)\n"
"v_mfma_f32_16x16x32_bf16 v[76:79], a[16:19], a[192:195], v[16:19]\n"
"v_exp_f32 v101, v101\n"
"s_waitcnt lgkmcnt(2)\n"
"v_mfma_f32_16x16x32_bf16 v[76:79], a[20:23], a[196:199], v[76:79]\n"
"v_exp_f32 v102, v102\n"
"s_waitcnt lgkmcnt(1)\n"
"v_mfma_f32_16x16x32_bf16 v[76:79], a[24:27], a[200:203], v[76:79]\n"
"v_exp_f32 v103, v103\n"
"s_waitcnt lgkmcnt(0)\n"
"v_mfma_f32_16x16x32_bf16 v[76:79], a[28:31], a[204:207], v[76:79]\n"
"v_exp_f32 v116, v116\n"
"v_mfma_f32_16x16x32_bf16 v[92:95], a[16:19], a[208:211], v[20:23]\n"
"v_exp_f32 v117, v117\n"
"v_mfma_f32_16x16x32_bf16 v[92:95], a[20:23], a[212:215], v[92:95]\n"
"v_exp_f32 v118, v118\n"
"v_mfma_f32_16x16x32_bf16 v[92:95], a[24:27], a[216:219], v[92:95]\n"
"v_exp_f32 v119, v119\n"
"v_mfma_f32_16x16x32_bf16 v[92:95], a[28:31], a[220:223], v[92:95]\n"
"v_exp_f32 v72, v72\n"
"v_mfma_f32_16x16x32_bf16 v[108:111], a[16:19], a[224:227], v[24:27]\n"
"buffer_load_dwordx4 v4, s[48:51], s61 offen offset:2048 nt lds\n"
"v_mfma_f32_16x16x32_bf16 v[108:111], a[20:23], a[228:231], v[108:111]\n"
"v_exp_f32 v73, v73\n"
"v_mfma_f32_16x16x32_bf16 v[108:111], a[24:27], a[232:235], v[108:111]\n"
"v_exp_f32 v74, v74\n"
"v_mfma_f32_16x16x32_bf16 v[108:111], a[28:31], a[236:239], v[108:111]\n"
"v_exp_f32 v75, v75\n"
"v_mfma_f32_16x16x32_bf16 v[124:127], a[16:19], a[240:243], v[28:31]\n"
"v_exp_f32 v88, v88\n"
"v_mfma_f32_16x16x32_bf16 v[124:127], a[20:23], a[244:247], v[124:127]\n"
"v_exp_f32 v89, v89\n"
"v_mfma_f32_16x16x32_bf16 v[124:127], a[24:27], a[248:251], v[124:127]\n"
"v_exp_f32 v90, v90\n"
"v_mfma_f32_16x16x32_bf16 v[124:127], a[28:31], a[252:255], v[124:127]\n"
"v_exp_f32 v91, v91\n"
"v_mfma_f32_16x16x32_bf16 a[96:99], a[112:115], v[32:35], a[96:99]\n"
"v_exp_f32 v104, v104\n"
"v_mfma_f32_16x16x32_bf16 a[96:99], a[112:115], v[36:39], a[96:99]\n"
"v_exp_f32 v105, v105\n"
"v_mfma_f32_16x16x32_bf16 v[128:131], a[32:35], v[32:35], v[128:131]\n"
"v_exp_f32 v106, v106\n"
"v_mfma_f32_16x16x32_bf16 v[128:131], a[36:39], v[36:39], v[128:131]\n"
"v_exp_f32 v107, v107\n"
"v_mfma_f32_16x16x32_bf16 v[132:135], a[40:43], v[32:35], v[132:135]\n"
"buffer_load_dwordx4 v5, s[48:51], s61 offen offset:3072 nt lds\n"
"v_mfma_f32_16x16x32_bf16 v[132:135], a[44:47], v[36:39], v[132:135]\n"
"v_xor_b32 v6, s64, v6\n"
"v_xor_b32 v7, s64, v7\n"
"v_mfma_f32_16x16x32_bf16 v[136:139], a[48:51], v[32:35], v[136:139]\n"
"v_xor_b32 v8, s64, v8\n"
"v_xor_b32 v9, s64, v9\n"
"v_mfma_f32_16x16x32_bf16 v[136:139], a[52:55], v[36:39], v[136:139]\n"
"v_xor_b32 v10, s65, v10\n"
"v_xor_b32 v11, s65, v11\n"
"v_mfma_f32_16x16x32_bf16 v[140:143], a[56:59], v[32:35], v[140:143]\n"
"s_xor_b32 s62, s62, s65\n"
"s_xor_b32 s63, s63, s64\n"
"v_mfma_f32_16x16x32_bf16 v[140:143], a[60:63], v[36:39], v[140:143]\n"
"s_add_u32 s60, s60, 0x4000\n"
"s_add_u32 s61, s61, 0x4000\n"
"v_mfma_f32_16x16x32_bf16 v[144:147], a[64:67], v[32:35], v[144:147]\n"
"s_xor_b32 s64, s64, 0xc000\n"
"s_xor_b32 s65, s65, 0xc000\n"
"v_mfma_f32_16x16x32_bf16 v[144:147], a[68:71], v[36:39], v[144:147]\n"
"v_exp_f32 v120, v120\n"
"v_mfma_f32_16x16x32_bf16 v[148:151], a[72:75], v[32:35], v[148:151]\n"
"v_exp_f32 v121, v121\n"
"v_mfma_f32_16x16x32_bf16 v[148:151], a[76:79], v[36:39], v[148:151]\n"
"v_exp_f32 v122, v122\n"
"v_mfma_f32_16x16x32_bf16 v[152:155], a[80:83], v[32:35], v[152:155]\n"
"v_exp_f32 v123, v123\n"
"v_mfma_f32_16x16x32_bf16 v[152:155], a[84:87], v[36:39], v[152:155]\n"
"v_exp_f32 v76, v76\n"
"v_mfma_f32_16x16x32_bf16 v[156:159], a[88:91], v[32:35], v[156:159]\n"
"v_cvt_pk_bf16_f32 v32, v64, v65\n"
"v_cvt_pk_bf16_f32 v33, v66, v67\n"
"v_mfma_f32_16x16x32_bf16 v[156:159], a[92:95], v[36:39], v[156:159]\n"
"v_cvt_pk_bf16_f32 v34, v68, v69\n"
"v_cvt_pk_bf16_f32 v35, v70, v71\n"
"v_mfma_f32_16x16x32_bf16 a[100:103], a[112:115], v[40:43], a[100:103]\n"
"v_cvt_pk_bf16_f32 v36, v72, v73\n"
"v_cvt_pk_bf16_f32 v37, v74, v75\n"
"v_mfma_f32_16x16x32_bf16 a[100:103], a[112:115], v[44:47], a[100:103]\n"
"v_exp_f32 v77, v77\n"
"v_mfma_f32_16x16x32_bf16 v[160:163], a[32:35], v[40:43], v[160:163]\n"
"v_exp_f32 v78, v78\n"
"v_mfma_f32_16x16x32_bf16 v[160:163], a[36:39], v[44:47], v[160:163]\n"
"v_exp_f32 v79, v79\n"
"v_mfma_f32_16x16x32_bf16 v[164:167], a[40:43], v[40:43], v[164:167]\n"
"v_cvt_pk_bf16_f32 v38, v76, v77\n"
"v_cvt_pk_bf16_f32 v39, v78, v79\n"
"v_mfma_f32_16x16x32_bf16 v[164:167], a[44:47], v[44:47], v[164:167]\n"
"v_exp_f32 v92, v92\n"
"v_mfma_f32_16x16x32_bf16 v[168:171], a[48:51], v[40:43], v[168:171]\n"
"v_exp_f32 v93, v93\n"
"v_mfma_f32_16x16x32_bf16 v[168:171], a[52:55], v[44:47], v[168:171]\n"
"v_exp_f32 v94, v94\n"
"v_mfma_f32_16x16x32_bf16 v[172:175], a[56:59], v[40:43], v[172:175]\n"
"v_exp_f32 v95, v95\n"
"v_mfma_f32_16x16x32_bf16 v[172:175], a[60:63], v[44:47], v[172:175]\n"
"v_exp_f32 v108, v108\n"
"v_mfma_f32_16x16x32_bf16 v[176:179], a[64:67], v[40:43], v[176:179]\n"
"v_exp_f32 v109, v109\n"
"v_mfma_f32_16x16x32_bf16 v[176:179], a[68:71], v[44:47], v[176:179]\n"
"v_exp_f32 v110, v110\n"
"v_mfma_f32_16x16x32_bf16 v[180:183], a[72:75], v[40:43], v[180:183]\n"
"v_exp_f32 v111, v111\n"
"v_mfma_f32_16x16x32_bf16 v[180:183], a[76:79], v[44:47], v[180:183]\n"
"v_exp_f32 v124, v124\n"
"v_mfma_f32_16x16x32_bf16 v[184:187], a[80:83], v[40:43], v[184:187]\n"
"v_exp_f32 v125, v125\n"
"v_mfma_f32_16x16x32_bf16 v[184:187], a[84:87], v[44:47], v[184:187]\n"
"v_exp_f32 v126, v126\n"
"v_mfma_f32_16x16x32_bf16 v[188:191], a[88:91], v[40:43], v[188:191]\n"
"v_cvt_pk_bf16_f32 v40, v80, v81\n"
"v_cvt_pk_bf16_f32 v41, v82, v83\n"
"v_mfma_f32_16x16x32_bf16 v[188:191], a[92:95], v[44:47], v[188:191]\n"
"v_cvt_pk_bf16_f32 v42, v84, v85\n"
"v_cvt_pk_bf16_f32 v43, v86, v87\n"
"v_mfma_f32_16x16x32_bf16 a[104:107], a[112:115], v[48:51], a[104:107]\n"
"v_cvt_pk_bf16_f32 v44, v88, v89\n"
"v_cvt_pk_bf16_f32 v45, v90, v91\n"
"v_mfma_f32_16x16x32_bf16 a[104:107], a[112:115], v[52:55], a[104:107]\n"
"v_cvt_pk_bf16_f32 v46, v92, v93\n"
"v_cvt_pk_bf16_f32 v47, v94, v95\n"
"v_mfma_f32_16x16x32_bf16 v[192:195], a[32:35], v[48:51], v[192:195]\n"
"v_exp_f32 v127, v127\n"
"v_mfma_f32_16x16x32_bf16 v[192:195], a[36:39], v[52:55], v[192:195]\n"
"v_mfma_f32_16x16x32_bf16 v[196:199], a[40:43], v[48:51], v[196:199]\n"
"v_mfma_f32_16x16x32_bf16 v[196:199], a[44:47], v[52:55], v[196:199]\n"
"v_mfma_f32_16x16x32_bf16 v[200:203], a[48:51], v[48:51], v[200:203]\n"
"v_mfma_f32_16x16x32_bf16 v[200:203], a[52:55], v[52:55], v[200:203]\n"
"v_mfma_f32_16x16x32_bf16 v[204:207], a[56:59], v[48:51], v[204:207]\n"
"v_mfma_f32_16x16x32_bf16 v[204:207], a[60:63], v[52:55], v[204:207]\n"
"v_mfma_f32_16x16x32_bf16 v[208:211], a[64:67], v[48:51], v[208:211]\n"
"v_mfma_f32_16x16x32_bf16 v[208:211], a[68:71], v[52:55], v[208:211]\n"
"v_mfma_f32_16x16x32_bf16 v[212:215], a[72:75], v[48:51], v[212:215]\n"
"v_mfma_f32_16x16x32_bf16 v[212:215], a[76:79], v[52:55], v[212:215]\n"
"v_mfma_f32_16x16x32_bf16 v[216:219], a[80:83], v[48:51], v[216:219]\n"
"v_mfma_f32_16x16x32_bf16 v[216:219], a[84:87], v[52:55], v[216:219]\n"
"v_mfma_f32_16x16x32_bf16 v[220:223], a[88:91], v[48:51], v[220:223]\n"
"s_waitcnt vmcnt(16)\n"
"s_barrier\n"
"v_mfma_f32_16x16x32_bf16 v[220:223], a[92:95], v[52:55], v[220:223]\n"
"ds_read_b128 a[0:3], v6 offset:0\n"
"ds_read_b128 a[4:7], v7 offset:0\n"
"v_mfma_f32_16x16x32_bf16 a[108:111], a[112:115], v[56:59], a[108:111]\n"
"ds_read_b128 a[8:11], v8 offset:0\n"
"ds_read_b128 a[12:15], v9 offset:0\n"
"v_mfma_f32_16x16x32_bf16 a[108:111], a[112:115], v[60:63], a[108:111]\n"
"s_mov_b32 m0, s62\n"
"v_cvt_pk_bf16_f32 v48, v96, v97\n"
"v_mfma_f32_16x16x32_bf16 v[224:227], a[32:35], v[56:59], v[224:227]\n"
"buffer_load_dwordx4 v0, s[44:47], s60 offen offset:0 nt lds\n"
"v_mfma_f32_16x16x32_bf16 v[224:227], a[36:39], v[60:63], v[224:227]\n"
"v_cvt_pk_bf16_f32 v49, v98, v99\n"
"ds_read_b128 a[16:19], v6 offset:4096\n"
"v_mfma_f32_16x16x32_bf16 v[228:231], a[40:43], v[56:59], v[228:231]\n"
"ds_read_b128 a[20:23], v7 offset:4096\n"
"ds_read_b128 a[24:27], v8 offset:4096\n"
"v_mfma_f32_16x16x32_bf16 v[228:231], a[44:47], v[60:63], v[228:231]\n"
"ds_read_b128 a[28:31], v9 offset:4096\n"
"v_cvt_pk_bf16_f32 v50, v100, v101\n"
"v_mfma_f32_16x16x32_bf16 v[232:235], a[48:51], v[56:59], v[232:235]\n"
"v_cvt_pk_bf16_f32 v51, v102, v103\n"
"v_cvt_pk_bf16_f32 v52, v104, v105\n"
"v_mfma_f32_16x16x32_bf16 v[232:235], a[52:55], v[60:63], v[232:235]\n"
"v_cvt_pk_bf16_f32 v53, v106, v107\n"
"v_cvt_pk_bf16_f32 v54, v108, v109\n"
"v_mfma_f32_16x16x32_bf16 v[236:239], a[56:59], v[56:59], v[236:239]\n"
"v_cvt_pk_bf16_f32 v55, v110, v111\n"
"ds_read_b64_tr_b16 a[32:33], v10 offset:0\n"
"v_mfma_f32_16x16x32_bf16 v[236:239], a[60:63], v[60:63], v[236:239]\n"
"ds_read_b64_tr_b16 a[34:35], v10 offset:4096\n"
"ds_read_b64_tr_b16 a[36:37], v10 offset:8192\n"
"v_mfma_f32_16x16x32_bf16 v[240:243], a[64:67], v[56:59], v[240:243]\n"
"ds_read_b64_tr_b16 a[38:39], v10 offset:12288\n"
"ds_read_b64_tr_b16 a[40:41], v11 offset:0\n"
"v_mfma_f32_16x16x32_bf16 v[240:243], a[68:71], v[60:63], v[240:243]\n"
"ds_read_b64_tr_b16 a[42:43], v11 offset:4096\n"
"ds_read_b64_tr_b16 a[44:45], v11 offset:8192\n"
"v_mfma_f32_16x16x32_bf16 v[244:247], a[72:75], v[56:59], v[244:247]\n"
"ds_read_b64_tr_b16 a[46:47], v11 offset:12288\n"
"ds_read_b64_tr_b16 a[48:49], v10 offset:256\n"
"v_mfma_f32_16x16x32_bf16 v[244:247], a[76:79], v[60:63], v[244:247]\n"
"ds_read_b64_tr_b16 a[50:51], v10 offset:4352\n"
"ds_read_b64_tr_b16 a[52:53], v10 offset:8448\n"
"v_mfma_f32_16x16x32_bf16 v[248:251], a[80:83], v[56:59], v[248:251]\n"
"buffer_load_dwordx4 v1, s[44:47], s60 offen offset:1024 nt lds\n"
"v_mfma_f32_16x16x32_bf16 v[248:251], a[84:87], v[60:63], v[248:251]\n"
"ds_read_b64_tr_b16 a[54:55], v10 offset:12544\n"
"ds_read_b64_tr_b16 a[56:57], v11 offset:256\n"
"v_mfma_f32_16x16x32_bf16 v[252:255], a[88:91], v[56:59], v[252:255]\n"
"v_cvt_pk_bf16_f32 v56, v112, v113\n"
"v_cvt_pk_bf16_f32 v57, v114, v115\n"
"v_mfma_f32_16x16x32_bf16 v[252:255], a[92:95], v[60:63], v[252:255]\n"
"s_sub_u32 s66, s66, 1\n"
"s_cmp_lg_u32 s66, 0\n"
"s_cbranch_scc1 .LG1a_%=\n"
"s_waitcnt lgkmcnt(15)\n"
"v_mfma_f32_16x16x32_bf16 v[64:67], a[0:3], a[192:195], v[16:19]\n"
"v_cvt_pk_bf16_f32 v58, v116, v117\n"
"v_cvt_pk_bf16_f32 v59, v118, v119\n"
"s_mov_b32 s40, %[wol]\n"
"s_and_b32 s41, %[woh], 0xffff\n"
"s_mov_b32 s42, 1047808\n"
"s_lshl_b32 s74, %[ksp], 6\n"
"v_add_u32 v2, s74, v4\n"
"v_and_b32 v2, 0xff, v2\n"
"v_and_b32 v3, 0xffffff00, v4\n"
"v_lshl_add_u32 v0, v3, 2, v2\n"
"v_add_u32 v2, s74, v5\n"
"v_and_b32 v2, 0xff, v2\n"
"v_and_b32 v3, 0xffffff00, v5\n"
"v_lshl_add_u32 v1, v3, 2, v2\n"
"s_mul_i32 s74, %[ntp], 0x22222222\n"
"s_xor_b32 s68, s74, 0x75364210\n"
"s_mov_b32 s69, -1\n"
"v_mfma_f32_16x16x32_bf16 v[64:67], a[4:7], a[196:199], v[64:67]\n"
"v_cvt_pk_bf16_f32 v60, v120, v121\n"
"v_cvt_pk_bf16_f32 v61, v122, v123\n"
"v_mfma_f32_16x16x32_bf16 v[64:67], a[8:11], a[200:203], v[64:67]\n"
"v_cvt_pk_bf16_f32 v62, v124, v125\n"
"v_cvt_pk_bf16_f32 v63, v126, v127\n"
"v_mfma_f32_16x16x32_bf16 v[64:67], a[12:15], a[204:207], v[64:67]\n"
"ds_read_b64_tr_b16 a[58:59], v11 offset:4352\n"
"ds_read_b64_tr_b16 a[60:61], v11 offset:8448\n"
"v_mfma_f32_16x16x32_bf16 v[80:83], a[0:3], a[208:211], v[20:23]\n"
"ds_read_b64_tr_b16 a[62:63], v11 offset:12544\n"
"ds_read_b64_tr_b16 a[64:65], v10 offset:512\n"
"v_mfma_f32_16x16x32_bf16 v[80:83], a[4:7], a[212:215], v[80:83]\n"
"ds_read_b64_tr_b16 a[66:67], v10 offset:4608\n"
"ds_read_b64_tr_b16 a[68:69], v10 offset:8704\n"
"v_mfma_f32_16x16x32_bf16 v[80:83], a[8:11], a[216:219], v[80:83]\n"
"ds_read_b64_tr_b16 a[70:71], v10 offset:12800\n"
"ds_read_b64_tr_b16 a[72:73], v11 offset:512\n"
"v_mfma_f32_16x16x32_bf16 v[80:83], a[12:15], a[220:223], v[80:83]\n"
"ds_read_b64_tr_b16 a[74:75], v11 offset:4608\n"
"ds_read_b64_tr_b16 a[76:77], v11 offset:8704\n"
"v_mfma_f32_16x16x32_bf16 v[96:99], a[0:3], a[224:227], v[24:27]\n"
"buffer_store_dwordx4 a[128:131], v12, s[52:55], s56 offen offset:0 sc1\n"
"v_mfma_f32_16x16x32_bf16 v[96:99], a[4:7], a[228:231], v[96:99]\n"
"ds_read_b64_tr_b16 a[78:79], v11 offset:12800\n"
"ds_read_b64_tr_b16 a[80:81], v10 offset:768\n"
"v_mfma_f32_16x16x32_bf16 v[96:99], a[8:11], a[232:235], v[96:99]\n"
"ds_read_b64_tr_b16 a[82:83], v10 offset:4864\n"
"ds_read_b64_tr_b16 a[84:85], v10 offset:8960\n"
"v_mfma_f32_16x16x32_bf16 v[96:99], a[12:15], a[236:239], v[96:99]\n"
"ds_read_b64_tr_b16 a[86:87], v10 offset:13056\n"
"ds_read_b64_tr_b16 a[88:89], v11 offset:768\n"
"v_mfma_f32_16x16x32_bf16 v[112:115], a[0:3], a[240:243], v[28:31]\n"
"ds_read_b128 a[0:3], v6 offset:8192\n"
"ds_read_b64_tr_b16 a[90:91], v11 offset:4864\n"
"v_mfma_f32_16x16x32_bf16 v[112:115], a[4:7], a[244:247], v[112:115]\n"
"ds_read_b128 a[4:7], v7 offset:8192\n"
"ds_read_b64_tr_b16 a[92:93], v11 offset:8960\n"
"buffer_store_dwordx4 a[144:147], v12, s[52:55], s56 offen offset:1024 sc1\n"
"v_mfma_f32_16x16x32_bf16 v[112:115], a[8:11], a[248:251], v[112:115]\n"
"ds_read_b128 a[8:11], v8 offset:8192\n"
"ds_read_b64_tr_b16 a[94:95], v11 offset:13056\n"
"s_add_u32 s75, s77, 0\n"
"s_add_u32 m0, s75, 0\n"
"s_bfe_u32 s76, s68, 0x40004\n"
"s_lshl_b32 s76, s76, 17\n"
"buffer_load_dwordx4 v0, s[40:43], s76 offen offset:0 lds\n"
"v_mfma_f32_16x16x32_bf16 v[112:115], a[12:15], a[252:255], v[112:115]\n"
"ds_read_b128 a[12:15], v9 offset:8192\n"
"v_mfma_f32_16x16x32_bf16 v[68:71], a[16:19], a[192:195], v[16:19]\n"
"v_exp_f32 v64, v64\n"
"v_mfma_f32_16x16x32_bf16 v[68:71], a[20:23], a[196:199], v[68:71]\n"
"v_exp_f32 v65, v65\n"
"s_waitcnt lgkmcnt(15)\n"
"v_mfma_f32_16x16x32_bf16 v[68:71], a[24:27], a[200:203], v[68:71]\n"
"v_exp_f32 v66, v66\n"
"buffer_store_dwordx4 a[160:163], v12, s[52:55], s56 offen offset:2048 sc1\n"
"v_mfma_f32_16x16x32_bf16 v[68:71], a[28:31], a[204:207], v[68:71]\n"
"v_exp_f32 v67, v67\n"
"v_mfma_f32_16x16x32_bf16 v[84:87], a[16:19], a[208:211], v[20:23]\n"
"buffer_store_dwordx4 a[176:179], v12, s[52:55], s56 offen offset:3072 sc1\n"
"v_mfma_f32_16x16x32_bf16 v[84:87], a[20:23], a[212:215], v[84:87]\n"
"v_exp_f32 v80, v80\n"
"v_mfma_f32_16x16x32_bf16 v[84:87], a[24:27], a[216:219], v[84:87]\n"
"v_exp_f32 v81, v81\n"
"v_mfma_f32_16x16x32_bf16 v[84:87], a[28:31], a[220:223], v[84:87]\n"
"v_exp_f32 v82, v82\n"
"v_mfma_f32_16x16x32_bf16 v[100:103], a[16:19], a[224:227], v[24:27]\n"
"v_exp_f32 v83, v83\n"
"v_mfma_f32_16x16x32_bf16 v[100:103], a[20:23], a[228:231], v[100:103]\n"
"v_exp_f32 v96, v96\n"
"v_mfma_f32_16x16x32_bf16 v[100:103], a[24:27], a[232:235], v[100:103]\n"
"v_exp_f32 v97, v97\n"
"v_mfma_f32_16x16x32_bf16 v[100:103], a[28:31], a[236:239], v[100:103]\n"
"v_exp_f32 v98, v98\n"
"s_add_u32 m0, s75, 0\n"
"s_add_u32 s76, s76, 3072\n"
"buffer_load_dwordx4 v1, s[40:43], s76 offen offset:1024 lds\n"
"v_mfma_f32_16x16x32_bf16 v[116:119], a[16:19], a[240:243], v[28:31]\n"
"ds_read_b128 a[16:19], v6 offset:12288\n"
"buffer_store_dwordx4 a[132:135], v12, s[52:55], s57 offen offset:0 sc1\n"
"v_mfma_f32_16x16x32_bf16 v[116:119], a[20:23], a[244:247], v[116:119]\n"
"ds_read_b128 a[20:23], v7 offset:12288\n"
"v_mfma_f32_16x16x32_bf16 v[116:119], a[24:27], a[248:251], v[116:119]\n"
"ds_read_b128 a[24:27], v8 offset:12288\n"
"v_mfma_f32_16x16x32_bf16 v[116:119], a[28:31], a[252:255], v[116:119]\n"
"ds_read_b128 a[28:31], v9 offset:12288\n"
"s_waitcnt lgkmcnt(10)\n"
"v_mfma_f32_16x16x32_bf16 v[72:75], a[0:3], a[192:195], v[16:19]\n"
"buffer_store_dwordx4 a[148:151], v12, s[52:55], s57 offen offset:1024 sc1\n"
"s_waitcnt lgkmcnt(8)\n"
"v_mfma_f32_16x16x32_bf16 v[72:75], a[4:7], a[196:199], v[72:75]\n"
"v_exp_f32 v99, v99\n"
"s_waitcnt lgkmcnt(6)\n"
"v_mfma_f32_16x16x32_bf16 v[72:75], a[8:11], a[200:203], v[72:75]\n"
"v_exp_f32 v112, v112\n"
"s_waitcnt lgkmcnt(4)\n"
"v_mfma_f32_16x16x32_bf16 v[72:75], a[12:15], a[204:207], v[72:75]\n"
"v_exp_f32 v113, v113\n"
"v_mfma_f32_16x16x32_bf16 v[88:91], a[0:3], a[208:211], v[20:23]\n"
"v_exp_f32 v114, v114\n"
"v_mfma_f32_16x16x32_bf16 v[88:91], a[4:7], a[212:215], v[88:91]\n"
"v_exp_f32 v115, v115\n"
"v_mfma_f32_16x16x32_bf16 v[88:91], a[8:11], a[216:219], v[88:91]\n"
"v_exp_f32 v68, v68\n"
"buffer_store_dwordx4 a[164:167], v12, s[52:55], s57 offen offset:2048 sc1\n"
"v_mfma_f32_16x16x32_bf16 v[88:91], a[12:15], a[220:223], v[88:91]\n"
"v_exp_f32 v69, v69\n"
"v_mfma_f32_16x16x32_bf16 v[104:107], a[0:3], a[224:227], v[24:27]\n"
"v_exp_f32 v70, v70\n"
"s_add_u32 m0, s75, 0\n"
"s_add_u32 s76, s76, 3072\n"
"buffer_load_dwordx4 v0, s[40:43], s76 offen offset:2048 lds\n"
"v_mfma_f32_16x16x32_bf16 v[104:107], a[4:7], a[228:231], v[104:107]\n"
"v_exp_f32 v71, v71\n"
"v_mfma_f32_16x16x32_bf16 v[104:107], a[8:11], a[232:235], v[104:107]\n"
"v_exp_f32 v84, v84\n"
"v_mfma_f32_16x16x32_bf16 v[104:107], a[12:15], a[236:239], v[104:107]\n"
"v_exp_f32 v85, v85\n"
"buffer_store_dwordx4 a[180:183], v12, s[52:55], s57 offen offset:3072 sc1\n"
"v_mfma_f32_16x16x32_bf16 v[120:123], a[0:3], a[240:243], v[28:31]\n"
"buffer_store_dwordx4 a[136:139], v12, s[52:55], s58 offen offset:0 sc1\n"
"v_mfma_f32_16x16x32_bf16 v[120:123], a[4:7], a[244:247], v[120:123]\n"
"v_exp_f32 v86, v86\n"
"v_mfma_f32_16x16x32_bf16 v[120:123], a[8:11], a[248:251], v[120:123]\n"
"v_exp_f32 v87, v87\n"
"v_mfma_f32_16x16x32_bf16 v[120:123], a[12:15], a[252:255], v[120:123]\n"
"v_exp_f32 v100, v100\n"
"s_waitcnt lgkmcnt(3)\n"
"v_mfma_f32_16x16x32_bf16 v[76:79], a[16:19], a[192:195], v[16:19]\n"
"v_exp_f32 v101, v101\n"
"s_waitcnt lgkmcnt(2)\n"
"v_mfma_f32_16x16x32_bf16 v[76:79], a[20:23], a[196:199], v[76:79]\n"
"v_exp_f32 v102, v102\n"
"s_waitcnt lgkmcnt(1)\n"
"v_mfma_f32_16x16x32_bf16 v[76:79], a[24:27], a[200:203], v[76:79]\n"
"v_exp_f32 v103, v103\n"
"s_waitcnt lgkmcnt(0)\n"
"v_mfma_f32_16x16x32_bf16 v[76:79], a[28:31], a[204:207], v[76:79]\n"
"v_exp_f32 v116, v116\n"
"v_mfma_f32_16x16x32_bf16 v[92:95], a[16:19], a[208:211], v[20:23]\n"
"v_exp_f32 v117, v117\n"
"v_mfma_f32_16x16x32_bf16 v[92:95], a[20:23], a[212:215], v[92:95]\n"
"v_exp_f32 v118, v118\n"
"buffer_store_dwordx4 a[152:155], v12, s[52:55], s58 offen offset:1024 sc1\n"
"s_add_u32 m0, s75, 0\n"
"s_add_u32 s76, s76, 3072\n"
"buffer_load_dwordx4 v1, s[40:43], s76 offen offset:3072 lds\n"
"v_mfma_f32_16x16x32_bf16 v[92:95], a[24:27], a[216:219], v[92:95]\n"
"v_exp_f32 v119, v119\n"
"v_mfma_f32_16x16x32_bf16 v[92:95], a[28:31], a[220:223], v[92:95]\n"
"v_exp_f32 v72, v72\n"
"v_mfma_f32_16x16x32_bf16 v[108:111], a[16:19], a[224:227], v[24:27]\n"
"buffer_store_dwordx4 a[168:171], v12, s[52:55], s58 offen offset:2048 sc1\n"
"v_mfma_f32_16x16x32_bf16 v[108:111], a[20:23], a[228:231], v[108:111]\n"
"v_exp_f32 v73, v73\n"
"v_mfma_f32_16x16x32_bf16 v[108:111], a[24:27], a[232:235], v[108:111]\n"
"v_exp_f32 v74, v74\n"
"v_mfma_f32_16x16x32_bf16 v[108:111], a[28:31], a[236:239], v[108:111]\n"
"v_exp_f32 v75, v75\n"
"v_mfma_f32_16x16x32_bf16 v[124:127], a[16:19], a[240:243], v[28:31]\n"
"v_exp_f32 v88, v88\n"
"v_mfma_f32_16x16x32_bf16 v[124:127], a[20:23], a[244:247], v[124:127]\n"
"v_exp_f32 v89, v89\n"
"v_mfma_f32_16x16x32_bf16 v[124:127], a[24:27], a[248:251], v[124:127]\n"
"v_exp_f32 v90, v90\n"
"v_mfma_f32_16x16x32_bf16 v[124:127], a[28:31], a[252:255], v[124:127]\n"
"v_exp_f32 v91, v91\n"
"buffer_store_dwordx4 a[184:187], v12, s[52:55], s58 offen offset:3072 sc1\n"
"v_mfma_f32_16x16x32_bf16 a[96:99], a[112:115], v[32:35], a[96:99]\n"
"v_exp_f32 v104, v104\n"
"v_mfma_f32_16x16x32_bf16 a[96:99], a[112:115], v[36:39], a[96:99]\n"
"v_exp_f32 v105, v105\n"
"v_mfma_f32_16x16x32_bf16 v[128:131], a[32:35], v[32:35], v[128:131]\n"
"v_exp_f32 v106, v106\n"
"s_add_u32 m0, s75, 16384\n"
"s_add_u32 s76, s76, 56320\n"
"buffer_load_dwordx4 v0, s[40:43], s76 offen offset:0 lds\n"
"v_mfma_f32_16x16x32_bf16 v[128:131], a[36:39], v[36:39], v[128:131]\n"
"v_exp_f32 v107, v107\n"
"v_mfma_f32_16x16x32_bf16 v[132:135], a[40:43], v[32:35], v[132:135]\n"
"buffer_store_dwordx4 a[140:143], v12, s[52:55], s59 offen offset:0 sc1\n"
"v_mfma_f32_16x16x32_bf16 v[132:135], a[44:47], v[36:39], v[132:135]\n"
"v_xor_b32 v6, s64, v6\n"
"v_xor_b32 v7, s64, v7\n"
"v_mfma_f32_16x16x32_bf16 v[136:139], a[48:51], v[32:35], v[136:139]\n"
"v_xor_b32 v8, s64, v8\n"
"v_xor_b32 v9, s64, v9\n"
"v_mfma_f32_16x16x32_bf16 v[136:139], a[52:55], v[36:39], v[136:139]\n"
"v_xor_b32 v10, s65, v10\n"
"v_xor_b32 v11, s65, v11\n"
"v_mfma_f32_16x16x32_bf16 v[140:143], a[56:59], v[32:35], v[140:143]\n"
"s_xor_b32 s62, s62, s65\n"
"s_xor_b32 s63, s63, s64\n"
"v_mfma_f32_16x16x32_bf16 v[140:143], a[60:63], v[36:39], v[140:143]\n"
"s_add_u32 s60, s60, 0x4000\n"
"s_add_u32 s61, s61, 0x4000\n"
"buffer_store_dwordx4 a[156:159], v12, s[52:55], s59 offen offset:1024 sc1\n"
"v_mfma_f32_16x16x32_bf16 v[144:147], a[64:67], v[32:35], v[144:147]\n"
"s_xor_b32 s64, s64, 0xc000\n"
"s_xor_b32 s65, s65, 0xc000\n"
"v_mfma_f32_16x16x32_bf16 v[144:147], a[68:71], v[36:39], v[144:147]\n"
"v_exp_f32 v120, v120\n"
"v_mfma_f32_16x16x32_bf16 v[148:151], a[72:75], v[32:35], v[148:151]\n"
"v_exp_f32 v121, v121\n"
"v_mfma_f32_16x16x32_bf16 v[148:151], a[76:79], v[36:39], v[148:151]\n"
"v_exp_f32 v122, v122\n"
"v_mfma_f32_16x16x32_bf16 v[152:155], a[80:83], v[32:35], v[152:155]\n"
"v_exp_f32 v123, v123\n"
"buffer_store_dwordx4 a[172:175], v12, s[52:55], s59 offen offset:2048 sc1\n"
"v_mfma_f32_16x16x32_bf16 v[152:155], a[84:87], v[36:39], v[152:155]\n"
"v_exp_f32 v76, v76\n"
"s_add_u32 m0, s75, 16384\n"
"s_add_u32 s76, s76, 3072\n"
"buffer_load_dwordx4 v1, s[40:43], s76 offen offset:1024 lds\n"
"v_mfma_f32_16x16x32_bf16 v[156:159], a[88:91], v[32:35], v[156:159]\n"
"v_cvt_pk_bf16_f32 v32, v64, v65\n"
"v_cvt_pk_bf16_f32 v33, v66, v67\n"
"v_mfma_f32_16x16x32_bf16 v[156:159], a[92:95], v[36:39], v[156:159]\n"
"v_cvt_pk_bf16_f32 v34, v68, v69\n"
"v_cvt_pk_bf16_f32 v35, v70, v71\n"
"v_mfma_f32_16x16x32_bf16 a[100:103], a[112:115], v[40:43], a[100:103]\n"
"v_cvt_pk_bf16_f32 v36, v72, v73\n"
"v_cvt_pk_bf16_f32 v37, v74, v75\n"
"v_mfma_f32_16x16x32_bf16 a[100:103], a[112:115], v[44:47], a[100:103]\n"
"v_exp_f32 v77, v77\n"
"buffer_store_dwordx4 a[188:191], v12, s[52:55], s59 offen offset:3072 sc1\n"
"v_mfma_f32_16x16x32_bf16 v[160:163], a[32:35], v[40:43], v[160:163]\n"
"v_exp_f32 v78, v78\n"
"v_mfma_f32_16x16x32_bf16 v[160:163], a[36:39], v[44:47], v[160:163]\n"
"v_exp_f32 v79, v79\n"
"v_mfma_f32_16x16x32_bf16 v[164:167], a[40:43], v[40:43], v[164:167]\n"
"v_cvt_pk_bf16_f32 v38, v76, v77\n"
"v_cvt_pk_bf16_f32 v39, v78, v79\n"
"v_mfma_f32_16x16x32_bf16 v[164:167], a[44:47], v[44:47], v[164:167]\n"
"v_exp_f32 v92, v92\n"
"v_mfma_f32_16x16x32_bf16 v[168:171], a[48:51], v[40:43], v[168:171]\n"
"v_exp_f32 v93, v93\n"
"v_mfma_f32_16x16x32_bf16 v[168:171], a[52:55], v[44:47], v[168:171]\n"
"v_exp_f32 v94, v94\n"
"v_mfma_f32_16x16x32_bf16 v[172:175], a[56:59], v[40:43], v[172:175]\n"
"v_exp_f32 v95, v95\n"
"v_mfma_f32_16x16x32_bf16 v[172:175], a[60:63], v[44:47], v[172:175]\n"
"v_exp_f32 v108, v108\n"
"v_mfma_f32_16x16x32_bf16 v[176:179], a[64:67], v[40:43], v[176:179]\n"
"v_exp_f32 v109, v109\n"
"s_add_u32 m0, s75, 16384\n"
"s_add_u32 s76, s76, 3072\n"
"buffer_load_dwordx4 v0, s[40:43], s76 offen offset:2048 lds\n"
"v_mfma_f32_16x16x32_bf16 v[176:179], a[68:71], v[44:47], v[176:179]\n"
"v_exp_f32 v110, v110\n"
"v_mfma_f32_16x16x32_bf16 v[180:183], a[72:75], v[40:43], v[180:183]\n"
"v_exp_f32 v111, v111\n"
"v_mfma_f32_16x16x32_bf16 v[180:183], a[76:79], v[44:47], v[180:183]\n"
"v_exp_f32 v124, v124\n"
"v_mfma_f32_16x16x32_bf16 v[184:187], a[80:83], v[40:43], v[184:187]\n"
"v_exp_f32 v125, v125\n"
"v_mfma_f32_16x16x32_bf16 v[184:187], a[84:87], v[44:47], v[184:187]\n"
"v_exp_f32 v126, v126\n"
"v_mfma_f32_16x16x32_bf16 v[188:191], a[88:91], v[40:43], v[188:191]\n"
"v_cvt_pk_bf16_f32 v40, v80, v81\n"
"v_cvt_pk_bf16_f32 v41, v82, v83\n"
"v_mfma_f32_16x16x32_bf16 v[188:191], a[92:95], v[44:47], v[188:191]\n"
"v_cvt_pk_bf16_f32 v42, v84, v85\n"
"v_cvt_pk_bf16_f32 v43, v86, v87\n"
"v_mfma_f32_16x16x32_bf16 a[104:107], a[112:115], v[48:51], a[104:107]\n"
"v_cvt_pk_bf16_f32 v44, v88, v89\n"
"v_cvt_pk_bf16_f32 v45, v90, v91\n"
"v_mfma_f32_16x16x32_bf16 a[104:107], a[112:115], v[52:55], a[104:107]\n"
"v_cvt_pk_bf16_f32 v46, v92, v93\n"
"v_cvt_pk_bf16_f32 v47, v94, v95\n"
"v_mfma_f32_16x16x32_bf16 v[192:195], a[32:35], v[48:51], v[192:195]\n"
"v_exp_f32 v127, v127\n"
"v_mfma_f32_16x16x32_bf16 v[192:195], a[36:39], v[52:55], v[192:195]\n"
"s_add_u32 m0, s75, 16384\n"
"s_add_u32 s76, s76, 3072\n"
"buffer_load_dwordx4 v1, s[40:43], s76 offen offset:3072 lds\n"
"s_xor_b32 s75, s75, 0x8000\n"
"v_mfma_f32_16x16x32_bf16 v[196:199], a[40:43], v[48:51], v[196:199]\n"
"v_mfma_f32_16x16x32_bf16 v[196:199], a[44:47], v[52:55], v[196:199]\n"
"v_mfma_f32_16x16x32_bf16 v[200:203], a[48:51], v[48:51], v[200:203]\n"
"v_mfma_f32_16x16x32_bf16 v[200:203], a[52:55], v[52:55], v[200:203]\n"
"v_mfma_f32_16x16x32_bf16 v[204:207], a[56:59], v[48:51], v[204:207]\n"
"v_mfma_f32_16x16x32_bf16 v[204:207], a[60:63], v[52:55], v[204:207]\n"
"v_mfma_f32_16x16x32_bf16 v[208:211], a[64:67], v[48:51], v[208:211]\n"
"v_mfma_f32_16x16x32_bf16 v[208:211], a[68:71], v[52:55], v[208:211]\n"
"v_mfma_f32_16x16x32_bf16 v[212:215], a[72:75], v[48:51], v[212:215]\n"
"v_mfma_f32_16x16x32_bf16 v[212:215], a[76:79], v[52:55], v[212:215]\n"
"v_mfma_f32_16x16x32_bf16 v[216:219], a[80:83], v[48:51], v[216:219]\n"
"v_mfma_f32_16x16x32_bf16 v[216:219], a[84:87], v[52:55], v[216:219]\n"
"v_mfma_f32_16x16x32_bf16 v[220:223], a[88:91], v[48:51], v[220:223]\n"
"s_waitcnt vmcnt(34)\n"
"s_barrier\n"
"v_mfma_f32_16x16x32_bf16 v[220:223], a[92:95], v[52:55], v[220:223]\n"
"ds_read_b128 a[0:3], v6 offset:0\n"
"ds_read_b128 a[4:7], v7 offset:0\n"
"v_mfma_f32_16x16x32_bf16 a[108:111], a[112:115], v[56:59], a[108:111]\n"
"ds_read_b128 a[8:11], v8 offset:0\n"
"ds_read_b128 a[12:15], v9 offset:0\n"
"v_mfma_f32_16x16x32_bf16 a[108:111], a[112:115], v[60:63], a[108:111]\n"
"v_cvt_pk_bf16_f32 v48, v96, v97\n"
"v_mfma_f32_16x16x32_bf16 v[224:227], a[32:35], v[56:59], v[224:227]\n"
"s_add_u32 s75, s77, 65536\n"
"s_add_u32 m0, s75, 0\n"
"s_bfe_u32 s76, s68, 0x40000\n"
"s_lshl_b32 s76, s76, 17\n"
"buffer_load_dwordx4 v0, s[40:43], s76 offen offset:0 lds\n"
"v_mfma_f32_16x16x32_bf16 v[224:227], a[36:39], v[60:63], v[224:227]\n"
"v_cvt_pk_bf16_f32 v49, v98, v99\n"
"ds_read_b128 a[16:19], v6 offset:4096\n"
"v_mfma_f32_16x16x32_bf16 v[228:231], a[40:43], v[56:59], v[228:231]\n"
"ds_read_b128 a[20:23], v7 offset:4096\n"
"ds_read_b128 a[24:27], v8 offset:4096\n"
"v_mfma_f32_16x16x32_bf16 v[228:231], a[44:47], v[60:63], v[228:231]\n"
"ds_read_b128 a[28:31], v9 offset:4096\n"
"v_cvt_pk_bf16_f32 v50, v100, v101\n"
"v_mfma_f32_16x16x32_bf16 v[232:235], a[48:51], v[56:59], v[232:235]\n"
"v_cvt_pk_bf16_f32 v51, v102, v103\n"
"v_cvt_pk_bf16_f32 v52, v104, v105\n"
"v_mfma_f32_16x16x32_bf16 v[232:235], a[52:55], v[60:63], v[232:235]\n"
"v_cvt_pk_bf16_f32 v53, v106, v107\n"
"v_cvt_pk_bf16_f32 v54, v108, v109\n"
"v_mfma_f32_16x16x32_bf16 v[236:239], a[56:59], v[56:59], v[236:239]\n"
"v_cvt_pk_bf16_f32 v55, v110, v111\n"
"ds_read_b64_tr_b16 a[32:33], v10 offset:0\n"
"v_mfma_f32_16x16x32_bf16 v[236:239], a[60:63], v[60:63], v[236:239]\n"
"ds_read_b64_tr_b16 a[34:35], v10 offset:4096\n"
"ds_read_b64_tr_b16 a[36:37], v10 offset:8192\n"
"v_mfma_f32_16x16x32_bf16 v[240:243], a[64:67], v[56:59], v[240:243]\n"
"ds_read_b64_tr_b16 a[38:39], v10 offset:12288\n"
"ds_read_b64_tr_b16 a[40:41], v11 offset:0\n"
"v_mfma_f32_16x16x32_bf16 v[240:243], a[68:71], v[60:63], v[240:243]\n"
"ds_read_b64_tr_b16 a[42:43], v11 offset:4096\n"
"ds_read_b64_tr_b16 a[44:45], v11 offset:8192\n"
"v_mfma_f32_16x16x32_bf16 v[244:247], a[72:75], v[56:59], v[244:247]\n"
"ds_read_b64_tr_b16 a[46:47], v11 offset:12288\n"
"ds_read_b64_tr_b16 a[48:49], v10 offset:256\n"
"v_mfma_f32_16x16x32_bf16 v[244:247], a[76:79], v[60:63], v[244:247]\n"
"ds_read_b64_tr_b16 a[50:51], v10 offset:4352\n"
"ds_read_b64_tr_b16 a[52:53], v10 offset:8448\n"
"v_mfma_f32_16x16x32_bf16 v[248:251], a[80:83], v[56:59], v[248:251]\n"
"s_add_u32 m0, s75, 0\n"
"s_add_u32 s76, s76, 3072\n"
"buffer_load_dwordx4 v1, s[40:43], s76 offen offset:1024 lds\n"
"v_mfma_f32_16x16x32_bf16 v[248:251], a[84:87], v[60:63], v[248:251]\n"
"ds_read_b64_tr_b16 a[54:55], v10 offset:12544\n"
"ds_read_b64_tr_b16 a[56:57], v11 offset:256\n"
"v_mfma_f32_16x16x32_bf16 v[252:255], a[88:91], v[56:59], v[252:255]\n"
"v_cvt_pk_bf16_f32 v56, v112, v113\n"
"v_cvt_pk_bf16_f32 v57, v114, v115\n"
"v_mfma_f32_16x16x32_bf16 v[252:255], a[92:95], v[60:63], v[252:255]\n"
"s_waitcnt lgkmcnt(15)\n"
"v_mfma_f32_16x16x32_bf16 v[64:67], a[0:3], a[192:195], v[16:19]\n"
"v_cvt_pk_bf16_f32 v58, v116, v117\n"
"v_cvt_pk_bf16_f32 v59, v118, v119\n"
"v_mfma_f32_16x16x32_bf16 v[64:67], a[4:7], a[196:199], v[64:67]\n"
"v_cvt_pk_bf16_f32 v60, v120, v121\n"
"v_cvt_pk_bf16_f32 v61, v122, v123\n"
"v_mfma_f32_16x16x32_bf16 v[64:67], a[8:11], a[200:203], v[64:67]\n"
"v_cvt_pk_bf16_f32 v62, v124, v125\n"
"v_cvt_pk_bf16_f32 v63, v126, v127\n"
"v_mfma_f32_16x16x32_bf16 v[64:67], a[12:15], a[204:207], v[64:67]\n"
"ds_read_b64_tr_b16 a[58:59], v11 offset:4352\n"
"ds_read_b64_tr_b16 a[60:61], v11 offset:8448\n"
"v_mfma_f32_16x16x32_bf16 v[80:83], a[0:3], a[208:211], v[20:23]\n"
"ds_read_b64_tr_b16 a[62:63], v11 offset:12544\n"
"ds_read_b64_tr_b16 a[64:65], v10 offset:512\n"
"v_mfma_f32_16x16x32_bf16 v[80:83], a[4:7], a[212:215], v[80:83]\n"
"ds_read_b64_tr_b16 a[66:67], v10 offset:4608\n"
"ds_read_b64_tr_b16 a[68:69], v10 offset:8704\n"
"v_mfma_f32_16x16x32_bf16 v[80:83], a[8:11], a[216:219], v[80:83]\n"
"ds_read_b64_tr_b16 a[70:71], v10 offset:12800\n"
"ds_read_b64_tr_b16 a[72:73], v11 offset:512\n"
"v_mfma_f32_16x16x32_bf16 v[80:83], a[12:15], a[220:223], v[80:83]\n"
"ds_read_b64_tr_b16 a[74:75], v11 offset:4608\n"
"ds_read_b64_tr_b16 a[76:77], v11 offset:8704\n"
"v_mfma_f32_16x16x32_bf16 v[96:99], a[0:3], a[224:227], v[24:27]\n"
"v_mfma_f32_16x16x32_bf16 v[96:99], a[4:7], a[228:231], v[96:99]\n"
"ds_read_b64_tr_b16 a[78:79], v11 offset:12800\n"
"ds_read_b64_tr_b16 a[80:81], v10 offset:768\n"
"v_mfma_f32_16x16x32_bf16 v[96:99], a[8:11], a[232:235], v[96:99]\n"
"ds_read_b64_tr_b16 a[82:83], v10 offset:4864\n"
"ds_read_b64_tr_b16 a[84:85], v10 offset:8960\n"
"v_mfma_f32_16x16x32_bf16 v[96:99], a[12:15], a[236:239], v[96:99]\n"
"ds_read_b64_tr_b16 a[86:87], v10 offset:13056\n"
"ds_read_b64_tr_b16 a[88:89], v11 offset:768\n"
"v_mfma_f32_16x16x32_bf16 v[112:115], a[0:3], a[240:243], v[28:31]\n"
"ds_read_b128 a[0:3], v6 offset:8192\n"
"ds_read_b64_tr_b16 a[90:91], v11 offset:4864\n"
"v_mfma_f32_16x16x32_bf16 v[112:115], a[4:7], a[244:247], v[112:115]\n"
"ds_read_b128 a[4:7], v7 offset:8192\n"
"ds_read_b64_tr_b16 a[92:93], v11 offset:8960\n"
"v_mfma_f32_16x16x32_bf16 v[112:115], a[8:11], a[248:251], v[112:115]\n"
"ds_read_b128 a[8:11], v8 offset:8192\n"
"ds_read_b64_tr_b16 a[94:95], v11 offset:13056\n"
"v_mfma_f32_16x16x32_bf16 v[112:115], a[12:15], a[252:255], v[112:115]\n"
"ds_read_b128 a[12:15], v9 offset:8192\n"
"v_mfma_f32_16x16x32_bf16 v[68:71], a[16:19], a[192:195], v[16:19]\n"
"v_exp_f32 v64, v64\n"
"v_mfma_f32_16x16x32_bf16 v[68:71], a[20:23], a[196:199], v[68:71]\n"
"v_exp_f32 v65, v65\n"
"s_waitcnt lgkmcnt(15)\n"
"v_mfma_f32_16x16x32_bf16 v[68:71], a[24:27], a[200:203], v[68:71]\n"
"v_exp_f32 v66, v66\n"
"s_add_u32 m0, s75, 0\n"
"s_add_u32 s76, s76, 3072\n"
"buffer_load_dwordx4 v0, s[40:43], s76 offen offset:2048 lds\n"
"v_mfma_f32_16x16x32_bf16 v[68:71], a[28:31], a[204:207], v[68:71]\n"
"v_exp_f32 v67, v67\n"
"v_mfma_f32_16x16x32_bf16 v[84:87], a[16:19], a[208:211], v[20:23]\n"
"v_mfma_f32_16x16x32_bf16 v[84:87], a[20:23], a[212:215], v[84:87]\n"
"v_exp_f32 v80, v80\n"
"v_mfma_f32_16x16x32_bf16 v[84:87], a[24:27], a[216:219], v[84:87]\n"
"v_exp_f32 v81, v81\n"
"v_mfma_f32_16x16x32_bf16 v[84:87], a[28:31], a[220:223], v[84:87]\n"
"v_exp_f32 v82, v82\n"
"v_mfma_f32_16x16x32_bf16 v[100:103], a[16:19], a[224:227], v[24:27]\n"
"v_exp_f32 v83, v83\n"
"v_mfma_f32_16x16x32_bf16 v[100:103], a[20:23], a[228:231], v[100:103]\n"
"v_exp_f32 v96, v96\n"
"v_mfma_f32_16x16x32_bf16 v[100:103], a[24:27], a[232:235], v[100:103]\n"
"v_exp_f32 v97, v97\n"
"v_mfma_f32_16x16x32_bf16 v[100:103], a[28:31], a[236:239], v[100:103]\n"
"v_exp_f32 v98, v98\n"
"v_mfma_f32_16x16x32_bf16 v[116:119], a[16:19], a[240:243], v[28:31]\n"
"ds_read_b128 a[16:19], v6 offset:12288\n"
"v_mfma_f32_16x16x32_bf16 v[116:119], a[20:23], a[244:247], v[116:119]\n"
"ds_read_b128 a[20:23], v7 offset:12288\n"
"v_mfma_f32_16x16x32_bf16 v[116:119], a[24:27], a[248:251], v[116:119]\n"
"ds_read_b128 a[24:27], v8 offset:12288\n"
"v_mfma_f32_16x16x32_bf16 v[116:119], a[28:31], a[252:255], v[116:119]\n"
"ds_read_b128 a[28:31], v9 offset:12288\n"
"s_waitcnt lgkmcnt(10)\n"
"v_mfma_f32_16x16x32_bf16 v[72:75], a[0:3], a[192:195], v[16:19]\n"
"s_add_u32 m0, s75, 0\n"
"s_add_u32 s76, s76, 3072\n"
"buffer_load_dwordx4 v1, s[40:43], s76 offen offset:3072 lds\n"
"s_waitcnt lgkmcnt(8)\n"
"v_mfma_f32_16x16x32_bf16 v[72:75], a[4:7], a[196:199], v[72:75]\n"
"v_exp_f32 v99, v99\n"
"s_waitcnt lgkmcnt(6)\n"
"v_mfma_f32_16x16x32_bf16 v[72:75], a[8:11], a[200:203], v[72:75]\n"
"v_exp_f32 v112, v112\n"
"s_waitcnt lgkmcnt(4)\n"
"v_mfma_f32_16x16x32_bf16 v[72:75], a[12:15], a[204:207], v[72:75]\n"
"v_exp_f32 v113, v113\n"
"v_mfma_f32_16x16x32_bf16 v[88:91], a[0:3], a[208:211], v[20:23]\n"
"v_exp_f32 v114, v114\n"
"v_mfma_f32_16x16x32_bf16 v[88:91], a[4:7], a[212:215], v[88:91]\n"
"v_exp_f32 v115, v115\n"
"v_mfma_f32_16x16x32_bf16 v[88:91], a[8:11], a[216:219], v[88:91]\n"
"v_exp_f32 v68, v68\n"
"v_mfma_f32_16x16x32_bf16 v[88:91], a[12:15], a[220:223], v[88:91]\n"
"v_exp_f32 v69, v69\n"
"v_mfma_f32_16x16x32_bf16 v[104:107], a[0:3], a[224:227], v[24:27]\n"
"v_exp_f32 v70, v70\n"
"v_mfma_f32_16x16x32_bf16 v[104:107], a[4:7], a[228:231], v[104:107]\n"
"v_exp_f32 v71, v71\n"
"v_mfma_f32_16x16x32_bf16 v[104:107], a[8:11], a[232:235], v[104:107]\n"
"v_exp_f32 v84, v84\n"
"v_mfma_f32_16x16x32_bf16 v[104:107], a[12:15], a[236:239], v[104:107]\n"
"v_exp_f32 v85, v85\n"
"v_mfma_f32_16x16x32_bf16 v[120:123], a[0:3], a[240:243], v[28:31]\n"
"s_add_u32 m0, s75, 16384\n"
"s_add_u32 s76, s76, 56320\n"
"buffer_load_dwordx4 v0, s[40:43], s76 offen offset:0 lds\n"
"v_mfma_f32_16x16x32_bf16 v[120:123], a[4:7], a[244:247], v[120:123]\n"
"v_exp_f32 v86, v86\n"
"v_mfma_f32_16x16x32_bf16 v[120:123], a[8:11], a[248:251], v[120:123]\n"
"v_exp_f32 v87, v87\n"
"v_mfma_f32_16x16x32_bf16 v[120:123], a[12:15], a[252:255], v[120:123]\n"
"v_exp_f32 v100, v100\n"
"s_waitcnt lgkmcnt(3)\n"
"v_mfma_f32_16x16x32_bf16 v[76:79], a[16:19], a[192:195], v[16:19]\n"
"v_exp_f32 v101, v101\n"
"s_waitcnt lgkmcnt(2)\n"
"v_mfma_f32_16x16x32_bf16 v[76:79], a[20:23], a[196:199], v[76:79]\n"
"v_exp_f32 v102, v102\n"
"s_waitcnt lgkmcnt(1)\n"
"v_mfma_f32_16x16x32_bf16 v[76:79], a[24:27], a[200:203], v[76:79]\n"
"v_exp_f32 v103, v103\n"
"s_waitcnt lgkmcnt(0)\n"
"v_mfma_f32_16x16x32_bf16 v[76:79], a[28:31], a[204:207], v[76:79]\n"
"v_exp_f32 v116, v116\n"
"v_mfma_f32_16x16x32_bf16 v[92:95], a[16:19], a[208:211], v[20:23]\n"
"v_exp_f32 v117, v117\n"
"v_mfma_f32_16x16x32_bf16 v[92:95], a[20:23], a[212:215], v[92:95]\n"
"v_exp_f32 v118, v118\n"
"v_mfma_f32_16x16x32_bf16 v[92:95], a[24:27], a[216:219], v[92:95]\n"
"v_exp_f32 v119, v119\n"
"v_mfma_f32_16x16x32_bf16 v[92:95], a[28:31], a[220:223], v[92:95]\n"
"v_exp_f32 v72, v72\n"
"v_mfma_f32_16x16x32_bf16 v[108:111], a[16:19], a[224:227], v[24:27]\n"
"s_add_u32 m0, s75, 16384\n"
"s_add_u32 s76, s76, 3072\n"
"buffer_load_dwordx4 v1, s[40:43], s76 offen offset:1024 lds\n"
"v_mfma_f32_16x16x32_bf16 v[108:111], a[20:23], a[228:231], v[108:111]\n"
"v_exp_f32 v73, v73\n"
"v_mfma_f32_16x16x32_bf16 v[108:111], a[24:27], a[232:235], v[108:111]\n"
"v_exp_f32 v74, v74\n"
"v_mfma_f32_16x16x32_bf16 v[108:111], a[28:31], a[236:239], v[108:111]\n"
"v_exp_f32 v75, v75\n"
"v_mfma_f32_16x16x32_bf16 v[124:127], a[16:19], a[240:243], v[28:31]\n"
"v_exp_f32 v88, v88\n"
"v_mfma_f32_16x16x32_bf16 v[124:127], a[20:23], a[244:247], v[124:127]\n"
"v_exp_f32 v89, v89\n"
"v_mfma_f32_16x16x32_bf16 v[124:127], a[24:27], a[248:251], v[124:127]\n"
"v_exp_f32 v90, v90\n"
"v_mfma_f32_16x16x32_bf16 v[124:127], a[28:31], a[252:255], v[124:127]\n"
"v_exp_f32 v91, v91\n"
"v_mfma_f32_16x16x32_bf16 a[96:99], a[112:115], v[32:35], a[96:99]\n"
"v_exp_f32 v104, v104\n"
"v_mfma_f32_16x16x32_bf16 a[96:99], a[112:115], v[36:39], a[96:99]\n"
"v_exp_f32 v105, v105\n"
"v_mfma_f32_16x16x32_bf16 v[128:131], a[32:35], v[32:35], v[128:131]\n"
"v_exp_f32 v106, v106\n"
"s_mov_b32 s54, 1843200\n"
"buffer_load_dword a119, v13, s[36:39], %[ma] offen offset:128 sc1\n"
"s_waitcnt vmcnt(9)\n"
"s_cmp_lg_u64 s[70:71], 0\n"
"s_cbranch_scc1 .Lsga_%=\n"
"s_mov_b64 exec, 15\n"
"buffer_store_dwordx2 a[116:117], v14, s[36:39], %[mw] offen sc1\n"
"s_mov_b64 exec, -1\n"
".Lsga_%=:\n"
"v_mfma_f32_16x16x32_bf16 v[128:131], a[36:39], v[36:39], v[128:131]\n"
"v_exp_f32 v107, v107\n"
"v_mfma_f32_16x16x32_bf16 v[132:135], a[40:43], v[32:35], v[132:135]\n"
"s_add_u32 m0, s75, 16384\n"
"s_add_u32 s76, s76, 3072\n"
"buffer_load_dwordx4 v0, s[40:43], s76 offen offset:2048 lds\n"
"v_mfma_f32_16x16x32_bf16 v[132:135], a[44:47], v[36:39], v[132:135]\n"
"v_xor_b32 v6, s64, v6\n"
"v_xor_b32 v7, s64, v7\n"
"v_mfma_f32_16x16x32_bf16 v[136:139], a[48:51], v[32:35], v[136:139]\n"
"v_xor_b32 v8, s64, v8\n"
"v_xor_b32 v9, s64, v9\n"
"v_mfma_f32_16x16x32_bf16 v[136:139], a[52:55], v[36:39], v[136:139]\n"
"v_xor_b32 v10, s65, v10\n"
"v_xor_b32 v11, s65, v11\n"
"v_mfma_f32_16x16x32_bf16 v[140:143], a[56:59], v[32:35], v[140:143]\n"
"s_xor_b32 s62, s62, s65\n"
"s_xor_b32 s63, s63, s64\n"
"v_mfma_f32_16x16x32_bf16 v[140:143], a[60:63], v[36:39], v[140:143]\n"
"s_add_u32 s60, s60, 0x4000\n"
"s_add_u32 s61, s61, 0x4000\n"
"v_mfma_f32_16x16x32_bf16 v[144:147], a[64:67], v[32:35], v[144:147]\n"
"s_xor_b32 s64, s64, 0xc000\n"
"s_xor_b32 s65, s65, 0xc000\n"
"v_mfma_f32_16x16x32_bf16 v[144:147], a[68:71], v[36:39], v[144:147]\n"
"v_exp_f32 v120, v120\n"
"v_mfma_f32_16x16x32_bf16 v[148:151], a[72:75], v[32:35], v[148:151]\n"
"v_exp_f32 v121, v121\n"
"v_mfma_f32_16x16x32_bf16 v[148:151], a[76:79], v[36:39], v[148:151]\n"
"v_exp_f32 v122, v122\n"
"v_mfma_f32_16x16x32_bf16 v[152:155], a[80:83], v[32:35], v[152:155]\n"
"v_exp_f32 v123, v123\n"
"v_mfma_f32_16x16x32_bf16 v[152:155], a[84:87], v[36:39], v[152:155]\n"
"v_exp_f32 v76, v76\n"
"v_mfma_f32_16x16x32_bf16 v[156:159], a[88:91], v[32:35], v[156:159]\n"
"v_cvt_pk_bf16_f32 v32, v64, v65\n"
"v_cvt_pk_bf16_f32 v33, v66, v67\n"
"v_mfma_f32_16x16x32_bf16 v[156:159], a[92:95], v[36:39], v[156:159]\n"
"v_cvt_pk_bf16_f32 v34, v68, v69\n"
"v_cvt_pk_bf16_f32 v35, v70, v71\n"
"v_mfma_f32_16x16x32_bf16 a[100:103], a[112:115], v[40:43], a[100:103]\n"
"v_cvt_pk_bf16_f32 v36, v72, v73\n"
"v_cvt_pk_bf16_f32 v37, v74, v75\n"
"v_mfma_f32_16x16x32_bf16 a[100:103], a[112:115], v[44:47], a[100:103]\n"
"v_exp_f32 v77, v77\n"
"v_mfma_f32_16x16x32_bf16 v[160:163], a[32:35], v[40:43], v[160:163]\n"
"v_exp_f32 v78, v78\n"
"v_mfma_f32_16x16x32_bf16 v[160:163], a[36:39], v[44:47], v[160:163]\n"
"v_exp_f32 v79, v79\n"
"v_mfma_f32_16x16x32_bf16 v[164:167], a[40:43], v[40:43], v[164:167]\n"
"v_cvt_pk_bf16_f32 v38, v76, v77\n"
"v_cvt_pk_bf16_f32 v39, v78, v79\n"
"v_mfma_f32_16x16x32_bf16 v[164:167], a[44:47], v[44:47], v[164:167]\n"
"v_exp_f32 v92, v92\n"
"v_mfma_f32_16x16x32_bf16 v[168:171], a[48:51], v[40:43], v[168:171]\n"
"v_exp_f32 v93, v93\n"
"s_add_u32 m0, s75, 16384\n"
"s_add_u32 s76, s76, 3072\n"
"buffer_load_dwordx4 v1, s[40:43], s76 offen offset:3072 lds\n"
"s_xor_b32 s75, s75, 0x10000\n"
"v_mfma_f32_16x16x32_bf16 v[168:171], a[52:55], v[44:47], v[168:171]\n"
"v_exp_f32 v94, v94\n"
"v_mfma_f32_16x16x32_bf16 v[172:175], a[56:59], v[40:43], v[172:175]\n"
"v_exp_f32 v95, v95\n"
"v_mfma_f32_16x16x32_bf16 v[172:175], a[60:63], v[44:47], v[172:175]\n"
"v_exp_f32 v108, v108\n"
"v_mfma_f32_16x16x32_bf16 v[176:179], a[64:67], v[40:43], v[176:179]\n"
"v_exp_f32 v109, v109\n"
"v_mfma_f32_16x16x32_bf16 v[176:179], a[68:71], v[44:47], v[176:179]\n"
"v_exp_f32 v110, v110\n"
"v_mfma_f32_16x16x32_bf16 v[180:183], a[72:75], v[40:43], v[180:183]\n"
"v_exp_f32 v111, v111\n"
"v_mfma_f32_16x16x32_bf16 v[180:183], a[76:79], v[44:47], v[180:183]\n"
"v_exp_f32 v124, v124\n"
"v_mfma_f32_16x16x32_bf16 v[184:187], a[80:83], v[40:43], v[184:187]\n"
"v_exp_f32 v125, v125\n"
"v_mfma_f32_16x16x32_bf16 v[184:187], a[84:87], v[44:47], v[184:187]\n"
"v_exp_f32 v126, v126\n"
"v_mfma_f32_16x16x32_bf16 v[188:191], a[88:91], v[40:43], v[188:191]\n"
"v_cvt_pk_bf16_f32 v40, v80, v81\n"
"v_cvt_pk_bf16_f32 v41, v82, v83\n"
"v_mfma_f32_16x16x32_bf16 v[188:191], a[92:95], v[44:47], v[188:191]\n"
"v_cvt_pk_bf16_f32 v42, v84, v85\n"
"v_cvt_pk_bf16_f32 v43, v86, v87\n"
"v_mfma_f32_16x16x32_bf16 a[104:107], a[112:115], v[48:51], a[104:107]\n"
"v_cvt_pk_bf16_f32 v44, v88, v89\n"
"v_cvt_pk_bf16_f32 v45, v90, v91\n"
"v_mfma_f32_16x16x32_bf16 a[104:107], a[112:115], v[52:55], a[104:107]\n"
"v_cvt_pk_bf16_f32 v46, v92, v93\n"
"v_cvt_pk_bf16_f32 v47, v94, v95\n"
"v_mfma_f32_16x16x32_bf16 v[192:195], a[32:35], v[48:51], v[192:195]\n"
"v_exp_f32 v127, v127\n"
"v_mfma_f32_16x16x32_bf16 v[192:195], a[36:39], v[52:55], v[192:195]\n"
"v_mfma_f32_16x16x32_bf16 v[196:199], a[40:43], v[48:51], v[196:199]\n"
"v_mfma_f32_16x16x32_bf16 v[196:199], a[44:47], v[52:55], v[196:199]\n"
"v_mfma_f32_16x16x32_bf16 v[200:203], a[48:51], v[48:51], v[200:203]\n"
"v_mfma_f32_16x16x32_bf16 v[200:203], a[52:55], v[52:55], v[200:203]\n"
"v_mfma_f32_16x16x32_bf16 v[204:207], a[56:59], v[48:51], v[204:207]\n"
"v_mfma_f32_16x16x32_bf16 v[204:207], a[60:63], v[52:55], v[204:207]\n"
"v_mfma_f32_16x16x32_bf16 v[208:211], a[64:67], v[48:51], v[208:211]\n"
"v_mfma_f32_16x16x32_bf16 v[208:211], a[68:71], v[52:55], v[208:211]\n"
"v_mfma_f32_16x16x32_bf16 v[212:215], a[72:75], v[48:51], v[212:215]\n"
"v_mfma_f32_16x16x32_bf16 v[212:215], a[76:79], v[52:55], v[212:215]\n"
"v_mfma_f32_16x16x32_bf16 v[216:219], a[80:83], v[48:51], v[216:219]\n"
"v_mfma_f32_16x16x32_bf16 v[216:219], a[84:87], v[52:55], v[216:219]\n"
"v_mfma_f32_16x16x32_bf16 v[220:223], a[88:91], v[48:51], v[220:223]\n"
"s_waitcnt vmcnt(63)\n"
"s_barrier\n"
"v_mfma_f32_16x16x32_bf16 v[220:223], a[92:95], v[52:55], v[220:223]\n"
"ds_read_b128 a[0:3], v6 offset:0\n"
"ds_read_b128 a[4:7], v7 offset:0\n"
"v_mfma_f32_16x16x32_bf16 a[108:111], a[112:115], v[56:59], a[108:111]\n"
"ds_read_b128 a[8:11], v8 offset:0\n"
"ds_read_b128 a[12:15], v9 offset:0\n"
"v_mfma_f32_16x16x32_bf16 a[108:111], a[112:115], v[60:63], a[108:111]\n"
"v_cvt_pk_bf16_f32 v48, v96, v97\n"
"v_mfma_f32_16x16x32_bf16 v[224:227], a[32:35], v[56:59], v[224:227]\n"
"s_add_u32 s75, s77, 32768\n"
"s_add_u32 m0, s75, 0\n"
"s_bfe_u32 s76, s68, 0x40008\n"
"s_lshl_b32 s76, s76, 17\n"
"buffer_load_dwordx4 v0, s[40:43], s76 offen offset:0 lds\n"
"v_mfma_f32_16x16x32_bf16 v[224:227], a[36:39], v[60:63], v[224:227]\n"
"v_cvt_pk_bf16_f32 v49, v98, v99\n"
"ds_read_b128 a[16:19], v6 offset:4096\n"
"v_mfma_f32_16x16x32_bf16 v[228:231], a[40:43], v[56:59], v[228:231]\n"
"ds_read_b128 a[20:23], v7 offset:4096\n"
"ds_read_b128 a[24:27], v8 offset:4096\n"
"v_mfma_f32_16x16x32_bf16 v[228:231], a[44:47], v[60:63], v[228:231]\n"
"ds_read_b128 a[28:31], v9 offset:4096\n"
"v_cvt_pk_bf16_f32 v50, v100, v101\n"
"v_mfma_f32_16x16x32_bf16 v[232:235], a[48:51], v[56:59], v[232:235]\n"
"v_cvt_pk_bf16_f32 v51, v102, v103\n"
"v_cvt_pk_bf16_f32 v52, v104, v105\n"
"v_mfma_f32_16x16x32_bf16 v[232:235], a[52:55], v[60:63], v[232:235]\n"
"v_cvt_pk_bf16_f32 v53, v106, v107\n"
"v_cvt_pk_bf16_f32 v54, v108, v109\n"
"v_mfma_f32_16x16x32_bf16 v[236:239], a[56:59], v[56:59], v[236:239]\n"
"v_cvt_pk_bf16_f32 v55, v110, v111\n"
"ds_read_b64_tr_b16 a[32:33], v10 offset:0\n"
"v_mfma_f32_16x16x32_bf16 v[236:239], a[60:63], v[60:63], v[236:239]\n"
"ds_read_b64_tr_b16 a[34:35], v10 offset:4096\n"
"ds_read_b64_tr_b16 a[36:37], v10 offset:8192\n"
"v_mfma_f32_16x16x32_bf16 v[240:243], a[64:67], v[56:59], v[240:243]\n"
"ds_read_b64_tr_b16 a[38:39], v10 offset:12288\n"
"ds_read_b64_tr_b16 a[40:41], v11 offset:0\n"
"v_mfma_f32_16x16x32_bf16 v[240:243], a[68:71], v[60:63], v[240:243]\n"
"ds_read_b64_tr_b16 a[42:43], v11 offset:4096\n"
"ds_read_b64_tr_b16 a[44:45], v11 offset:8192\n"
"v_mfma_f32_16x16x32_bf16 v[244:247], a[72:75], v[56:59], v[244:247]\n"
"ds_read_b64_tr_b16 a[46:47], v11 offset:12288\n"
"ds_read_b64_tr_b16 a[48:49], v10 offset:256\n"
"v_mfma_f32_16x16x32_bf16 v[244:247], a[76:79], v[60:63], v[244:247]\n"
"ds_read_b64_tr_b16 a[50:51], v10 offset:4352\n"
"ds_read_b64_tr_b16 a[52:53], v10 offset:8448\n"
"v_mfma_f32_16x16x32_bf16 v[248:251], a[80:83], v[56:59], v[248:251]\n"
"s_add_u32 m0, s75, 0\n"
"s_add_u32 s76, s76, 3072\n"
"buffer_load_dwordx4 v1, s[40:43], s76 offen offset:1024 lds\n"
"v_mfma_f32_16x16x32_bf16 v[248:251], a[84:87], v[60:63], v[248:251]\n"
"ds_read_b64_tr_b16 a[54:55], v10 offset:12544\n"
"ds_read_b64_tr_b16 a[56:57], v11 offset:256\n"
"v_mfma_f32_16x16x32_bf16 v[252:255], a[88:91], v[56:59], v[252:255]\n"
"v_cvt_pk_bf16_f32 v56, v112, v113\n"
"v_cvt_pk_bf16_f32 v57, v114, v115\n"
"v_mfma_f32_16x16x32_bf16 v[252:255], a[92:95], v[60:63], v[252:255]\n"
"s_add_u32 m0, s75, 0\n"
"s_add_u32 s76, s76, 3072\n"
"buffer_load_dwordx4 v0, s[40:43], s76 offen offset:2048 lds\n"
"v_cvt_pk_bf16_f32 v58, v116, v117\n"
"v_cvt_pk_bf16_f32 v59, v118, v119\n"
"buffer_load_dword v20, v13, s[36:39], %[xa] offen offset:4 sc1\n"
"v_cvt_pk_bf16_f32 v60, v120, v121\n"
"v_cvt_pk_bf16_f32 v61, v122, v123\n"
"v_cvt_pk_bf16_f32 v62, v124, v125\n"
"v_cvt_pk_bf16_f32 v63, v126, v127\n"
"ds_read_b64_tr_b16 a[58:59], v11 offset:4352\n"
"ds_read_b64_tr_b16 a[60:61], v11 offset:8448\n"
"ds_read_b64_tr_b16 a[62:63], v11 offset:12544\n"
"ds_read_b64_tr_b16 a[64:65], v10 offset:512\n"
"ds_read_b64_tr_b16 a[66:67], v10 offset:4608\n"
"ds_read_b64_tr_b16 a[68:69], v10 offset:8704\n"
"ds_read_b64_tr_b16 a[70:71], v10 offset:12800\n"
"ds_read_b64_tr_b16 a[72:73], v11 offset:512\n"
"ds_read_b64_tr_b16 a[74:75], v11 offset:4608\n"
"ds_read_b64_tr_b16 a[76:77], v11 offset:8704\n"
"s_add_u32 m0, s75, 0\n"
"s_add_u32 s76, s76, 3072\n"
"buffer_load_dwordx4 v1, s[40:43], s76 offen offset:3072 lds\n"
"ds_read_b64_tr_b16 a[78:79], v11 offset:12800\n"
"ds_read_b64_tr_b16 a[80:81], v10 offset:768\n"
"ds_read_b64_tr_b16 a[82:83], v10 offset:4864\n"
"ds_read_b64_tr_b16 a[84:85], v10 offset:8960\n"
"ds_read_b64_tr_b16 a[86:87], v10 offset:13056\n"
"ds_read_b64_tr_b16 a[88:89], v11 offset:768\n"
"ds_read_b64_tr_b16 a[90:91], v11 offset:4864\n"
"ds_read_b64_tr_b16 a[92:93], v11 offset:8960\n"
"ds_read_b64_tr_b16 a[94:95], v11 offset:13056\n"
"s_add_u32 m0, s75, 16384\n"
"s_add_u32 s76, s76, 56320\n"
"buffer_load_dwordx4 v0, s[40:43], s76 offen offset:0 lds\n"
"s_add_u32 m0, s75, 16384\n"
"s_add_u32 s76, s76, 3072\n"
"buffer_load_dwordx4 v1, s[40:43], s76 offen offset:1024 lds\n"
"v_mbcnt_lo_u32_b32 v26, -1, 0\n"
"v_mbcnt_hi_u32_b32 v26, -1, v26\n"
"v_and_b32 v27, 15, v26\n"
"v_lshrrev_b32 v28, 4, v26\n"
"s_add_u32 m0, s75, 16384\n"
"s_add_u32 s76, s76, 3072\n"
"buffer_load_dwordx4 v0, s[40:43], s76 offen offset:2048 lds\n"
"v_lshrrev_b32 v29, 2, v27\n"
"v_and_b32 v30, 1, v28\n"
"v_lshl_add_u32 v29, v30, 2, v29\n"
"v_lshlrev_b32 v29, 5, v29\n"
"v_and_b32 v30, 3, v26\n"
"v_lshl_add_u32 v29, v30, 3, v29\n"
"v_lshl_add_u32 v2, v28, 10, v29\n"
"v_add_u32 v2, 0x10000, v2\n"
"v_xor_b32 v3, 0x80, v2\n"
"v_mov_b32 v4, v12\n"
"s_add_u32 m0, s75, 16384\n"
"s_add_u32 s76, s76, 3072\n"
"buffer_load_dwordx4 v1, s[40:43], s76 offen offset:3072 lds\n"
"s_xor_b32 s75, s75, 0x10000\n"
"s_lshr_b32 s65, s68, 8\n"
"s_lshr_b64 s[68:69], s[68:69], 12\n"
"s_add_u32 s75, s77, 98304\n"
"v_lshlrev_b32 v5, 11, v27\n"
"v_lshl_add_u32 v5, v28, 3, v5\n"
"s_lshl_b32 s63, %[wv], 17\n"
"v_add_u32 v5, s63, v5\n"
"v_lshlrev_b32 v6, 4, v26\n"
"v_add_u32 v6, s77, v6\n"
"v_lshlrev_b32 v8, 3, v30\n"
"s_lshl_b32 s63, %[ntp], 13\n"
"s_sub_u32 s44, s52, s63\n"
"s_subb_u32 s45, s53, 0\n"
"s_lshl_b32 s63, %[ksp], 15\n"
"s_sub_u32 s46, 1966080, s63\n"
"s_mov_b32 s48, %[yl]\n"
"s_and_b32 s49, %[yh], 0xffff\n"
"s_mov_b32 s50, 489536\n"
"s_mov_b32 s62, 3\n"
"v_mfma_f32_16x16x32_bf16 a[96:99], a[112:115], v[32:35], a[96:99]\n"
"v_mfma_f32_16x16x32_bf16 a[96:99], a[112:115], v[36:39], a[96:99]\n"
"s_waitcnt lgkmcnt(15)\n"
"v_mfma_f32_16x16x32_bf16 v[128:131], a[32:35], v[32:35], v[128:131]\n"
"v_mfma_f32_16x16x32_bf16 v[128:131], a[36:39], v[36:39], v[128:131]\n"
"v_mfma_f32_16x16x32_bf16 v[132:135], a[40:43], v[32:35], v[132:135]\n"
"s_nop 3\n"
"v_accvgpr_read_b32 v14, a96\n"
"v_mfma_f32_16x16x32_bf16 v[132:135], a[44:47], v[36:39], v[132:135]\n"
"v_cmp_ngt_f32 vcc, s67, v14\n"
"s_mov_b32 s72, 4096\n"
"s_or_b64 s[70:71], s[70:71], vcc\n"
"v_mfma_f32_16x16x32_bf16 v[136:139], a[48:51], v[32:35], v[136:139]\n"
"v_rcp_f32 v14, v14\n"
"v_mfma_f32_16x16x32_bf16 v[136:139], a[52:55], v[36:39], v[136:139]\n"
"v_mfma_f32_16x16x32_bf16 v[140:143], a[56:59], v[32:35], v[140:143]\n"
"v_mul_f32 v128, v128, v14\n"
"v_mul_f32 v129, v129, v14\n"
"v_mfma_f32_16x16x32_bf16 v[140:143], a[60:63], v[36:39], v[140:143]\n"
"v_mul_f32 v130, v130, v14\n"
"v_mul_f32 v131, v131, v14\n"
"s_waitcnt lgkmcnt(14)\n"
"v_mfma_f32_16x16x32_bf16 v[144:147], a[64:67], v[32:35], v[144:147]\n"
"v_mul_f32 v132, v132, v14\n"
"v_mul_f32 v133, v133, v14\n"
"s_waitcnt lgkmcnt(12)\n"
"v_mfma_f32_16x16x32_bf16 v[144:147], a[68:71], v[36:39], v[144:147]\n"
"v_mul_f32 v134, v134, v14\n"
"v_mul_f32 v135, v135, v14\n"
"s_waitcnt lgkmcnt(10)\n"
"v_mfma_f32_16x16x32_bf16 v[148:151], a[72:75], v[32:35], v[148:151]\n"
"v_cvt_pk_bf16_f32 v128, v128, v129\n"
"v_cvt_pk_bf16_f32 v129, v130, v131\n"
"s_waitcnt lgkmcnt(8)\n"
"v_mfma_f32_16x16x32_bf16 v[148:151], a[76:79], v[36:39], v[148:151]\n"
"v_cvt_pk_bf16_f32 v130, v132, v133\n"
"v_cvt_pk_bf16_f32 v131, v134, v135\n"
"s_waitcnt lgkmcnt(6)\n"
"v_mfma_f32_16x16x32_bf16 v[152:155], a[80:83], v[32:35], v[152:155]\n"
"buffer_store_dwordx4 v[128:131], v12, s[52:55], s72 offen offset:0 sc1\n"
"s_waitcnt lgkmcnt(4)\n"
"v_mfma_f32_16x16x32_bf16 v[152:155], a[84:87], v[36:39], v[152:155]\n"
"v_accvgpr_write_b32 a192, v128\n"
"v_accvgpr_write_b32 a193, v129\n"
"s_waitcnt lgkmcnt(2)\n"
"v_mfma_f32_16x16x32_bf16 v[156:159], a[88:91], v[32:35], v[156:159]\n"
"v_accvgpr_write_b32 a194, v130\n"
"v_accvgpr_write_b32 a195, v131\n"
"s_waitcnt lgkmcnt(0)\n"
"v_mfma_f32_16x16x32_bf16 v[156:159], a[92:95], v[36:39], v[156:159]\n"
"v_mul_f32 v136, v136, v14\n"
"v_mul_f32 v137, v137, v14\n"
"v_mfma_f32_16x16x32_bf16 a[100:103], a[112:115], v[40:43], a[100:103]\n"
"buffer_load_dwordx2 v[24:25], v13, s[36:39], %[ma] offen sc1\n"
"v_mfma_f32_16x16x32_bf16 a[100:103], a[112:115], v[44:47], a[100:103]\n"
"v_mul_f32 v138, v138, v14\n"
"v_mul_f32 v139, v139, v14\n"
"v_mfma_f32_16x16x32_bf16 v[160:163], a[32:35], v[40:43], v[160:163]\n"
"v_mul_f32 v140, v140, v14\n"
"v_mul_f32 v141, v141, v14\n"
"v_mfma_f32_16x16x32_bf16 v[160:163], a[36:39], v[44:47], v[160:163]\n"
"v_mul_f32 v142, v142, v14\n"
"v_mul_f32 v143, v143, v14\n"
"v_mfma_f32_16x16x32_bf16 v[164:167], a[40:43], v[40:43], v[164:167]\n"
"v_cvt_pk_bf16_f32 v136, v136, v137\n"
"v_cvt_pk_bf16_f32 v137, v138, v139\n"
"v_mfma_f32_16x16x32_bf16 v[164:167], a[44:47], v[44:47], v[164:167]\n"
"v_cvt_pk_bf16_f32 v138, v140, v141\n"
"v_cvt_pk_bf16_f32 v139, v142, v143\n"
"v_mfma_f32_16x16x32_bf16 v[168:171], a[48:51], v[40:43], v[168:171]\n"
"buffer_store_dwordx4 v[136:139], v12, s[52:55], s72 offen offset:1024 sc1\n"
"v_mfma_f32_16x16x32_bf16 v[168:171], a[52:55], v[44:47], v[168:171]\n"
"v_accvgpr_write_b32 a208, v136\n"
"v_accvgpr_write_b32 a209, v137\n"
"v_mfma_f32_16x16x32_bf16 v[172:175], a[56:59], v[40:43], v[172:175]\n"
"v_accvgpr_write_b32 a210, v138\n"
"v_accvgpr_write_b32 a211, v139\n"
"v_mfma_f32_16x16x32_bf16 v[172:175], a[60:63], v[44:47], v[172:175]\n"
"v_mul_f32 v144, v144, v14\n"
"v_mul_f32 v145, v145, v14\n"
"v_mfma_f32_16x16x32_bf16 v[176:179], a[64:67], v[40:43], v[176:179]\n"
"v_mul_f32 v146, v146, v14\n"
"v_mul_f32 v147, v147, v14\n"
"v_mfma_f32_16x16x32_bf16 v[176:179], a[68:71], v[44:47], v[176:179]\n"
"v_mul_f32 v148, v148, v14\n"
"v_mul_f32 v149, v149, v14\n"
"v_mfma_f32_16x16x32_bf16 v[180:183], a[72:75], v[40:43], v[180:183]\n"
"v_mul_f32 v150, v150, v14\n"
"v_mul_f32 v151, v151, v14\n"
"v_mfma_f32_16x16x32_bf16 v[180:183], a[76:79], v[44:47], v[180:183]\n"
"v_cvt_pk_bf16_f32 v144, v144, v145\n"
"v_cvt_pk_bf16_f32 v145, v146, v147\n"
"v_mfma_f32_16x16x32_bf16 v[184:187], a[80:83], v[40:43], v[184:187]\n"
"v_cvt_pk_bf16_f32 v146, v148, v149\n"
"v_cvt_pk_bf16_f32 v147, v150, v151\n"
"v_mfma_f32_16x16x32_bf16 v[184:187], a[84:87], v[44:47], v[184:187]\n"
"buffer_store_dwordx4 v[144:147], v12, s[52:55], s72 offen offset:2048 sc1\n"
"v_mfma_f32_16x16x32_bf16 v[188:191], a[88:91], v[40:43], v[188:191]\n"
"v_accvgpr_write_b32 a224, v144\n"
"v_accvgpr_write_b32 a225, v145\n"
"v_mfma_f32_16x16x32_bf16 v[188:191], a[92:95], v[44:47], v[188:191]\n"
"v_accvgpr_write_b32 a226, v146\n"
"v_accvgpr_write_b32 a227, v147\n"
"v_mfma_f32_16x16x32_bf16 a[104:107], a[112:115], v[48:51], a[104:107]\n"
"v_mul_f32 v152, v152, v14\n"
"v_mul_f32 v153, v153, v14\n"
"v_mfma_f32_16x16x32_bf16 a[104:107], a[112:115], v[52:55], a[104:107]\n"
"v_mul_f32 v154, v154, v14\n"
"v_mul_f32 v155, v155, v14\n"
"v_mfma_f32_16x16x32_bf16 v[192:195], a[32:35], v[48:51], v[192:195]\n"
"v_mul_f32 v156, v156, v14\n"
"v_mul_f32 v157, v157, v14\n"
"v_mfma_f32_16x16x32_bf16 v[192:195], a[36:39], v[52:55], v[192:195]\n"
"v_mul_f32 v158, v158, v14\n"
"v_mul_f32 v159, v159, v14\n"
"v_mfma_f32_16x16x32_bf16 v[196:199], a[40:43], v[48:51], v[196:199]\n"
"v_cvt_pk_bf16_f32 v152, v152, v153\n"
"v_cvt_pk_bf16_f32 v153, v154, v155\n"
"v_mfma_f32_16x16x32_bf16 v[196:199], a[44:47], v[52:55], v[196:199]\n"
"v_cvt_pk_bf16_f32 v154, v156, v157\n"
"v_cvt_pk_bf16_f32 v155, v158, v159\n"
"v_mfma_f32_16x16x32_bf16 v[200:203], a[48:51], v[48:51], v[200:203]\n"
"buffer_store_dwordx4 v[152:155], v12, s[52:55], s72 offen offset:3072 sc1\n"
"v_mfma_f32_16x16x32_bf16 v[200:203], a[52:55], v[52:55], v[200:203]\n"
"v_accvgpr_write_b32 a240, v152\n"
"v_accvgpr_write_b32 a241, v153\n"
"v_mfma_f32_16x16x32_bf16 v[204:207], a[56:59], v[48:51], v[204:207]\n"
"v_accvgpr_write_b32 a242, v154\n"
"v_accvgpr_write_b32 a243, v155\n"
"v_mfma_f32_16x16x32_bf16 v[204:207], a[60:63], v[52:55], v[204:207]\n"
"v_accvgpr_read_b32 v15, a100\n"
"v_accvgpr_read_b32 v14, a104\n"
"v_mfma_f32_16x16x32_bf16 v[208:211], a[64:67], v[48:51], v[208:211]\n"
"v_cmp_ngt_f32 vcc, s67, v15\n"
"s_mov_b32 s73, 135168\n"
"s_or_b64 s[70:71], s[70:71], vcc\n"
"v_mfma_f32_16x16x32_bf16 v[208:211], a[68:71], v[52:55], v[208:211]\n"
"v_rcp_f32 v15, v15\n"
"v_mfma_f32_16x16x32_bf16 v[212:215], a[72:75], v[48:51], v[212:215]\n"
"v_mul_f32 v160, v160, v15\n"
"v_mul_f32 v161, v161, v15\n"
"v_mfma_f32_16x16x32_bf16 v[212:215], a[76:79], v[52:55], v[212:215]\n"
"v_mul_f32 v162, v162, v15\n"
"v_mul_f32 v163, v163, v15\n"
"v_mfma_f32_16x16x32_bf16 v[216:219], a[80:83], v[48:51], v[216:219]\n"
"v_mul_f32 v164, v164, v15\n"
"v_mul_f32 v165, v165, v15\n"
"v_mfma_f32_16x16x32_bf16 v[216:219], a[84:87], v[52:55], v[216:219]\n"
"v_mul_f32 v166, v166, v15\n"
"v_mul_f32 v167, v167, v15\n"
"v_mfma_f32_16x16x32_bf16 v[220:223], a[88:91], v[48:51], v[220:223]\n"
"v_cvt_pk_bf16_f32 v160, v160, v161\n"
"v_cvt_pk_bf16_f32 v161, v162, v163\n"
"v_mfma_f32_16x16x32_bf16 v[220:223], a[92:95], v[52:55], v[220:223]\n"
"v_cvt_pk_bf16_f32 v162, v164, v165\n"
"v_cvt_pk_bf16_f32 v163, v166, v167\n"
"v_mfma_f32_16x16x32_bf16 a[108:111], a[112:115], v[56:59], a[108:111]\n"
"buffer_store_dwordx4 v[160:163], v12, s[52:55], s73 offen offset:0 sc1\n"
"v_mfma_f32_16x16x32_bf16 a[108:111], a[112:115], v[60:63], a[108:111]\n"
"v_accvgpr_write_b32 a196, v160\n"
"v_accvgpr_write_b32 a197, v161\n"
"v_mfma_f32_16x16x32_bf16 v[224:227], a[32:35], v[56:59], v[224:227]\n"
"v_accvgpr_write_b32 a198, v162\n"
"v_accvgpr_write_b32 a199, v163\n"
"v_mfma_f32_16x16x32_bf16 v[224:227], a[36:39], v[60:63], v[224:227]\n"
"v_mul_f32 v168, v168, v15\n"
"v_mul_f32 v169, v169, v15\n"
"v_mfma_f32_16x16x32_bf16 v[228:231], a[40:43], v[56:59], v[228:231]\n"
"v_mul_f32 v170, v170, v15\n"
"v_mul_f32 v171, v171, v15\n"
"v_mfma_f32_16x16x32_bf16 v[228:231], a[44:47], v[60:63], v[228:231]\n"
"v_mul_f32 v172, v172, v15\n"
"v_mul_f32 v173, v173, v15\n"
"v_mfma_f32_16x16x32_bf16 v[232:235], a[48:51], v[56:59], v[232:235]\n"
"v_mul_f32 v174, v174, v15\n"
"v_mul_f32 v175, v175, v15\n"
"v_mfma_f32_16x16x32_bf16 v[232:235], a[52:55], v[60:63], v[232:235]\n"
"v_cvt_pk_bf16_f32 v168, v168, v169\n"
"v_cvt_pk_bf16_f32 v169, v170, v171\n"
"v_mfma_f32_16x16x32_bf16 v[236:239], a[56:59], v[56:59], v[236:239]\n"
"v_cvt_pk_bf16_f32 v170, v172, v173\n"
"v_cvt_pk_bf16_f32 v171, v174, v175\n"
"v_mfma_f32_16x16x32_bf16 v[236:239], a[60:63], v[60:63], v[236:239]\n"
"buffer_store_dwordx4 v[168:171], v12, s[52:55], s73 offen offset:1024 sc1\n"
"v_mfma_f32_16x16x32_bf16 v[240:243], a[64:67], v[56:59], v[240:243]\n"
"v_accvgpr_read_b32 v16, a116\n"
"v_accvgpr_read_b32 v17, a117\n"
"v_accvgpr_read_b32 v18, a118\n"
"s_waitcnt vmcnt(18)\n"
"v_accvgpr_read_b32 v19, a119\n"
"v_mfma_f32_16x16x32_bf16 v[240:243], a[68:71], v[60:63], v[240:243]\n"
"v_accvgpr_write_b32 a212, v168\n"
"v_accvgpr_write_b32 a213, v169\n"
"v_mfma_f32_16x16x32_bf16 v[244:247], a[72:75], v[56:59], v[244:247]\n"
"v_accvgpr_write_b32 a214, v170\n"
"v_accvgpr_write_b32 a215, v171\n"
"v_mfma_f32_16x16x32_bf16 v[244:247], a[76:79], v[60:63], v[244:247]\n"
"v_mul_f32 v176, v176, v15\n"
"v_mul_f32 v177, v177, v15\n"
"v_mfma_f32_16x16x32_bf16 v[248:251], a[80:83], v[56:59], v[248:251]\n"
"v_mul_f32 v178, v178, v15\n"
"v_mul_f32 v179, v179, v15\n"
"v_mfma_f32_16x16x32_bf16 v[248:251], a[84:87], v[60:63], v[248:251]\n"
"v_mul_f32 v180, v180, v15\n"
"v_mul_f32 v181, v181, v15\n"
"v_mfma_f32_16x16x32_bf16 v[252:255], a[88:91], v[56:59], v[252:255]\n"
"v_mul_f32 v182, v182, v15\n"
"v_mul_f32 v183, v183, v15\n"
"v_mfma_f32_16x16x32_bf16 v[252:255], a[92:95], v[60:63], v[252:255]\n"
"v_cvt_pk_bf16_f32 v176, v176, v177\n"
"v_cvt_pk_bf16_f32 v177, v178, v179\n"
"v_cvt_pk_bf16_f32 v178, v180, v181\n"
"v_cvt_pk_bf16_f32 v179, v182, v183\n"
"buffer_store_dwordx4 v[176:179], v12, s[52:55], s73 offen offset:2048 sc1\n"
"v_accvgpr_write_b32 a228, v176\n"
"v_accvgpr_write_b32 a229, v177\n"
"v_accvgpr_write_b32 a230, v178\n"
"v_accvgpr_write_b32 a231, v179\n"
"v_mul_f32 v184, v184, v15\n"
"v_mul_f32 v185, v185, v15\n"
"v_mul_f32 v186, v186, v15\n"
"v_mul_f32 v187, v187, v15\n"
"v_mul_f32 v188, v188, v15\n"
"v_mul_f32 v189, v189, v15\n"
"v_mul_f32 v190, v190, v15\n"
"v_mul_f32 v191, v191, v15\n"
"v_cvt_pk_bf16_f32 v184, v184, v185\n"
"v_cvt_pk_bf16_f32 v185, v186, v187\n"
"v_cvt_pk_bf16_f32 v186, v188, v189\n"
"v_cvt_pk_bf16_f32 v187, v190, v191\n"
"buffer_store_dwordx4 v[184:187], v12, s[52:55], s73 offen offset:3072 sc1\n"
"v_accvgpr_write_b32 a244, v184\n"
"v_accvgpr_write_b32 a245, v185\n"
"v_accvgpr_write_b32 a246, v186\n"
"v_accvgpr_write_b32 a247, v187\n"
"v_cmp_ngt_f32 vcc, s67, v14\n"
"s_mov_b32 s72, 266240\n"
"s_or_b64 s[70:71], s[70:71], vcc\n"
"v_rcp_f32 v14, v14\n"
"s_nop 0\n"
"v_mul_f32 v192, v192, v14\n"
"v_mul_f32 v193, v193, v14\n"
"v_mul_f32 v194, v194, v14\n"
"v_mul_f32 v195, v195, v14\n"
"v_mul_f32 v196, v196, v14\n"
"v_mul_f32 v197, v197, v14\n"
"v_mul_f32 v198, v198, v14\n"
"v_mul_f32 v199, v199, v14\n"
"v_cvt_pk_bf16_f32 v192, v192, v193\n"
"v_cvt_pk_bf16_f32 v193, v194, v195\n"
"v_cvt_pk_bf16_f32 v194, v196, v197\n"
"v_cvt_pk_bf16_f32 v195, v198, v199\n"
"buffer_store_dwordx4 v[192:195], v12, s[52:55], s72 offen offset:0 sc1\n"
"v_accvgpr_write_b32 a200, v192\n"
"v_accvgpr_write_b32 a201, v193\n"
"v_accvgpr_write_b32 a202, v194\n"
"v_accvgpr_write_b32 a203, v195\n"
"v_mul_f32 v200, v200, v14\n"
"v_mul_f32 v201, v201, v14\n"
"v_mul_f32 v202, v202, v14\n"
"v_mul_f32 v203, v203, v14\n"
"v_mul_f32 v204, v204, v14\n"
"v_mul_f32 v205, v205, v14\n"
"v_mul_f32 v206, v206, v14\n"
"v_mul_f32 v207, v207, v14\n"
"v_cvt_pk_bf16_f32 v200, v200, v201\n"
"v_cvt_pk_bf16_f32 v201, v202, v203\n"
"v_cvt_pk_bf16_f32 v202, v204, v205\n"
"v_cvt_pk_bf16_f32 v203, v206, v207\n"
"buffer_store_dwordx4 v[200:203], v12, s[52:55], s72 offen offset:1024 sc1\n"
"v_accvgpr_write_b32 a216, v200\n"
"v_accvgpr_write_b32 a217, v201\n"
"v_accvgpr_write_b32 a218, v202\n"
"v_accvgpr_write_b32 a219, v203\n"
"v_mul_f32 v208, v208, v14\n"
"v_mul_f32 v209, v209, v14\n"
"v_mul_f32 v210, v210, v14\n"
"v_mul_f32 v211, v211, v14\n"
"v_mul_f32 v212, v212, v14\n"
"v_mul_f32 v213, v213, v14\n"
"v_mul_f32 v214, v214, v14\n"
"v_mul_f32 v215, v215, v14\n"
"v_cvt_pk_bf16_f32 v208, v208, v209\n"
"v_cvt_pk_bf16_f32 v209, v210, v211\n"
"v_cvt_pk_bf16_f32 v210, v212, v213\n"
"v_cvt_pk_bf16_f32 v211, v214, v215\n"
"buffer_store_dwordx4 v[208:211], v12, s[52:55], s72 offen offset:2048 sc1\n"
"v_accvgpr_write_b32 a232, v208\n"
"v_accvgpr_write_b32 a233, v209\n"
"v_accvgpr_write_b32 a234, v210\n"
"v_accvgpr_write_b32 a235, v211\n"
"v_mul_f32 v216, v216, v14\n"
"v_mul_f32 v217, v217, v14\n"
"v_mul_f32 v218, v218, v14\n"
"v_mul_f32 v219, v219, v14\n"
"v_mul_f32 v220, v220, v14\n"
"v_mul_f32 v221, v221, v14\n"
"v_mul_f32 v222, v222, v14\n"
"v_mul_f32 v223, v223, v14\n"
"v_cvt_pk_bf16_f32 v216, v216, v217\n"
"v_cvt_pk_bf16_f32 v217, v218, v219\n"
"v_cvt_pk_bf16_f32 v218, v220, v221\n"
"v_cvt_pk_bf16_f32 v219, v222, v223\n"
"buffer_store_dwordx4 v[216:219], v12, s[52:55], s72 offen offset:3072 sc1\n"
"v_accvgpr_write_b32 a248, v216\n"
"v_accvgpr_write_b32 a249, v217\n"
"v_accvgpr_write_b32 a250, v218\n"
"v_accvgpr_write_b32 a251, v219\n"
"v_accvgpr_read_b32 v15, a108\n"
"v_cmp_ngt_f32 vcc, s67, v15\n"
"s_mov_b32 s73, 397312\n"
"s_or_b64 s[70:71], s[70:71], vcc\n"
"v_rcp_f32 v15, v15\n"
"s_nop 0\n"
"v_mul_f32 v224, v224, v15\n"
"v_mul_f32 v225, v225, v15\n"
"v_mul_f32 v226, v226, v15\n"
"v_mul_f32 v227, v227, v15\n"
"v_mul_f32 v228, v228, v15\n"
"v_mul_f32 v229, v229, v15\n"
"v_mul_f32 v230, v230, v15\n"
"v_mul_f32 v231, v231, v15\n"
"v_cvt_pk_bf16_f32 v224, v224, v225\n"
"v_cvt_pk_bf16_f32 v225, v226, v227\n"
"v_cvt_pk_bf16_f32 v226, v228, v229\n"
"v_cvt_pk_bf16_f32 v227, v230, v231\n"
"buffer_store_dwordx4 v[224:227], v12, s[52:55], s73 offen offset:0 sc1\n"
"v_accvgpr_write_b32 a204, v224\n"
"v_accvgpr_write_b32 a205, v225\n"
"v_accvgpr_write_b32 a206, v226\n"
"v_accvgpr_write_b32 a207, v227\n"
"v_mul_f32 v232, v232, v15\n"
"v_mul_f32 v233, v233, v15\n"
"v_mul_f32 v234, v234, v15\n"
"v_mul_f32 v235, v235, v15\n"
"v_mul_f32 v236, v236, v15\n"
"v_mul_f32 v237, v237, v15\n"
"v_mul_f32 v238, v238, v15\n"
"v_mul_f32 v239, v239, v15\n"
"v_cvt_pk_bf16_f32 v232, v232, v233\n"
"v_cvt_pk_bf16_f32 v233, v234, v235\n"
"v_cvt_pk_bf16_f32 v234, v236, v237\n"
"v_cvt_pk_bf16_f32 v235, v238, v239\n"
"buffer_store_dwordx4 v[232:235], v12, s[52:55], s73 offen offset:1024 sc1\n"
"v_accvgpr_write_b32 a220, v232\n"
"v_accvgpr_write_b32 a221, v233\n"
"v_accvgpr_write_b32 a222, v234\n"
"v_accvgpr_write_b32 a223, v235\n"
"v_mul_f32 v240, v240, v15\n"
"v_mul_f32 v241, v241, v15\n"
"v_mul_f32 v242, v242, v15\n"
"v_mul_f32 v243, v243, v15\n"
"v_mul_f32 v244, v244, v15\n"
"v_mul_f32 v245, v245, v15\n"
"v_mul_f32 v246, v246, v15\n"
"v_mul_f32 v247, v247, v15\n"
"v_cvt_pk_bf16_f32 v240, v240, v241\n"
"v_cvt_pk_bf16_f32 v241, v242, v243\n"
"v_cvt_pk_bf16_f32 v242, v244, v245\n"
"v_cvt_pk_bf16_f32 v243, v246, v247\n"
"buffer_store_dwordx4 v[240:243], v12, s[52:55], s73 offen offset:2048 sc1\n"
"v_accvgpr_write_b32 a236, v240\n"
"v_accvgpr_write_b32 a237, v241\n"
"v_accvgpr_write_b32 a238, v242\n"
"v_accvgpr_write_b32 a239, v243\n"
"v_mul_f32 v248, v248, v15\n"
"v_mul_f32 v249, v249, v15\n"
"v_mul_f32 v250, v250, v15\n"
"v_mul_f32 v251, v251, v15\n"
"v_mul_f32 v252, v252, v15\n"
"v_mul_f32 v253, v253, v15\n"
"v_mul_f32 v254, v254, v15\n"
"v_mul_f32 v255, v255, v15\n"
"v_cvt_pk_bf16_f32 v248, v248, v249\n"
"v_cvt_pk_bf16_f32 v249, v250, v251\n"
"v_cvt_pk_bf16_f32 v250, v252, v253\n"
"v_cvt_pk_bf16_f32 v251, v254, v255\n"
"buffer_store_dwordx4 v[248:251], v12, s[52:55], s73 offen offset:3072 sc1\n"
"v_accvgpr_write_b32 a252, v248\n"
"v_accvgpr_write_b32 a253, v249\n"
"v_accvgpr_write_b32 a254, v250\n"
"v_accvgpr_write_b32 a255, v251\n"
"s_or_b32 %[bad], s70, s71\n"
"s_lshl_b32 s63, %[wv], 2\n"
"s_add_u32 s63, s63, 131072\n"
"v_mov_b32 v64, s63\n"
"v_mov_b32 v65, %[bad]\n"
"v_mov_b32 v66, 131072\n"
"ds_write_b32 v64, v65\n"
"s_waitcnt lgkmcnt(0)\n"
"s_waitcnt vmcnt(26)\n"
"s_barrier\n"
"ds_read_b128 v[68:71], v66\n"
"ds_read_b64_tr_b16 a[0:1], v2 offset:0\n"
"ds_read_b64_tr_b16 a[2:3], v2 offset:4096\n"
"ds_read_b64_tr_b16 a[4:5], v2 offset:8192\n"
"ds_read_b64_tr_b16 a[6:7], v2 offset:12288\n"
"ds_read_b64_tr_b16 a[8:9], v3 offset:0\n"
"ds_read_b64_tr_b16 a[10:11], v3 offset:4096\n"
"ds_read_b64_tr_b16 a[12:13], v3 offset:8192\n"
"ds_read_b64_tr_b16 a[14:15], v3 offset:12288\n"
"ds_read_b64_tr_b16 a[16:17], v2 offset:256\n"
"ds_read_b64_tr_b16 a[18:19], v2 offset:4352\n"
"ds_read_b64_tr_b16 a[20:21], v2 offset:8448\n"
"ds_read_b64_tr_b16 a[22:23], v2 offset:12544\n"
"ds_read_b64_tr_b16 a[24:25], v3 offset:256\n"
"ds_read_b64_tr_b16 a[26:27], v3 offset:4352\n"
"ds_read_b64_tr_b16 a[28:29], v3 offset:8448\n"
"ds_read_b64_tr_b16 a[30:31], v3 offset:12544\n"
"ds_read_b64_tr_b16 a[32:33], v2 offset:512\n"
"ds_read_b64_tr_b16 a[34:35], v2 offset:4608\n"
"ds_read_b64_tr_b16 a[36:37], v2 offset:8704\n"
"ds_read_b64_tr_b16 a[38:39], v2 offset:12800\n"
"ds_read_b64_tr_b16 a[40:41], v3 offset:512\n"
"ds_read_b64_tr_b16 a[42:43], v3 offset:4608\n"
"ds_read_b64_tr_b16 a[44:45], v3 offset:8704\n"
"ds_read_b64_tr_b16 a[46:47], v3 offset:12800\n"
"ds_read_b64_tr_b16 a[48:49], v2 offset:768\n"
"ds_read_b64_tr_b16 a[50:51], v2 offset:4864\n"
"ds_read_b64_tr_b16 a[52:53], v2 offset:8960\n"
"ds_read_b64_tr_b16 a[54:55], v2 offset:13056\n"
"ds_read_b64_tr_b16 a[56:57], v3 offset:768\n"
"ds_read_b64_tr_b16 a[58:59], v3 offset:4864\n"
"s_waitcnt lgkmcnt(15)\n"
"v_or3_b32 v68, v68, v69, v70\n"
"v_or_b32 v68, v68, v71\n"
"s_nop 1\n"
"v_readfirstlane_b32 %[any], v68\n"
"s_nop 3\n"
"s_cmp_lg_u32 %[any], 0\n"
"s_cbranch_scc1 .Lza_%=\n"
"v_mfma_f32_16x16x32_bf16 v[128:131], a[0:3], a[128:131], 0\n"
"ds_read_b64_tr_b16 a[60:61], v3 offset:8960\n"
"ds_read_b64_tr_b16 a[62:63], v3 offset:13056\n"
"v_mfma_f32_16x16x32_bf16 v[128:131], a[4:7], a[144:147], v[128:131]\n"
"v_mfma_f32_16x16x32_bf16 v[160:163], a[0:3], a[132:135], 0\n"
"s_add_u32 s63, %[ksp], 1\n"
"s_and_b32 s63, s63, 3\n"
"s_add_u32 s63, s63, %[gr4]\n"
"s_mul_i32 s63, s63, 3\n"
"s_add_u32 s63, s63, 0\n"
"s_mul_i32 s52, s63, 15360\n"
"s_add_u32 s63, %[ksp], 2\n"
"s_and_b32 s63, s63, 3\n"
"s_add_u32 s63, s63, %[gr4]\n"
"s_mul_i32 s63, s63, 3\n"
"s_add_u32 s63, s63, 1\n"
"s_mul_i32 s53, s63, 15360\n"
"s_add_u32 s63, %[ksp], 3\n"
"s_and_b32 s63, s63, 3\n"
"s_add_u32 s63, s63, %[gr4]\n"
"s_mul_i32 s63, s63, 3\n"
"s_add_u32 s63, s63, 2\n"
"s_mul_i32 s54, s63, 15360\n"
"s_add_u32 s63, %[gr4], %[ksp]\n"
"s_mul_i32 s55, s63, 46080\n"
"s_add_u32 s66, s55, 15360\n"
"s_add_u32 s67, s55, 30720\n"
"v_mfma_f32_16x16x32_bf16 v[160:163], a[4:7], a[148:151], v[160:163]\n"
"v_mfma_f32_16x16x32_bf16 v[192:195], a[0:3], a[136:139], 0\n"
"v_mfma_f32_16x16x32_bf16 v[192:195], a[4:7], a[152:155], v[192:195]\n"
"v_mfma_f32_16x16x32_bf16 v[224:227], a[0:3], a[140:143], 0\n"
"ds_read_b64_tr_b16 a[0:1], v2 offset:16384\n"
"ds_read_b64_tr_b16 a[2:3], v2 offset:20480\n"
"v_mfma_f32_16x16x32_bf16 v[224:227], a[4:7], a[156:159], v[224:227]\n"
"v_mfma_f32_16x16x32_bf16 v[132:135], a[8:11], a[128:131], 0\n"
"ds_read_b64_tr_b16 a[4:5], v2 offset:24576\n"
"ds_read_b64_tr_b16 a[6:7], v2 offset:28672\n"
"v_mfma_f32_16x16x32_bf16 v[132:135], a[12:15], a[144:147], v[132:135]\n"
"v_mfma_f32_16x16x32_bf16 v[164:167], a[8:11], a[132:135], 0\n"
"s_waitcnt vmcnt(22)\n"
"v_add_u32 v20, 1, v20\n"
"s_mov_b64 exec, 15\n"
"buffer_store_dword v20, v8, s[36:39], %[xw] offen sc1\n"
"s_mov_b64 exec, -1\n"
"v_mfma_f32_16x16x32_bf16 v[164:167], a[12:15], a[148:151], v[164:167]\n"
"v_mfma_f32_16x16x32_bf16 v[196:199], a[8:11], a[136:139], 0\n"
"v_mfma_f32_16x16x32_bf16 v[196:199], a[12:15], a[152:155], v[196:199]\n"
"v_mfma_f32_16x16x32_bf16 v[228:231], a[8:11], a[140:143], 0\n"
"ds_read_b64_tr_b16 a[8:9], v3 offset:16384\n"
"ds_read_b64_tr_b16 a[10:11], v3 offset:20480\n"
"v_mfma_f32_16x16x32_bf16 v[228:231], a[12:15], a[156:159], v[228:231]\n"
"ds_read_b64_tr_b16 a[12:13], v3 offset:24576\n"
"ds_read_b64_tr_b16 a[14:15], v3 offset:28672\n"
"v_mfma_f32_16x16x32_bf16 v[136:139], a[16:19], a[128:131], 0\n"
"v_mfma_f32_16x16x32_bf16 v[136:139], a[20:23], a[144:147], v[136:139]\n"
"v_mfma_f32_16x16x32_bf16 v[168:171], a[16:19], a[132:135], 0\n"
"v_mfma_f32_16x16x32_bf16 v[168:171], a[20:23], a[148:151], v[168:171]\n"
"v_mfma_f32_16x16x32_bf16 v[200:203], a[16:19], a[136:139], 0\n"
"v_mfma_f32_16x16x32_bf16 v[200:203], a[20:23], a[152:155], v[200:203]\n"
"v_mfma_f32_16x16x32_bf16 v[232:235], a[16:19], a[140:143], 0\n"
"ds_read_b64_tr_b16 a[16:17], v2 offset:16640\n"
"ds_read_b64_tr_b16 a[18:19], v2 offset:20736\n"
"v_mfma_f32_16x16x32_bf16 v[232:235], a[20:23], a[156:159], v[232:235]\n"
"ds_read_b64_tr_b16 a[20:21], v2 offset:24832\n"
"ds_read_b64_tr_b16 a[22:23], v2 offset:28928\n"
"v_mfma_f32_16x16x32_bf16 v[140:143], a[24:27], a[128:131], 0\n"
"s_waitcnt lgkmcnt(15)\n"
"v_mfma_f32_16x16x32_bf16 v[140:143], a[28:31], a[144:147], v[140:143]\n"
"v_mfma_f32_16x16x32_bf16 v[172:175], a[24:27], a[132:135], 0\n"
"v_mfma_f32_16x16x32_bf16 v[172:175], a[28:31], a[148:151], v[172:175]\n"
"v_mfma_f32_16x16x32_bf16 v[204:207], a[24:27], a[136:139], 0\n"
"v_mfma_f32_16x16x32_bf16 v[204:207], a[28:31], a[152:155], v[204:207]\n"
"v_mfma_f32_16x16x32_bf16 v[236:239], a[24:27], a[140:143], 0\n"
"ds_read_b64_tr_b16 a[24:25], v3 offset:16640\n"
"ds_read_b64_tr_b16 a[26:27], v3 offset:20736\n"
"v_mfma_f32_16x16x32_bf16 v[236:239], a[28:31], a[156:159], v[236:239]\n"
"v_mfma_f32_16x16x32_bf16 v[144:147], a[32:35], a[128:131], 0\n"
"ds_read_b64_tr_b16 a[28:29], v3 offset:24832\n"
"ds_read_b64_tr_b16 a[30:31], v3 offset:28928\n"
"v_mfma_f32_16x16x32_bf16 v[144:147], a[36:39], a[144:147], v[144:147]\n"
"v_mfma_f32_16x16x32_bf16 v[176:179], a[32:35], a[132:135], 0\n"
"v_mfma_f32_16x16x32_bf16 v[176:179], a[36:39], a[148:151], v[176:179]\n"
"v_mfma_f32_16x16x32_bf16 v[208:211], a[32:35], a[136:139], 0\n"
"v_mfma_f32_16x16x32_bf16 v[208:211], a[36:39], a[152:155], v[208:211]\n"
"v_mfma_f32_16x16x32_bf16 v[240:243], a[32:35], a[140:143], 0\n"
"ds_read_b64_tr_b16 a[32:33], v2 offset:16896\n"
"ds_read_b64_tr_b16 a[34:35], v2 offset:20992\n"
"v_mfma_f32_16x16x32_bf16 v[240:243], a[36:39], a[156:159], v[240:243]\n"
"ds_read_b64_tr_b16 a[36:37], v2 offset:25088\n"
"ds_read_b64_tr_b16 a[38:39], v2 offset:29184\n"
"v_mfma_f32_16x16x32_bf16 v[148:151], a[40:43], a[128:131], 0\n"
"v_mfma_f32_16x16x32_bf16 v[148:151], a[44:47], a[144:147], v[148:151]\n"
"v_mfma_f32_16x16x32_bf16 v[180:183], a[40:43], a[132:135], 0\n"
"v_mfma_f32_16x16x32_bf16 v[180:183], a[44:47], a[148:151], v[180:183]\n"
"v_mfma_f32_16x16x32_bf16 v[212:215], a[40:43], a[136:139], 0\n"
"v_mfma_f32_16x16x32_bf16 v[212:215], a[44:47], a[152:155], v[212:215]\n"
"v_mfma_f32_16x16x32_bf16 v[244:247], a[40:43], a[140:143], 0\n"
"ds_read_b64_tr_b16 a[40:41], v3 offset:16896\n"
"ds_read_b64_tr_b16 a[42:43], v3 offset:20992\n"
"v_mfma_f32_16x16x32_bf16 v[244:247], a[44:47], a[156:159], v[244:247]\n"
"ds_read_b64_tr_b16 a[44:45], v3 offset:25088\n"
"ds_read_b64_tr_b16 a[46:47], v3 offset:29184\n"
"v_mfma_f32_16x16x32_bf16 v[152:155], a[48:51], a[128:131], 0\n"
"v_mfma_f32_16x16x32_bf16 v[152:155], a[52:55], a[144:147], v[152:155]\n"
"s_and_b32 s63, s65, 15\n"
"s_lshl_b32 s58, s63, 12\n"
"s_lshr_b32 s65, s65, 4\n"
"s_add_u32 s59, s58, 131072\n"
"s_add_u32 s60, s58, 262144\n"
"s_add_u32 s61, s58, 393216\n"
"s_waitcnt vmcnt(16)\n"
"v_cmp_ne_u32 vcc, v24, v17\n"
"s_nop 1\n"
"s_mov_b64 s[78:79], vcc\n"
"v_cmp_ne_u32 vcc, v25, v16\n"
"s_nop 1\n"
"s_or_b64 s[78:79], s[78:79], vcc\n"
"s_cbranch_scc1 .Ls0a_%=\n"
".Lr0a_%=:\n"
"v_mfma_f32_16x16x32_bf16 v[184:187], a[48:51], a[132:135], 0\n"
"v_mfma_f32_16x16x32_bf16 v[184:187], a[52:55], a[148:151], v[184:187]\n"
"v_mfma_f32_16x16x32_bf16 v[216:219], a[48:51], a[136:139], 0\n"
"v_mfma_f32_16x16x32_bf16 v[216:219], a[52:55], a[152:155], v[216:219]\n"
"v_mfma_f32_16x16x32_bf16 v[248:251], a[48:51], a[140:143], 0\n"
"ds_read_b64_tr_b16 a[48:49], v2 offset:17152\n"
"ds_read_b64_tr_b16 a[50:51], v2 offset:21248\n"
"v_mfma_f32_16x16x32_bf16 v[248:251], a[52:55], a[156:159], v[248:251]\n"
"ds_read_b64_tr_b16 a[52:53], v2 offset:25344\n"
"ds_read_b64_tr_b16 a[54:55], v2 offset:29440\n"
"s_waitcnt lgkmcnt(15)\n"
"v_mfma_f32_16x16x32_bf16 v[156:159], a[56:59], a[128:131], 0\n"
"v_mfma_f32_16x16x32_bf16 v[156:159], a[60:63], a[144:147], v[156:159]\n"
"v_mfma_f32_16x16x32_bf16 v[188:191], a[56:59], a[132:135], 0\n"
"v_mfma_f32_16x16x32_bf16 v[188:191], a[60:63], a[148:151], v[188:191]\n"
"v_mfma_f32_16x16x32_bf16 v[220:223], a[56:59], a[136:139], 0\n"
"buffer_load_dwordx4 a[128:131], v4, s[44:47], s58 offen offset:0 sc1\n"
"v_mfma_f32_16x16x32_bf16 v[220:223], a[60:63], a[152:155], v[220:223]\n"
"v_mfma_f32_16x16x32_bf16 v[252:255], a[56:59], a[140:143], 0\n"
"ds_read_b64_tr_b16 a[56:57], v3 offset:17152\n"
"ds_read_b64_tr_b16 a[58:59], v3 offset:21248\n"
"v_mfma_f32_16x16x32_bf16 v[252:255], a[60:63], a[156:159], v[252:255]\n"
"ds_read_b64_tr_b16 a[60:61], v3 offset:25344\n"
"ds_read_b64_tr_b16 a[62:63], v3 offset:29440\n"
"v_mfma_f32_16x16x32_bf16 v[128:131], a[0:3], a[160:163], v[128:131]\n"
"v_mfma_f32_16x16x32_bf16 v[128:131], a[4:7], a[176:179], v[128:131]\n"
"v_mfma_f32_16x16x32_bf16 v[160:163], a[0:3], a[164:167], v[160:163]\n"
"v_mfma_f32_16x16x32_bf16 v[160:163], a[4:7], a[180:183], v[160:163]\n"
"buffer_load_dwordx4 a[132:135], v4, s[44:47], s59 offen offset:0 sc1\n"
"v_mfma_f32_16x16x32_bf16 v[192:195], a[0:3], a[168:171], v[192:195]\n"
"s_waitcnt vmcnt(63)\n"
"s_barrier\n"
"v_xor_b32 v2, 0x10000, v2\n"
"v_xor_b32 v3, 0x10000, v3\n"
"v_mfma_f32_16x16x32_bf16 v[192:195], a[4:7], a[184:187], v[192:195]\n"
"s_add_u32 m0, s75, 0\n"
"s_and_b32 s76, s68, 15\n"
"s_lshl_b32 s76, s76, 17\n"
"s_lshr_b64 s[68:69], s[68:69], 4\n"
"buffer_load_dwordx4 v0, s[40:43], s76 offen offset:0 lds\n"
"v_mfma_f32_16x16x32_bf16 v[224:227], a[0:3], a[172:175], v[224:227]\n"
"ds_read_b64_tr_b16 a[0:1], v2 offset:0\n"
"ds_read_b64_tr_b16 a[2:3], v2 offset:4096\n"
"v_mfma_f32_16x16x32_bf16 v[224:227], a[4:7], a[188:191], v[224:227]\n"
"ds_read_b64_tr_b16 a[4:5], v2 offset:8192\n"
"ds_read_b64_tr_b16 a[6:7], v2 offset:12288\n"
"v_mfma_f32_16x16x32_bf16 v[132:135], a[8:11], a[160:163], v[132:135]\n"
"v_mfma_f32_16x16x32_bf16 v[132:135], a[12:15], a[176:179], v[132:135]\n"
"v_mfma_f32_16x16x32_bf16 v[164:167], a[8:11], a[164:167], v[164:167]\n"
"buffer_load_dwordx4 a[136:139], v4, s[44:47], s60 offen offset:0 sc1\n"
"v_mfma_f32_16x16x32_bf16 v[164:167], a[12:15], a[180:183], v[164:167]\n"
"v_mfma_f32_16x16x32_bf16 v[196:199], a[8:11], a[168:171], v[196:199]\n"
"s_add_u32 m0, s75, 0\n"
"s_add_u32 s76, s76, 3072\n"
"buffer_load_dwordx4 v1, s[40:43], s76 offen offset:1024 lds\n"
"v_mfma_f32_16x16x32_bf16 v[196:199], a[12:15], a[184:187], v[196:199]\n"
"v_mfma_f32_16x16x32_bf16 v[228:231], a[8:11], a[172:175], v[228:231]\n"
"ds_read_b64_tr_b16 a[8:9], v3 offset:0\n"
"ds_read_b64_tr_b16 a[10:11], v3 offset:4096\n"
"v_mfma_f32_16x16x32_bf16 v[228:231], a[12:15], a[188:191], v[228:231]\n"
"ds_read_b64_tr_b16 a[12:13], v3 offset:8192\n"
"ds_read_b64_tr_b16 a[14:15], v3 offset:12288\n"
"v_mfma_f32_16x16x32_bf16 v[136:139], a[16:19], a[160:163], v[136:139]\n"
"v_mfma_f32_16x16x32_bf16 v[136:139], a[20:23], a[176:179], v[136:139]\n"
"buffer_load_dwordx4 a[140:143], v4, s[44:47], s61 offen offset:0 sc1\n"
"v_mfma_f32_16x16x32_bf16 v[168:171], a[16:19], a[164:167], v[168:171]\n"
"v_mfma_f32_16x16x32_bf16 v[168:171], a[20:23], a[180:183], v[168:171]\n"
"s_add_u32 m0, s75, 0\n"
"s_add_u32 s76, s76, 3072\n"
"buffer_load_dwordx4 v0, s[40:43], s76 offen offset:2048 lds\n"
"v_mfma_f32_16x16x32_bf16 v[200:203], a[16:19], a[168:171], v[200:203]\n"
"v_mfma_f32_16x16x32_bf16 v[200:203], a[20:23], a[184:187], v[200:203]\n"
"v_mfma_f32_16x16x32_bf16 v[232:235], a[16:19], a[172:175], v[232:235]\n"
"ds_read_b64_tr_b16 a[16:17], v2 offset:256\n"
"ds_read_b64_tr_b16 a[18:19], v2 offset:4352\n"
"v_mfma_f32_16x16x32_bf16 v[232:235], a[20:23], a[188:191], v[232:235]\n"
"ds_read_b64_tr_b16 a[20:21], v2 offset:8448\n"
"ds_read_b64_tr_b16 a[22:23], v2 offset:12544\n"
"s_waitcnt lgkmcnt(15)\n"
"v_mfma_f32_16x16x32_bf16 v[140:143], a[24:27], a[160:163], v[140:143]\n"
"buffer_load_dwordx4 a[144:147], v4, s[44:47], s58 offen offset:1024 sc1\n"
"v_mfma_f32_16x16x32_bf16 v[140:143], a[28:31], a[176:179], v[140:143]\n"
"v_mfma_f32_16x16x32_bf16 v[172:175], a[24:27], a[164:167], v[172:175]\n"
"s_add_u32 m0, s75, 0\n"
"s_add_u32 s76, s76, 3072\n"
"buffer_load_dwordx4 v1, s[40:43], s76 offen offset:3072 lds\n"
"v_mfma_f32_16x16x32_bf16 v[172:175], a[28:31], a[180:183], v[172:175]\n"
"v_mfma_f32_16x16x32_bf16 v[204:207], a[24:27], a[168:171], v[204:207]\n"
"v_mfma_f32_16x16x32_bf16 v[204:207], a[28:31], a[184:187], v[204:207]\n"
"v_mfma_f32_16x16x32_bf16 v[236:239], a[24:27], a[172:175], v[236:239]\n"
"ds_read_b64_tr_b16 a[24:25], v3 offset:256\n"
"ds_read_b64_tr_b16 a[26:27], v3 offset:4352\n"
"v_mfma_f32_16x16x32_bf16 v[236:239], a[28:31], a[188:191], v[236:239]\n"
"buffer_load_dwordx4 a[148:151], v4, s[44:47], s59 offen offset:1024 sc1\n"
"v_mfma_f32_16x16x32_bf16 v[144:147], a[32:35], a[160:163], v[144:147]\n"
"ds_read_b64_tr_b16 a[28:29], v3 offset:8448\n"
"ds_read_b64_tr_b16 a[30:31], v3 offset:12544\n"
"v_mfma_f32_16x16x32_bf16 v[144:147], a[36:39], a[176:179], v[144:147]\n"
"s_add_u32 m0, s75, 16384\n"
"s_add_u32 s76, s76, 56320\n"
"buffer_load_dwordx4 v0, s[40:43], s76 offen offset:0 lds\n"
"v_mfma_f32_16x16x32_bf16 v[176:179], a[32:35], a[164:167], v[176:179]\n"
"v_mfma_f32_16x16x32_bf16 v[176:179], a[36:39], a[180:183], v[176:179]\n"
"v_mfma_f32_16x16x32_bf16 v[208:211], a[32:35], a[168:171], v[208:211]\n"
"v_mfma_f32_16x16x32_bf16 v[208:211], a[36:39], a[184:187], v[208:211]\n"
"v_mfma_f32_16x16x32_bf16 v[240:243], a[32:35], a[172:175], v[240:243]\n"
"buffer_load_dwordx4 a[152:155], v4, s[44:47], s60 offen offset:1024 sc1\n"
"v_mfma_f32_16x16x32_bf16 v[240:243], a[36:39], a[188:191], v[240:243]\n"
"ds_read_b64_tr_b16 a[32:33], v2 offset:512\n"
"ds_read_b64_tr_b16 a[34:35], v2 offset:4608\n"
"v_mfma_f32_16x16x32_bf16 v[148:151], a[40:43], a[160:163], v[148:151]\n"
"s_add_u32 m0, s75, 16384\n"
"s_add_u32 s76, s76, 3072\n"
"buffer_load_dwordx4 v1, s[40:43], s76 offen offset:1024 lds\n"
"v_mfma_f32_16x16x32_bf16 v[148:151], a[44:47], a[176:179], v[148:151]\n"
"ds_read_b64_tr_b16 a[36:37], v2 offset:8704\n"
"ds_read_b64_tr_b16 a[38:39], v2 offset:12800\n"
"v_mfma_f32_16x16x32_bf16 v[180:183], a[40:43], a[164:167], v[180:183]\n"
"v_mfma_f32_16x16x32_bf16 v[180:183], a[44:47], a[180:183], v[180:183]\n"
"v_mfma_f32_16x16x32_bf16 v[212:215], a[40:43], a[168:171], v[212:215]\n"
"v_mfma_f32_16x16x32_bf16 v[212:215], a[44:47], a[184:187], v[212:215]\n"
"buffer_load_dwordx4 a[156:159], v4, s[44:47], s61 offen offset:1024 sc1\n"
"v_mfma_f32_16x16x32_bf16 v[244:247], a[40:43], a[172:175], v[244:247]\n"
"ds_read_b64_tr_b16 a[40:41], v3 offset:512\n"
"ds_read_b64_tr_b16 a[42:43], v3 offset:4608\n"
"v_mfma_f32_16x16x32_bf16 v[244:247], a[44:47], a[188:191], v[244:247]\n"
"s_add_u32 m0, s75, 16384\n"
"s_add_u32 s76, s76, 3072\n"
"buffer_load_dwordx4 v0, s[40:43], s76 offen offset:2048 lds\n"
"v_mfma_f32_16x16x32_bf16 v[152:155], a[48:51], a[160:163], v[152:155]\n"
"ds_read_b64_tr_b16 a[44:45], v3 offset:8704\n"
"ds_read_b64_tr_b16 a[46:47], v3 offset:12800\n"
"v_mfma_f32_16x16x32_bf16 v[152:155], a[52:55], a[176:179], v[152:155]\n"
"v_mfma_f32_16x16x32_bf16 v[184:187], a[48:51], a[164:167], v[184:187]\n"
"v_mfma_f32_16x16x32_bf16 v[184:187], a[52:55], a[180:183], v[184:187]\n"
"v_mfma_f32_16x16x32_bf16 v[216:219], a[48:51], a[168:171], v[216:219]\n"
"v_mfma_f32_16x16x32_bf16 v[216:219], a[52:55], a[184:187], v[216:219]\n"
"v_mfma_f32_16x16x32_bf16 v[248:251], a[48:51], a[172:175], v[248:251]\n"
"s_add_u32 m0, s75, 16384\n"
"s_add_u32 s76, s76, 3072\n"
"buffer_load_dwordx4 v1, s[40:43], s76 offen offset:3072 lds\n"
"s_xor_b32 s75, s75, 0x8000\n"
"v_mfma_f32_16x16x32_bf16 v[248:251], a[52:55], a[188:191], v[248:251]\n"
"ds_read_b64_tr_b16 a[48:49], v2 offset:768\n"
"ds_read_b64_tr_b16 a[50:51], v2 offset:4864\n"
"s_waitcnt lgkmcnt(15)\n"
"v_mfma_f32_16x16x32_bf16 v[156:159], a[56:59], a[160:163], v[156:159]\n"
"ds_read_b64_tr_b16 a[52:53], v2 offset:8960\n"
"ds_read_b64_tr_b16 a[54:55], v2 offset:13056\n"
"v_mfma_f32_16x16x32_bf16 v[156:159], a[60:63], a[176:179], v[156:159]\n"
"v_mfma_f32_16x16x32_bf16 v[188:191], a[56:59], a[164:167], v[188:191]\n"
"v_mfma_f32_16x16x32_bf16 v[188:191], a[60:63], a[180:183], v[188:191]\n"
"v_mfma_f32_16x16x32_bf16 v[220:223], a[56:59], a[168:171], v[220:223]\n"
"v_mfma_f32_16x16x32_bf16 v[220:223], a[60:63], a[184:187], v[220:223]\n"
"v_mfma_f32_16x16x32_bf16 v[252:255], a[56:59], a[172:175], v[252:255]\n"
"ds_read_b64_tr_b16 a[56:57], v3 offset:768\n"
"ds_read_b64_tr_b16 a[58:59], v3 offset:4864\n"
"v_mfma_f32_16x16x32_bf16 v[252:255], a[60:63], a[188:191], v[252:255]\n"
"v_mfma_f32_16x16x32_bf16 v[128:131], a[0:3], a[192:195], v[128:131]\n"
"ds_read_b64_tr_b16 a[60:61], v3 offset:8960\n"
"ds_read_b64_tr_b16 a[62:63], v3 offset:13056\n"
"v_mfma_f32_16x16x32_bf16 v[128:131], a[4:7], a[208:211], v[128:131]\n"
"buffer_load_dwordx4 a[160:163], v4, s[44:47], s58 offen offset:2048 sc1\n"
"v_mfma_f32_16x16x32_bf16 v[160:163], a[0:3], a[196:199], v[160:163]\n"
"v_mfma_f32_16x16x32_bf16 v[160:163], a[4:7], a[212:215], v[160:163]\n"
"v_mfma_f32_16x16x32_bf16 v[192:195], a[0:3], a[200:203], v[192:195]\n"
"v_mfma_f32_16x16x32_bf16 v[192:195], a[4:7], a[216:219], v[192:195]\n"
"v_mfma_f32_16x16x32_bf16 v[224:227], a[0:3], a[204:207], v[224:227]\n"
"ds_read_b64_tr_b16 a[0:1], v2 offset:16384\n"
"ds_read_b64_tr_b16 a[2:3], v2 offset:20480\n"
"v_mfma_f32_16x16x32_bf16 v[224:227], a[4:7], a[220:223], v[224:227]\n"
"buffer_load_dwordx4 a[164:167], v4, s[44:47], s59 offen offset:2048 sc1\n"
"v_mfma_f32_16x16x32_bf16 v[132:135], a[8:11], a[192:195], v[132:135]\n"
"ds_read_b64_tr_b16 a[4:5], v2 offset:24576\n"
"ds_read_b64_tr_b16 a[6:7], v2 offset:28672\n"
"v_mfma_f32_16x16x32_bf16 v[132:135], a[12:15], a[208:211], v[132:135]\n"
"s_waitcnt vmcnt(19)\n"
"s_mov_b64 exec, 15\n"
"buffer_store_dwordx2 v[18:19], v8, s[36:39], %[mw] offen offset:128 sc1\n"
"s_mov_b64 exec, -1\n"
"v_mfma_f32_16x16x32_bf16 v[164:167], a[8:11], a[196:199], v[164:167]\n"
"v_mfma_f32_16x16x32_bf16 v[164:167], a[12:15], a[212:215], v[164:167]\n"
"v_mfma_f32_16x16x32_bf16 v[196:199], a[8:11], a[200:203], v[196:199]\n"
"v_mfma_f32_16x16x32_bf16 v[196:199], a[12:15], a[216:219], v[196:199]\n"
"buffer_load_dwordx4 a[168:171], v4, s[44:47], s60 offen offset:2048 sc1\n"
"v_mfma_f32_16x16x32_bf16 v[228:231], a[8:11], a[204:207], v[228:231]\n"
"ds_read_b64_tr_b16 a[8:9], v3 offset:16384\n"
"ds_read_b64_tr_b16 a[10:11], v3 offset:20480\n"
"v_mfma_f32_16x16x32_bf16 v[228:231], a[12:15], a[220:223], v[228:231]\n"
"ds_read_b64_tr_b16 a[12:13], v3 offset:24576\n"
"ds_read_b64_tr_b16 a[14:15], v3 offset:28672\n"
"v_mfma_f32_16x16x32_bf16 v[136:139], a[16:19], a[192:195], v[136:139]\n"
"s_waitcnt lgkmcnt(15)\n"
"v_mfma_f32_16x16x32_bf16 v[136:139], a[20:23], a[208:211], v[136:139]\n"
"v_mfma_f32_16x16x32_bf16 v[168:171], a[16:19], a[196:199], v[168:171]\n"
"v_mfma_f32_16x16x32_bf16 v[168:171], a[20:23], a[212:215], v[168:171]\n"
"buffer_load_dwordx4 a[172:175], v4, s[44:47], s61 offen offset:2048 sc1\n"
"v_mfma_f32_16x16x32_bf16 v[200:203], a[16:19], a[200:203], v[200:203]\n"
"v_mfma_f32_16x16x32_bf16 v[200:203], a[20:23], a[216:219], v[200:203]\n"
"v_mfma_f32_16x16x32_bf16 v[232:235], a[16:19], a[204:207], v[232:235]\n"
"ds_read_b64_tr_b16 a[16:17], v2 offset:16640\n"
"ds_read_b64_tr_b16 a[18:19], v2 offset:20736\n"
"v_mfma_f32_16x16x32_bf16 v[232:235], a[20:23], a[220:223], v[232:235]\n"
"ds_read_b64_tr_b16 a[20:21], v2 offset:24832\n"
"ds_read_b64_tr_b16 a[22:23], v2 offset:28928\n"
"v_mfma_f32_16x16x32_bf16 v[140:143], a[24:27], a[192:195], v[140:143]\n"
"v_mfma_f32_16x16x32_bf16 v[140:143], a[28:31], a[208:211], v[140:143]\n"
"buffer_load_dwordx4 a[176:179], v4, s[44:47], s58 offen offset:3072 sc1\n"
"v_mfma_f32_16x16x32_bf16 v[172:175], a[24:27], a[196:199], v[172:175]\n"
"v_mfma_f32_16x16x32_bf16 v[172:175], a[28:31], a[212:215], v[172:175]\n"
"v_mfma_f32_16x16x32_bf16 v[204:207], a[24:27], a[200:203], v[204:207]\n"
"v_mfma_f32_16x16x32_bf16 v[204:207], a[28:31], a[216:219], v[204:207]\n"
"v_mfma_f32_16x16x32_bf16 v[236:239], a[24:27], a[204:207], v[236:239]\n"
"ds_read_b64_tr_b16 a[24:25], v3 offset:16640\n"
"ds_read_b64_tr_b16 a[26:27], v3 offset:20736\n"
"v_mfma_f32_16x16x32_bf16 v[236:239], a[28:31], a[220:223], v[236:239]\n"
"buffer_load_dwordx4 a[180:183], v4, s[44:47], s59 offen offset:3072 sc1\n"
"v_mfma_f32_16x16x32_bf16 v[144:147], a[32:35], a[192:195], v[144:147]\n"
"ds_read_b64_tr_b16 a[28:29], v3 offset:24832\n"
"ds_read_b64_tr_b16 a[30:31], v3 offset:28928\n"
"v_mfma_f32_16x16x32_bf16 v[144:147], a[36:39], a[208:211], v[144:147]\n"
"v_mfma_f32_16x16x32_bf16 v[176:179], a[32:35], a[196:199], v[176:179]\n"
"v_mfma_f32_16x16x32_bf16 v[176:179], a[36:39], a[212:215], v[176:179]\n"
"v_mfma_f32_16x16x32_bf16 v[208:211], a[32:35], a[200:203], v[208:211]\n"
"v_mfma_f32_16x16x32_bf16 v[208:211], a[36:39], a[216:219], v[208:211]\n"
"buffer_load_dwordx4 a[184:187], v4, s[44:47], s60 offen offset:3072 sc1\n"
"v_mfma_f32_16x16x32_bf16 v[240:243], a[32:35], a[204:207], v[240:243]\n"
"ds_read_b64_tr_b16 a[32:33], v2 offset:16896\n"
"ds_read_b64_tr_b16 a[34:35], v2 offset:20992\n"
"v_mfma_f32_16x16x32_bf16 v[240:243], a[36:39], a[220:223], v[240:243]\n"
"ds_read_b64_tr_b16 a[36:37], v2 offset:25088\n"
"ds_read_b64_tr_b16 a[38:39], v2 offset:29184\n"
"v_mfma_f32_16x16x32_bf16 v[148:151], a[40:43], a[192:195], v[148:151]\n"
"v_mfma_f32_16x16x32_bf16 v[148:151], a[44:47], a[208:211], v[148:151]\n"
"v_mfma_f32_16x16x32_bf16 v[180:183], a[40:43], a[196:199], v[180:183]\n"
"v_mfma_f32_16x16x32_bf16 v[180:183], a[44:47], a[212:215], v[180:183]\n"
"buffer_load_dwordx4 a[188:191], v4, s[44:47], s61 offen offset:3072 sc1\n"
"v_mfma_f32_16x16x32_bf16 v[212:215], a[40:43], a[200:203], v[212:215]\n"
"v_mfma_f32_16x16x32_bf16 v[212:215], a[44:47], a[216:219], v[212:215]\n"
"v_mfma_f32_16x16x32_bf16 v[244:247], a[40:43], a[204:207], v[244:247]\n"
"ds_read_b64_tr_b16 a[40:41], v3 offset:16896\n"
"ds_read_b64_tr_b16 a[42:43], v3 offset:20992\n"
"v_mfma_f32_16x16x32_bf16 v[244:247], a[44:47], a[220:223], v[244:247]\n"
"ds_read_b64_tr_b16 a[44:45], v3 offset:25088\n"
"ds_read_b64_tr_b16 a[46:47], v3 offset:29184\n"
"s_waitcnt lgkmcnt(15)\n"
"v_mfma_f32_16x16x32_bf16 v[152:155], a[48:51], a[192:195], v[152:155]\n"
"v_mfma_f32_16x16x32_bf16 v[152:155], a[52:55], a[208:211], v[152:155]\n"
"s_and_b32 s63, s65, 15\n"
"s_lshl_b32 s58, s63, 12\n"
"s_lshr_b32 s65, s65, 4\n"
"s_add_u32 s59, s58, 131072\n"
"s_add_u32 s60, s58, 262144\n"
"s_add_u32 s61, s58, 393216\n"
"v_mfma_f32_16x16x32_bf16 v[184:187], a[48:51], a[196:199], v[184:187]\n"
"v_mfma_f32_16x16x32_bf16 v[184:187], a[52:55], a[212:215], v[184:187]\n"
"v_mfma_f32_16x16x32_bf16 v[216:219], a[48:51], a[200:203], v[216:219]\n"
"v_mfma_f32_16x16x32_bf16 v[216:219], a[52:55], a[216:219], v[216:219]\n"
"v_mfma_f32_16x16x32_bf16 v[248:251], a[48:51], a[204:207], v[248:251]\n"
"ds_read_b64_tr_b16 a[48:49], v2 offset:17152\n"
"ds_read_b64_tr_b16 a[50:51], v2 offset:21248\n"
"v_mfma_f32_16x16x32_bf16 v[248:251], a[52:55], a[220:223], v[248:251]\n"
"ds_read_b64_tr_b16 a[52:53], v2 offset:25344\n"
"ds_read_b64_tr_b16 a[54:55], v2 offset:29440\n"
"v_mfma_f32_16x16x32_bf16 v[156:159], a[56:59], a[192:195], v[156:159]\n"
"v_mfma_f32_16x16x32_bf16 v[156:159], a[60:63], a[208:211], v[156:159]\n"
"v_mfma_f32_16x16x32_bf16 v[188:191], a[56:59], a[196:199], v[188:191]\n"
"v_mfma_f32_16x16x32_bf16 v[188:191], a[60:63], a[212:215], v[188:191]\n"
"v_mfma_f32_16x16x32_bf16 v[220:223], a[56:59], a[200:203], v[220:223]\n"
"buffer_load_dwordx4 a[192:195], v4, s[44:47], s58 offen offset:0 sc1\n"
"v_mfma_f32_16x16x32_bf16 v[220:223], a[60:63], a[216:219], v[220:223]\n"
"v_mfma_f32_16x16x32_bf16 v[252:255], a[56:59], a[204:207], v[252:255]\n"
"ds_read_b64_tr_b16 a[56:57], v3 offset:17152\n"
"ds_read_b64_tr_b16 a[58:59], v3 offset:21248\n"
"v_mfma_f32_16x16x32_bf16 v[252:255], a[60:63], a[220:223], v[252:255]\n"
"ds_read_b64_tr_b16 a[60:61], v3 offset:25344\n"
"ds_read_b64_tr_b16 a[62:63], v3 offset:29440\n"
"v_mfma_f32_16x16x32_bf16 v[128:131], a[0:3], a[224:227], v[128:131]\n"
"v_mfma_f32_16x16x32_bf16 v[128:131], a[4:7], a[240:243], v[128:131]\n"
"v_mfma_f32_16x16x32_bf16 v[160:163], a[0:3], a[228:231], v[160:163]\n"
"v_mfma_f32_16x16x32_bf16 v[160:163], a[4:7], a[244:247], v[160:163]\n"
"buffer_load_dwordx4 a[196:199], v4, s[44:47], s59 offen offset:0 sc1\n"
"v_mfma_f32_16x16x32_bf16 v[192:195], a[0:3], a[232:235], v[192:195]\n"
"s_waitcnt vmcnt(63)\n"
"s_barrier\n"
"v_xor_b32 v2, 0x8000, v2\n"
"v_xor_b32 v3, 0x8000, v3\n"
"v_mfma_f32_16x16x32_bf16 v[192:195], a[4:7], a[248:251], v[192:195]\n"
"s_add_u32 m0, s75, 0\n"
"s_and_b32 s76, s68, 15\n"
"s_lshl_b32 s76, s76, 17\n"
"s_lshr_b64 s[68:69], s[68:69], 4\n"
"buffer_load_dwordx4 v0, s[40:43], s76 offen offset:0 lds\n"
"v_mfma_f32_16x16x32_bf16 v[224:227], a[0:3], a[236:239], v[224:227]\n"
"ds_read_b64_tr_b16 a[0:1], v2 offset:0\n"
"ds_read_b64_tr_b16 a[2:3], v2 offset:4096\n"
"v_mfma_f32_16x16x32_bf16 v[224:227], a[4:7], a[252:255], v[224:227]\n"
"ds_read_b64_tr_b16 a[4:5], v2 offset:8192\n"
"ds_read_b64_tr_b16 a[6:7], v2 offset:12288\n"
"v_mfma_f32_16x16x32_bf16 v[132:135], a[8:11], a[224:227], v[132:135]\n"
"v_mfma_f32_16x16x32_bf16 v[132:135], a[12:15], a[240:243], v[132:135]\n"
"v_mfma_f32_16x16x32_bf16 v[164:167], a[8:11], a[228:231], v[164:167]\n"
"buffer_load_dwordx4 a[200:203], v4, s[44:47], s60 offen offset:0 sc1\n"
"v_mfma_f32_16x16x32_bf16 v[164:167], a[12:15], a[244:247], v[164:167]\n"
"v_mfma_f32_16x16x32_bf16 v[196:199], a[8:11], a[232:235], v[196:199]\n"
"s_add_u32 m0, s75, 0\n"
"s_add_u32 s76, s76, 3072\n"
"buffer_load_dwordx4 v1, s[40:43], s76 offen offset:1024 lds\n"
"v_mfma_f32_16x16x32_bf16 v[196:199], a[12:15], a[248:251], v[196:199]\n"
"v_mfma_f32_16x16x32_bf16 v[228:231], a[8:11], a[236:239], v[228:231]\n"
"ds_read_b64_tr_b16 a[8:9], v3 offset:0\n"
"ds_read_b64_tr_b16 a[10:11], v3 offset:4096\n"
"v_mfma_f32_16x16x32_bf16 v[228:231], a[12:15], a[252:255], v[228:231]\n"
"ds_read_b64_tr_b16 a[12:13], v3 offset:8192\n"
"ds_read_b64_tr_b16 a[14:15], v3 offset:12288\n"
"s_waitcnt lgkmcnt(15)\n"
"v_mfma_f32_16x16x32_bf16 v[136:139], a[16:19], a[224:227], v[136:139]\n"
"v_mfma_f32_16x16x32_bf16 v[136:139], a[20:23], a[240:243], v[136:139]\n"
"buffer_load_dwordx4 a[204:207], v4, s[44:47], s61 offen offset:0 sc1\n"
"v_mfma_f32_16x16x32_bf16 v[168:171], a[16:19], a[228:231], v[168:171]\n"
"v_mfma_f32_16x16x32_bf16 v[168:171], a[20:23], a[244:247], v[168:171]\n"
"s_add_u32 m0, s75, 0\n"
"s_add_u32 s76, s76, 3072\n"
"buffer_load_dwordx4 v0, s[40:43], s76 offen offset:2048 lds\n"
"v_mfma_f32_16x16x32_bf16 v[200:203], a[16:19], a[232:235], v[200:203]\n"
"v_mfma_f32_16x16x32_bf16 v[200:203], a[20:23], a[248:251], v[200:203]\n"
"v_mfma_f32_16x16x32_bf16 v[232:235], a[16:19], a[236:239], v[232:235]\n"
"ds_read_b64_tr_b16 a[16:17], v2 offset:256\n"
"ds_read_b64_tr_b16 a[18:19], v2 offset:4352\n"
"v_mfma_f32_16x16x32_bf16 v[232:235], a[20:23], a[252:255], v[232:235]\n"
"ds_read_b64_tr_b16 a[20:21], v2 offset:8448\n"
"ds_read_b64_tr_b16 a[22:23], v2 offset:12544\n"
"v_mfma_f32_16x16x32_bf16 v[140:143], a[24:27], a[224:227], v[140:143]\n"
"buffer_load_dwordx4 a[208:211], v4, s[44:47], s58 offen offset:1024 sc1\n"
"v_mfma_f32_16x16x32_bf16 v[140:143], a[28:31], a[240:243], v[140:143]\n"
"v_mfma_f32_16x16x32_bf16 v[172:175], a[24:27], a[228:231], v[172:175]\n"
"s_add_u32 m0, s75, 0\n"
"s_add_u32 s76, s76, 3072\n"
"buffer_load_dwordx4 v1, s[40:43], s76 offen offset:3072 lds\n"
"v_mfma_f32_16x16x32_bf16 v[172:175], a[28:31], a[244:247], v[172:175]\n"
"v_mfma_f32_16x16x32_bf16 v[204:207], a[24:27], a[232:235], v[204:207]\n"
"v_mfma_f32_16x16x32_bf16 v[204:207], a[28:31], a[248:251], v[204:207]\n"
"v_mfma_f32_16x16x32_bf16 v[236:239], a[24:27], a[236:239], v[236:239]\n"
"ds_read_b64_tr_b16 a[24:25], v3 offset:256\n"
"ds_read_b64_tr_b16 a[26:27], v3 offset:4352\n"
"v_mfma_f32_16x16x32_bf16 v[236:239], a[28:31], a[252:255], v[236:239]\n"
"buffer_load_dwordx4 a[212:215], v4, s[44:47], s59 offen offset:1024 sc1\n"
"v_mfma_f32_16x16x32_bf16 v[144:147], a[32:35], a[224:227], v[144:147]\n"
"ds_read_b64_tr_b16 a[28:29], v3 offset:8448\n"
"ds_read_b64_tr_b16 a[30:31], v3 offset:12544\n"
"v_mfma_f32_16x16x32_bf16 v[144:147], a[36:39], a[240:243], v[144:147]\n"
"s_add_u32 m0, s75, 16384\n"
"s_add_u32 s76, s76, 56320\n"
"buffer_load_dwordx4 v0, s[40:43], s76 offen offset:0 lds\n"
"v_mfma_f32_16x16x32_bf16 v[176:179], a[32:35], a[228:231], v[176:179]\n"
"v_mfma_f32_16x16x32_bf16 v[176:179], a[36:39], a[244:247], v[176:179]\n"
"v_mfma_f32_16x16x32_bf16 v[208:211], a[32:35], a[232:235], v[208:211]\n"
"v_mfma_f32_16x16x32_bf16 v[208:211], a[36:39], a[248:251], v[208:211]\n"
"v_mfma_f32_16x16x32_bf16 v[240:243], a[32:35], a[236:239], v[240:243]\n"
"buffer_load_dwordx4 a[216:219], v4, s[44:47], s60 offen offset:1024 sc1\n"
"v_mfma_f32_16x16x32_bf16 v[240:243], a[36:39], a[252:255], v[240:243]\n"
"ds_read_b64_tr_b16 a[32:33], v2 offset:512\n"
"ds_read_b64_tr_b16 a[34:35], v2 offset:4608\n"
"v_mfma_f32_16x16x32_bf16 v[148:151], a[40:43], a[224:227], v[148:151]\n"
"s_add_u32 m0, s75, 16384\n"
"s_add_u32 s76, s76, 3072\n"
"buffer_load_dwordx4 v1, s[40:43], s76 offen offset:1024 lds\n"
"v_mfma_f32_16x16x32_bf16 v[148:151], a[44:47], a[240:243], v[148:151]\n"
"ds_read_b64_tr_b16 a[36:37], v2 offset:8704\n"
"ds_read_b64_tr_b16 a[38:39], v2 offset:12800\n"
"v_mfma_f32_16x16x32_bf16 v[180:183], a[40:43], a[228:231], v[180:183]\n"
"v_mfma_f32_16x16x32_bf16 v[180:183], a[44:47], a[244:247], v[180:183]\n"
"v_mfma_f32_16x16x32_bf16 v[212:215], a[40:43], a[232:235], v[212:215]\n"
"v_mfma_f32_16x16x32_bf16 v[212:215], a[44:47], a[248:251], v[212:215]\n"
"buffer_load_dwordx4 a[220:223], v4, s[44:47], s61 offen offset:1024 sc1\n"
"v_mfma_f32_16x16x32_bf16 v[244:247], a[40:43], a[236:239], v[244:247]\n"
"ds_read_b64_tr_b16 a[40:41], v3 offset:512\n"
"ds_read_b64_tr_b16 a[42:43], v3 offset:4608\n"
"v_mfma_f32_16x16x32_bf16 v[244:247], a[44:47], a[252:255], v[244:247]\n"
"s_add_u32 m0, s75, 16384\n"
"s_add_u32 s76, s76, 3072\n"
"buffer_load_dwordx4 v0, s[40:43], s76 offen offset:2048 lds\n"
"s_waitcnt lgkmcnt(15)\n"
"v_mfma_f32_16x16x32_bf16 v[152:155], a[48:51], a[224:227], v[152:155]\n"
"ds_read_b64_tr_b16 a[44:45], v3 offset:8704\n"
"ds_read_b64_tr_b16 a[46:47], v3 offset:12800\n"
"v_mfma_f32_16x16x32_bf16 v[152:155], a[52:55], a[240:243], v[152:155]\n"
"v_mfma_f32_16x16x32_bf16 v[184:187], a[48:51], a[228:231], v[184:187]\n"
"v_mfma_f32_16x16x32_bf16 v[184:187], a[52:55], a[244:247], v[184:187]\n"
"v_mfma_f32_16x16x32_bf16 v[216:219], a[48:51], a[232:235], v[216:219]\n"
"v_mfma_f32_16x16x32_bf16 v[216:219], a[52:55], a[248:251], v[216:219]\n"
"v_mfma_f32_16x16x32_bf16 v[248:251], a[48:51], a[236:239], v[248:251]\n"
"s_add_u32 m0, s75, 16384\n"
"s_add_u32 s76, s76, 3072\n"
"buffer_load_dwordx4 v1, s[40:43], s76 offen offset:3072 lds\n"
"s_xor_b32 s75, s75, 0x10000\n"
"v_mfma_f32_16x16x32_bf16 v[248:251], a[52:55], a[252:255], v[248:251]\n"
"ds_read_b64_tr_b16 a[48:49], v2 offset:768\n"
"ds_read_b64_tr_b16 a[50:51], v2 offset:4864\n"
"v_mfma_f32_16x16x32_bf16 v[156:159], a[56:59], a[224:227], v[156:159]\n"
"ds_read_b64_tr_b16 a[52:53], v2 offset:8960\n"
"ds_read_b64_tr_b16 a[54:55], v2 offset:13056\n"
"v_mfma_f32_16x16x32_bf16 v[156:159], a[60:63], a[240:243], v[156:159]\n"
"v_mfma_f32_16x16x32_bf16 v[188:191], a[56:59], a[228:231], v[188:191]\n"
"v_mfma_f32_16x16x32_bf16 v[188:191], a[60:63], a[244:247], v[188:191]\n"
"v_mfma_f32_16x16x32_bf16 v[220:223], a[56:59], a[232:235], v[220:223]\n"
"v_mfma_f32_16x16x32_bf16 v[220:223], a[60:63], a[248:251], v[220:223]\n"
"v_mfma_f32_16x16x32_bf16 v[252:255], a[56:59], a[236:239], v[252:255]\n"
"ds_read_b64_tr_b16 a[56:57], v3 offset:768\n"
"ds_read_b64_tr_b16 a[58:59], v3 offset:4864\n"
"v_mfma_f32_16x16x32_bf16 v[252:255], a[60:63], a[252:255], v[252:255]\n"
".LLa_%=:\n"
"s_waitcnt vmcnt(40)\n"
"v_mfma_f32_16x16x32_bf16 v[128:131], a[0:3], a[128:131], v[128:131]\n"
"ds_read_b64_tr_b16 a[60:61], v3 offset:8960\n"
"ds_read_b64_tr_b16 a[62:63], v3 offset:13056\n"
"s_waitcnt vmcnt(32)\n"
"v_mfma_f32_16x16x32_bf16 v[128:131], a[4:7], a[144:147], v[128:131]\n"
"buffer_load_dwordx4 a[224:227], v4, s[44:47], s58 offen offset:2048 sc1\n"
"v_mfma_f32_16x16x32_bf16 v[160:163], a[0:3], a[132:135], v[160:163]\n"
"s_waitcnt vmcnt(31)\n"
"v_mfma_f32_16x16x32_bf16 v[160:163], a[4:7], a[148:151], v[160:163]\n"
"v_mfma_f32_16x16x32_bf16 v[192:195], a[0:3], a[136:139], v[192:195]\n"
"s_waitcnt vmcnt(29)\n"
"v_mfma_f32_16x16x32_bf16 v[192:195], a[4:7], a[152:155], v[192:195]\n"
"v_mfma_f32_16x16x32_bf16 v[224:227], a[0:3], a[140:143], v[224:227]\n"
"ds_read_b64_tr_b16 a[0:1], v2 offset:16384\n"
"ds_read_b64_tr_b16 a[2:3], v2 offset:20480\n"
"s_waitcnt vmcnt(27)\n"
"v_mfma_f32_16x16x32_bf16 v[224:227], a[4:7], a[156:159], v[224:227]\n"
"buffer_load_dwordx4 a[228:231], v4, s[44:47], s59 offen offset:2048 sc1\n"
"v_mfma_f32_16x16x32_bf16 v[132:135], a[8:11], a[128:131], v[132:135]\n"
"ds_read_b64_tr_b16 a[4:5], v2 offset:24576\n"
"ds_read_b64_tr_b16 a[6:7], v2 offset:28672\n"
"s_waitcnt lgkmcnt(15)\n"
"v_mfma_f32_16x16x32_bf16 v[132:135], a[12:15], a[144:147], v[132:135]\n"
"v_mfma_f32_16x16x32_bf16 v[164:167], a[8:11], a[132:135], v[164:167]\n"
"v_mfma_f32_16x16x32_bf16 v[164:167], a[12:15], a[148:151], v[164:167]\n"
"v_mfma_f32_16x16x32_bf16 v[196:199], a[8:11], a[136:139], v[196:199]\n"
"v_mfma_f32_16x16x32_bf16 v[196:199], a[12:15], a[152:155], v[196:199]\n"
"buffer_load_dwordx4 a[232:235], v4, s[44:47], s60 offen offset:2048 sc1\n"
"v_mfma_f32_16x16x32_bf16 v[228:231], a[8:11], a[140:143], v[228:231]\n"
"ds_read_b64_tr_b16 a[8:9], v3 offset:16384\n"
"ds_read_b64_tr_b16 a[10:11], v3 offset:20480\n"
"v_mfma_f32_16x16x32_bf16 v[228:231], a[12:15], a[156:159], v[228:231]\n"
"ds_read_b64_tr_b16 a[12:13], v3 offset:24576\n"
"ds_read_b64_tr_b16 a[14:15], v3 offset:28672\n"
"v_mfma_f32_16x16x32_bf16 v[136:139], a[16:19], a[128:131], v[136:139]\n"
"v_mfma_f32_16x16x32_bf16 v[136:139], a[20:23], a[144:147], v[136:139]\n"
"v_mfma_f32_16x16x32_bf16 v[168:171], a[16:19], a[132:135], v[168:171]\n"
"v_mfma_f32_16x16x32_bf16 v[168:171], a[20:23], a[148:151], v[168:171]\n"
"buffer_load_dwordx4 a[236:239], v4, s[44:47], s61 offen offset:2048 sc1\n"
"v_mfma_f32_16x16x32_bf16 v[200:203], a[16:19], a[136:139], v[200:203]\n"
"v_mfma_f32_16x16x32_bf16 v[200:203], a[20:23], a[152:155], v[200:203]\n"
"v_mfma_f32_16x16x32_bf16 v[232:235], a[16:19], a[140:143], v[232:235]\n"
"ds_read_b64_tr_b16 a[16:17], v2 offset:16640\n"
"ds_read_b64_tr_b16 a[18:19], v2 offset:20736\n"
"v_mfma_f32_16x16x32_bf16 v[232:235], a[20:23], a[156:159], v[232:235]\n"
"ds_read_b64_tr_b16 a[20:21], v2 offset:24832\n"
"ds_read_b64_tr_b16 a[22:23], v2 offset:28928\n"
"v_mfma_f32_16x16x32_bf16 v[140:143], a[24:27], a[128:131], v[140:143]\n"
"v_mfma_f32_16x16x32_bf16 v[140:143], a[28:31], a[144:147], v[140:143]\n"
"buffer_load_dwordx4 a[240:243], v4, s[44:47], s58 offen offset:3072 sc1\n"
"v_mfma_f32_16x16x32_bf16 v[172:175], a[24:27], a[132:135], v[172:175]\n"
"v_mfma_f32_16x16x32_bf16 v[172:175], a[28:31], a[148:151], v[172:175]\n"
"v_mfma_f32_16x16x32_bf16 v[204:207], a[24:27], a[136:139], v[204:207]\n"
"v_mfma_f32_16x16x32_bf16 v[204:207], a[28:31], a[152:155], v[204:207]\n"
"v_mfma_f32_16x16x32_bf16 v[236:239], a[24:27], a[140:143], v[236:239]\n"
"ds_read_b64_tr_b16 a[24:25], v3 offset:16640\n"
"ds_read_b64_tr_b16 a[26:27], v3 offset:20736\n"
"v_mfma_f32_16x16x32_bf16 v[236:239], a[28:31], a[156:159], v[236:239]\n"
"buffer_load_dwordx4 a[244:247], v4, s[44:47], s59 offen offset:3072 sc1\n"
"v_mfma_f32_16x16x32_bf16 v[144:147], a[32:35], a[128:131], v[144:147]\n"
"ds_read_b64_tr_b16 a[28:29], v3 offset:24832\n"
"ds_read_b64_tr_b16 a[30:31], v3 offset:28928\n"
"v_mfma_f32_16x16x32_bf16 v[144:147], a[36:39], a[144:147], v[144:147]\n"
"v_mfma_f32_16x16x32_bf16 v[176:179], a[32:35], a[132:135], v[176:179]\n"
"v_mfma_f32_16x16x32_bf16 v[176:179], a[36:39], a[148:151], v[176:179]\n"
"v_mfma_f32_16x16x32_bf16 v[208:211], a[32:35], a[136:139], v[208:211]\n"
"v_mfma_f32_16x16x32_bf16 v[208:211], a[36:39], a[152:155], v[208:211]\n"
"buffer_load_dwordx4 a[248:251], v4, s[44:47], s60 offen offset:3072 sc1\n"
"v_mfma_f32_16x16x32_bf16 v[240:243], a[32:35], a[140:143], v[240:243]\n"
"ds_read_b64_tr_b16 a[32:33], v2 offset:16896\n"
"ds_read_b64_tr_b16 a[34:35], v2 offset:20992\n"
"v_mfma_f32_16x16x32_bf16 v[240:243], a[36:39], a[156:159], v[240:243]\n"
"ds_read_b64_tr_b16 a[36:37], v2 offset:25088\n"
"ds_read_b64_tr_b16 a[38:39], v2 offset:29184\n"
"s_waitcnt lgkmcnt(15)\n"
"v_mfma_f32_16x16x32_bf16 v[148:151], a[40:43], a[128:131], v[148:151]\n"
"v_mfma_f32_16x16x32_bf16 v[148:151], a[44:47], a[144:147], v[148:151]\n"
"v_mfma_f32_16x16x32_bf16 v[180:183], a[40:43], a[132:135], v[180:183]\n"
"v_mfma_f32_16x16x32_bf16 v[180:183], a[44:47], a[148:151], v[180:183]\n"
"buffer_load_dwordx4 a[252:255], v4, s[44:47], s61 offen offset:3072 sc1\n"
"v_mfma_f32_16x16x32_bf16 v[212:215], a[40:43], a[136:139], v[212:215]\n"
"v_mfma_f32_16x16x32_bf16 v[212:215], a[44:47], a[152:155], v[212:215]\n"
"v_mfma_f32_16x16x32_bf16 v[244:247], a[40:43], a[140:143], v[244:247]\n"
"ds_read_b64_tr_b16 a[40:41], v3 offset:16896\n"
"ds_read_b64_tr_b16 a[42:43], v3 offset:20992\n"
"v_mfma_f32_16x16x32_bf16 v[244:247], a[44:47], a[156:159], v[244:247]\n"
"ds_read_b64_tr_b16 a[44:45], v3 offset:25088\n"
"ds_read_b64_tr_b16 a[46:47], v3 offset:29184\n"
"v_mfma_f32_16x16x32_bf16 v[152:155], a[48:51], a[128:131], v[152:155]\n"
"v_mfma_f32_16x16x32_bf16 v[152:155], a[52:55], a[144:147], v[152:155]\n"
"s_and_b32 s63, s65, 15\n"
"s_lshl_b32 s58, s63, 12\n"
"s_lshr_b32 s65, s65, 4\n"
"s_add_u32 s59, s58, 131072\n"
"s_add_u32 s60, s58, 262144\n"
"s_add_u32 s61, s58, 393216\n"
"s_cmp_eq_u32 s62, 1\n"
"s_cselect_b32 s46, 0, s46\n"
"v_mfma_f32_16x16x32_bf16 v[184:187], a[48:51], a[132:135], v[184:187]\n"
"v_mfma_f32_16x16x32_bf16 v[184:187], a[52:55], a[148:151], v[184:187]\n"
"v_mfma_f32_16x16x32_bf16 v[216:219], a[48:51], a[136:139], v[216:219]\n"
"v_mfma_f32_16x16x32_bf16 v[216:219], a[52:55], a[152:155], v[216:219]\n"
"v_mfma_f32_16x16x32_bf16 v[248:251], a[48:51], a[140:143], v[248:251]\n"
"ds_read_b64_tr_b16 a[48:49], v2 offset:17152\n"
"ds_read_b64_tr_b16 a[50:51], v2 offset:21248\n"
"v_mfma_f32_16x16x32_bf16 v[248:251], a[52:55], a[156:159], v[248:251]\n"
"ds_read_b64_tr_b16 a[52:53], v2 offset:25344\n"
"ds_read_b64_tr_b16 a[54:55], v2 offset:29440\n"
"v_mfma_f32_16x16x32_bf16 v[156:159], a[56:59], a[128:131], v[156:159]\n"
"v_mfma_f32_16x16x32_bf16 v[156:159], a[60:63], a[144:147], v[156:159]\n"
"v_mfma_f32_16x16x32_bf16 v[188:191], a[56:59], a[132:135], v[188:191]\n"
"v_mfma_f32_16x16x32_bf16 v[188:191], a[60:63], a[148:151], v[188:191]\n"
"v_mfma_f32_16x16x32_bf16 v[220:223], a[56:59], a[136:139], v[220:223]\n"
"buffer_load_dwordx4 a[128:131], v4, s[44:47], s58 offen offset:0 sc1\n"
"v_mfma_f32_16x16x32_bf16 v[220:223], a[60:63], a[152:155], v[220:223]\n"
"v_mfma_f32_16x16x32_bf16 v[252:255], a[56:59], a[140:143], v[252:255]\n"
"ds_read_b64_tr_b16 a[56:57], v3 offset:17152\n"
"ds_read_b64_tr_b16 a[58:59], v3 offset:21248\n"
"v_mfma_f32_16x16x32_bf16 v[252:255], a[60:63], a[156:159], v[252:255]\n"
"ds_read_b64_tr_b16 a[60:61], v3 offset:25344\n"
"ds_read_b64_tr_b16 a[62:63], v3 offset:29440\n"
"s_waitcnt vmcnt(32)\n"
"v_mfma_f32_16x16x32_bf16 v[128:131], a[0:3], a[160:163], v[128:131]\n"
"buffer_load_dwordx2 v[24:25], v13, s[36:39], %[ma] offen offset:128 sc1\n"
"buffer_load_dword v21, v13, s[36:39], %[xa] offen sc1\n"
"s_waitcnt vmcnt(30)\n"
"v_mfma_f32_16x16x32_bf16 v[128:131], a[4:7], a[176:179], v[128:131]\n"
"v_mfma_f32_16x16x32_bf16 v[160:163], a[0:3], a[164:167], v[160:163]\n"
"s_waitcnt vmcnt(29)\n"
"v_mfma_f32_16x16x32_bf16 v[160:163], a[4:7], a[180:183], v[160:163]\n"
"buffer_load_dwordx4 a[132:135], v4, s[44:47], s59 offen offset:0 sc1\n"
"v_mfma_f32_16x16x32_bf16 v[192:195], a[0:3], a[168:171], v[192:195]\n"
"s_waitcnt vmcnt(63)\n"
"s_barrier\n"
"v_xor_b32 v2, 0x10000, v2\n"
"v_xor_b32 v3, 0x10000, v3\n"
"s_waitcnt vmcnt(29)\n"
"v_mfma_f32_16x16x32_bf16 v[192:195], a[4:7], a[184:187], v[192:195]\n"
"s_add_u32 m0, s75, 0\n"
"s_and_b32 s76, s68, 15\n"
"s_lshl_b32 s76, s76, 17\n"
"s_lshr_b64 s[68:69], s[68:69], 4\n"
"buffer_load_dwordx4 v0, s[40:43], s76 offen offset:0 lds\n"
"v_mfma_f32_16x16x32_bf16 v[224:227], a[0:3], a[172:175], v[224:227]\n"
"ds_read_b64_tr_b16 a[0:1], v2 offset:0\n"
"ds_read_b64_tr_b16 a[2:3], v2 offset:4096\n"
"s_waitcnt vmcnt(29)\n"
"v_mfma_f32_16x16x32_bf16 v[224:227], a[4:7], a[188:191], v[224:227]\n"
"ds_read_b64_tr_b16 a[4:5], v2 offset:8192\n"
"ds_read_b64_tr_b16 a[6:7], v2 offset:12288\n"
"s_waitcnt lgkmcnt(15)\n"
"v_mfma_f32_16x16x32_bf16 v[132:135], a[8:11], a[160:163], v[132:135]\n"
"v_mfma_f32_16x16x32_bf16 v[132:135], a[12:15], a[176:179], v[132:135]\n"
"v_mfma_f32_16x16x32_bf16 v[164:167], a[8:11], a[164:167], v[164:167]\n"
"buffer_load_dwordx4 a[136:139], v4, s[44:47], s60 offen offset:0 sc1\n"
"v_mfma_f32_16x16x32_bf16 v[164:167], a[12:15], a[180:183], v[164:167]\n"
"v_mfma_f32_16x16x32_bf16 v[196:199], a[8:11], a[168:171], v[196:199]\n"
"s_add_u32 m0, s75, 0\n"
"s_add_u32 s76, s76, 3072\n"
"buffer_load_dwordx4 v1, s[40:43], s76 offen offset:1024 lds\n"
"v_mfma_f32_16x16x32_bf16 v[196:199], a[12:15], a[184:187], v[196:199]\n"
"v_mfma_f32_16x16x32_bf16 v[228:231], a[8:11], a[172:175], v[228:231]\n"
"ds_read_b64_tr_b16 a[8:9], v3 offset:0\n"
"ds_read_b64_tr_b16 a[10:11], v3 offset:4096\n"
"v_mfma_f32_16x16x32_bf16 v[228:231], a[12:15], a[188:191], v[228:231]\n"
"ds_read_b64_tr_b16 a[12:13], v3 offset:8192\n"
"ds_read_b64_tr_b16 a[14:15], v3 offset:12288\n"
"v_mfma_f32_16x16x32_bf16 v[136:139], a[16:19], a[160:163], v[136:139]\n"
"v_mfma_f32_16x16x32_bf16 v[136:139], a[20:23], a[176:179], v[136:139]\n"
"buffer_load_dwordx4 a[140:143], v4, s[44:47], s61 offen offset:0 sc1\n"
"v_mfma_f32_16x16x32_bf16 v[168:171], a[16:19], a[164:167], v[168:171]\n"
"v_mfma_f32_16x16x32_bf16 v[168:171], a[20:23], a[180:183], v[168:171]\n"
"s_add_u32 m0, s75, 0\n"
"s_add_u32 s76, s76, 3072\n"
"buffer_load_dwordx4 v0, s[40:43], s76 offen offset:2048 lds\n"
"v_mfma_f32_16x16x32_bf16 v[200:203], a[16:19], a[168:171], v[200:203]\n"
"v_mfma_f32_16x16x32_bf16 v[200:203], a[20:23], a[184:187], v[200:203]\n"
"v_mfma_f32_16x16x32_bf16 v[232:235], a[16:19], a[172:175], v[232:235]\n"
"ds_read_b64_tr_b16 a[16:17], v2 offset:256\n"
"ds_read_b64_tr_b16 a[18:19], v2 offset:4352\n"
"v_mfma_f32_16x16x32_bf16 v[232:235], a[20:23], a[188:191], v[232:235]\n"
"ds_read_b64_tr_b16 a[20:21], v2 offset:8448\n"
"ds_read_b64_tr_b16 a[22:23], v2 offset:12544\n"
"v_mfma_f32_16x16x32_bf16 v[140:143], a[24:27], a[160:163], v[140:143]\n"
"buffer_load_dwordx4 a[144:147], v4, s[44:47], s58 offen offset:1024 sc1\n"
"v_mfma_f32_16x16x32_bf16 v[140:143], a[28:31], a[176:179], v[140:143]\n"
"v_mfma_f32_16x16x32_bf16 v[172:175], a[24:27], a[164:167], v[172:175]\n"
"s_add_u32 m0, s75, 0\n"
"s_add_u32 s76, s76, 3072\n"
"buffer_load_dwordx4 v1, s[40:43], s76 offen offset:3072 lds\n"
"v_mfma_f32_16x16x32_bf16 v[172:175], a[28:31], a[180:183], v[172:175]\n"
"v_mfma_f32_16x16x32_bf16 v[204:207], a[24:27], a[168:171], v[204:207]\n"
"v_mfma_f32_16x16x32_bf16 v[204:207], a[28:31], a[184:187], v[204:207]\n"
"v_mfma_f32_16x16x32_bf16 v[236:239], a[24:27], a[172:175], v[236:239]\n"
"ds_read_b64_tr_b16 a[24:25], v3 offset:256\n"
"ds_read_b64_tr_b16 a[26:27], v3 offset:4352\n"
"v_mfma_f32_16x16x32_bf16 v[236:239], a[28:31], a[188:191], v[236:239]\n"
"buffer_load_dwordx4 a[148:151], v4, s[44:47], s59 offen offset:1024 sc1\n"
"v_mfma_f32_16x16x32_bf16 v[144:147], a[32:35], a[160:163], v[144:147]\n"
"ds_read_b64_tr_b16 a[28:29], v3 offset:8448\n"
"ds_read_b64_tr_b16 a[30:31], v3 offset:12544\n"
"v_mfma_f32_16x16x32_bf16 v[144:147], a[36:39], a[176:179], v[144:147]\n"
"s_add_u32 m0, s75, 16384\n"
"s_add_u32 s76, s76, 56320\n"
"buffer_load_dwordx4 v0, s[40:43], s76 offen offset:0 lds\n"
"v_mfma_f32_16x16x32_bf16 v[176:179], a[32:35], a[164:167], v[176:179]\n"
"v_mfma_f32_16x16x32_bf16 v[176:179], a[36:39], a[180:183], v[176:179]\n"
"v_mfma_f32_16x16x32_bf16 v[208:211], a[32:35], a[168:171], v[208:211]\n"
"v_mfma_f32_16x16x32_bf16 v[208:211], a[36:39], a[184:187], v[208:211]\n"
"v_mfma_f32_16x16x32_bf16 v[240:243], a[32:35], a[172:175], v[240:243]\n"
"buffer_load_dwordx4 a[152:155], v4, s[44:47], s60 offen offset:1024 sc1\n"
"v_mfma_f32_16x16x32_bf16 v[240:243], a[36:39], a[188:191], v[240:243]\n"
"ds_read_b64_tr_b16 a[32:33], v2 offset:512\n"
"ds_read_b64_tr_b16 a[34:35], v2 offset:4608\n"
"s_waitcnt lgkmcnt(15)\n"
"v_mfma_f32_16x16x32_bf16 v[148:151], a[40:43], a[160:163], v[148:151]\n"
"s_add_u32 m0, s75, 16384\n"
"s_add_u32 s76, s76, 3072\n"
"buffer_load_dwordx4 v1, s[40:43], s76 offen offset:1024 lds\n"
"v_mfma_f32_16x16x32_bf16 v[148:151], a[44:47], a[176:179], v[148:151]\n"
"ds_read_b64_tr_b16 a[36:37], v2 offset:8704\n"
"ds_read_b64_tr_b16 a[38:39], v2 offset:12800\n"
"v_mfma_f32_16x16x32_bf16 v[180:183], a[40:43], a[164:167], v[180:183]\n"
"v_mfma_f32_16x16x32_bf16 v[180:183], a[44:47], a[180:183], v[180:183]\n"
"v_mfma_f32_16x16x32_bf16 v[212:215], a[40:43], a[168:171], v[212:215]\n"
"v_mfma_f32_16x16x32_bf16 v[212:215], a[44:47], a[184:187], v[212:215]\n"
"buffer_load_dwordx4 a[156:159], v4, s[44:47], s61 offen offset:1024 sc1\n"
"v_mfma_f32_16x16x32_bf16 v[244:247], a[40:43], a[172:175], v[244:247]\n"
"ds_read_b64_tr_b16 a[40:41], v3 offset:512\n"
"ds_read_b64_tr_b16 a[42:43], v3 offset:4608\n"
"v_mfma_f32_16x16x32_bf16 v[244:247], a[44:47], a[188:191], v[244:247]\n"
"s_add_u32 m0, s75, 16384\n"
"s_add_u32 s76, s76, 3072\n"
"buffer_load_dwordx4 v0, s[40:43], s76 offen offset:2048 lds\n"
"v_mfma_f32_16x16x32_bf16 v[152:155], a[48:51], a[160:163], v[152:155]\n"
"ds_read_b64_tr_b16 a[44:45], v3 offset:8704\n"
"ds_read_b64_tr_b16 a[46:47], v3 offset:12800\n"
"v_mfma_f32_16x16x32_bf16 v[152:155], a[52:55], a[176:179], v[152:155]\n"
"v_mfma_f32_16x16x32_bf16 v[184:187], a[48:51], a[164:167], v[184:187]\n"
"v_mfma_f32_16x16x32_bf16 v[184:187], a[52:55], a[180:183], v[184:187]\n"
"v_mfma_f32_16x16x32_bf16 v[216:219], a[48:51], a[168:171], v[216:219]\n"
"v_mfma_f32_16x16x32_bf16 v[216:219], a[52:55], a[184:187], v[216:219]\n"
"v_mfma_f32_16x16x32_bf16 v[248:251], a[48:51], a[172:175], v[248:251]\n"
"s_add_u32 m0, s75, 16384\n"
"s_add_u32 s76, s76, 3072\n"
"buffer_load_dwordx4 v1, s[40:43], s76 offen offset:3072 lds\n"
"s_xor_b32 s75, s75, 0x8000\n"
"v_mfma_f32_16x16x32_bf16 v[248:251], a[52:55], a[188:191], v[248:251]\n"
"ds_read_b64_tr_b16 a[48:49], v2 offset:768\n"
"ds_read_b64_tr_b16 a[50:51], v2 offset:4864\n"
"v_mfma_f32_16x16x32_bf16 v[156:159], a[56:59], a[160:163], v[156:159]\n"
"ds_read_b64_tr_b16 a[52:53], v2 offset:8960\n"
"ds_read_b64_tr_b16 a[54:55], v2 offset:13056\n"
"v_mfma_f32_16x16x32_bf16 v[156:159], a[60:63], a[176:179], v[156:159]\n"
"v_mfma_f32_16x16x32_bf16 v[188:191], a[56:59], a[164:167], v[188:191]\n"
"v_mfma_f32_16x16x32_bf16 v[188:191], a[60:63], a[180:183], v[188:191]\n"
"v_mfma_f32_16x16x32_bf16 v[220:223], a[56:59], a[168:171], v[220:223]\n"
"v_mfma_f32_16x16x32_bf16 v[220:223], a[60:63], a[184:187], v[220:223]\n"
"v_mfma_f32_16x16x32_bf16 v[252:255], a[56:59], a[172:175], v[252:255]\n"
"ds_read_b64_tr_b16 a[56:57], v3 offset:768\n"
"ds_read_b64_tr_b16 a[58:59], v3 offset:4864\n"
"v_mfma_f32_16x16x32_bf16 v[252:255], a[60:63], a[188:191], v[252:255]\n"
"s_waitcnt vmcnt(41)\n"
"v_mfma_f32_16x16x32_bf16 v[128:131], a[0:3], a[192:195], v[128:131]\n"
"ds_read_b64_tr_b16 a[60:61], v3 offset:8960\n"
"ds_read_b64_tr_b16 a[62:63], v3 offset:13056\n"
"s_waitcnt lgkmcnt(15)\n"
"s_waitcnt vmcnt(34)\n"
"v_mfma_f32_16x16x32_bf16 v[128:131], a[4:7], a[208:211], v[128:131]\n"
"buffer_load_dwordx4 a[160:163], v4, s[44:47], s58 offen offset:2048 sc1\n"
"v_mfma_f32_16x16x32_bf16 v[160:163], a[0:3], a[196:199], v[160:163]\n"
"s_waitcnt vmcnt(33)\n"
"v_mfma_f32_16x16x32_bf16 v[160:163], a[4:7], a[212:215], v[160:163]\n"
"v_mfma_f32_16x16x32_bf16 v[192:195], a[0:3], a[200:203], v[192:195]\n"
"s_waitcnt vmcnt(31)\n"
"v_mfma_f32_16x16x32_bf16 v[192:195], a[4:7], a[216:219], v[192:195]\n"
"v_mfma_f32_16x16x32_bf16 v[224:227], a[0:3], a[204:207], v[224:227]\n"
"ds_read_b64_tr_b16 a[0:1], v2 offset:16384\n"
"ds_read_b64_tr_b16 a[2:3], v2 offset:20480\n"
"s_waitcnt vmcnt(29)\n"
"v_mfma_f32_16x16x32_bf16 v[224:227], a[4:7], a[220:223], v[224:227]\n"
"buffer_load_dwordx4 a[164:167], v4, s[44:47], s59 offen offset:2048 sc1\n"
"v_mfma_f32_16x16x32_bf16 v[132:135], a[8:11], a[192:195], v[132:135]\n"
"ds_read_b64_tr_b16 a[4:5], v2 offset:24576\n"
"ds_read_b64_tr_b16 a[6:7], v2 offset:28672\n"
"v_mfma_f32_16x16x32_bf16 v[132:135], a[12:15], a[208:211], v[132:135]\n"
"v_mfma_f32_16x16x32_bf16 v[164:167], a[8:11], a[196:199], v[164:167]\n"
"v_mfma_f32_16x16x32_bf16 v[164:167], a[12:15], a[212:215], v[164:167]\n"
"v_mfma_f32_16x16x32_bf16 v[196:199], a[8:11], a[200:203], v[196:199]\n"
"v_mfma_f32_16x16x32_bf16 v[196:199], a[12:15], a[216:219], v[196:199]\n"
"buffer_load_dwordx4 a[168:171], v4, s[44:47], s60 offen offset:2048 sc1\n"
"v_mfma_f32_16x16x32_bf16 v[228:231], a[8:11], a[204:207], v[228:231]\n"
"ds_read_b64_tr_b16 a[8:9], v3 offset:16384\n"
"ds_read_b64_tr_b16 a[10:11], v3 offset:20480\n"
"v_mfma_f32_16x16x32_bf16 v[228:231], a[12:15], a[220:223], v[228:231]\n"
"ds_read_b64_tr_b16 a[12:13], v3 offset:24576\n"
"ds_read_b64_tr_b16 a[14:15], v3 offset:28672\n"
"v_mfma_f32_16x16x32_bf16 v[136:139], a[16:19], a[192:195], v[136:139]\n"
"v_mfma_f32_16x16x32_bf16 v[136:139], a[20:23], a[208:211], v[136:139]\n"
"v_mfma_f32_16x16x32_bf16 v[168:171], a[16:19], a[196:199], v[168:171]\n"
"v_mfma_f32_16x16x32_bf16 v[168:171], a[20:23], a[212:215], v[168:171]\n"
"buffer_load_dwordx4 a[172:175], v4, s[44:47], s61 offen offset:2048 sc1\n"
"v_mfma_f32_16x16x32_bf16 v[200:203], a[16:19], a[200:203], v[200:203]\n"
"v_mfma_f32_16x16x32_bf16 v[200:203], a[20:23], a[216:219], v[200:203]\n"
"v_mfma_f32_16x16x32_bf16 v[232:235], a[16:19], a[204:207], v[232:235]\n"
"ds_read_b64_tr_b16 a[16:17], v2 offset:16640\n"
"ds_read_b64_tr_b16 a[18:19], v2 offset:20736\n"
"v_mfma_f32_16x16x32_bf16 v[232:235], a[20:23], a[220:223], v[232:235]\n"
"ds_read_b64_tr_b16 a[20:21], v2 offset:24832\n"
"ds_read_b64_tr_b16 a[22:23], v2 offset:28928\n"
"v_mfma_f32_16x16x32_bf16 v[140:143], a[24:27], a[192:195], v[140:143]\n"
"v_mfma_f32_16x16x32_bf16 v[140:143], a[28:31], a[208:211], v[140:143]\n"
"buffer_load_dwordx4 a[176:179], v4, s[44:47], s58 offen offset:3072 sc1\n"
"v_mfma_f32_16x16x32_bf16 v[172:175], a[24:27], a[196:199], v[172:175]\n"
"v_mfma_f32_16x16x32_bf16 v[172:175], a[28:31], a[212:215], v[172:175]\n"
"v_mfma_f32_16x16x32_bf16 v[204:207], a[24:27], a[200:203], v[204:207]\n"
"v_mfma_f32_16x16x32_bf16 v[204:207], a[28:31], a[216:219], v[204:207]\n"
"v_mfma_f32_16x16x32_bf16 v[236:239], a[24:27], a[204:207], v[236:239]\n"
"ds_read_b64_tr_b16 a[24:25], v3 offset:16640\n"
"ds_read_b64_tr_b16 a[26:27], v3 offset:20736\n"
"v_mfma_f32_16x16x32_bf16 v[236:239], a[28:31], a[220:223], v[236:239]\n"
"buffer_load_dwordx4 a[180:183], v4, s[44:47], s59 offen offset:3072 sc1\n"
"s_waitcnt lgkmcnt(15)\n"
"v_mfma_f32_16x16x32_bf16 v[144:147], a[32:35], a[192:195], v[144:147]\n"
"ds_read_b64_tr_b16 a[28:29], v3 offset:24832\n"
"ds_read_b64_tr_b16 a[30:31], v3 offset:28928\n"
"v_mfma_f32_16x16x32_bf16 v[144:147], a[36:39], a[208:211], v[144:147]\n"
"v_mfma_f32_16x16x32_bf16 v[176:179], a[32:35], a[196:199], v[176:179]\n"
"v_mfma_f32_16x16x32_bf16 v[176:179], a[36:39], a[212:215], v[176:179]\n"
"v_mfma_f32_16x16x32_bf16 v[208:211], a[32:35], a[200:203], v[208:211]\n"
"v_mfma_f32_16x16x32_bf16 v[208:211], a[36:39], a[216:219], v[208:211]\n"
"buffer_load_dwordx4 a[184:187], v4, s[44:47], s60 offen offset:3072 sc1\n"
"v_mfma_f32_16x16x32_bf16 v[240:243], a[32:35], a[204:207], v[240:243]\n"
"ds_read_b64_tr_b16 a[32:33], v2 offset:16896\n"
"ds_read_b64_tr_b16 a[34:35], v2 offset:20992\n"
"v_mfma_f32_16x16x32_bf16 v[240:243], a[36:39], a[220:223], v[240:243]\n"
"ds_read_b64_tr_b16 a[36:37], v2 offset:25088\n"
"ds_read_b64_tr_b16 a[38:39], v2 offset:29184\n"
"v_mfma_f32_16x16x32_bf16 v[148:151], a[40:43], a[192:195], v[148:151]\n"
"v_mfma_f32_16x16x32_bf16 v[148:151], a[44:47], a[208:211], v[148:151]\n"
"v_mfma_f32_16x16x32_bf16 v[180:183], a[40:43], a[196:199], v[180:183]\n"
"v_mfma_f32_16x16x32_bf16 v[180:183], a[44:47], a[212:215], v[180:183]\n"
"buffer_load_dwordx4 a[188:191], v4, s[44:47], s61 offen offset:3072 sc1\n"
"v_mfma_f32_16x16x32_bf16 v[212:215], a[40:43], a[200:203], v[212:215]\n"
"v_mfma_f32_16x16x32_bf16 v[212:215], a[44:47], a[216:219], v[212:215]\n"
"v_mfma_f32_16x16x32_bf16 v[244:247], a[40:43], a[204:207], v[244:247]\n"
"ds_read_b64_tr_b16 a[40:41], v3 offset:16896\n"
"ds_read_b64_tr_b16 a[42:43], v3 offset:20992\n"
"v_mfma_f32_16x16x32_bf16 v[244:247], a[44:47], a[220:223], v[244:247]\n"
"ds_read_b64_tr_b16 a[44:45], v3 offset:25088\n"
"ds_read_b64_tr_b16 a[46:47], v3 offset:29184\n"
"v_mfma_f32_16x16x32_bf16 v[152:155], a[48:51], a[192:195], v[152:155]\n"
"v_mfma_f32_16x16x32_bf16 v[152:155], a[52:55], a[208:211], v[152:155]\n"
"s_and_b32 s63, s65, 15\n"
"s_lshl_b32 s58, s63, 12\n"
"s_lshr_b32 s65, s65, 4\n"
"s_add_u32 s59, s58, 131072\n"
"s_add_u32 s60, s58, 262144\n"
"s_add_u32 s61, s58, 393216\n"
"s_waitcnt vmcnt(24)\n"
"v_cmp_ne_u32 vcc, v24, v19\n"
"s_nop 1\n"
"s_mov_b64 s[78:79], vcc\n"
"v_cmp_ne_u32 vcc, v25, v18\n"
"s_cmp_eq_u32 s62, 3\n"
"s_cselect_b64 s[70:71], -1, 0\n"
"s_or_b64 s[78:79], s[78:79], vcc\n"
"s_and_b64 s[78:79], s[78:79], s[70:71]\n"
"s_cbranch_scc1 .Ls1a_%=\n"
".Lr1a_%=:\n"
"v_mfma_f32_16x16x32_bf16 v[184:187], a[48:51], a[196:199], v[184:187]\n"
"v_mfma_f32_16x16x32_bf16 v[184:187], a[52:55], a[212:215], v[184:187]\n"
"v_mfma_f32_16x16x32_bf16 v[216:219], a[48:51], a[200:203], v[216:219]\n"
"v_mfma_f32_16x16x32_bf16 v[216:219], a[52:55], a[216:219], v[216:219]\n"
"v_mfma_f32_16x16x32_bf16 v[248:251], a[48:51], a[204:207], v[248:251]\n"
"ds_read_b64_tr_b16 a[48:49], v2 offset:17152\n"
"ds_read_b64_tr_b16 a[50:51], v2 offset:21248\n"
"v_mfma_f32_16x16x32_bf16 v[248:251], a[52:55], a[220:223], v[248:251]\n"
"ds_read_b64_tr_b16 a[52:53], v2 offset:25344\n"
"ds_read_b64_tr_b16 a[54:55], v2 offset:29440\n"
"v_mfma_f32_16x16x32_bf16 v[156:159], a[56:59], a[192:195], v[156:159]\n"
"s_waitcnt lgkmcnt(15)\n"
"v_mfma_f32_16x16x32_bf16 v[156:159], a[60:63], a[208:211], v[156:159]\n"
"v_mfma_f32_16x16x32_bf16 v[188:191], a[56:59], a[196:199], v[188:191]\n"
"v_mfma_f32_16x16x32_bf16 v[188:191], a[60:63], a[212:215], v[188:191]\n"
"v_mfma_f32_16x16x32_bf16 v[220:223], a[56:59], a[200:203], v[220:223]\n"
"buffer_load_dwordx4 a[192:195], v4, s[44:47], s58 offen offset:0 sc1\n"
"v_mfma_f32_16x16x32_bf16 v[220:223], a[60:63], a[216:219], v[220:223]\n"
"v_mfma_f32_16x16x32_bf16 v[252:255], a[56:59], a[204:207], v[252:255]\n"
"ds_read_b64_tr_b16 a[56:57], v3 offset:17152\n"
"ds_read_b64_tr_b16 a[58:59], v3 offset:21248\n"
"v_mfma_f32_16x16x32_bf16 v[252:255], a[60:63], a[220:223], v[252:255]\n"
"ds_read_b64_tr_b16 a[60:61], v3 offset:25344\n"
"ds_read_b64_tr_b16 a[62:63], v3 offset:29440\n"
"v_mfma_f32_16x16x32_bf16 v[128:131], a[0:3], a[224:227], v[128:131]\n"
"v_mfma_f32_16x16x32_bf16 v[128:131], a[4:7], a[240:243], v[128:131]\n"
"v_mfma_f32_16x16x32_bf16 v[160:163], a[0:3], a[228:231], v[160:163]\n"
"v_mfma_f32_16x16x32_bf16 v[160:163], a[4:7], a[244:247], v[160:163]\n"
"buffer_load_dwordx4 a[196:199], v4, s[44:47], s59 offen offset:0 sc1\n"
"v_mfma_f32_16x16x32_bf16 v[192:195], a[0:3], a[232:235], v[192:195]\n"
"s_waitcnt vmcnt(63)\n"
"s_barrier\n"
"v_xor_b32 v2, 0x8000, v2\n"
"v_xor_b32 v3, 0x8000, v3\n"
"v_mfma_f32_16x16x32_bf16 v[192:195], a[4:7], a[248:251], v[192:195]\n"
"s_add_u32 m0, s75, 0\n"
"s_and_b32 s76, s68, 15\n"
"s_lshl_b32 s76, s76, 17\n"
"s_lshr_b64 s[68:69], s[68:69], 4\n"
"buffer_load_dwordx4 v0, s[40:43], s76 offen offset:0 lds\n"
"v_mfma_f32_16x16x32_bf16 v[224:227], a[0:3], a[236:239], v[224:227]\n"
"ds_read_b64_tr_b16 a[0:1], v2 offset:0\n"
"ds_read_b64_tr_b16 a[2:3], v2 offset:4096\n"
"v_mfma_f32_16x16x32_bf16 v[224:227], a[4:7], a[252:255], v[224:227]\n"
"ds_read_b64_tr_b16 a[4:5], v2 offset:8192\n"
"ds_read_b64_tr_b16 a[6:7], v2 offset:12288\n"
"v_mfma_f32_16x16x32_bf16 v[132:135], a[8:11], a[224:227], v[132:135]\n"
"v_mfma_f32_16x16x32_bf16 v[132:135], a[12:15], a[240:243], v[132:135]\n"
"v_mfma_f32_16x16x32_bf16 v[164:167], a[8:11], a[228:231], v[164:167]\n"
"buffer_load_dwordx4 a[200:203], v4, s[44:47], s60 offen offset:0 sc1\n"
"v_mfma_f32_16x16x32_bf16 v[164:167], a[12:15], a[244:247], v[164:167]\n"
"v_mfma_f32_16x16x32_bf16 v[196:199], a[8:11], a[232:235], v[196:199]\n"
"s_add_u32 m0, s75, 0\n"
"s_add_u32 s76, s76, 3072\n"
"buffer_load_dwordx4 v1, s[40:43], s76 offen offset:1024 lds\n"
"v_mfma_f32_16x16x32_bf16 v[196:199], a[12:15], a[248:251], v[196:199]\n"
"v_mfma_f32_16x16x32_bf16 v[228:231], a[8:11], a[236:239], v[228:231]\n"
"ds_read_b64_tr_b16 a[8:9], v3 offset:0\n"
"ds_read_b64_tr_b16 a[10:11], v3 offset:4096\n"
"v_mfma_f32_16x16x32_bf16 v[228:231], a[12:15], a[252:255], v[228:231]\n"
"ds_read_b64_tr_b16 a[12:13], v3 offset:8192\n"
"ds_read_b64_tr_b16 a[14:15], v3 offset:12288\n"
"v_mfma_f32_16x16x32_bf16 v[136:139], a[16:19], a[224:227], v[136:139]\n"
"v_mfma_f32_16x16x32_bf16 v[136:139], a[20:23], a[240:243], v[136:139]\n"
"buffer_load_dwordx4 a[204:207], v4, s[44:47], s61 offen offset:0 sc1\n"
"v_mfma_f32_16x16x32_bf16 v[168:171], a[16:19], a[228:231], v[168:171]\n"
"v_mfma_f32_16x16x32_bf16 v[168:171], a[20:23], a[244:247], v[168:171]\n"
"s_add_u32 m0, s75, 0\n"
"s_add_u32 s76, s76, 3072\n"
"buffer_load_dwordx4 v0, s[40:43], s76 offen offset:2048 lds\n"
"v_mfma_f32_16x16x32_bf16 v[200:203], a[16:19], a[232:235], v[200:203]\n"
"v_mfma_f32_16x16x32_bf16 v[200:203], a[20:23], a[248:251], v[200:203]\n"
"v_mfma_f32_16x16x32_bf16 v[232:235], a[16:19], a[236:239], v[232:235]\n"
"ds_read_b64_tr_b16 a[16:17], v2 offset:256\n"
"ds_read_b64_tr_b16 a[18:19], v2 offset:4352\n"
"v_mfma_f32_16x16x32_bf16 v[232:235], a[20:23], a[252:255], v[232:235]\n"
"ds_read_b64_tr_b16 a[20:21], v2 offset:8448\n"
"ds_read_b64_tr_b16 a[22:23], v2 offset:12544\n"
"s_waitcnt lgkmcnt(15)\n"
"v_mfma_f32_16x16x32_bf16 v[140:143], a[24:27], a[224:227], v[140:143]\n"
"buffer_load_dwordx4 a[208:211], v4, s[44:47], s58 offen offset:1024 sc1\n"
"v_mfma_f32_16x16x32_bf16 v[140:143], a[28:31], a[240:243], v[140:143]\n"
"v_mfma_f32_16x16x32_bf16 v[172:175], a[24:27], a[228:231], v[172:175]\n"
"s_add_u32 m0, s75, 0\n"
"s_add_u32 s76, s76, 3072\n"
"buffer_load_dwordx4 v1, s[40:43], s76 offen offset:3072 lds\n"
"v_mfma_f32_16x16x32_bf16 v[172:175], a[28:31], a[244:247], v[172:175]\n"
"v_mfma_f32_16x16x32_bf16 v[204:207], a[24:27], a[232:235], v[204:207]\n"
"v_mfma_f32_16x16x32_bf16 v[204:207], a[28:31], a[248:251], v[204:207]\n"
"v_mfma_f32_16x16x32_bf16 v[236:239], a[24:27], a[236:239], v[236:239]\n"
"ds_read_b64_tr_b16 a[24:25], v3 offset:256\n"
"ds_read_b64_tr_b16 a[26:27], v3 offset:4352\n"
"v_mfma_f32_16x16x32_bf16 v[236:239], a[28:31], a[252:255], v[236:239]\n"
"buffer_load_dwordx4 a[212:215], v4, s[44:47], s59 offen offset:1024 sc1\n"
"v_mfma_f32_16x16x32_bf16 v[144:147], a[32:35], a[224:227], v[144:147]\n"
"ds_read_b64_tr_b16 a[28:29], v3 offset:8448\n"
"ds_read_b64_tr_b16 a[30:31], v3 offset:12544\n"
"v_mfma_f32_16x16x32_bf16 v[144:147], a[36:39], a[240:243], v[144:147]\n"
"s_add_u32 m0, s75, 16384\n"
"s_add_u32 s76, s76, 56320\n"
"buffer_load_dwordx4 v0, s[40:43], s76 offen offset:0 lds\n"
"v_mfma_f32_16x16x32_bf16 v[176:179], a[32:35], a[228:231], v[176:179]\n"
"v_mfma_f32_16x16x32_bf16 v[176:179], a[36:39], a[244:247], v[176:179]\n"
"v_mfma_f32_16x16x32_bf16 v[208:211], a[32:35], a[232:235], v[208:211]\n"
"v_mfma_f32_16x16x32_bf16 v[208:211], a[36:39], a[248:251], v[208:211]\n"
"v_mfma_f32_16x16x32_bf16 v[240:243], a[32:35], a[236:239], v[240:243]\n"
"buffer_load_dwordx4 a[216:219], v4, s[44:47], s60 offen offset:1024 sc1\n"
"v_mfma_f32_16x16x32_bf16 v[240:243], a[36:39], a[252:255], v[240:243]\n"
"ds_read_b64_tr_b16 a[32:33], v2 offset:512\n"
"ds_read_b64_tr_b16 a[34:35], v2 offset:4608\n"
"v_mfma_f32_16x16x32_bf16 v[148:151], a[40:43], a[224:227], v[148:151]\n"
"s_add_u32 m0, s75, 16384\n"
"s_add_u32 s76, s76, 3072\n"
"buffer_load_dwordx4 v1, s[40:43], s76 offen offset:1024 lds\n"
"v_mfma_f32_16x16x32_bf16 v[148:151], a[44:47], a[240:243], v[148:151]\n"
"ds_read_b64_tr_b16 a[36:37], v2 offset:8704\n"
"ds_read_b64_tr_b16 a[38:39], v2 offset:12800\n"
"v_mfma_f32_16x16x32_bf16 v[180:183], a[40:43], a[228:231], v[180:183]\n"
"v_mfma_f32_16x16x32_bf16 v[180:183], a[44:47], a[244:247], v[180:183]\n"
"v_mfma_f32_16x16x32_bf16 v[212:215], a[40:43], a[232:235], v[212:215]\n"
"v_mfma_f32_16x16x32_bf16 v[212:215], a[44:47], a[248:251], v[212:215]\n"
"buffer_load_dwordx4 a[220:223], v4, s[44:47], s61 offen offset:1024 sc1\n"
"v_mfma_f32_16x16x32_bf16 v[244:247], a[40:43], a[236:239], v[244:247]\n"
"ds_read_b64_tr_b16 a[40:41], v3 offset:512\n"
"ds_read_b64_tr_b16 a[42:43], v3 offset:4608\n"
"v_mfma_f32_16x16x32_bf16 v[244:247], a[44:47], a[252:255], v[244:247]\n"
"s_add_u32 m0, s75, 16384\n"
"s_add_u32 s76, s76, 3072\n"
"buffer_load_dwordx4 v0, s[40:43], s76 offen offset:2048 lds\n"
"v_mfma_f32_16x16x32_bf16 v[152:155], a[48:51], a[224:227], v[152:155]\n"
"ds_read_b64_tr_b16 a[44:45], v3 offset:8704\n"
"ds_read_b64_tr_b16 a[46:47], v3 offset:12800\n"
"v_mfma_f32_16x16x32_bf16 v[152:155], a[52:55], a[240:243], v[152:155]\n"
"v_mfma_f32_16x16x32_bf16 v[184:187], a[48:51], a[228:231], v[184:187]\n"
"v_mfma_f32_16x16x32_bf16 v[184:187], a[52:55], a[244:247], v[184:187]\n"
"v_mfma_f32_16x16x32_bf16 v[216:219], a[48:51], a[232:235], v[216:219]\n"
"v_mfma_f32_16x16x32_bf16 v[216:219], a[52:55], a[248:251], v[216:219]\n"
"v_mfma_f32_16x16x32_bf16 v[248:251], a[48:51], a[236:239], v[248:251]\n"
"s_add_u32 m0, s75, 16384\n"
"s_add_u32 s76, s76, 3072\n"
"buffer_load_dwordx4 v1, s[40:43], s76 offen offset:3072 lds\n"
"s_xor_b32 s75, s75, 0x10000\n"
"v_mfma_f32_16x16x32_bf16 v[248:251], a[52:55], a[252:255], v[248:251]\n"
"ds_read_b64_tr_b16 a[48:49], v2 offset:768\n"
"ds_read_b64_tr_b16 a[50:51], v2 offset:4864\n"
"s_waitcnt lgkmcnt(15)\n"
"v_mfma_f32_16x16x32_bf16 v[156:159], a[56:59], a[224:227], v[156:159]\n"
"ds_read_b64_tr_b16 a[52:53], v2 offset:8960\n"
"ds_read_b64_tr_b16 a[54:55], v2 offset:13056\n"
"v_mfma_f32_16x16x32_bf16 v[156:159], a[60:63], a[240:243], v[156:159]\n"
"v_mfma_f32_16x16x32_bf16 v[188:191], a[56:59], a[228:231], v[188:191]\n"
"v_mfma_f32_16x16x32_bf16 v[188:191], a[60:63], a[244:247], v[188:191]\n"
"v_mfma_f32_16x16x32_bf16 v[220:223], a[56:59], a[232:235], v[220:223]\n"
"v_mfma_f32_16x16x32_bf16 v[220:223], a[60:63], a[248:251], v[220:223]\n"
"v_mfma_f32_16x16x32_bf16 v[252:255], a[56:59], a[236:239], v[252:255]\n"
"ds_read_b64_tr_b16 a[56:57], v3 offset:768\n"
"ds_read_b64_tr_b16 a[58:59], v3 offset:4864\n"
"v_mfma_f32_16x16x32_bf16 v[252:255], a[60:63], a[252:255], v[252:255]\n"
"s_sub_u32 s62, s62, 1\n"
"s_cmp_lg_u32 s62, 0\n"
"s_cbranch_scc1 .LLa_%=\n"
"v_cvt_pk_bf16_f32 v136, v136, v137\n"
"v_cvt_pk_bf16_f32 v137, v138, v139\n"
"v_cvt_pk_bf16_f32 v138, v140, v141\n"
"v_cvt_pk_bf16_f32 v139, v142, v143\n"
"buffer_store_dwordx4 v[136:139], v6, s[36:39], s52 offen offset:0 sc1\n"
"v_cvt_pk_bf16_f32 v144, v144, v145\n"
"v_cvt_pk_bf16_f32 v145, v146, v147\n"
"v_cvt_pk_bf16_f32 v146, v148, v149\n"
"v_cvt_pk_bf16_f32 v147, v150, v151\n"
"buffer_store_dwordx4 v[144:147], v6, s[36:39], s53 offen offset:0 sc1\n"
"v_cvt_pk_bf16_f32 v152, v152, v153\n"
"v_cvt_pk_bf16_f32 v153, v154, v155\n"
"v_cvt_pk_bf16_f32 v154, v156, v157\n"
"v_cvt_pk_bf16_f32 v155, v158, v159\n"
"buffer_store_dwordx4 v[152:155], v6, s[36:39], s54 offen offset:0 sc1\n"
"v_cvt_pk_bf16_f32 v168, v168, v169\n"
"v_cvt_pk_bf16_f32 v169, v170, v171\n"
"v_cvt_pk_bf16_f32 v170, v172, v173\n"
"v_cvt_pk_bf16_f32 v171, v174, v175\n"
"buffer_store_dwordx4 v[168:171], v6, s[36:39], s52 offen offset:1024 sc1\n"
"v_cvt_pk_bf16_f32 v176, v176, v177\n"
"v_cvt_pk_bf16_f32 v177, v178, v179\n"
"v_cvt_pk_bf16_f32 v178, v180, v181\n"
"v_cvt_pk_bf16_f32 v179, v182, v183\n"
"buffer_store_dwordx4 v[176:179], v6, s[36:39], s53 offen offset:1024 sc1\n"
"v_cvt_pk_bf16_f32 v184, v184, v185\n"
"v_cvt_pk_bf16_f32 v185, v186, v187\n"
"v_cvt_pk_bf16_f32 v186, v188, v189\n"
"v_cvt_pk_bf16_f32 v187, v190, v191\n"
"buffer_store_dwordx4 v[184:187], v6, s[36:39], s54 offen offset:1024 sc1\n"
"v_cvt_pk_bf16_f32 v200, v200, v201\n"
"v_cvt_pk_bf16_f32 v201, v202, v203\n"
"v_cvt_pk_bf16_f32 v202, v204, v205\n"
"v_cvt_pk_bf16_f32 v203, v206, v207\n"
"buffer_store_dwordx4 v[200:203], v6, s[36:39], s52 offen offset:2048 sc1\n"
"v_cvt_pk_bf16_f32 v208, v208, v209\n"
"v_cvt_pk_bf16_f32 v209, v210, v211\n"
"v_cvt_pk_bf16_f32 v210, v212, v213\n"
"v_cvt_pk_bf16_f32 v211, v214, v215\n"
"buffer_store_dwordx4 v[208:211], v6, s[36:39], s53 offen offset:2048 sc1\n"
"v_cvt_pk_bf16_f32 v216, v216, v217\n"
"v_cvt_pk_bf16_f32 v217, v218, v219\n"
"v_cvt_pk_bf16_f32 v218, v220, v221\n"
"v_cvt_pk_bf16_f32 v219, v222, v223\n"
"buffer_store_dwordx4 v[216:219], v6, s[36:39], s54 offen offset:2048 sc1\n"
"v_cvt_pk_bf16_f32 v232, v232, v233\n"
"v_cvt_pk_bf16_f32 v233, v234, v235\n"
"v_cvt_pk_bf16_f32 v234, v236, v237\n"
"v_cvt_pk_bf16_f32 v235, v238, v239\n"
"buffer_store_dwordx4 v[232:235], v6, s[36:39], s52 offen offset:3072 sc1\n"
"v_cvt_pk_bf16_f32 v240, v240, v241\n"
"v_cvt_pk_bf16_f32 v241, v242, v243\n"
"v_cvt_pk_bf16_f32 v242, v244, v245\n"
"v_cvt_pk_bf16_f32 v243, v246, v247\n"
"buffer_store_dwordx4 v[240:243], v6, s[36:39], s53 offen offset:3072 sc1\n"
"v_cvt_pk_bf16_f32 v248, v248, v249\n"
"v_cvt_pk_bf16_f32 v249, v250, v251\n"
"v_cvt_pk_bf16_f32 v250, v252, v253\n"
"v_cvt_pk_bf16_f32 v251, v254, v255\n"
"buffer_store_dwordx4 v[248:251], v6, s[36:39], s54 offen offset:3072 sc1\n"
"s_waitcnt vmcnt(0)\n"
"s_mov_b64 exec, 15\n"
"buffer_store_dwordx2 v[20:21], v8, s[36:39], %[xw] offen sc1\n"
"s_mov_b64 exec, -1\n"
"s_mov_b32 s64, 0x200000\n"
".Lpxa_%=:\n"
"buffer_load_dwordx2 v[22:23], v13, s[36:39], %[xa] offen offset:0 sc1\n"
"s_waitcnt vmcnt(0)\n"
"v_cmp_ne_u32 vcc, v22, v21\n"
"s_nop 1\n"
"s_cmp_eq_u64 vcc, 0\n"
"s_cbranch_scc1 .Lexa_%=\n"
"v_mov_b32 v21, v22\n"
"s_mov_b64 exec, 15\n"
"buffer_store_dwordx2 v[20:21], v8, s[36:39], %[xw] offen offset:0 sc1\n"
"s_mov_b64 exec, -1\n"
".Lexa_%=:\n"
"v_cmp_ne_u32 vcc, v23, v20\n"
"s_nop 1\n"
"s_cmp_eq_u64 vcc, 0\n"
"s_cbranch_scc1 .Lqxa_%=\n"
"s_sleep 1\n"
"s_sub_u32 s64, s64, 1\n"
"s_cmp_lg_u32 s64, 0\n"
"s_cbranch_scc1 .Lpxa_%=\n"
".Lqxa_%=:\n"
"s_waitcnt vmcnt(0)\n"
"buffer_load_dwordx4 v[136:139], v6, s[36:39], s55 offen offset:0 sc1\n"
"buffer_load_dwordx4 v[144:147], v6, s[36:39], s66 offen offset:0 sc1\n"
"buffer_load_dwordx4 v[152:155], v6, s[36:39], s67 offen offset:0 sc1\n"
"buffer_load_dwordx4 v[168:171], v6, s[36:39], s55 offen offset:1024 sc1\n"
"buffer_load_dwordx4 v[176:179], v6, s[36:39], s66 offen offset:1024 sc1\n"
"buffer_load_dwordx4 v[184:187], v6, s[36:39], s67 offen offset:1024 sc1\n"
"buffer_load_dwordx4 v[200:203], v6, s[36:39], s55 offen offset:2048 sc1\n"
"buffer_load_dwordx4 v[208:211], v6, s[36:39], s66 offen offset:2048 sc1\n"
"buffer_load_dwordx4 v[216:219], v6, s[36:39], s67 offen offset:2048 sc1\n"
"buffer_load_dwordx4 v[232:235], v6, s[36:39], s55 offen offset:3072 sc1\n"
"buffer_load_dwordx4 v[240:243], v6, s[36:39], s66 offen offset:3072 sc1\n"
"buffer_load_dwordx4 v[248:251], v6, s[36:39], s67 offen offset:3072 sc1\n"
"s_waitcnt vmcnt(11)\n"
"v_lshlrev_b32 v140, 16, v136\n"
"v_and_b32 v141, 0xffff0000, v136\n"
"v_add_f32 v128, v128, v140\n"
"v_add_f32 v129, v129, v141\n"
"v_lshlrev_b32 v142, 16, v137\n"
"v_and_b32 v143, 0xffff0000, v137\n"
"v_add_f32 v130, v130, v142\n"
"v_add_f32 v131, v131, v143\n"
"v_lshlrev_b32 v140, 16, v138\n"
"v_and_b32 v141, 0xffff0000, v138\n"
"v_add_f32 v132, v132, v140\n"
"v_add_f32 v133, v133, v141\n"
"v_lshlrev_b32 v142, 16, v139\n"
"v_and_b32 v143, 0xffff0000, v139\n"
"v_add_f32 v134, v134, v142\n"
"v_add_f32 v135, v135, v143\n"
"s_waitcnt vmcnt(10)\n"
"v_lshlrev_b32 v148, 16, v144\n"
"v_and_b32 v149, 0xffff0000, v144\n"
"v_add_f32 v128, v128, v148\n"
"v_add_f32 v129, v129, v149\n"
"v_lshlrev_b32 v150, 16, v145\n"
"v_and_b32 v151, 0xffff0000, v145\n"
"v_add_f32 v130, v130, v150\n"
"v_add_f32 v131, v131, v151\n"
"v_lshlrev_b32 v148, 16, v146\n"
"v_and_b32 v149, 0xffff0000, v146\n"
"v_add_f32 v132, v132, v148\n"
"v_add_f32 v133, v133, v149\n"
"v_lshlrev_b32 v150, 16, v147\n"
"v_and_b32 v151, 0xffff0000, v147\n"
"v_add_f32 v134, v134, v150\n"
"v_add_f32 v135, v135, v151\n"
"s_waitcnt vmcnt(9)\n"
"v_lshlrev_b32 v156, 16, v152\n"
"v_and_b32 v157, 0xffff0000, v152\n"
"v_add_f32 v128, v128, v156\n"
"v_add_f32 v129, v129, v157\n"
"v_lshlrev_b32 v158, 16, v153\n"
"v_and_b32 v159, 0xffff0000, v153\n"
"v_add_f32 v130, v130, v158\n"
"v_add_f32 v131, v131, v159\n"
"v_lshlrev_b32 v156, 16, v154\n"
"v_and_b32 v157, 0xffff0000, v154\n"
"v_add_f32 v132, v132, v156\n"
"v_add_f32 v133, v133, v157\n"
"v_lshlrev_b32 v158, 16, v155\n"
"v_and_b32 v159, 0xffff0000, v155\n"
"v_add_f32 v134, v134, v158\n"
"v_add_f32 v135, v135, v159\n"
"v_cvt_pk_bf16_f32 v128, v128, v129\n"
"v_cvt_pk_bf16_f32 v129, v130, v131\n"
"v_cvt_pk_bf16_f32 v130, v132, v133\n"
"v_cvt_pk_bf16_f32 v131, v134, v135\n"
"s_mov_b32 s63, 0\n"
"buffer_store_dwordx2 v[128:129], v5, s[48:51], s63 offen offset:0\n"
"buffer_store_dwordx2 v[130:131], v5, s[48:51], s63 offen offset:32\n"
"s_waitcnt vmcnt(10)\n"
"v_lshlrev_b32 v172, 16, v168\n"
"v_and_b32 v173, 0xffff0000, v168\n"
"v_add_f32 v160, v160, v172\n"
"v_add_f32 v161, v161, v173\n"
"v_lshlrev_b32 v174, 16, v169\n"
"v_and_b32 v175, 0xffff0000, v169\n"
"v_add_f32 v162, v162, v174\n"
"v_add_f32 v163, v163, v175\n"
"v_lshlrev_b32 v172, 16, v170\n"
"v_and_b32 v173, 0xffff0000, v170\n"
"v_add_f32 v164, v164, v172\n"
"v_add_f32 v165, v165, v173\n"
"v_lshlrev_b32 v174, 16, v171\n"
"v_and_b32 v175, 0xffff0000, v171\n"
"v_add_f32 v166, v166, v174\n"
"v_add_f32 v167, v167, v175\n"
"s_waitcnt vmcnt(9)\n"
"v_lshlrev_b32 v180, 16, v176\n"
"v_and_b32 v181, 0xffff0000, v176\n"
"v_add_f32 v160, v160, v180\n"
"v_add_f32 v161, v161, v181\n"
"v_lshlrev_b32 v182, 16, v177\n"
"v_and_b32 v183, 0xffff0000, v177\n"
"v_add_f32 v162, v162, v182\n"
"v_add_f32 v163, v163, v183\n"
"v_lshlrev_b32 v180, 16, v178\n"
"v_and_b32 v181, 0xffff0000, v178\n"
"v_add_f32 v164, v164, v180\n"
"v_add_f32 v165, v165, v181\n"
"v_lshlrev_b32 v182, 16, v179\n"
"v_and_b32 v183, 0xffff0000, v179\n"
"v_add_f32 v166, v166, v182\n"
"v_add_f32 v167, v167, v183\n"
"s_waitcnt vmcnt(8)\n"
"v_lshlrev_b32 v188, 16, v184\n"
"v_and_b32 v189, 0xffff0000, v184\n"
"v_add_f32 v160, v160, v188\n"
"v_add_f32 v161, v161, v189\n"
"v_lshlrev_b32 v190, 16, v185\n"
"v_and_b32 v191, 0xffff0000, v185\n"
"v_add_f32 v162, v162, v190\n"
"v_add_f32 v163, v163, v191\n"
"v_lshlrev_b32 v188, 16, v186\n"
"v_and_b32 v189, 0xffff0000, v186\n"
"v_add_f32 v164, v164, v188\n"
"v_add_f32 v165, v165, v189\n"
"v_lshlrev_b32 v190, 16, v187\n"
"v_and_b32 v191, 0xffff0000, v187\n"
"v_add_f32 v166, v166, v190\n"
"v_add_f32 v167, v167, v191\n"
"v_cvt_pk_bf16_f32 v160, v160, v161\n"
"v_cvt_pk_bf16_f32 v161, v162, v163\n"
"v_cvt_pk_bf16_f32 v162, v164, v165\n"
"v_cvt_pk_bf16_f32 v163, v166, v167\n"
"s_mov_b32 s63, 32768\n"
"buffer_store_dwordx2 v[160:161], v5, s[48:51], s63 offen offset:0\n"
"buffer_store_dwordx2 v[162:163], v5, s[48:51], s63 offen offset:32\n"
"s_waitcnt vmcnt(9)\n"
"v_lshlrev_b32 v204, 16, v200\n"
"v_and_b32 v205, 0xffff0000, v200\n"
"v_add_f32 v192, v192, v204\n"
"v_add_f32 v193, v193, v205\n"
"v_lshlrev_b32 v206, 16, v201\n"
"v_and_b32 v207, 0xffff0000, v201\n"
"v_add_f32 v194, v194, v206\n"
"v_add_f32 v195, v195, v207\n"
"v_lshlrev_b32 v204, 16, v202\n"
"v_and_b32 v205, 0xffff0000, v202\n"
"v_add_f32 v196, v196, v204\n"
"v_add_f32 v197, v197, v205\n"
"v_lshlrev_b32 v206, 16, v203\n"
"v_and_b32 v207, 0xffff0000, v203\n"
"v_add_f32 v198, v198, v206\n"
"v_add_f32 v199, v199, v207\n"
"s_waitcnt vmcnt(8)\n"
"v_lshlrev_b32 v212, 16, v208\n"
"v_and_b32 v213, 0xffff0000, v208\n"
"v_add_f32 v192, v192, v212\n"
"v_add_f32 v193, v193, v213\n"
"v_lshlrev_b32 v214, 16, v209\n"
"v_and_b32 v215, 0xffff0000, v209\n"
"v_add_f32 v194, v194, v214\n"
"v_add_f32 v195, v195, v215\n"
"v_lshlrev_b32 v212, 16, v210\n"
"v_and_b32 v213, 0xffff0000, v210\n"
"v_add_f32 v196, v196, v212\n"
"v_add_f32 v197, v197, v213\n"
"v_lshlrev_b32 v214, 16, v211\n"
"v_and_b32 v215, 0xffff0000, v211\n"
"v_add_f32 v198, v198, v214\n"
"v_add_f32 v199, v199, v215\n"
"s_waitcnt vmcnt(7)\n"
"v_lshlrev_b32 v220, 16, v216\n"
"v_and_b32 v221, 0xffff0000, v216\n"
"v_add_f32 v192, v192, v220\n"
"v_add_f32 v193, v193, v221\n"
"v_lshlrev_b32 v222, 16, v217\n"
"v_and_b32 v223, 0xffff0000, v217\n"
"v_add_f32 v194, v194, v222\n"
"v_add_f32 v195, v195, v223\n"
"v_lshlrev_b32 v220, 16, v218\n"
"v_and_b32 v221, 0xffff0000, v218\n"
"v_add_f32 v196, v196, v220\n"
"v_add_f32 v197, v197, v221\n"
"v_lshlrev_b32 v222, 16, v219\n"
"v_and_b32 v223, 0xffff0000, v219\n"
"v_add_f32 v198, v198, v222\n"
"v_add_f32 v199, v199, v223\n"
"v_cvt_pk_bf16_f32 v192, v192, v193\n"
"v_cvt_pk_bf16_f32 v193, v194, v195\n"
"v_cvt_pk_bf16_f32 v194, v196, v197\n"
"v_cvt_pk_bf16_f32 v195, v198, v199\n"
"s_mov_b32 s63, 65536\n"
"buffer_store_dwordx2 v[192:193], v5, s[48:51], s63 offen offset:0\n"
"buffer_store_dwordx2 v[194:195], v5, s[48:51], s63 offen offset:32\n"
"s_waitcnt vmcnt(8)\n"
"v_lshlrev_b32 v236, 16, v232\n"
"v_and_b32 v237, 0xffff0000, v232\n"
"v_add_f32 v224, v224, v236\n"
"v_add_f32 v225, v225, v237\n"
"v_lshlrev_b32 v238, 16, v233\n"
"v_and_b32 v239, 0xffff0000, v233\n"
"v_add_f32 v226, v226, v238\n"
"v_add_f32 v227, v227, v239\n"
"v_lshlrev_b32 v236, 16, v234\n"
"v_and_b32 v237, 0xffff0000, v234\n"
"v_add_f32 v228, v228, v236\n"
"v_add_f32 v229, v229, v237\n"
"v_lshlrev_b32 v238, 16, v235\n"
"v_and_b32 v239, 0xffff0000, v235\n"
"v_add_f32 v230, v230, v238\n"
"v_add_f32 v231, v231, v239\n"
"s_waitcnt vmcnt(7)\n"
"v_lshlrev_b32 v244, 16, v240\n"
"v_and_b32 v245, 0xffff0000, v240\n"
"v_add_f32 v224, v224, v244\n"
"v_add_f32 v225, v225, v245\n"
"v_lshlrev_b32 v246, 16, v241\n"
"v_and_b32 v247, 0xffff0000, v241\n"
"v_add_f32 v226, v226, v246\n"
"v_add_f32 v227, v227, v247\n"
"v_lshlrev_b32 v244, 16, v242\n"
"v_and_b32 v245, 0xffff0000, v242\n"
"v_add_f32 v228, v228, v244\n"
"v_add_f32 v229, v229, v245\n"
"v_lshlrev_b32 v246, 16, v243\n"
"v_and_b32 v247, 0xffff0000, v243\n"
"v_add_f32 v230, v230, v246\n"
"v_add_f32 v231, v231, v247\n"
"s_waitcnt vmcnt(6)\n"
"v_lshlrev_b32 v252, 16, v248\n"
"v_and_b32 v253, 0xffff0000, v248\n"
"v_add_f32 v224, v224, v252\n"
"v_add_f32 v225, v225, v253\n"
"v_lshlrev_b32 v254, 16, v249\n"
"v_and_b32 v255, 0xffff0000, v249\n"
"v_add_f32 v226, v226, v254\n"
"v_add_f32 v227, v227, v255\n"
"v_lshlrev_b32 v252, 16, v250\n"
"v_and_b32 v253, 0xffff0000, v250\n"
"v_add_f32 v228, v228, v252\n"
"v_add_f32 v229, v229, v253\n"
"v_lshlrev_b32 v254, 16, v251\n"
"v_and_b32 v255, 0xffff0000, v251\n"
"v_add_f32 v230, v230, v254\n"
"v_add_f32 v231, v231, v255\n"
"v_cvt_pk_bf16_f32 v224, v224, v225\n"
"v_cvt_pk_bf16_f32 v225, v226, v227\n"
"v_cvt_pk_bf16_f32 v226, v228, v229\n"
"v_cvt_pk_bf16_f32 v227, v230, v231\n"
"s_mov_b32 s63, 98304\n"
"buffer_store_dwordx2 v[224:225], v5, s[48:51], s63 offen offset:0\n"
"buffer_store_dwordx2 v[226:227], v5, s[48:51], s63 offen offset:32\n"
"s_branch .Lza_%=\n"
".Ls0a_%=:\n"
"s_mov_b32 s64, 0x200000\n"
".Lpaa_%=:\n"
"buffer_load_dwordx2 v[22:23], v13, s[36:39], %[ma] offen offset:0 sc1\n"
"s_waitcnt vmcnt(0)\n"
"v_cmp_ne_u32 vcc, v22, v17\n"
"s_nop 1\n"
"s_cmp_eq_u64 vcc, 0\n"
"s_cbranch_scc1 .Leaa_%=\n"
"v_mov_b32 v17, v22\n"
"s_mov_b64 exec, 15\n"
"buffer_store_dwordx2 v[16:17], v8, s[36:39], %[mw] offen offset:0 sc1\n"
"s_mov_b64 exec, -1\n"
".Leaa_%=:\n"
"v_cmp_ne_u32 vcc, v23, v16\n"
"s_nop 1\n"
"s_cmp_eq_u64 vcc, 0\n"
"s_cbranch_scc1 .Lqaa_%=\n"
"s_sleep 1\n"
"s_sub_u32 s64, s64, 1\n"
"s_cmp_lg_u32 s64, 0\n"
"s_cbranch_scc1 .Lpaa_%=\n"
".Lqaa_%=:\n"
"s_waitcnt vmcnt(0)\n"
"s_branch .Lr0a_%=\n"
".Ls1a_%=:\n"
"s_mov_b32 s64, 0x200000\n"
".Lpca_%=:\n"
"buffer_load_dwordx2 v[22:23], v13, s[36:39], %[ma] offen offset:128 sc1\n"
"s_waitcnt vmcnt(0)\n"
"v_cmp_ne_u32 vcc, v22, v19\n"
"s_nop 1\n"
"s_cmp_eq_u64 vcc, 0\n"
"s_cbranch_scc1 .Leca_%=\n"
"v_mov_b32 v19, v22\n"
"s_mov_b64 exec, 15\n"
"buffer_store_dwordx2 v[18:19], v8, s[36:39], %[mw] offen offset:128 sc1\n"
"s_mov_b64 exec, -1\n"
".Leca_%=:\n"
"v_cmp_ne_u32 vcc, v23, v18\n"
"s_nop 1\n"
"s_cmp_eq_u64 vcc, 0\n"
"s_cbranch_scc1 .Lqca_%=\n"
"s_sleep 1\n"
"s_sub_u32 s64, s64, 1\n"
"s_cmp_lg_u32 s64, 0\n"
"s_cbranch_scc1 .Lpca_%=\n"
".Lqca_%=:\n"
"s_waitcnt vmcnt(0)\n"
"s_branch .Lr1a_%=\n"
".Lza_%=:\n"
"s_waitcnt vmcnt(0) lgkmcnt(0)\n"
"s_branch .Lend_%=\n"
".Lw3_%=:\n"
"s_mov_b32 s36, %[xl]\n"
"s_and_b32 s37, %[xh], 0xffff\n"
"s_mov_b32 s38, 245760\n"
"s_mov_b32 s39, 0x20000\n"
"s_mov_b32 s40, %[wl]\n"
"s_and_b32 s41, %[wh], 0xffff\n"
"s_mov_b32 s42, 4186624\n"
"s_mov_b32 s43, 0x20000\n"
"s_mov_b32 s44, %[kl]\n"
"s_and_b32 s45, %[kh], 0xffff\n"
"s_mov_b32 s46, 262144\n"
"s_mov_b32 s47, 0x20000\n"
"s_mov_b32 s48, %[vl]\n"
"s_and_b32 s49, %[vh], 0xffff\n"
"s_mov_b32 s50, 262144\n"
"s_mov_b32 s51, 0x20000\n"
"s_add_u32 s52, %[wsl], %[oof]\n"
"s_addc_u32 s53, %[wsh], 0\n"
"s_and_b32 s53, s53, 0xffff\n"
"s_mov_b32 s54, 1843200\n"
"s_mov_b32 s55, 0x20000\n"
"v_mbcnt_lo_u32_b32 v20, -1, 0\n"
"v_mbcnt_hi_u32_b32 v20, -1, v20\n"
"s_lshl_b32 s77, %[wv], 12\n"
"v_and_b32 v21, 15, v20\n"
"v_lshrrev_b32 v22, 4, v20\n"
"v_bfe_u32 v23, v20, 1, 2\n"
"v_bfe_u32 v24, v20, 3, 1\n"
"v_and_b32 v25, 1, v20\n"
"v_lshl_add_u32 v26, v22, 1, v24\n"
"v_lshlrev_b32 v26, 5, v26\n"
"v_lshl_add_u32 v26, v25, 4, v26\n"
"s_lshl_b32 s74, %[wv], 17\n"
"v_lshl_add_u32 v12, v23, 13, v26\n"
"v_add_u32 v12, s74, v12\n"
"v_xor_b32 v13, 32, v12\n"
"v_lshrrev_b32 v27, 3, v20\n"
"v_and_b32 v28, 7, v20\n"
"v_xor_b32 v28, v28, v22\n"
"v_lshlrev_b32 v28, 4, v28\n"
"s_lshl_b32 s74, %[wv], 16\n"
"v_lshl_add_u32 v10, v27, 10, v28\n"
"v_add_u32 v10, s74, v10\n"
"v_xor_b32 v11, 64, v10\n"
"s_mov_b32 s56, 39808\n"
"s_mov_b32 s57, 0xfff97400\n"
"s_add_u32 s58, s77, 0x8000\n"
"s_lshl_b32 s59, %[wv], 13\n"
"s_add_u32 s59, s59, 131072\n"
"s_add_u32 m0, s58, 0\n"
"s_add_u32 s57, s57, 0x68c00\n"
"buffer_load_dwordx4 v12, s[40:43], s57 offen offset:0 nt lds\n"
"s_add_u32 m0, s58, 0\n"
"s_add_u32 s57, s57, 0x7c00\n"
"buffer_load_dwordx4 v12, s[40:43], s57 offen offset:1024 nt lds\n"
"s_add_u32 m0, s58, 0\n"
"s_add_u32 s57, s57, 0x7c00\n"
"buffer_load_dwordx4 v13, s[40:43], s57 offen offset:2048 nt lds\n"
"s_add_u32 m0, s58, 0\n"
"s_add_u32 s57, s57, 0x7c00\n"
"buffer_load_dwordx4 v13, s[40:43], s57 offen offset:3072 nt lds\n"
"s_add_u32 m0, s58, 16128\n"
"s_add_u32 s57, s57, 0xfffe8c00\n"
"buffer_load_dwordx4 v12, s[40:43], s57 offen offset:256 nt lds\n"
"s_add_u32 m0, s58, 16128\n"
"s_add_u32 s57, s57, 0x7c00\n"
"buffer_load_dwordx4 v12, s[40:43], s57 offen offset:1280 nt lds\n"
"s_add_u32 m0, s58, 16128\n"
"s_add_u32 s57, s57, 0x7c00\n"
"buffer_load_dwordx4 v13, s[40:43], s57 offen offset:2304 nt lds\n"
"s_add_u32 m0, s58, 16128\n"
"s_add_u32 s57, s57, 0x7c00\n"
"buffer_load_dwordx4 v13, s[40:43], s57 offen offset:3328 nt lds\n"
"s_xor_b32 s58, s58, 0x10000\n"
"s_add_u32 m0, s59, 0\n"
"s_add_u32 s56, s56, 0xffff6480\n"
"buffer_load_dwordx4 v10, s[36:39], s56 offen offset:0 lds\n"
"s_add_u32 m0, s59, 0\n"
"s_add_u32 s56, s56, 0x1c00\n"
"buffer_load_dwordx4 v11, s[36:39], s56 offen offset:1024 lds\n"
"s_add_u32 m0, s59, 0\n"
"s_add_u32 s56, s56, 0x1c00\n"
"buffer_load_dwordx4 v10, s[36:39], s56 offen offset:2048 lds\n"
"s_add_u32 m0, s59, 0\n"
"s_add_u32 s56, s56, 0x1c00\n"
"buffer_load_dwordx4 v11, s[36:39], s56 offen offset:3072 lds\n"
"s_add_u32 m0, s59, 4096\n"
"s_add_u32 s56, s56, 0x2c00\n"
"buffer_load_dwordx4 v10, s[36:39], s56 offen offset:0 lds\n"
"s_add_u32 m0, s59, 4096\n"
"s_add_u32 s56, s56, 0x1c00\n"
"buffer_load_dwordx4 v11, s[36:39], s56 offen offset:1024 lds\n"
"s_add_u32 m0, s58, 0\n"
"s_add_u32 s57, s57, 0x68c00\n"
"buffer_load_dwordx4 v12, s[40:43], s57 offen offset:0 nt lds\n"
"s_add_u32 m0, s58, 0\n"
"s_add_u32 s57, s57, 0x7c00\n"
"buffer_load_dwordx4 v12, s[40:43], s57 offen offset:1024 nt lds\n"
"s_add_u32 m0, s58, 0\n"
"s_add_u32 s57, s57, 0x7c00\n"
"buffer_load_dwordx4 v13, s[40:43], s57 offen offset:2048 nt lds\n"
"s_add_u32 m0, s58, 0\n"
"s_add_u32 s57, s57, 0x7c00\n"
"buffer_load_dwordx4 v13, s[40:43], s57 offen offset:3072 nt lds\n"
"s_add_u32 m0, s58, 16128\n"
"s_add_u32 s57, s57, 0xfffe8c00\n"
"buffer_load_dwordx4 v12, s[40:43], s57 offen offset:256 nt lds\n"
"s_add_u32 m0, s58, 16128\n"
"s_add_u32 s57, s57, 0x7c00\n"
"buffer_load_dwordx4 v12, s[40:43], s57 offen offset:1280 nt lds\n"
"s_add_u32 m0, s58, 16128\n"
"s_add_u32 s57, s57, 0x7c00\n"
"buffer_load_dwordx4 v13, s[40:43], s57 offen offset:2304 nt lds\n"
"s_add_u32 m0, s58, 16128\n"
"s_add_u32 s57, s57, 0x7c00\n"
"buffer_load_dwordx4 v13, s[40:43], s57 offen offset:3328 nt lds\n"
"s_xor_b32 s58, s58, 0x10000\n"
"v_lshl_add_u32 v4, v23, 8, v26\n"
"v_add_u32 v4, s77, v4\n"
"v_xor_b32 v5, 32, v4\n"
"v_xor_b32 v29, v21, v22\n"
"v_lshlrev_b32 v29, 4, v29\n"
"v_lshl_add_u32 v0, v22, 8, v29\n"
"v_add_u32 v0, s77, v0\n"
"v_xor_b32 v1, 64, v0\n"
"v_xor_b32 v2, 128, v0\n"
"v_xor_b32 v3, 192, v0\n"
"v_lshl_add_u32 v15, v21, 8, v29\n"
"v_lshlrev_b32 v14, 10, v22\n"
"v_lshrrev_b32 v30, 1, v21\n"
"v_xor_b32 v30, v30, v22\n"
"v_lshlrev_b32 v30, 4, v30\n"
"v_lshl_add_u32 v6, v21, 7, v30\n"
"v_add_u32 v6, s59, v6\n"
"v_xor_b32 v7, 64, v6\n"
"v_lshrrev_b32 v30, 2, v21\n"
"v_and_b32 v31, 1, v22\n"
"v_lshl_add_u32 v30, v31, 2, v30\n"
"v_lshlrev_b32 v30, 5, v30\n"
"v_and_b32 v31, 3, v20\n"
"v_lshl_add_u32 v30, v31, 3, v30\n"
"v_lshl_add_u32 v8, v22, 11, v30\n"
"v_add_u32 v8, 0x8000, v8\n"
"v_xor_b32 v9, 0x80, v8\n"
"s_mov_b32 s60, 0xc000\n"
"s_mov_b32 s61, 0x8000\n"
"s_add_u32 s62, s77, 32768\n"
"s_add_u32 s63, s77, 114688\n"
"s_mov_b32 s64, 0x4000\n"
"s_mov_b32 s65, 0x8000\n"
"s_mov_b32 s67, 0x53800000\n"
"s_mov_b32 s68, 0x3e0293ee\n"
"s_mov_b32 s69, 0x3e0293ee\n"
"s_mov_b64 s[70:71], 0\n"
"s_mov_b32 s75, 0\n"
"s_mov_b32 s76, s77\n"
"s_mov_b32 s66, 3\n"
"v_mov_b32 v16, 0\n"
"v_mov_b32 v17, 0\n"
"v_mov_b32 v18, 0\n"
"v_mov_b32 v19, 0\n"
"v_mov_b32 v20, 0\n"
"v_mov_b32 v21, 0\n"
"v_mov_b32 v22, 0\n"
"v_mov_b32 v23, 0\n"
"v_mov_b32 v24, 0\n"
"v_mov_b32 v25, 0\n"
"v_mov_b32 v26, 0\n"
"v_mov_b32 v27, 0\n"
"v_mov_b32 v28, 0\n"
"v_mov_b32 v29, 0\n"
"v_mov_b32 v30, 0\n"
"v_mov_b32 v31, 0\n"
"v_mov_b32 v32, 0\n"
"v_mov_b32 v33, 0\n"
"v_mov_b32 v34, 0\n"
"v_mov_b32 v35, 0\n"
"v_mov_b32 v36, 0\n"
"v_mov_b32 v37, 0\n"
"v_mov_b32 v38, 0\n"
"v_mov_b32 v39, 0\n"
"v_mov_b32 v40, 0\n"
"v_mov_b32 v41, 0\n"
"v_mov_b32 v42, 0\n"
"v_mov_b32 v43, 0\n"
"v_mov_b32 v44, 0\n"
"v_mov_b32 v45, 0\n"
"v_mov_b32 v46, 0\n"
"v_mov_b32 v47, 0\n"
"v_mov_b32 v48, 0\n"
"v_mov_b32 v49, 0\n"
"v_mov_b32 v50, 0\n"
"v_mov_b32 v51, 0\n"
"v_mov_b32 v52, 0\n"
"v_mov_b32 v53, 0\n"
"v_mov_b32 v54, 0\n"
"v_mov_b32 v55, 0\n"
"v_mov_b32 v56, 0\n"
"v_mov_b32 v57, 0\n"
"v_mov_b32 v58, 0\n"
"v_mov_b32 v59, 0\n"
"v_mov_b32 v60, 0\n"
"v_mov_b32 v61, 0\n"
"v_mov_b32 v62, 0\n"
"v_mov_b32 v63, 0\n"
"v_mov_b32 v64, 0\n"
"v_mov_b32 v65, 0\n"
"v_mov_b32 v66, 0\n"
"v_mov_b32 v67, 0\n"
"v_mov_b32 v68, 0\n"
"v_mov_b32 v69, 0\n"
"v_mov_b32 v70, 0\n"
"v_mov_b32 v71, 0\n"
"v_mov_b32 v72, 0\n"
"v_mov_b32 v73, 0\n"
"v_mov_b32 v74, 0\n"
"v_mov_b32 v75, 0\n"
"v_mov_b32 v76, 0\n"
"v_mov_b32 v77, 0\n"
"v_mov_b32 v78, 0\n"
"v_mov_b32 v79, 0\n"
"v_mov_b32 v80, 0\n"
"v_mov_b32 v81, 0\n"
"v_mov_b32 v82, 0\n"
"v_mov_b32 v83, 0\n"
"v_mov_b32 v84, 0\n"
"v_mov_b32 v85, 0\n"
"v_mov_b32 v86, 0\n"
"v_mov_b32 v87, 0\n"
"v_mov_b32 v88, 0\n"
"v_mov_b32 v89, 0\n"
"v_mov_b32 v90, 0\n"
"v_mov_b32 v91, 0\n"
"v_mov_b32 v92, 0\n"
"v_mov_b32 v93, 0\n"
"v_mov_b32 v94, 0\n"
"v_mov_b32 v95, 0\n"
"v_mov_b32 v96, 0\n"
"v_mov_b32 v97, 0\n"
"v_mov_b32 v98, 0\n"
"v_mov_b32 v99, 0\n"
"v_mov_b32 v100, 0\n"
"v_mov_b32 v101, 0\n"
"v_mov_b32 v102, 0\n"
"v_mov_b32 v103, 0\n"
"v_mov_b32 v104, 0\n"
"v_mov_b32 v105, 0\n"
"v_mov_b32 v106, 0\n"
"v_mov_b32 v107, 0\n"
"v_mov_b32 v108, 0\n"
"v_mov_b32 v109, 0\n"
"v_mov_b32 v110, 0\n"
"v_mov_b32 v111, 0\n"
"v_mov_b32 v112, 0\n"
"v_mov_b32 v113, 0\n"
"v_mov_b32 v114, 0\n"
"v_mov_b32 v115, 0\n"
"v_mov_b32 v116, 0\n"
"v_mov_b32 v117, 0\n"
"v_mov_b32 v118, 0\n"
"v_mov_b32 v119, 0\n"
"v_mov_b32 v120, 0\n"
"v_mov_b32 v121, 0\n"
"v_mov_b32 v122, 0\n"
"v_mov_b32 v123, 0\n"
"v_mov_b32 v124, 0\n"
"v_mov_b32 v125, 0\n"
"v_mov_b32 v126, 0\n"
"v_mov_b32 v127, 0\n"
"v_mov_b32 v128, 0\n"
"v_mov_b32 v129, 0\n"
"v_mov_b32 v130, 0\n"
"v_mov_b32 v131, 0\n"
"v_mov_b32 v132, 0\n"
"v_mov_b32 v133, 0\n"
"v_mov_b32 v134, 0\n"
"v_mov_b32 v135, 0\n"
"v_mov_b32 v136, 0\n"
"v_mov_b32 v137, 0\n"
"v_mov_b32 v138, 0\n"
"v_mov_b32 v139, 0\n"
"v_mov_b32 v140, 0\n"
"v_mov_b32 v141, 0\n"
"v_mov_b32 v142, 0\n"
"v_mov_b32 v143, 0\n"
"v_mov_b32 v144, 0\n"
"v_mov_b32 v145, 0\n"
"v_mov_b32 v146, 0\n"
"v_mov_b32 v147, 0\n"
"v_mov_b32 v148, 0\n"
"v_mov_b32 v149, 0\n"
"v_mov_b32 v150, 0\n"
"v_mov_b32 v151, 0\n"
"v_mov_b32 v152, 0\n"
"v_mov_b32 v153, 0\n"
"v_mov_b32 v154, 0\n"
"v_mov_b32 v155, 0\n"
"v_mov_b32 v156, 0\n"
"v_mov_b32 v157, 0\n"
"v_mov_b32 v158, 0\n"
"v_mov_b32 v159, 0\n"
"v_mov_b32 v160, 0\n"
"v_mov_b32 v161, 0\n"
"v_mov_b32 v162, 0\n"
"v_mov_b32 v163, 0\n"
"v_mov_b32 v164, 0\n"
"v_mov_b32 v165, 0\n"
"v_mov_b32 v166, 0\n"
"v_mov_b32 v167, 0\n"
"v_mov_b32 v168, 0\n"
"v_mov_b32 v169, 0\n"
"v_mov_b32 v170, 0\n"
"v_mov_b32 v171, 0\n"
"v_mov_b32 v172, 0\n"
"v_mov_b32 v173, 0\n"
"v_mov_b32 v174, 0\n"
"v_mov_b32 v175, 0\n"
"v_mov_b32 v176, 0\n"
"v_mov_b32 v177, 0\n"
"v_mov_b32 v178, 0\n"
"v_mov_b32 v179, 0\n"
"v_mov_b32 v180, 0\n"
"v_mov_b32 v181, 0\n"
"v_mov_b32 v182, 0\n"
"v_mov_b32 v183, 0\n"
"v_mov_b32 v184, 0\n"
"v_mov_b32 v185, 0\n"
"v_mov_b32 v186, 0\n"
"v_mov_b32 v187, 0\n"
"v_mov_b32 v188, 0\n"
"v_mov_b32 v189, 0\n"
"v_mov_b32 v190, 0\n"
"v_mov_b32 v191, 0\n"
"v_mov_b32 v192, 0\n"
"v_mov_b32 v193, 0\n"
"v_mov_b32 v194, 0\n"
"v_mov_b32 v195, 0\n"
"v_mov_b32 v196, 0\n"
"v_mov_b32 v197, 0\n"
"v_mov_b32 v198, 0\n"
"v_mov_b32 v199, 0\n"
"v_mov_b32 v200, 0\n"
"v_mov_b32 v201, 0\n"
"v_mov_b32 v202, 0\n"
"v_mov_b32 v203, 0\n"
"v_mov_b32 v204, 0\n"
"v_mov_b32 v205, 0\n"
"v_mov_b32 v206, 0\n"
"v_mov_b32 v207, 0\n"
"v_mov_b32 v208, 0\n"
"v_mov_b32 v209, 0\n"
"v_mov_b32 v210, 0\n"
"v_mov_b32 v211, 0\n"
"v_mov_b32 v212, 0\n"
"v_mov_b32 v213, 0\n"
"v_mov_b32 v214, 0\n"
"v_mov_b32 v215, 0\n"
"v_mov_b32 v216, 0\n"
"v_mov_b32 v217, 0\n"
"v_mov_b32 v218, 0\n"
"v_mov_b32 v219, 0\n"
"v_mov_b32 v220, 0\n"
"v_mov_b32 v221, 0\n"
"v_mov_b32 v222, 0\n"
"v_mov_b32 v223, 0\n"
"v_mov_b32 v224, 0\n"
"v_mov_b32 v225, 0\n"
"v_mov_b32 v226, 0\n"
"v_mov_b32 v227, 0\n"
"v_mov_b32 v228, 0\n"
"v_mov_b32 v229, 0\n"
"v_mov_b32 v230, 0\n"
"v_mov_b32 v231, 0\n"
"v_mov_b32 v232, 0\n"
"v_mov_b32 v233, 0\n"
"v_mov_b32 v234, 0\n"
"v_mov_b32 v235, 0\n"
"v_mov_b32 v236, 0\n"
"v_mov_b32 v237, 0\n"
"v_mov_b32 v238, 0\n"
"v_mov_b32 v239, 0\n"
"v_mov_b32 v240, 0\n"
"v_mov_b32 v241, 0\n"
"v_mov_b32 v242, 0\n"
"v_mov_b32 v243, 0\n"
"v_mov_b32 v244, 0\n"
"v_mov_b32 v245, 0\n"
"v_mov_b32 v246, 0\n"
"v_mov_b32 v247, 0\n"
"v_mov_b32 v248, 0\n"
"v_mov_b32 v249, 0\n"
"v_mov_b32 v250, 0\n"
"v_mov_b32 v251, 0\n"
"v_mov_b32 v252, 0\n"
"v_mov_b32 v253, 0\n"
"v_mov_b32 v254, 0\n"
"v_mov_b32 v255, 0\n"
"v_accvgpr_write_b32 a204, 0\n"
"v_accvgpr_write_b32 a205, 0\n"
"v_accvgpr_write_b32 a206, 0\n"
"v_accvgpr_write_b32 a207, 0\n"
"v_accvgpr_write_b32 a220, 0\n"
"v_accvgpr_write_b32 a221, 0\n"
"v_accvgpr_write_b32 a222, 0\n"
"v_accvgpr_write_b32 a223, 0\n"
"v_accvgpr_write_b32 a236, 0\n"
"v_accvgpr_write_b32 a237, 0\n"
"v_accvgpr_write_b32 a238, 0\n"
"v_accvgpr_write_b32 a239, 0\n"
"s_waitcnt lgkmcnt(0)\n"
"s_waitcnt vmcnt(14)\n"
"s_barrier\n"
"ds_read_b64_tr_b16 a[64:65], v8 offset:16384\n"
"ds_read_b64_tr_b16 a[66:67], v8 offset:17408\n"
"ds_read_b64_tr_b16 a[68:69], v8 offset:24576\n"
"ds_read_b64_tr_b16 a[70:71], v8 offset:25600\n"
"ds_read_b64_tr_b16 a[72:73], v9 offset:16384\n"
"ds_read_b64_tr_b16 a[74:75], v9 offset:17408\n"
"ds_read_b64_tr_b16 a[76:77], v9 offset:24576\n"
"ds_read_b64_tr_b16 a[78:79], v9 offset:25600\n"
"ds_read_b64_tr_b16 a[80:81], v8 offset:16640\n"
"ds_read_b64_tr_b16 a[82:83], v8 offset:17664\n"
"ds_read_b64_tr_b16 a[84:85], v8 offset:24832\n"
"ds_read_b64_tr_b16 a[86:87], v8 offset:25856\n"
"ds_read_b64_tr_b16 a[88:89], v9 offset:16640\n"
"ds_read_b64_tr_b16 a[90:91], v9 offset:17664\n"
"ds_read_b64_tr_b16 a[92:93], v9 offset:24832\n"
"ds_read_b64_tr_b16 a[94:95], v9 offset:25856\n"
"ds_read_b64_tr_b16 a[96:97], v8 offset:16896\n"
"ds_read_b64_tr_b16 a[98:99], v8 offset:17920\n"
"ds_read_b64_tr_b16 a[100:101], v8 offset:25088\n"
"ds_read_b64_tr_b16 a[102:103], v8 offset:26112\n"
"ds_read_b64_tr_b16 a[104:105], v9 offset:16896\n"
"ds_read_b64_tr_b16 a[106:107], v9 offset:17920\n"
"s_waitcnt vmcnt(8)\n"
"ds_read_b128 a[0:3], v6 offset:0\n"
"ds_read_b128 a[4:7], v7 offset:0\n"
"ds_read_b128 a[8:11], v6 offset:2048\n"
"ds_read_b128 a[12:15], v7 offset:2048\n"
"ds_read_b128 a[16:19], v6 offset:4096\n"
"ds_read_b128 a[20:23], v7 offset:4096\n"
"ds_read_b64_tr_b16 a[108:109], v9 offset:25088\n"
"ds_read_b64_tr_b16 a[110:111], v9 offset:26112\n"
"ds_read_b64_tr_b16 a[112:113], v8 offset:17152\n"
"ds_read_b64_tr_b16 a[114:115], v8 offset:18176\n"
"ds_read_b64_tr_b16 a[116:117], v8 offset:25344\n"
"ds_read_b64_tr_b16 a[118:119], v8 offset:26368\n"
"ds_read_b64_tr_b16 a[120:121], v9 offset:17152\n"
"ds_read_b64_tr_b16 a[122:123], v9 offset:18176\n"
".LQb_%=:\n"
"s_waitcnt lgkmcnt(13)\n"
"v_mfma_f32_16x16x32_bf16 v[16:19], a[64:67], a[0:3], v[16:19]\n"
"ds_read_b64_tr_b16 a[124:125], v9 offset:25344\n"
"ds_read_b64_tr_b16 a[126:127], v9 offset:26368\n"
"s_waitcnt lgkmcnt(14)\n"
"v_mfma_f32_16x16x32_bf16 v[16:19], a[68:71], a[4:7], v[16:19]\n"
"s_add_u32 m0, s59, 0\n"
"s_add_u32 s56, s56, 0xffff6480\n"
"s_waitcnt lgkmcnt(10)\n"
"buffer_load_dwordx4 v10, s[36:39], s56 offen offset:0 lds\n"
"v_mfma_f32_16x16x32_bf16 v[44:47], a[64:67], a[8:11], v[44:47]\n"
"v_mfma_f32_16x16x32_bf16 v[44:47], a[68:71], a[12:15], v[44:47]\n"
"v_mfma_f32_16x16x32_bf16 v[72:75], a[64:67], a[16:19], v[72:75]\n"
"ds_read_b64_tr_b16 a[64:65], v8 offset:0\n"
"ds_read_b64_tr_b16 a[66:67], v8 offset:1024\n"
"v_mfma_f32_16x16x32_bf16 v[72:75], a[68:71], a[20:23], v[72:75]\n"
"s_add_u32 m0, s76, 0\n"
"s_nop 0\n"
"buffer_load_dwordx4 v0, s[44:47], s75 offen offset:0 nt lds\n"
"v_mfma_f32_16x16x32_bf16 v[20:23], a[72:75], a[0:3], v[20:23]\n"
"ds_read_b64_tr_b16 a[68:69], v8 offset:8192\n"
"ds_read_b64_tr_b16 a[70:71], v8 offset:9216\n"
"v_mfma_f32_16x16x32_bf16 v[20:23], a[76:79], a[4:7], v[20:23]\n"
"v_mfma_f32_16x16x32_bf16 v[48:51], a[72:75], a[8:11], v[48:51]\n"
"v_mfma_f32_16x16x32_bf16 v[48:51], a[76:79], a[12:15], v[48:51]\n"
"s_add_u32 m0, s59, 0\n"
"s_add_u32 s56, s56, 0x1c00\n"
"buffer_load_dwordx4 v11, s[36:39], s56 offen offset:1024 lds\n"
"v_mfma_f32_16x16x32_bf16 v[76:79], a[72:75], a[16:19], v[76:79]\n"
"ds_read_b64_tr_b16 a[72:73], v9 offset:0\n"
"ds_read_b64_tr_b16 a[74:75], v9 offset:1024\n"
"v_mfma_f32_16x16x32_bf16 v[76:79], a[76:79], a[20:23], v[76:79]\n"
"ds_read_b64_tr_b16 a[76:77], v9 offset:8192\n"
"ds_read_b64_tr_b16 a[78:79], v9 offset:9216\n"
"v_mfma_f32_16x16x32_bf16 v[24:27], a[80:83], a[0:3], v[24:27]\n"
"v_mfma_f32_16x16x32_bf16 v[24:27], a[84:87], a[4:7], v[24:27]\n"
"v_mfma_f32_16x16x32_bf16 v[52:55], a[80:83], a[8:11], v[52:55]\n"
"v_mfma_f32_16x16x32_bf16 v[52:55], a[84:87], a[12:15], v[52:55]\n"
"v_mfma_f32_16x16x32_bf16 v[80:83], a[80:83], a[16:19], v[80:83]\n"
"ds_read_b64_tr_b16 a[80:81], v8 offset:256\n"
"ds_read_b64_tr_b16 a[82:83], v8 offset:1280\n"
"v_mfma_f32_16x16x32_bf16 v[80:83], a[84:87], a[20:23], v[80:83]\n"
"s_add_u32 m0, s59, 0\n"
"s_add_u32 s56, s56, 0x1c00\n"
"buffer_load_dwordx4 v10, s[36:39], s56 offen offset:2048 lds\n"
"v_mfma_f32_16x16x32_bf16 v[28:31], a[88:91], a[0:3], v[28:31]\n"
"s_add_u32 m0, s76, 0\n"
"s_nop 0\n"
"buffer_load_dwordx4 v1, s[44:47], s75 offen offset:1024 nt lds\n"
"v_mfma_f32_16x16x32_bf16 v[28:31], a[92:95], a[4:7], v[28:31]\n"
"ds_read_b64_tr_b16 a[84:85], v8 offset:8448\n"
"ds_read_b64_tr_b16 a[86:87], v8 offset:9472\n"
"v_mfma_f32_16x16x32_bf16 v[56:59], a[88:91], a[8:11], v[56:59]\n"
"v_mfma_f32_16x16x32_bf16 v[56:59], a[92:95], a[12:15], v[56:59]\n"
"v_mfma_f32_16x16x32_bf16 v[84:87], a[88:91], a[16:19], v[84:87]\n"
"ds_read_b64_tr_b16 a[88:89], v9 offset:256\n"
"ds_read_b64_tr_b16 a[90:91], v9 offset:1280\n"
"v_mfma_f32_16x16x32_bf16 v[84:87], a[92:95], a[20:23], v[84:87]\n"
"ds_read_b64_tr_b16 a[92:93], v9 offset:8448\n"
"ds_read_b64_tr_b16 a[94:95], v9 offset:9472\n"
"v_mfma_f32_16x16x32_bf16 v[32:35], a[96:99], a[0:3], v[32:35]\n"
"v_mfma_f32_16x16x32_bf16 v[32:35], a[100:103], a[4:7], v[32:35]\n"
"s_add_u32 m0, s59, 0\n"
"s_add_u32 s56, s56, 0x1c00\n"
"buffer_load_dwordx4 v11, s[36:39], s56 offen offset:3072 lds\n"
"v_mfma_f32_16x16x32_bf16 v[60:63], a[96:99], a[8:11], v[60:63]\n"
"v_mfma_f32_16x16x32_bf16 v[60:63], a[100:103], a[12:15], v[60:63]\n"
"v_mfma_f32_16x16x32_bf16 v[88:91], a[96:99], a[16:19], v[88:91]\n"
"ds_read_b64_tr_b16 a[96:97], v8 offset:512\n"
"ds_read_b64_tr_b16 a[98:99], v8 offset:1536\n"
"v_mfma_f32_16x16x32_bf16 v[88:91], a[100:103], a[20:23], v[88:91]\n"
"s_add_u32 m0, s76, 0\n"
"s_nop 0\n"
"buffer_load_dwordx4 v2, s[44:47], s75 offen offset:2048 nt lds\n"
"v_mfma_f32_16x16x32_bf16 v[36:39], a[104:107], a[0:3], v[36:39]\n"
"ds_read_b64_tr_b16 a[100:101], v8 offset:8704\n"
"ds_read_b64_tr_b16 a[102:103], v8 offset:9728\n"
"s_waitcnt lgkmcnt(15)\n"
"v_mfma_f32_16x16x32_bf16 v[36:39], a[108:111], a[4:7], v[36:39]\n"
"v_mfma_f32_16x16x32_bf16 v[64:67], a[104:107], a[8:11], v[64:67]\n"
"v_mfma_f32_16x16x32_bf16 v[64:67], a[108:111], a[12:15], v[64:67]\n"
"s_add_u32 m0, s59, 4096\n"
"s_add_u32 s56, s56, 0x2c00\n"
"buffer_load_dwordx4 v10, s[36:39], s56 offen offset:0 lds\n"
"v_mfma_f32_16x16x32_bf16 v[92:95], a[104:107], a[16:19], v[92:95]\n"
"ds_read_b64_tr_b16 a[104:105], v9 offset:512\n"
"ds_read_b64_tr_b16 a[106:107], v9 offset:1536\n"
"v_mfma_f32_16x16x32_bf16 v[92:95], a[108:111], a[20:23], v[92:95]\n"
"ds_read_b64_tr_b16 a[108:109], v9 offset:8704\n"
"ds_read_b64_tr_b16 a[110:111], v9 offset:9728\n"
"v_mfma_f32_16x16x32_bf16 v[40:43], a[112:115], a[0:3], v[40:43]\n"
"v_mfma_f32_16x16x32_bf16 v[40:43], a[116:119], a[4:7], v[40:43]\n"
"v_mfma_f32_16x16x32_bf16 v[68:71], a[112:115], a[8:11], v[68:71]\n"
"v_mfma_f32_16x16x32_bf16 v[68:71], a[116:119], a[12:15], v[68:71]\n"
"v_mfma_f32_16x16x32_bf16 v[96:99], a[112:115], a[16:19], v[96:99]\n"
"ds_read_b64_tr_b16 a[112:113], v8 offset:768\n"
"ds_read_b64_tr_b16 a[114:115], v8 offset:1792\n"
"v_mfma_f32_16x16x32_bf16 v[96:99], a[116:119], a[20:23], v[96:99]\n"
"s_add_u32 m0, s59, 4096\n"
"s_add_u32 s56, s56, 0x1c00\n"
"buffer_load_dwordx4 v11, s[36:39], s56 offen offset:1024 lds\n"
"v_mfma_f32_16x16x32_bf16 a[204:207], a[120:123], a[0:3], a[204:207]\n"
"s_add_u32 m0, s76, 0\n"
"s_nop 0\n"
"buffer_load_dwordx4 v3, s[44:47], s75 offen offset:3072 nt lds\n"
"v_mfma_f32_16x16x32_bf16 a[204:207], a[124:127], a[4:7], a[204:207]\n"
"ds_read_b64_tr_b16 a[116:117], v8 offset:8960\n"
"ds_read_b64_tr_b16 a[118:119], v8 offset:9984\n"
"v_mfma_f32_16x16x32_bf16 a[220:223], a[120:123], a[8:11], a[220:223]\n"
"v_mfma_f32_16x16x32_bf16 a[220:223], a[124:127], a[12:15], a[220:223]\n"
"v_mfma_f32_16x16x32_bf16 a[236:239], a[120:123], a[16:19], a[236:239]\n"
"ds_read_b64_tr_b16 a[120:121], v9 offset:768\n"
"ds_read_b64_tr_b16 a[122:123], v9 offset:1792\n"
"v_mfma_f32_16x16x32_bf16 a[236:239], a[124:127], a[20:23], a[236:239]\n"
"ds_read_b64_tr_b16 a[124:125], v9 offset:8960\n"
"ds_read_b64_tr_b16 a[126:127], v9 offset:9984\n"
"v_mfma_f32_16x16x32_bf16 v[128:131], a[64:67], a[0:3], v[128:131]\n"
"v_mfma_f32_16x16x32_bf16 v[128:131], a[68:71], a[4:7], v[128:131]\n"
"v_mfma_f32_16x16x32_bf16 v[160:163], a[64:67], a[8:11], v[160:163]\n"
"v_mfma_f32_16x16x32_bf16 v[160:163], a[68:71], a[12:15], v[160:163]\n"
"v_mfma_f32_16x16x32_bf16 v[192:195], a[64:67], a[16:19], v[192:195]\n"
"v_mfma_f32_16x16x32_bf16 v[192:195], a[68:71], a[20:23], v[192:195]\n"
"s_waitcnt lgkmcnt(15)\n"
"v_mfma_f32_16x16x32_bf16 v[132:135], a[72:75], a[0:3], v[132:135]\n"
"s_waitcnt lgkmcnt(0)\n"
"s_waitcnt vmcnt(10)\n"
"s_barrier\n"
"v_xor_b32 v8, 0x10000, v8\n"
"v_xor_b32 v9, 0x10000, v9\n"
"v_mfma_f32_16x16x32_bf16 v[132:135], a[76:79], a[4:7], v[132:135]\n"
"s_add_u32 m0, s58, 0\n"
"s_add_u32 s57, s57, 0x68c00\n"
"buffer_load_dwordx4 v12, s[40:43], s57 offen offset:0 nt lds\n"
"v_mfma_f32_16x16x32_bf16 v[164:167], a[72:75], a[8:11], v[164:167]\n"
"ds_read_b64_tr_b16 a[64:65], v8 offset:16384\n"
"ds_read_b64_tr_b16 a[66:67], v8 offset:17408\n"
"v_mfma_f32_16x16x32_bf16 v[164:167], a[76:79], a[12:15], v[164:167]\n"
"ds_read_b64_tr_b16 a[68:69], v8 offset:24576\n"
"ds_read_b64_tr_b16 a[70:71], v8 offset:25600\n"
"v_mfma_f32_16x16x32_bf16 v[196:199], a[72:75], a[16:19], v[196:199]\n"
"ds_read_b64_tr_b16 a[72:73], v9 offset:16384\n"
"ds_read_b64_tr_b16 a[74:75], v9 offset:17408\n"
"v_mfma_f32_16x16x32_bf16 v[196:199], a[76:79], a[20:23], v[196:199]\n"
"s_add_u32 m0, s58, 0\n"
"s_add_u32 s57, s57, 0x7c00\n"
"buffer_load_dwordx4 v12, s[40:43], s57 offen offset:1024 nt lds\n"
"v_mfma_f32_16x16x32_bf16 v[136:139], a[80:83], a[0:3], v[136:139]\n"
"ds_read_b64_tr_b16 a[76:77], v9 offset:24576\n"
"ds_read_b64_tr_b16 a[78:79], v9 offset:25600\n"
"v_mfma_f32_16x16x32_bf16 v[136:139], a[84:87], a[4:7], v[136:139]\n"
"v_mfma_f32_16x16x32_bf16 v[168:171], a[80:83], a[8:11], v[168:171]\n"
"v_mfma_f32_16x16x32_bf16 v[168:171], a[84:87], a[12:15], v[168:171]\n"
"s_add_u32 m0, s58, 0\n"
"s_add_u32 s57, s57, 0x7c00\n"
"buffer_load_dwordx4 v13, s[40:43], s57 offen offset:2048 nt lds\n"
"v_mfma_f32_16x16x32_bf16 v[200:203], a[80:83], a[16:19], v[200:203]\n"
"ds_read_b64_tr_b16 a[80:81], v8 offset:16640\n"
"ds_read_b64_tr_b16 a[82:83], v8 offset:17664\n"
"v_mfma_f32_16x16x32_bf16 v[200:203], a[84:87], a[20:23], v[200:203]\n"
"ds_read_b64_tr_b16 a[84:85], v8 offset:24832\n"
"ds_read_b64_tr_b16 a[86:87], v8 offset:25856\n"
"v_mfma_f32_16x16x32_bf16 v[140:143], a[88:91], a[0:3], v[140:143]\n"
"v_mfma_f32_16x16x32_bf16 v[140:143], a[92:95], a[4:7], v[140:143]\n"
"s_add_u32 m0, s58, 0\n"
"s_add_u32 s57, s57, 0x7c00\n"
"buffer_load_dwordx4 v13, s[40:43], s57 offen offset:3072 nt lds\n"
"v_mfma_f32_16x16x32_bf16 v[172:175], a[88:91], a[8:11], v[172:175]\n"
"v_mfma_f32_16x16x32_bf16 v[172:175], a[92:95], a[12:15], v[172:175]\n"
"v_mfma_f32_16x16x32_bf16 v[204:207], a[88:91], a[16:19], v[204:207]\n"
"ds_read_b64_tr_b16 a[88:89], v9 offset:16640\n"
"ds_read_b64_tr_b16 a[90:91], v9 offset:17664\n"
"v_mfma_f32_16x16x32_bf16 v[204:207], a[92:95], a[20:23], v[204:207]\n"
"s_add_u32 m0, s58, 16128\n"
"s_add_u32 s57, s57, 0xfffe8c00\n"
"buffer_load_dwordx4 v12, s[40:43], s57 offen offset:256 nt lds\n"
"v_mfma_f32_16x16x32_bf16 v[144:147], a[96:99], a[0:3], v[144:147]\n"
"ds_read_b64_tr_b16 a[92:93], v9 offset:24832\n"
"ds_read_b64_tr_b16 a[94:95], v9 offset:25856\n"
"v_mfma_f32_16x16x32_bf16 v[144:147], a[100:103], a[4:7], v[144:147]\n"
"v_mfma_f32_16x16x32_bf16 v[176:179], a[96:99], a[8:11], v[176:179]\n"
"v_mfma_f32_16x16x32_bf16 v[176:179], a[100:103], a[12:15], v[176:179]\n"
"s_add_u32 m0, s58, 16128\n"
"s_add_u32 s57, s57, 0x7c00\n"
"buffer_load_dwordx4 v12, s[40:43], s57 offen offset:1280 nt lds\n"
"v_mfma_f32_16x16x32_bf16 v[208:211], a[96:99], a[16:19], v[208:211]\n"
"ds_read_b64_tr_b16 a[96:97], v8 offset:16896\n"
"ds_read_b64_tr_b16 a[98:99], v8 offset:17920\n"
"v_mfma_f32_16x16x32_bf16 v[208:211], a[100:103], a[20:23], v[208:211]\n"
"ds_read_b64_tr_b16 a[100:101], v8 offset:25088\n"
"ds_read_b64_tr_b16 a[102:103], v8 offset:26112\n"
"v_mfma_f32_16x16x32_bf16 v[148:151], a[104:107], a[0:3], v[148:151]\n"
"v_mfma_f32_16x16x32_bf16 v[148:151], a[108:111], a[4:7], v[148:151]\n"
"s_add_u32 m0, s58, 16128\n"
"s_add_u32 s57, s57, 0x7c00\n"
"buffer_load_dwordx4 v13, s[40:43], s57 offen offset:2304 nt lds\n"
"v_mfma_f32_16x16x32_bf16 v[180:183], a[104:107], a[8:11], v[180:183]\n"
"v_mfma_f32_16x16x32_bf16 v[180:183], a[108:111], a[12:15], v[180:183]\n"
"v_mfma_f32_16x16x32_bf16 v[212:215], a[104:107], a[16:19], v[212:215]\n"
"ds_read_b64_tr_b16 a[104:105], v9 offset:16896\n"
"ds_read_b64_tr_b16 a[106:107], v9 offset:17920\n"
"v_mfma_f32_16x16x32_bf16 v[212:215], a[108:111], a[20:23], v[212:215]\n"
"s_waitcnt vmcnt(8)\n"
"ds_read_b128 a[32:35], v6 offset:0\n"
"v_mfma_f32_16x16x32_bf16 v[152:155], a[112:115], a[0:3], v[152:155]\n"
"s_add_u32 m0, s58, 16128\n"
"s_add_u32 s57, s57, 0x7c00\n"
"buffer_load_dwordx4 v13, s[40:43], s57 offen offset:3328 nt lds\n"
"s_xor_b32 s58, s58, 0x10000\n"
"v_mfma_f32_16x16x32_bf16 v[152:155], a[116:119], a[4:7], v[152:155]\n"
"ds_read_b128 a[36:39], v7 offset:0\n"
"ds_read_b128 a[40:43], v6 offset:2048\n"
"v_mfma_f32_16x16x32_bf16 v[184:187], a[112:115], a[8:11], v[184:187]\n"
"ds_read_b128 a[44:47], v7 offset:2048\n"
"ds_read_b128 a[48:51], v6 offset:4096\n"
"v_mfma_f32_16x16x32_bf16 v[184:187], a[116:119], a[12:15], v[184:187]\n"
"ds_read_b128 a[52:55], v7 offset:4096\n"
"ds_read_b64_tr_b16 a[108:109], v9 offset:25088\n"
"v_mfma_f32_16x16x32_bf16 v[216:219], a[112:115], a[16:19], v[216:219]\n"
"ds_read_b64_tr_b16 a[110:111], v9 offset:26112\n"
"ds_read_b64_tr_b16 a[112:113], v8 offset:17152\n"
"v_mfma_f32_16x16x32_bf16 v[216:219], a[116:119], a[20:23], v[216:219]\n"
"ds_read_b64_tr_b16 a[114:115], v8 offset:18176\n"
"ds_read_b64_tr_b16 a[116:117], v8 offset:25344\n"
"v_mfma_f32_16x16x32_bf16 v[156:159], a[120:123], a[0:3], v[156:159]\n"
"ds_read_b64_tr_b16 a[118:119], v8 offset:26368\n"
"v_mfma_f32_16x16x32_bf16 v[156:159], a[124:127], a[4:7], v[156:159]\n"
"v_mfma_f32_16x16x32_bf16 v[188:191], a[120:123], a[8:11], v[188:191]\n"
"v_mfma_f32_16x16x32_bf16 v[188:191], a[124:127], a[12:15], v[188:191]\n"
"v_mfma_f32_16x16x32_bf16 v[220:223], a[120:123], a[16:19], v[220:223]\n"
"ds_read_b64_tr_b16 a[120:121], v9 offset:17152\n"
"ds_read_b64_tr_b16 a[122:123], v9 offset:18176\n"
"v_mfma_f32_16x16x32_bf16 v[220:223], a[124:127], a[20:23], v[220:223]\n"
"s_waitcnt lgkmcnt(13)\n"
"v_mfma_f32_16x16x32_bf16 v[16:19], a[64:67], a[32:35], v[16:19]\n"
"ds_read_b64_tr_b16 a[124:125], v9 offset:25344\n"
"ds_read_b64_tr_b16 a[126:127], v9 offset:26368\n"
"s_waitcnt lgkmcnt(14)\n"
"v_mfma_f32_16x16x32_bf16 v[16:19], a[68:71], a[36:39], v[16:19]\n"
"s_add_u32 m0, s59, 0\n"
"s_add_u32 s56, s56, 0xffff6480\n"
"s_waitcnt lgkmcnt(10)\n"
"buffer_load_dwordx4 v10, s[36:39], s56 offen offset:0 lds\n"
"v_mfma_f32_16x16x32_bf16 v[44:47], a[64:67], a[40:43], v[44:47]\n"
"v_mfma_f32_16x16x32_bf16 v[44:47], a[68:71], a[44:47], v[44:47]\n"
"v_mfma_f32_16x16x32_bf16 v[72:75], a[64:67], a[48:51], v[72:75]\n"
"ds_read_b64_tr_b16 a[64:65], v8 offset:0\n"
"ds_read_b64_tr_b16 a[66:67], v8 offset:1024\n"
"v_mfma_f32_16x16x32_bf16 v[72:75], a[68:71], a[52:55], v[72:75]\n"
"s_add_u32 m0, s76, 65536\n"
"s_nop 0\n"
"buffer_load_dwordx4 v4, s[48:51], s75 offen offset:0 nt lds\n"
"v_mfma_f32_16x16x32_bf16 v[20:23], a[72:75], a[32:35], v[20:23]\n"
"ds_read_b64_tr_b16 a[68:69], v8 offset:8192\n"
"ds_read_b64_tr_b16 a[70:71], v8 offset:9216\n"
"v_mfma_f32_16x16x32_bf16 v[20:23], a[76:79], a[36:39], v[20:23]\n"
"v_mfma_f32_16x16x32_bf16 v[48:51], a[72:75], a[40:43], v[48:51]\n"
"v_mfma_f32_16x16x32_bf16 v[48:51], a[76:79], a[44:47], v[48:51]\n"
"s_add_u32 m0, s59, 0\n"
"s_add_u32 s56, s56, 0x1c00\n"
"buffer_load_dwordx4 v11, s[36:39], s56 offen offset:1024 lds\n"
"v_mfma_f32_16x16x32_bf16 v[76:79], a[72:75], a[48:51], v[76:79]\n"
"ds_read_b64_tr_b16 a[72:73], v9 offset:0\n"
"ds_read_b64_tr_b16 a[74:75], v9 offset:1024\n"
"v_mfma_f32_16x16x32_bf16 v[76:79], a[76:79], a[52:55], v[76:79]\n"
"ds_read_b64_tr_b16 a[76:77], v9 offset:8192\n"
"ds_read_b64_tr_b16 a[78:79], v9 offset:9216\n"
"v_mfma_f32_16x16x32_bf16 v[24:27], a[80:83], a[32:35], v[24:27]\n"
"v_mfma_f32_16x16x32_bf16 v[24:27], a[84:87], a[36:39], v[24:27]\n"
"v_mfma_f32_16x16x32_bf16 v[52:55], a[80:83], a[40:43], v[52:55]\n"
"v_mfma_f32_16x16x32_bf16 v[52:55], a[84:87], a[44:47], v[52:55]\n"
"v_mfma_f32_16x16x32_bf16 v[80:83], a[80:83], a[48:51], v[80:83]\n"
"ds_read_b64_tr_b16 a[80:81], v8 offset:256\n"
"ds_read_b64_tr_b16 a[82:83], v8 offset:1280\n"
"v_mfma_f32_16x16x32_bf16 v[80:83], a[84:87], a[52:55], v[80:83]\n"
"s_add_u32 m0, s59, 0\n"
"s_add_u32 s56, s56, 0x1c00\n"
"buffer_load_dwordx4 v10, s[36:39], s56 offen offset:2048 lds\n"
"v_mfma_f32_16x16x32_bf16 v[28:31], a[88:91], a[32:35], v[28:31]\n"
"s_add_u32 m0, s76, 65536\n"
"s_nop 0\n"
"buffer_load_dwordx4 v5, s[48:51], s75 offen offset:1024 nt lds\n"
"v_mfma_f32_16x16x32_bf16 v[28:31], a[92:95], a[36:39], v[28:31]\n"
"ds_read_b64_tr_b16 a[84:85], v8 offset:8448\n"
"ds_read_b64_tr_b16 a[86:87], v8 offset:9472\n"
"v_mfma_f32_16x16x32_bf16 v[56:59], a[88:91], a[40:43], v[56:59]\n"
"v_mfma_f32_16x16x32_bf16 v[56:59], a[92:95], a[44:47], v[56:59]\n"
"v_mfma_f32_16x16x32_bf16 v[84:87], a[88:91], a[48:51], v[84:87]\n"
"ds_read_b64_tr_b16 a[88:89], v9 offset:256\n"
"ds_read_b64_tr_b16 a[90:91], v9 offset:1280\n"
"v_mfma_f32_16x16x32_bf16 v[84:87], a[92:95], a[52:55], v[84:87]\n"
"ds_read_b64_tr_b16 a[92:93], v9 offset:8448\n"
"ds_read_b64_tr_b16 a[94:95], v9 offset:9472\n"
"v_mfma_f32_16x16x32_bf16 v[32:35], a[96:99], a[32:35], v[32:35]\n"
"v_mfma_f32_16x16x32_bf16 v[32:35], a[100:103], a[36:39], v[32:35]\n"
"s_add_u32 m0, s59, 0\n"
"s_add_u32 s56, s56, 0x1c00\n"
"buffer_load_dwordx4 v11, s[36:39], s56 offen offset:3072 lds\n"
"v_mfma_f32_16x16x32_bf16 v[60:63], a[96:99], a[40:43], v[60:63]\n"
"v_mfma_f32_16x16x32_bf16 v[60:63], a[100:103], a[44:47], v[60:63]\n"
"v_mfma_f32_16x16x32_bf16 v[88:91], a[96:99], a[48:51], v[88:91]\n"
"ds_read_b64_tr_b16 a[96:97], v8 offset:512\n"
"ds_read_b64_tr_b16 a[98:99], v8 offset:1536\n"
"v_mfma_f32_16x16x32_bf16 v[88:91], a[100:103], a[52:55], v[88:91]\n"
"s_add_u32 m0, s76, 65536\n"
"s_nop 0\n"
"buffer_load_dwordx4 v4, s[48:51], s75 offen offset:2048 nt lds\n"
"v_mfma_f32_16x16x32_bf16 v[36:39], a[104:107], a[32:35], v[36:39]\n"
"ds_read_b64_tr_b16 a[100:101], v8 offset:8704\n"
"ds_read_b64_tr_b16 a[102:103], v8 offset:9728\n"
"s_waitcnt lgkmcnt(15)\n"
"v_mfma_f32_16x16x32_bf16 v[36:39], a[108:111], a[36:39], v[36:39]\n"
"v_mfma_f32_16x16x32_bf16 v[64:67], a[104:107], a[40:43], v[64:67]\n"
"v_mfma_f32_16x16x32_bf16 v[64:67], a[108:111], a[44:47], v[64:67]\n"
"s_add_u32 m0, s59, 4096\n"
"s_add_u32 s56, s56, 0x2c00\n"
"buffer_load_dwordx4 v10, s[36:39], s56 offen offset:0 lds\n"
"v_mfma_f32_16x16x32_bf16 v[92:95], a[104:107], a[48:51], v[92:95]\n"
"ds_read_b64_tr_b16 a[104:105], v9 offset:512\n"
"ds_read_b64_tr_b16 a[106:107], v9 offset:1536\n"
"v_mfma_f32_16x16x32_bf16 v[92:95], a[108:111], a[52:55], v[92:95]\n"
"ds_read_b64_tr_b16 a[108:109], v9 offset:8704\n"
"ds_read_b64_tr_b16 a[110:111], v9 offset:9728\n"
"v_mfma_f32_16x16x32_bf16 v[40:43], a[112:115], a[32:35], v[40:43]\n"
"v_mfma_f32_16x16x32_bf16 v[40:43], a[116:119], a[36:39], v[40:43]\n"
"v_mfma_f32_16x16x32_bf16 v[68:71], a[112:115], a[40:43], v[68:71]\n"
"v_mfma_f32_16x16x32_bf16 v[68:71], a[116:119], a[44:47], v[68:71]\n"
"v_mfma_f32_16x16x32_bf16 v[96:99], a[112:115], a[48:51], v[96:99]\n"
"ds_read_b64_tr_b16 a[112:113], v8 offset:768\n"
"ds_read_b64_tr_b16 a[114:115], v8 offset:1792\n"
"v_mfma_f32_16x16x32_bf16 v[96:99], a[116:119], a[52:55], v[96:99]\n"
"s_add_u32 m0, s59, 4096\n"
"s_add_u32 s56, s56, 0x1c00\n"
"buffer_load_dwordx4 v11, s[36:39], s56 offen offset:1024 lds\n"
"v_mfma_f32_16x16x32_bf16 a[204:207], a[120:123], a[32:35], a[204:207]\n"
"s_add_u32 m0, s76, 65536\n"
"s_nop 0\n"
"buffer_load_dwordx4 v5, s[48:51], s75 offen offset:3072 nt lds\n"
"s_add_u32 s75, s75, 0x4000\n"
"s_lshr_b32 s74, s75, 15\n"
"s_mul_i32 s74, s74, 0x24000\n"
"s_add_u32 s76, s76, 0x4000\n"
"s_add_u32 s76, s76, s74\n"
"v_mfma_f32_16x16x32_bf16 a[204:207], a[124:127], a[36:39], a[204:207]\n"
"ds_read_b64_tr_b16 a[116:117], v8 offset:8960\n"
"ds_read_b64_tr_b16 a[118:119], v8 offset:9984\n"
"v_mfma_f32_16x16x32_bf16 a[220:223], a[120:123], a[40:43], a[220:223]\n"
"v_mfma_f32_16x16x32_bf16 a[220:223], a[124:127], a[44:47], a[220:223]\n"
"v_mfma_f32_16x16x32_bf16 a[236:239], a[120:123], a[48:51], a[236:239]\n"
"ds_read_b64_tr_b16 a[120:121], v9 offset:768\n"
"ds_read_b64_tr_b16 a[122:123], v9 offset:1792\n"
"v_mfma_f32_16x16x32_bf16 a[236:239], a[124:127], a[52:55], a[236:239]\n"
"ds_read_b64_tr_b16 a[124:125], v9 offset:8960\n"
"ds_read_b64_tr_b16 a[126:127], v9 offset:9984\n"
"v_mfma_f32_16x16x32_bf16 v[128:131], a[64:67], a[32:35], v[128:131]\n"
"v_mfma_f32_16x16x32_bf16 v[128:131], a[68:71], a[36:39], v[128:131]\n"
"v_mfma_f32_16x16x32_bf16 v[160:163], a[64:67], a[40:43], v[160:163]\n"
"v_mfma_f32_16x16x32_bf16 v[160:163], a[68:71], a[44:47], v[160:163]\n"
"v_mfma_f32_16x16x32_bf16 v[192:195], a[64:67], a[48:51], v[192:195]\n"
"v_mfma_f32_16x16x32_bf16 v[192:195], a[68:71], a[52:55], v[192:195]\n"
"s_waitcnt lgkmcnt(15)\n"
"v_mfma_f32_16x16x32_bf16 v[132:135], a[72:75], a[32:35], v[132:135]\n"
"s_waitcnt lgkmcnt(0)\n"
"s_waitcnt vmcnt(10)\n"
"s_barrier\n"
"v_xor_b32 v8, 0x10000, v8\n"
"v_xor_b32 v9, 0x10000, v9\n"
"v_mfma_f32_16x16x32_bf16 v[132:135], a[76:79], a[36:39], v[132:135]\n"
"s_add_u32 m0, s58, 0\n"
"s_add_u32 s57, s57, 0x68c00\n"
"buffer_load_dwordx4 v12, s[40:43], s57 offen offset:0 nt lds\n"
"v_mfma_f32_16x16x32_bf16 v[164:167], a[72:75], a[40:43], v[164:167]\n"
"ds_read_b64_tr_b16 a[64:65], v8 offset:16384\n"
"ds_read_b64_tr_b16 a[66:67], v8 offset:17408\n"
"v_mfma_f32_16x16x32_bf16 v[164:167], a[76:79], a[44:47], v[164:167]\n"
"ds_read_b64_tr_b16 a[68:69], v8 offset:24576\n"
"ds_read_b64_tr_b16 a[70:71], v8 offset:25600\n"
"v_mfma_f32_16x16x32_bf16 v[196:199], a[72:75], a[48:51], v[196:199]\n"
"ds_read_b64_tr_b16 a[72:73], v9 offset:16384\n"
"ds_read_b64_tr_b16 a[74:75], v9 offset:17408\n"
"v_mfma_f32_16x16x32_bf16 v[196:199], a[76:79], a[52:55], v[196:199]\n"
"s_add_u32 m0, s58, 0\n"
"s_add_u32 s57, s57, 0x7c00\n"
"buffer_load_dwordx4 v12, s[40:43], s57 offen offset:1024 nt lds\n"
"v_mfma_f32_16x16x32_bf16 v[136:139], a[80:83], a[32:35], v[136:139]\n"
"ds_read_b64_tr_b16 a[76:77], v9 offset:24576\n"
"ds_read_b64_tr_b16 a[78:79], v9 offset:25600\n"
"v_mfma_f32_16x16x32_bf16 v[136:139], a[84:87], a[36:39], v[136:139]\n"
"v_mfma_f32_16x16x32_bf16 v[168:171], a[80:83], a[40:43], v[168:171]\n"
"v_mfma_f32_16x16x32_bf16 v[168:171], a[84:87], a[44:47], v[168:171]\n"
"s_add_u32 m0, s58, 0\n"
"s_add_u32 s57, s57, 0x7c00\n"
"buffer_load_dwordx4 v13, s[40:43], s57 offen offset:2048 nt lds\n"
"v_mfma_f32_16x16x32_bf16 v[200:203], a[80:83], a[48:51], v[200:203]\n"
"ds_read_b64_tr_b16 a[80:81], v8 offset:16640\n"
"ds_read_b64_tr_b16 a[82:83], v8 offset:17664\n"
"v_mfma_f32_16x16x32_bf16 v[200:203], a[84:87], a[52:55], v[200:203]\n"
"ds_read_b64_tr_b16 a[84:85], v8 offset:24832\n"
"ds_read_b64_tr_b16 a[86:87], v8 offset:25856\n"
"v_mfma_f32_16x16x32_bf16 v[140:143], a[88:91], a[32:35], v[140:143]\n"
"v_mfma_f32_16x16x32_bf16 v[140:143], a[92:95], a[36:39], v[140:143]\n"
"s_add_u32 m0, s58, 0\n"
"s_add_u32 s57, s57, 0x7c00\n"
"buffer_load_dwordx4 v13, s[40:43], s57 offen offset:3072 nt lds\n"
"v_mfma_f32_16x16x32_bf16 v[172:175], a[88:91], a[40:43], v[172:175]\n"
"v_mfma_f32_16x16x32_bf16 v[172:175], a[92:95], a[44:47], v[172:175]\n"
"v_mfma_f32_16x16x32_bf16 v[204:207], a[88:91], a[48:51], v[204:207]\n"
"ds_read_b64_tr_b16 a[88:89], v9 offset:16640\n"
"ds_read_b64_tr_b16 a[90:91], v9 offset:17664\n"
"v_mfma_f32_16x16x32_bf16 v[204:207], a[92:95], a[52:55], v[204:207]\n"
"s_add_u32 m0, s58, 16128\n"
"s_add_u32 s57, s57, 0xfffe8c00\n"
"buffer_load_dwordx4 v12, s[40:43], s57 offen offset:256 nt lds\n"
"v_mfma_f32_16x16x32_bf16 v[144:147], a[96:99], a[32:35], v[144:147]\n"
"ds_read_b64_tr_b16 a[92:93], v9 offset:24832\n"
"ds_read_b64_tr_b16 a[94:95], v9 offset:25856\n"
"v_mfma_f32_16x16x32_bf16 v[144:147], a[100:103], a[36:39], v[144:147]\n"
"v_mfma_f32_16x16x32_bf16 v[176:179], a[96:99], a[40:43], v[176:179]\n"
"v_mfma_f32_16x16x32_bf16 v[176:179], a[100:103], a[44:47], v[176:179]\n"
"s_add_u32 m0, s58, 16128\n"
"s_add_u32 s57, s57, 0x7c00\n"
"buffer_load_dwordx4 v12, s[40:43], s57 offen offset:1280 nt lds\n"
"v_mfma_f32_16x16x32_bf16 v[208:211], a[96:99], a[48:51], v[208:211]\n"
"ds_read_b64_tr_b16 a[96:97], v8 offset:16896\n"
"ds_read_b64_tr_b16 a[98:99], v8 offset:17920\n"
"v_mfma_f32_16x16x32_bf16 v[208:211], a[100:103], a[52:55], v[208:211]\n"
"ds_read_b64_tr_b16 a[100:101], v8 offset:25088\n"
"ds_read_b64_tr_b16 a[102:103], v8 offset:26112\n"
"v_mfma_f32_16x16x32_bf16 v[148:151], a[104:107], a[32:35], v[148:151]\n"
"v_mfma_f32_16x16x32_bf16 v[148:151], a[108:111], a[36:39], v[148:151]\n"
"s_add_u32 m0, s58, 16128\n"
"s_add_u32 s57, s57, 0x7c00\n"
"buffer_load_dwordx4 v13, s[40:43], s57 offen offset:2304 nt lds\n"
"v_mfma_f32_16x16x32_bf16 v[180:183], a[104:107], a[40:43], v[180:183]\n"
"v_mfma_f32_16x16x32_bf16 v[180:183], a[108:111], a[44:47], v[180:183]\n"
"v_mfma_f32_16x16x32_bf16 v[212:215], a[104:107], a[48:51], v[212:215]\n"
"ds_read_b64_tr_b16 a[104:105], v9 offset:16896\n"
"ds_read_b64_tr_b16 a[106:107], v9 offset:17920\n"
"v_mfma_f32_16x16x32_bf16 v[212:215], a[108:111], a[52:55], v[212:215]\n"
"s_waitcnt vmcnt(8)\n"
"ds_read_b128 a[0:3], v6 offset:0\n"
"v_mfma_f32_16x16x32_bf16 v[152:155], a[112:115], a[32:35], v[152:155]\n"
"s_add_u32 m0, s58, 16128\n"
"s_add_u32 s57, s57, 0x7c00\n"
"buffer_load_dwordx4 v13, s[40:43], s57 offen offset:3328 nt lds\n"
"s_xor_b32 s58, s58, 0x10000\n"
"v_mfma_f32_16x16x32_bf16 v[152:155], a[116:119], a[36:39], v[152:155]\n"
"ds_read_b128 a[4:7], v7 offset:0\n"
"ds_read_b128 a[8:11], v6 offset:2048\n"
"v_mfma_f32_16x16x32_bf16 v[184:187], a[112:115], a[40:43], v[184:187]\n"
"ds_read_b128 a[12:15], v7 offset:2048\n"
"ds_read_b128 a[16:19], v6 offset:4096\n"
"v_mfma_f32_16x16x32_bf16 v[184:187], a[116:119], a[44:47], v[184:187]\n"
"ds_read_b128 a[20:23], v7 offset:4096\n"
"ds_read_b64_tr_b16 a[108:109], v9 offset:25088\n"
"v_mfma_f32_16x16x32_bf16 v[216:219], a[112:115], a[48:51], v[216:219]\n"
"ds_read_b64_tr_b16 a[110:111], v9 offset:26112\n"
"ds_read_b64_tr_b16 a[112:113], v8 offset:17152\n"
"v_mfma_f32_16x16x32_bf16 v[216:219], a[116:119], a[52:55], v[216:219]\n"
"ds_read_b64_tr_b16 a[114:115], v8 offset:18176\n"
"ds_read_b64_tr_b16 a[116:117], v8 offset:25344\n"
"v_mfma_f32_16x16x32_bf16 v[156:159], a[120:123], a[32:35], v[156:159]\n"
"ds_read_b64_tr_b16 a[118:119], v8 offset:26368\n"
"v_mfma_f32_16x16x32_bf16 v[156:159], a[124:127], a[36:39], v[156:159]\n"
"v_mfma_f32_16x16x32_bf16 v[188:191], a[120:123], a[40:43], v[188:191]\n"
"v_mfma_f32_16x16x32_bf16 v[188:191], a[124:127], a[44:47], v[188:191]\n"
"v_mfma_f32_16x16x32_bf16 v[220:223], a[120:123], a[48:51], v[220:223]\n"
"ds_read_b64_tr_b16 a[120:121], v9 offset:17152\n"
"ds_read_b64_tr_b16 a[122:123], v9 offset:18176\n"
"v_mfma_f32_16x16x32_bf16 v[220:223], a[124:127], a[52:55], v[220:223]\n"
"s_sub_u32 s66, s66, 1\n"
"s_cmp_lg_u32 s66, 0\n"
"s_cbranch_scc1 .LQb_%=\n"
"s_waitcnt lgkmcnt(13)\n"
"v_mfma_f32_16x16x32_bf16 v[16:19], a[64:67], a[0:3], v[16:19]\n"
"ds_read_b64_tr_b16 a[124:125], v9 offset:25344\n"
"ds_read_b64_tr_b16 a[126:127], v9 offset:26368\n"
"s_waitcnt lgkmcnt(14)\n"
"v_mfma_f32_16x16x32_bf16 v[16:19], a[68:71], a[4:7], v[16:19]\n"
"s_add_u32 m0, s59, 0\n"
"s_add_u32 s56, s56, 0xffff6480\n"
"s_waitcnt lgkmcnt(10)\n"
"buffer_load_dwordx4 v10, s[36:39], s56 offen offset:0 lds\n"
"v_mfma_f32_16x16x32_bf16 v[44:47], a[64:67], a[8:11], v[44:47]\n"
"v_mfma_f32_16x16x32_bf16 v[44:47], a[68:71], a[12:15], v[44:47]\n"
"v_mfma_f32_16x16x32_bf16 v[72:75], a[64:67], a[16:19], v[72:75]\n"
"ds_read_b64_tr_b16 a[64:65], v8 offset:0\n"
"ds_read_b64_tr_b16 a[66:67], v8 offset:1024\n"
"v_mfma_f32_16x16x32_bf16 v[72:75], a[68:71], a[20:23], v[72:75]\n"
"v_mfma_f32_16x16x32_bf16 v[20:23], a[72:75], a[0:3], v[20:23]\n"
"ds_read_b64_tr_b16 a[68:69], v8 offset:8192\n"
"ds_read_b64_tr_b16 a[70:71], v8 offset:9216\n"
"v_mfma_f32_16x16x32_bf16 v[20:23], a[76:79], a[4:7], v[20:23]\n"
"v_mfma_f32_16x16x32_bf16 v[48:51], a[72:75], a[8:11], v[48:51]\n"
"v_mfma_f32_16x16x32_bf16 v[48:51], a[76:79], a[12:15], v[48:51]\n"
"s_add_u32 m0, s59, 0\n"
"s_add_u32 s56, s56, 0x1c00\n"
"buffer_load_dwordx4 v11, s[36:39], s56 offen offset:1024 lds\n"
"v_mfma_f32_16x16x32_bf16 v[76:79], a[72:75], a[16:19], v[76:79]\n"
"ds_read_b64_tr_b16 a[72:73], v9 offset:0\n"
"ds_read_b64_tr_b16 a[74:75], v9 offset:1024\n"
"v_mfma_f32_16x16x32_bf16 v[76:79], a[76:79], a[20:23], v[76:79]\n"
"ds_read_b64_tr_b16 a[76:77], v9 offset:8192\n"
"ds_read_b64_tr_b16 a[78:79], v9 offset:9216\n"
"v_mfma_f32_16x16x32_bf16 v[24:27], a[80:83], a[0:3], v[24:27]\n"
"v_mfma_f32_16x16x32_bf16 v[24:27], a[84:87], a[4:7], v[24:27]\n"
"v_mfma_f32_16x16x32_bf16 v[52:55], a[80:83], a[8:11], v[52:55]\n"
"v_mfma_f32_16x16x32_bf16 v[52:55], a[84:87], a[12:15], v[52:55]\n"
"v_mfma_f32_16x16x32_bf16 v[80:83], a[80:83], a[16:19], v[80:83]\n"
"ds_read_b64_tr_b16 a[80:81], v8 offset:256\n"
"ds_read_b64_tr_b16 a[82:83], v8 offset:1280\n"
"v_mfma_f32_16x16x32_bf16 v[80:83], a[84:87], a[20:23], v[80:83]\n"
"s_add_u32 m0, s59, 0\n"
"s_add_u32 s56, s56, 0x1c00\n"
"buffer_load_dwordx4 v10, s[36:39], s56 offen offset:2048 lds\n"
"v_mfma_f32_16x16x32_bf16 v[28:31], a[88:91], a[0:3], v[28:31]\n"
"v_mfma_f32_16x16x32_bf16 v[28:31], a[92:95], a[4:7], v[28:31]\n"
"ds_read_b64_tr_b16 a[84:85], v8 offset:8448\n"
"ds_read_b64_tr_b16 a[86:87], v8 offset:9472\n"
"v_mfma_f32_16x16x32_bf16 v[56:59], a[88:91], a[8:11], v[56:59]\n"
"v_mfma_f32_16x16x32_bf16 v[56:59], a[92:95], a[12:15], v[56:59]\n"
"v_mfma_f32_16x16x32_bf16 v[84:87], a[88:91], a[16:19], v[84:87]\n"
"ds_read_b64_tr_b16 a[88:89], v9 offset:256\n"
"ds_read_b64_tr_b16 a[90:91], v9 offset:1280\n"
"v_mfma_f32_16x16x32_bf16 v[84:87], a[92:95], a[20:23], v[84:87]\n"
"ds_read_b64_tr_b16 a[92:93], v9 offset:8448\n"
"ds_read_b64_tr_b16 a[94:95], v9 offset:9472\n"
"v_mfma_f32_16x16x32_bf16 v[32:35], a[96:99], a[0:3], v[32:35]\n"
"v_mfma_f32_16x16x32_bf16 v[32:35], a[100:103], a[4:7], v[32:35]\n"
"s_add_u32 m0, s59, 0\n"
"s_add_u32 s56, s56, 0x1c00\n"
"buffer_load_dwordx4 v11, s[36:39], s56 offen offset:3072 lds\n"
"v_mfma_f32_16x16x32_bf16 v[60:63], a[96:99], a[8:11], v[60:63]\n"
"v_mfma_f32_16x16x32_bf16 v[60:63], a[100:103], a[12:15], v[60:63]\n"
"v_mfma_f32_16x16x32_bf16 v[88:91], a[96:99], a[16:19], v[88:91]\n"
"ds_read_b64_tr_b16 a[96:97], v8 offset:512\n"
"ds_read_b64_tr_b16 a[98:99], v8 offset:1536\n"
"v_mfma_f32_16x16x32_bf16 v[88:91], a[100:103], a[20:23], v[88:91]\n"
"v_mfma_f32_16x16x32_bf16 v[36:39], a[104:107], a[0:3], v[36:39]\n"
"ds_read_b64_tr_b16 a[100:101], v8 offset:8704\n"
"ds_read_b64_tr_b16 a[102:103], v8 offset:9728\n"
"s_waitcnt lgkmcnt(15)\n"
"v_mfma_f32_16x16x32_bf16 v[36:39], a[108:111], a[4:7], v[36:39]\n"
"v_mfma_f32_16x16x32_bf16 v[64:67], a[104:107], a[8:11], v[64:67]\n"
"v_mfma_f32_16x16x32_bf16 v[64:67], a[108:111], a[12:15], v[64:67]\n"
"s_add_u32 m0, s59, 4096\n"
"s_add_u32 s56, s56, 0x2c00\n"
"buffer_load_dwordx4 v10, s[36:39], s56 offen offset:0 lds\n"
"v_mfma_f32_16x16x32_bf16 v[92:95], a[104:107], a[16:19], v[92:95]\n"
"ds_read_b64_tr_b16 a[104:105], v9 offset:512\n"
"ds_read_b64_tr_b16 a[106:107], v9 offset:1536\n"
"v_mfma_f32_16x16x32_bf16 v[92:95], a[108:111], a[20:23], v[92:95]\n"
"ds_read_b64_tr_b16 a[108:109], v9 offset:8704\n"
"ds_read_b64_tr_b16 a[110:111], v9 offset:9728\n"
"v_mfma_f32_16x16x32_bf16 v[40:43], a[112:115], a[0:3], v[40:43]\n"
"v_mfma_f32_16x16x32_bf16 v[40:43], a[116:119], a[4:7], v[40:43]\n"
"v_mfma_f32_16x16x32_bf16 v[68:71], a[112:115], a[8:11], v[68:71]\n"
"v_mfma_f32_16x16x32_bf16 v[68:71], a[116:119], a[12:15], v[68:71]\n"
"v_mfma_f32_16x16x32_bf16 v[96:99], a[112:115], a[16:19], v[96:99]\n"
"ds_read_b64_tr_b16 a[112:113], v8 offset:768\n"
"ds_read_b64_tr_b16 a[114:115], v8 offset:1792\n"
"v_mfma_f32_16x16x32_bf16 v[96:99], a[116:119], a[20:23], v[96:99]\n"
"s_add_u32 m0, s59, 4096\n"
"s_add_u32 s56, s56, 0x1c00\n"
"buffer_load_dwordx4 v11, s[36:39], s56 offen offset:1024 lds\n"
"v_mfma_f32_16x16x32_bf16 a[204:207], a[120:123], a[0:3], a[204:207]\n"
"v_mfma_f32_16x16x32_bf16 a[204:207], a[124:127], a[4:7], a[204:207]\n"
"ds_read_b64_tr_b16 a[116:117], v8 offset:8960\n"
"ds_read_b64_tr_b16 a[118:119], v8 offset:9984\n"
"v_mfma_f32_16x16x32_bf16 a[220:223], a[120:123], a[8:11], a[220:223]\n"
"v_mfma_f32_16x16x32_bf16 a[220:223], a[124:127], a[12:15], a[220:223]\n"
"v_mfma_f32_16x16x32_bf16 a[236:239], a[120:123], a[16:19], a[236:239]\n"
"ds_read_b64_tr_b16 a[120:121], v9 offset:768\n"
"ds_read_b64_tr_b16 a[122:123], v9 offset:1792\n"
"v_mfma_f32_16x16x32_bf16 a[236:239], a[124:127], a[20:23], a[236:239]\n"
"ds_read_b64_tr_b16 a[124:125], v9 offset:8960\n"
"ds_read_b64_tr_b16 a[126:127], v9 offset:9984\n"
"v_mfma_f32_16x16x32_bf16 v[128:131], a[64:67], a[0:3], v[128:131]\n"
"v_mfma_f32_16x16x32_bf16 v[128:131], a[68:71], a[4:7], v[128:131]\n"
"v_mfma_f32_16x16x32_bf16 v[160:163], a[64:67], a[8:11], v[160:163]\n"
"v_mfma_f32_16x16x32_bf16 v[160:163], a[68:71], a[12:15], v[160:163]\n"
"v_mfma_f32_16x16x32_bf16 v[192:195], a[64:67], a[16:19], v[192:195]\n"
"v_mfma_f32_16x16x32_bf16 v[192:195], a[68:71], a[20:23], v[192:195]\n"
"s_waitcnt lgkmcnt(15)\n"
"v_mfma_f32_16x16x32_bf16 v[132:135], a[72:75], a[0:3], v[132:135]\n"
"s_waitcnt lgkmcnt(0)\n"
"s_waitcnt vmcnt(6)\n"
"s_barrier\n"
"v_xor_b32 v8, 0x10000, v8\n"
"v_xor_b32 v9, 0x10000, v9\n"
"v_mfma_f32_16x16x32_bf16 v[132:135], a[76:79], a[4:7], v[132:135]\n"
"s_add_u32 m0, s77, 49152\n"
"s_mov_b32 s75, 0x8000\n"
"buffer_load_dwordx4 v0, s[44:47], s75 offen offset:0 nt lds\n"
"buffer_load_dwordx4 v1, s[44:47], s75 offen offset:1024 nt lds\n"
"buffer_load_dwordx4 v2, s[44:47], s75 offen offset:2048 nt lds\n"
"buffer_load_dwordx4 v3, s[44:47], s75 offen offset:3072 nt lds\n"
"v_mfma_f32_16x16x32_bf16 v[164:167], a[72:75], a[8:11], v[164:167]\n"
"ds_read_b64_tr_b16 a[64:65], v8 offset:16384\n"
"ds_read_b64_tr_b16 a[66:67], v8 offset:17408\n"
"v_mfma_f32_16x16x32_bf16 v[164:167], a[76:79], a[12:15], v[164:167]\n"
"ds_read_b64_tr_b16 a[68:69], v8 offset:24576\n"
"ds_read_b64_tr_b16 a[70:71], v8 offset:25600\n"
"v_mfma_f32_16x16x32_bf16 v[196:199], a[72:75], a[16:19], v[196:199]\n"
"ds_read_b64_tr_b16 a[72:73], v9 offset:16384\n"
"ds_read_b64_tr_b16 a[74:75], v9 offset:17408\n"
"v_mfma_f32_16x16x32_bf16 v[196:199], a[76:79], a[20:23], v[196:199]\n"
"v_mfma_f32_16x16x32_bf16 v[136:139], a[80:83], a[0:3], v[136:139]\n"
"ds_read_b64_tr_b16 a[76:77], v9 offset:24576\n"
"ds_read_b64_tr_b16 a[78:79], v9 offset:25600\n"
"v_mfma_f32_16x16x32_bf16 v[136:139], a[84:87], a[4:7], v[136:139]\n"
"v_mfma_f32_16x16x32_bf16 v[168:171], a[80:83], a[8:11], v[168:171]\n"
"v_mfma_f32_16x16x32_bf16 v[168:171], a[84:87], a[12:15], v[168:171]\n"
"v_mfma_f32_16x16x32_bf16 v[200:203], a[80:83], a[16:19], v[200:203]\n"
"ds_read_b64_tr_b16 a[80:81], v8 offset:16640\n"
"ds_read_b64_tr_b16 a[82:83], v8 offset:17664\n"
"v_mfma_f32_16x16x32_bf16 v[200:203], a[84:87], a[20:23], v[200:203]\n"
"ds_read_b64_tr_b16 a[84:85], v8 offset:24832\n"
"ds_read_b64_tr_b16 a[86:87], v8 offset:25856\n"
"v_mfma_f32_16x16x32_bf16 v[140:143], a[88:91], a[0:3], v[140:143]\n"
"v_mfma_f32_16x16x32_bf16 v[140:143], a[92:95], a[4:7], v[140:143]\n"
"v_mfma_f32_16x16x32_bf16 v[172:175], a[88:91], a[8:11], v[172:175]\n"
"v_mfma_f32_16x16x32_bf16 v[172:175], a[92:95], a[12:15], v[172:175]\n"
"v_mfma_f32_16x16x32_bf16 v[204:207], a[88:91], a[16:19], v[204:207]\n"
"ds_read_b64_tr_b16 a[88:89], v9 offset:16640\n"
"ds_read_b64_tr_b16 a[90:91], v9 offset:17664\n"
"v_mfma_f32_16x16x32_bf16 v[204:207], a[92:95], a[20:23], v[204:207]\n"
"v_mfma_f32_16x16x32_bf16 v[144:147], a[96:99], a[0:3], v[144:147]\n"
"ds_read_b64_tr_b16 a[92:93], v9 offset:24832\n"
"ds_read_b64_tr_b16 a[94:95], v9 offset:25856\n"
"v_mfma_f32_16x16x32_bf16 v[144:147], a[100:103], a[4:7], v[144:147]\n"
"v_mfma_f32_16x16x32_bf16 v[176:179], a[96:99], a[8:11], v[176:179]\n"
"v_mfma_f32_16x16x32_bf16 v[176:179], a[100:103], a[12:15], v[176:179]\n"
"v_mfma_f32_16x16x32_bf16 v[208:211], a[96:99], a[16:19], v[208:211]\n"
"ds_read_b64_tr_b16 a[96:97], v8 offset:16896\n"
"ds_read_b64_tr_b16 a[98:99], v8 offset:17920\n"
"v_mfma_f32_16x16x32_bf16 v[208:211], a[100:103], a[20:23], v[208:211]\n"
"ds_read_b64_tr_b16 a[100:101], v8 offset:25088\n"
"ds_read_b64_tr_b16 a[102:103], v8 offset:26112\n"
"v_mfma_f32_16x16x32_bf16 v[148:151], a[104:107], a[0:3], v[148:151]\n"
"v_mfma_f32_16x16x32_bf16 v[148:151], a[108:111], a[4:7], v[148:151]\n"
"v_mfma_f32_16x16x32_bf16 v[180:183], a[104:107], a[8:11], v[180:183]\n"
"v_mfma_f32_16x16x32_bf16 v[180:183], a[108:111], a[12:15], v[180:183]\n"
"v_mfma_f32_16x16x32_bf16 v[212:215], a[104:107], a[16:19], v[212:215]\n"
"ds_read_b64_tr_b16 a[104:105], v9 offset:16896\n"
"ds_read_b64_tr_b16 a[106:107], v9 offset:17920\n"
"v_mfma_f32_16x16x32_bf16 v[212:215], a[108:111], a[20:23], v[212:215]\n"
"s_waitcnt vmcnt(4)\n"
"ds_read_b128 a[32:35], v6 offset:0\n"
"v_mfma_f32_16x16x32_bf16 v[152:155], a[112:115], a[0:3], v[152:155]\n"
"v_mfma_f32_16x16x32_bf16 v[152:155], a[116:119], a[4:7], v[152:155]\n"
"ds_read_b128 a[36:39], v7 offset:0\n"
"ds_read_b128 a[40:43], v6 offset:2048\n"
"v_mfma_f32_16x16x32_bf16 v[184:187], a[112:115], a[8:11], v[184:187]\n"
"ds_read_b128 a[44:47], v7 offset:2048\n"
"ds_read_b128 a[48:51], v6 offset:4096\n"
"v_mfma_f32_16x16x32_bf16 v[184:187], a[116:119], a[12:15], v[184:187]\n"
"ds_read_b128 a[52:55], v7 offset:4096\n"
"ds_read_b64_tr_b16 a[108:109], v9 offset:25088\n"
"v_mfma_f32_16x16x32_bf16 v[216:219], a[112:115], a[16:19], v[216:219]\n"
"ds_read_b64_tr_b16 a[110:111], v9 offset:26112\n"
"ds_read_b64_tr_b16 a[112:113], v8 offset:17152\n"
"v_mfma_f32_16x16x32_bf16 v[216:219], a[116:119], a[20:23], v[216:219]\n"
"ds_read_b64_tr_b16 a[114:115], v8 offset:18176\n"
"ds_read_b64_tr_b16 a[116:117], v8 offset:25344\n"
"v_mfma_f32_16x16x32_bf16 v[156:159], a[120:123], a[0:3], v[156:159]\n"
"ds_read_b64_tr_b16 a[118:119], v8 offset:26368\n"
"v_mfma_f32_16x16x32_bf16 v[156:159], a[124:127], a[4:7], v[156:159]\n"
"v_mfma_f32_16x16x32_bf16 v[188:191], a[120:123], a[8:11], v[188:191]\n"
"v_mfma_f32_16x16x32_bf16 v[188:191], a[124:127], a[12:15], v[188:191]\n"
"v_mfma_f32_16x16x32_bf16 v[220:223], a[120:123], a[16:19], v[220:223]\n"
"ds_read_b64_tr_b16 a[120:121], v9 offset:17152\n"
"ds_read_b64_tr_b16 a[122:123], v9 offset:18176\n"
"v_mfma_f32_16x16x32_bf16 v[220:223], a[124:127], a[20:23], v[220:223]\n"
"s_waitcnt lgkmcnt(13)\n"
"v_mfma_f32_16x16x32_bf16 v[16:19], a[64:67], a[32:35], v[16:19]\n"
"ds_read_b64_tr_b16 a[124:125], v9 offset:25344\n"
"ds_read_b64_tr_b16 a[126:127], v9 offset:26368\n"
"s_waitcnt lgkmcnt(14)\n"
"v_mfma_f32_16x16x32_bf16 v[16:19], a[68:71], a[36:39], v[16:19]\n"
"s_mov_b32 s40, %[wol]\n"
"s_and_b32 s41, %[woh], 0xffff\n"
"s_mov_b32 s42, 1047808\n"
"v_mbcnt_lo_u32_b32 v13, -1, 0\n"
"v_mbcnt_hi_u32_b32 v13, -1, v13\n"
"v_and_b32 v12, 1, v13\n"
"v_lshrrev_b32 v13, 1, v13\n"
"v_lshlrev_b32 v12, 7, v12\n"
"v_lshl_add_u32 v12, v13, 10, v12\n"
"s_mov_b32 m0, 161792\n"
"s_nop 0\n"
"buffer_load_dword v12, s[40:43], %[wto] offen lds\n"
"s_waitcnt lgkmcnt(13)\n"
"v_mfma_f32_16x16x32_bf16 v[44:47], a[64:67], a[40:43], v[44:47]\n"
"s_waitcnt lgkmcnt(12)\n"
"v_mfma_f32_16x16x32_bf16 v[44:47], a[68:71], a[44:47], v[44:47]\n"
"s_waitcnt lgkmcnt(11)\n"
"v_mfma_f32_16x16x32_bf16 v[72:75], a[64:67], a[48:51], v[72:75]\n"
"ds_read_b64_tr_b16 a[64:65], v8 offset:0\n"
"ds_read_b64_tr_b16 a[66:67], v8 offset:1024\n"
"s_waitcnt lgkmcnt(12)\n"
"v_mfma_f32_16x16x32_bf16 v[72:75], a[68:71], a[52:55], v[72:75]\n"
"v_mfma_f32_16x16x32_bf16 v[20:23], a[72:75], a[32:35], v[20:23]\n"
"ds_read_b64_tr_b16 a[68:69], v8 offset:8192\n"
"ds_read_b64_tr_b16 a[70:71], v8 offset:9216\n"
"v_mfma_f32_16x16x32_bf16 v[20:23], a[76:79], a[36:39], v[20:23]\n"
"v_mfma_f32_16x16x32_bf16 v[48:51], a[72:75], a[40:43], v[48:51]\n"
"v_mfma_f32_16x16x32_bf16 v[48:51], a[76:79], a[44:47], v[48:51]\n"
"v_mfma_f32_16x16x32_bf16 v[76:79], a[72:75], a[48:51], v[76:79]\n"
"ds_read_b64_tr_b16 a[72:73], v9 offset:0\n"
"ds_read_b64_tr_b16 a[74:75], v9 offset:1024\n"
"v_mfma_f32_16x16x32_bf16 v[76:79], a[76:79], a[52:55], v[76:79]\n"
"ds_read_b64_tr_b16 a[76:77], v9 offset:8192\n"
"ds_read_b64_tr_b16 a[78:79], v9 offset:9216\n"
"v_mfma_f32_16x16x32_bf16 v[24:27], a[80:83], a[32:35], v[24:27]\n"
"v_pk_mul_f32 v[16:17], v[16:17], s[68:69]\n"
"v_mfma_f32_16x16x32_bf16 v[24:27], a[84:87], a[36:39], v[24:27]\n"
"v_pk_mul_f32 v[18:19], v[18:19], s[68:69]\n"
"v_mfma_f32_16x16x32_bf16 v[52:55], a[80:83], a[40:43], v[52:55]\n"
"v_pk_mul_f32 v[20:21], v[20:21], s[68:69]\n"
"v_mfma_f32_16x16x32_bf16 v[52:55], a[84:87], a[44:47], v[52:55]\n"
"v_pk_mul_f32 v[22:23], v[22:23], s[68:69]\n"
"v_mfma_f32_16x16x32_bf16 v[80:83], a[80:83], a[48:51], v[80:83]\n"
"ds_read_b64_tr_b16 a[80:81], v8 offset:256\n"
"ds_read_b64_tr_b16 a[82:83], v8 offset:1280\n"
"v_mfma_f32_16x16x32_bf16 v[80:83], a[84:87], a[52:55], v[80:83]\n"
"v_cvt_pk_bf16_f32 v16, v16, v17\n"
"v_cvt_pk_bf16_f32 v17, v18, v19\n"
"v_mfma_f32_16x16x32_bf16 v[28:31], a[88:91], a[32:35], v[28:31]\n"
"v_cvt_pk_bf16_f32 v18, v20, v21\n"
"v_cvt_pk_bf16_f32 v19, v22, v23\n"
"v_mfma_f32_16x16x32_bf16 v[28:31], a[92:95], a[36:39], v[28:31]\n"
"ds_read_b64_tr_b16 a[84:85], v8 offset:8448\n"
"ds_read_b64_tr_b16 a[86:87], v8 offset:9472\n"
"v_mfma_f32_16x16x32_bf16 v[56:59], a[88:91], a[40:43], v[56:59]\n"
"v_permlane32_swap_b32 v16, v18\n"
"v_permlane32_swap_b32 v17, v19\n"
"v_mfma_f32_16x16x32_bf16 v[56:59], a[92:95], a[44:47], v[56:59]\n"
"v_permlane16_swap_b32 v16, v18\n"
"v_permlane16_swap_b32 v17, v19\n"
"v_mfma_f32_16x16x32_bf16 v[84:87], a[88:91], a[48:51], v[84:87]\n"
"ds_read_b64_tr_b16 a[88:89], v9 offset:256\n"
"ds_read_b64_tr_b16 a[90:91], v9 offset:1280\n"
"v_mfma_f32_16x16x32_bf16 v[84:87], a[92:95], a[52:55], v[84:87]\n"
"ds_read_b64_tr_b16 a[92:93], v9 offset:8448\n"
"ds_read_b64_tr_b16 a[94:95], v9 offset:9472\n"
"v_mfma_f32_16x16x32_bf16 v[32:35], a[96:99], a[32:35], v[32:35]\n"
"v_accvgpr_write_b32 a192, v16\n"
"v_accvgpr_write_b32 a193, v17\n"
"v_mfma_f32_16x16x32_bf16 v[32:35], a[100:103], a[36:39], v[32:35]\n"
"v_accvgpr_write_b32 a194, v18\n"
"v_accvgpr_write_b32 a195, v19\n"
"v_mfma_f32_16x16x32_bf16 v[60:63], a[96:99], a[40:43], v[60:63]\n"
"v_pk_mul_f32 v[44:45], v[44:45], s[68:69]\n"
"v_mfma_f32_16x16x32_bf16 v[60:63], a[100:103], a[44:47], v[60:63]\n"
"v_pk_mul_f32 v[46:47], v[46:47], s[68:69]\n"
"v_mfma_f32_16x16x32_bf16 v[88:91], a[96:99], a[48:51], v[88:91]\n"
"ds_read_b64_tr_b16 a[96:97], v8 offset:512\n"
"ds_read_b64_tr_b16 a[98:99], v8 offset:1536\n"
"v_mfma_f32_16x16x32_bf16 v[88:91], a[100:103], a[52:55], v[88:91]\n"
"v_pk_mul_f32 v[48:49], v[48:49], s[68:69]\n"
"v_mfma_f32_16x16x32_bf16 v[36:39], a[104:107], a[32:35], v[36:39]\n"
"ds_read_b64_tr_b16 a[100:101], v8 offset:8704\n"
"ds_read_b64_tr_b16 a[102:103], v8 offset:9728\n"
"s_waitcnt lgkmcnt(15)\n"
"v_mfma_f32_16x16x32_bf16 v[36:39], a[108:111], a[36:39], v[36:39]\n"
"v_pk_mul_f32 v[50:51], v[50:51], s[68:69]\n"
"v_mfma_f32_16x16x32_bf16 v[64:67], a[104:107], a[40:43], v[64:67]\n"
"v_cvt_pk_bf16_f32 v44, v44, v45\n"
"v_cvt_pk_bf16_f32 v45, v46, v47\n"
"v_mfma_f32_16x16x32_bf16 v[64:67], a[108:111], a[44:47], v[64:67]\n"
"v_cvt_pk_bf16_f32 v46, v48, v49\n"
"v_cvt_pk_bf16_f32 v47, v50, v51\n"
"v_mfma_f32_16x16x32_bf16 v[92:95], a[104:107], a[48:51], v[92:95]\n"
"ds_read_b64_tr_b16 a[104:105], v9 offset:512\n"
"ds_read_b64_tr_b16 a[106:107], v9 offset:1536\n"
"v_mfma_f32_16x16x32_bf16 v[92:95], a[108:111], a[52:55], v[92:95]\n"
"ds_read_b64_tr_b16 a[108:109], v9 offset:8704\n"
"ds_read_b64_tr_b16 a[110:111], v9 offset:9728\n"
"v_mfma_f32_16x16x32_bf16 v[40:43], a[112:115], a[32:35], v[40:43]\n"
"v_permlane32_swap_b32 v44, v46\n"
"v_permlane32_swap_b32 v45, v47\n"
"v_mfma_f32_16x16x32_bf16 v[40:43], a[116:119], a[36:39], v[40:43]\n"
"v_permlane16_swap_b32 v44, v46\n"
"v_permlane16_swap_b32 v45, v47\n"
"v_mfma_f32_16x16x32_bf16 v[68:71], a[112:115], a[40:43], v[68:71]\n"
"v_accvgpr_write_b32 a208, v44\n"
"v_accvgpr_write_b32 a209, v45\n"
"v_mfma_f32_16x16x32_bf16 v[68:71], a[116:119], a[44:47], v[68:71]\n"
"v_accvgpr_write_b32 a210, v46\n"
"v_accvgpr_write_b32 a211, v47\n"
"v_mfma_f32_16x16x32_bf16 v[96:99], a[112:115], a[48:51], v[96:99]\n"
"ds_read_b64_tr_b16 a[112:113], v8 offset:768\n"
"ds_read_b64_tr_b16 a[114:115], v8 offset:1792\n"
"v_mfma_f32_16x16x32_bf16 v[96:99], a[116:119], a[52:55], v[96:99]\n"
"v_pk_mul_f32 v[72:73], v[72:73], s[68:69]\n"
"v_mfma_f32_16x16x32_bf16 a[204:207], a[120:123], a[32:35], a[204:207]\n"
"v_pk_mul_f32 v[74:75], v[74:75], s[68:69]\n"
"v_mfma_f32_16x16x32_bf16 a[204:207], a[124:127], a[36:39], a[204:207]\n"
"ds_read_b64_tr_b16 a[116:117], v8 offset:8960\n"
"ds_read_b64_tr_b16 a[118:119], v8 offset:9984\n"
"v_mfma_f32_16x16x32_bf16 a[220:223], a[120:123], a[40:43], a[220:223]\n"
"v_pk_mul_f32 v[76:77], v[76:77], s[68:69]\n"
"v_mfma_f32_16x16x32_bf16 a[220:223], a[124:127], a[44:47], a[220:223]\n"
"v_pk_mul_f32 v[78:79], v[78:79], s[68:69]\n"
"v_mfma_f32_16x16x32_bf16 a[236:239], a[120:123], a[48:51], a[236:239]\n"
"ds_read_b64_tr_b16 a[120:121], v9 offset:768\n"
"ds_read_b64_tr_b16 a[122:123], v9 offset:1792\n"
"v_mfma_f32_16x16x32_bf16 a[236:239], a[124:127], a[52:55], a[236:239]\n"
"ds_read_b64_tr_b16 a[124:125], v9 offset:8960\n"
"ds_read_b64_tr_b16 a[126:127], v9 offset:9984\n"
"v_mfma_f32_16x16x32_bf16 v[128:131], a[64:67], a[32:35], v[128:131]\n"
"v_cvt_pk_bf16_f32 v72, v72, v73\n"
"v_cvt_pk_bf16_f32 v73, v74, v75\n"
"v_mfma_f32_16x16x32_bf16 v[128:131], a[68:71], a[36:39], v[128:131]\n"
"v_cvt_pk_bf16_f32 v74, v76, v77\n"
"v_cvt_pk_bf16_f32 v75, v78, v79\n"
"v_mfma_f32_16x16x32_bf16 v[160:163], a[64:67], a[40:43], v[160:163]\n"
"v_permlane32_swap_b32 v72, v74\n"
"v_permlane32_swap_b32 v73, v75\n"
"v_mfma_f32_16x16x32_bf16 v[160:163], a[68:71], a[44:47], v[160:163]\n"
"v_permlane16_swap_b32 v72, v74\n"
"v_permlane16_swap_b32 v73, v75\n"
"v_mfma_f32_16x16x32_bf16 v[192:195], a[64:67], a[48:51], v[192:195]\n"
"v_accvgpr_write_b32 a224, v72\n"
"v_accvgpr_write_b32 a225, v73\n"
"v_mfma_f32_16x16x32_bf16 v[192:195], a[68:71], a[52:55], v[192:195]\n"
"v_accvgpr_write_b32 a226, v74\n"
"v_accvgpr_write_b32 a227, v75\n"
"s_waitcnt lgkmcnt(15)\n"
"v_mfma_f32_16x16x32_bf16 v[132:135], a[72:75], a[32:35], v[132:135]\n"
"s_waitcnt lgkmcnt(0)\n"
"s_waitcnt vmcnt(63)\n"
"s_barrier\n"
"v_mfma_f32_16x16x32_bf16 v[132:135], a[76:79], a[36:39], v[132:135]\n"
"v_sub_u32 v10, v8, v14\n"
"v_sub_u32 v11, v9, v14\n"
"v_mfma_f32_16x16x32_bf16 v[164:167], a[72:75], a[40:43], v[164:167]\n"
"v_xor_b32 v6, 0, v15\n"
"v_xor_b32 v7, 64, v15\n"
"v_mfma_f32_16x16x32_bf16 v[164:167], a[76:79], a[44:47], v[164:167]\n"
"v_xor_b32 v8, 128, v15\n"
"v_xor_b32 v9, 192, v15\n"
"v_mfma_f32_16x16x32_bf16 v[196:199], a[72:75], a[48:51], v[196:199]\n"
"v_mbcnt_lo_u32_b32 v12, -1, 0\n"
"v_mbcnt_hi_u32_b32 v12, -1, v12\n"
"v_mfma_f32_16x16x32_bf16 v[196:199], a[76:79], a[52:55], v[196:199]\n"
"v_and_b32 v13, 3, v12\n"
"v_lshlrev_b32 v13, 5, v13\n"
"v_mfma_f32_16x16x32_bf16 v[136:139], a[80:83], a[32:35], v[136:139]\n"
"s_lshl_b32 s74, s77, 7\n"
"v_lshlrev_b32 v12, 4, v12\n"
"v_mfma_f32_16x16x32_bf16 v[136:139], a[84:87], a[36:39], v[136:139]\n"
"v_add_u32 v12, s74, v12\n"
"s_mov_b32 s66, 6\n"
"v_mfma_f32_16x16x32_bf16 v[168:171], a[80:83], a[40:43], v[168:171]\n"
"s_add_u32 s36, %[wsl], 0x2000000\n"
"s_addc_u32 s37, %[wsh], 0\n"
"s_and_b32 s37, s37, 0xffff\n"
"s_mov_b32 s38, 0x2000000\n"
"v_mfma_f32_16x16x32_bf16 v[168:171], a[84:87], a[44:47], v[168:171]\n"
"v_pk_mul_f32 v[128:129], v[128:129], s[68:69]\n"
"v_mfma_f32_16x16x32_bf16 v[200:203], a[80:83], a[48:51], v[200:203]\n"
"v_pk_mul_f32 v[130:131], v[130:131], s[68:69]\n"
"v_mfma_f32_16x16x32_bf16 v[200:203], a[84:87], a[52:55], v[200:203]\n"
"v_pk_mul_f32 v[132:133], v[132:133], s[68:69]\n"
"v_mfma_f32_16x16x32_bf16 v[140:143], a[88:91], a[32:35], v[140:143]\n"
"v_pk_mul_f32 v[134:135], v[134:135], s[68:69]\n"
"v_mfma_f32_16x16x32_bf16 v[140:143], a[92:95], a[36:39], v[140:143]\n"
"v_cvt_pk_bf16_f32 v128, v128, v129\n"
"v_cvt_pk_bf16_f32 v129, v130, v131\n"
"v_mfma_f32_16x16x32_bf16 v[172:175], a[88:91], a[40:43], v[172:175]\n"
"v_cvt_pk_bf16_f32 v130, v132, v133\n"
"v_cvt_pk_bf16_f32 v131, v134, v135\n"
"v_mfma_f32_16x16x32_bf16 v[172:175], a[92:95], a[44:47], v[172:175]\n"
"v_permlane32_swap_b32 v128, v130\n"
"v_permlane32_swap_b32 v129, v131\n"
"v_mfma_f32_16x16x32_bf16 v[204:207], a[88:91], a[48:51], v[204:207]\n"
"v_permlane16_swap_b32 v128, v130\n"
"v_permlane16_swap_b32 v129, v131\n"
"v_mfma_f32_16x16x32_bf16 v[204:207], a[92:95], a[52:55], v[204:207]\n"
"v_accvgpr_write_b32 a128, v128\n"
"v_accvgpr_write_b32 a129, v129\n"
"v_mfma_f32_16x16x32_bf16 v[144:147], a[96:99], a[32:35], v[144:147]\n"
"v_accvgpr_write_b32 a130, v130\n"
"v_accvgpr_write_b32 a131, v131\n"
"v_mfma_f32_16x16x32_bf16 v[144:147], a[100:103], a[36:39], v[144:147]\n"
"v_pk_mul_f32 v[24:25], v[24:25], s[68:69]\n"
"v_mfma_f32_16x16x32_bf16 v[176:179], a[96:99], a[40:43], v[176:179]\n"
"v_pk_mul_f32 v[26:27], v[26:27], s[68:69]\n"
"v_mfma_f32_16x16x32_bf16 v[176:179], a[100:103], a[44:47], v[176:179]\n"
"v_pk_mul_f32 v[28:29], v[28:29], s[68:69]\n"
"v_mfma_f32_16x16x32_bf16 v[208:211], a[96:99], a[48:51], v[208:211]\n"
"v_pk_mul_f32 v[30:31], v[30:31], s[68:69]\n"
"v_mfma_f32_16x16x32_bf16 v[208:211], a[100:103], a[52:55], v[208:211]\n"
"ds_read_b128 a[0:3], v6 offset:0\n"
"ds_read_b128 a[4:7], v7 offset:0\n"
"v_mfma_f32_16x16x32_bf16 v[148:151], a[104:107], a[32:35], v[148:151]\n"
"ds_read_b128 a[8:11], v8 offset:0\n"
"ds_read_b128 a[12:15], v9 offset:0\n"
"buffer_load_dword a96, v13, s[36:39], %[ma] offen offset:4 sc1\n"
"buffer_load_dword a97, v13, s[36:39], %[ma] offen offset:132 sc1\n"
"v_mfma_f32_16x16x32_bf16 v[148:151], a[108:111], a[36:39], v[148:151]\n"
"s_mov_b32 m0, s62\n"
"v_cvt_pk_bf16_f32 v24, v24, v25\n"
"v_mfma_f32_16x16x32_bf16 v[180:183], a[104:107], a[40:43], v[180:183]\n"
"buffer_load_dwordx4 v0, s[44:47], s60 offen offset:0 nt lds\n"
"v_mfma_f32_16x16x32_bf16 v[180:183], a[108:111], a[44:47], v[180:183]\n"
"ds_read_b128 a[16:19], v6 offset:4096\n"
"v_cvt_pk_bf16_f32 v25, v26, v27\n"
"v_mfma_f32_16x16x32_bf16 v[212:215], a[104:107], a[48:51], v[212:215]\n"
"ds_read_b128 a[20:23], v7 offset:4096\n"
"ds_read_b128 a[24:27], v8 offset:4096\n"
"v_mfma_f32_16x16x32_bf16 v[212:215], a[108:111], a[52:55], v[212:215]\n"
"ds_read_b128 a[28:31], v9 offset:4096\n"
"v_cvt_pk_bf16_f32 v26, v28, v29\n"
"v_mfma_f32_16x16x32_bf16 v[152:155], a[112:115], a[32:35], v[152:155]\n"
"v_cvt_pk_bf16_f32 v27, v30, v31\n"
"v_permlane32_swap_b32 v24, v26\n"
"v_mfma_f32_16x16x32_bf16 v[152:155], a[116:119], a[36:39], v[152:155]\n"
"v_permlane32_swap_b32 v25, v27\n"
"v_permlane16_swap_b32 v24, v26\n"
"v_mfma_f32_16x16x32_bf16 v[184:187], a[112:115], a[40:43], v[184:187]\n"
"v_permlane16_swap_b32 v25, v27\n"
"v_accvgpr_write_b32 a196, v24\n"
"v_mfma_f32_16x16x32_bf16 v[184:187], a[116:119], a[44:47], v[184:187]\n"
"v_accvgpr_write_b32 a197, v25\n"
"v_accvgpr_write_b32 a198, v26\n"
"v_mfma_f32_16x16x32_bf16 v[216:219], a[112:115], a[48:51], v[216:219]\n"
"v_accvgpr_write_b32 a199, v27\n"
"v_accvgpr_read_b32 v16, a204\n"
"v_mfma_f32_16x16x32_bf16 v[216:219], a[116:119], a[52:55], v[216:219]\n"
"buffer_load_dwordx4 v1, s[44:47], s60 offen offset:1024 nt lds\n"
"v_mfma_f32_16x16x32_bf16 v[156:159], a[120:123], a[32:35], v[156:159]\n"
"v_pk_mul_f32 v[52:53], v[52:53], s[68:69]\n"
"v_mfma_f32_16x16x32_bf16 v[156:159], a[124:127], a[36:39], v[156:159]\n"
"v_pk_mul_f32 v[54:55], v[54:55], s[68:69]\n"
"v_mfma_f32_16x16x32_bf16 v[188:191], a[120:123], a[40:43], v[188:191]\n"
"v_pk_mul_f32 v[56:57], v[56:57], s[68:69]\n"
"v_mfma_f32_16x16x32_bf16 v[188:191], a[124:127], a[44:47], v[188:191]\n"
"v_pk_mul_f32 v[58:59], v[58:59], s[68:69]\n"
"v_mfma_f32_16x16x32_bf16 v[220:223], a[120:123], a[48:51], v[220:223]\n"
"v_cvt_pk_bf16_f32 v52, v52, v53\n"
"v_cvt_pk_bf16_f32 v53, v54, v55\n"
"v_mfma_f32_16x16x32_bf16 v[220:223], a[124:127], a[52:55], v[220:223]\n"
"v_cvt_pk_bf16_f32 v54, v56, v57\n"
"v_cvt_pk_bf16_f32 v55, v58, v59\n"
"s_nop 0\n"
"v_permlane32_swap_b32 v52, v54\n"
"v_permlane32_swap_b32 v53, v55\n"
"s_nop 0\n"
"v_permlane16_swap_b32 v52, v54\n"
"v_permlane16_swap_b32 v53, v55\n"
"v_accvgpr_write_b32 a212, v52\n"
"v_accvgpr_write_b32 a213, v53\n"
"v_accvgpr_write_b32 a214, v54\n"
"v_accvgpr_write_b32 a215, v55\n"
"v_pk_mul_f32 v[80:81], v[80:81], s[68:69]\n"
"v_pk_mul_f32 v[82:83], v[82:83], s[68:69]\n"
"v_pk_mul_f32 v[84:85], v[84:85], s[68:69]\n"
"v_pk_mul_f32 v[86:87], v[86:87], s[68:69]\n"
"v_cvt_pk_bf16_f32 v80, v80, v81\n"
"v_cvt_pk_bf16_f32 v81, v82, v83\n"
"v_cvt_pk_bf16_f32 v82, v84, v85\n"
"v_cvt_pk_bf16_f32 v83, v86, v87\n"
"s_nop 0\n"
"v_permlane32_swap_b32 v80, v82\n"
"v_permlane32_swap_b32 v81, v83\n"
"s_nop 0\n"
"v_permlane16_swap_b32 v80, v82\n"
"v_permlane16_swap_b32 v81, v83\n"
"v_accvgpr_write_b32 a228, v80\n"
"v_accvgpr_write_b32 a229, v81\n"
"v_accvgpr_write_b32 a230, v82\n"
"v_accvgpr_write_b32 a231, v83\n"
"v_pk_mul_f32 v[32:33], v[32:33], s[68:69]\n"
"v_pk_mul_f32 v[34:35], v[34:35], s[68:69]\n"
"v_pk_mul_f32 v[36:37], v[36:37], s[68:69]\n"
"v_pk_mul_f32 v[38:39], v[38:39], s[68:69]\n"
"v_cvt_pk_bf16_f32 v32, v32, v33\n"
"v_cvt_pk_bf16_f32 v33, v34, v35\n"
"v_cvt_pk_bf16_f32 v34, v36, v37\n"
"v_cvt_pk_bf16_f32 v35, v38, v39\n"
"s_nop 0\n"
"v_permlane32_swap_b32 v32, v34\n"
"v_permlane32_swap_b32 v33, v35\n"
"s_nop 0\n"
"v_permlane16_swap_b32 v32, v34\n"
"v_permlane16_swap_b32 v33, v35\n"
"v_accvgpr_write_b32 a200, v32\n"
"v_accvgpr_write_b32 a201, v33\n"
"v_accvgpr_write_b32 a202, v34\n"
"v_accvgpr_write_b32 a203, v35\n"
"v_pk_mul_f32 v[60:61], v[60:61], s[68:69]\n"
"v_pk_mul_f32 v[62:63], v[62:63], s[68:69]\n"
"v_pk_mul_f32 v[64:65], v[64:65], s[68:69]\n"
"v_pk_mul_f32 v[66:67], v[66:67], s[68:69]\n"
"v_cvt_pk_bf16_f32 v60, v60, v61\n"
"v_cvt_pk_bf16_f32 v61, v62, v63\n"
"v_cvt_pk_bf16_f32 v62, v64, v65\n"
"v_cvt_pk_bf16_f32 v63, v66, v67\n"
"s_nop 0\n"
"v_permlane32_swap_b32 v60, v62\n"
"v_permlane32_swap_b32 v61, v63\n"
"s_nop 0\n"
"v_permlane16_swap_b32 v60, v62\n"
"v_permlane16_swap_b32 v61, v63\n"
"v_accvgpr_write_b32 a216, v60\n"
"v_accvgpr_write_b32 a217, v61\n"
"v_accvgpr_write_b32 a218, v62\n"
"v_accvgpr_write_b32 a219, v63\n"
"v_pk_mul_f32 v[88:89], v[88:89], s[68:69]\n"
"v_pk_mul_f32 v[90:91], v[90:91], s[68:69]\n"
"v_pk_mul_f32 v[92:93], v[92:93], s[68:69]\n"
"v_pk_mul_f32 v[94:95], v[94:95], s[68:69]\n"
"v_cvt_pk_bf16_f32 v88, v88, v89\n"
"v_cvt_pk_bf16_f32 v89, v90, v91\n"
"v_cvt_pk_bf16_f32 v90, v92, v93\n"
"v_cvt_pk_bf16_f32 v91, v94, v95\n"
"s_nop 0\n"
"v_permlane32_swap_b32 v88, v90\n"
"v_permlane32_swap_b32 v89, v91\n"
"s_nop 0\n"
"v_permlane16_swap_b32 v88, v90\n"
"v_permlane16_swap_b32 v89, v91\n"
"v_accvgpr_write_b32 a232, v88\n"
"v_accvgpr_write_b32 a233, v89\n"
"v_accvgpr_write_b32 a234, v90\n"
"v_accvgpr_write_b32 a235, v91\n"
"v_accvgpr_read_b32 v17, a205\n"
"v_accvgpr_read_b32 v18, a206\n"
"v_accvgpr_read_b32 v19, a207\n"
"v_pk_mul_f32 v[40:41], v[40:41], s[68:69]\n"
"v_pk_mul_f32 v[42:43], v[42:43], s[68:69]\n"
"v_pk_mul_f32 v[16:17], v[16:17], s[68:69]\n"
"v_pk_mul_f32 v[18:19], v[18:19], s[68:69]\n"
"v_cvt_pk_bf16_f32 v40, v40, v41\n"
"v_cvt_pk_bf16_f32 v41, v42, v43\n"
"v_cvt_pk_bf16_f32 v42, v16, v17\n"
"v_cvt_pk_bf16_f32 v43, v18, v19\n"
"s_nop 0\n"
"v_permlane32_swap_b32 v40, v42\n"
"v_permlane32_swap_b32 v41, v43\n"
"s_nop 0\n"
"v_permlane16_swap_b32 v40, v42\n"
"v_permlane16_swap_b32 v41, v43\n"
"v_accvgpr_write_b32 a204, v40\n"
"v_accvgpr_write_b32 a205, v41\n"
"v_accvgpr_write_b32 a206, v42\n"
"v_accvgpr_write_b32 a207, v43\n"
"v_accvgpr_read_b32 v44, a220\n"
"v_accvgpr_read_b32 v45, a221\n"
"v_accvgpr_read_b32 v46, a222\n"
"v_accvgpr_read_b32 v47, a223\n"
"v_pk_mul_f32 v[68:69], v[68:69], s[68:69]\n"
"v_pk_mul_f32 v[70:71], v[70:71], s[68:69]\n"
"v_pk_mul_f32 v[44:45], v[44:45], s[68:69]\n"
"v_pk_mul_f32 v[46:47], v[46:47], s[68:69]\n"
"v_cvt_pk_bf16_f32 v68, v68, v69\n"
"v_cvt_pk_bf16_f32 v69, v70, v71\n"
"v_cvt_pk_bf16_f32 v70, v44, v45\n"
"v_cvt_pk_bf16_f32 v71, v46, v47\n"
"s_nop 0\n"
"v_permlane32_swap_b32 v68, v70\n"
"v_permlane32_swap_b32 v69, v71\n"
"s_nop 0\n"
"v_permlane16_swap_b32 v68, v70\n"
"v_permlane16_swap_b32 v69, v71\n"
"v_accvgpr_write_b32 a220, v68\n"
"v_accvgpr_write_b32 a221, v69\n"
"v_accvgpr_write_b32 a222, v70\n"
"v_accvgpr_write_b32 a223, v71\n"
"v_accvgpr_read_b32 v72, a236\n"
"v_accvgpr_read_b32 v73, a237\n"
"v_accvgpr_read_b32 v74, a238\n"
"v_accvgpr_read_b32 v75, a239\n"
"v_pk_mul_f32 v[96:97], v[96:97], s[68:69]\n"
"v_pk_mul_f32 v[98:99], v[98:99], s[68:69]\n"
"v_pk_mul_f32 v[72:73], v[72:73], s[68:69]\n"
"v_pk_mul_f32 v[74:75], v[74:75], s[68:69]\n"
"v_cvt_pk_bf16_f32 v96, v96, v97\n"
"v_cvt_pk_bf16_f32 v97, v98, v99\n"
"v_cvt_pk_bf16_f32 v98, v72, v73\n"
"v_cvt_pk_bf16_f32 v99, v74, v75\n"
"s_nop 0\n"
"v_permlane32_swap_b32 v96, v98\n"
"v_permlane32_swap_b32 v97, v99\n"
"s_nop 0\n"
"v_permlane16_swap_b32 v96, v98\n"
"v_permlane16_swap_b32 v97, v99\n"
"v_accvgpr_write_b32 a236, v96\n"
"v_accvgpr_write_b32 a237, v97\n"
"v_accvgpr_write_b32 a238, v98\n"
"v_accvgpr_write_b32 a239, v99\n"
"v_pk_mul_f32 v[136:137], v[136:137], s[68:69]\n"
"v_pk_mul_f32 v[138:139], v[138:139], s[68:69]\n"
"v_pk_mul_f32 v[140:141], v[140:141], s[68:69]\n"
"v_pk_mul_f32 v[142:143], v[142:143], s[68:69]\n"
"v_cvt_pk_bf16_f32 v136, v136, v137\n"
"v_cvt_pk_bf16_f32 v137, v138, v139\n"
"v_cvt_pk_bf16_f32 v138, v140, v141\n"
"v_cvt_pk_bf16_f32 v139, v142, v143\n"
"s_nop 0\n"
"v_permlane32_swap_b32 v136, v138\n"
"v_permlane32_swap_b32 v137, v139\n"
"s_nop 0\n"
"v_permlane16_swap_b32 v136, v138\n"
"v_permlane16_swap_b32 v137, v139\n"
"v_accvgpr_write_b32 a132, v136\n"
"v_accvgpr_write_b32 a133, v137\n"
"v_accvgpr_write_b32 a134, v138\n"
"v_accvgpr_write_b32 a135, v139\n"
"s_waitcnt lgkmcnt(7)\n"
"v_mfma_f32_16x16x32_bf16 v[64:67], a[0:3], a[128:131], 0\n"
"v_pk_mul_f32 v[144:145], v[144:145], s[68:69]\n"
"v_pk_mul_f32 v[146:147], v[146:147], s[68:69]\n"
"v_pk_mul_f32 v[148:149], v[148:149], s[68:69]\n"
"v_pk_mul_f32 v[150:151], v[150:151], s[68:69]\n"
"v_cvt_pk_bf16_f32 v144, v144, v145\n"
"v_cvt_pk_bf16_f32 v145, v146, v147\n"
"v_cvt_pk_bf16_f32 v146, v148, v149\n"
"v_cvt_pk_bf16_f32 v147, v150, v151\n"
"s_nop 0\n"
"v_permlane32_swap_b32 v144, v146\n"
"v_permlane32_swap_b32 v145, v147\n"
"s_nop 0\n"
"v_permlane16_swap_b32 v144, v146\n"
"v_permlane16_swap_b32 v145, v147\n"
"v_accvgpr_write_b32 a136, v144\n"
"v_accvgpr_write_b32 a137, v145\n"
"v_accvgpr_write_b32 a138, v146\n"
"v_accvgpr_write_b32 a139, v147\n"
"s_waitcnt lgkmcnt(6)\n"
"v_mfma_f32_16x16x32_bf16 v[64:67], a[4:7], a[132:135], v[64:67]\n"
"v_pk_mul_f32 v[152:153], v[152:153], s[68:69]\n"
"v_pk_mul_f32 v[154:155], v[154:155], s[68:69]\n"
"v_pk_mul_f32 v[156:157], v[156:157], s[68:69]\n"
"v_pk_mul_f32 v[158:159], v[158:159], s[68:69]\n"
"v_cvt_pk_bf16_f32 v152, v152, v153\n"
"v_cvt_pk_bf16_f32 v153, v154, v155\n"
"v_cvt_pk_bf16_f32 v154, v156, v157\n"
"v_cvt_pk_bf16_f32 v155, v158, v159\n"
"s_nop 0\n"
"v_permlane32_swap_b32 v152, v154\n"
"v_permlane32_swap_b32 v153, v155\n"
"s_nop 0\n"
"v_permlane16_swap_b32 v152, v154\n"
"v_permlane16_swap_b32 v153, v155\n"
"v_accvgpr_write_b32 a140, v152\n"
"v_accvgpr_write_b32 a141, v153\n"
"v_accvgpr_write_b32 a142, v154\n"
"v_accvgpr_write_b32 a143, v155\n"
"s_waitcnt lgkmcnt(5)\n"
"v_mfma_f32_16x16x32_bf16 v[64:67], a[8:11], a[136:139], v[64:67]\n"
"buffer_load_dwordx4 v2, s[44:47], s60 offen offset:2048 nt lds\n"
"v_pk_mul_f32 v[160:161], v[160:161], s[68:69]\n"
"v_pk_mul_f32 v[162:163], v[162:163], s[68:69]\n"
"v_pk_mul_f32 v[164:165], v[164:165], s[68:69]\n"
"v_pk_mul_f32 v[166:167], v[166:167], s[68:69]\n"
"v_cvt_pk_bf16_f32 v160, v160, v161\n"
"v_cvt_pk_bf16_f32 v161, v162, v163\n"
"v_cvt_pk_bf16_f32 v162, v164, v165\n"
"v_cvt_pk_bf16_f32 v163, v166, v167\n"
"s_nop 0\n"
"v_permlane32_swap_b32 v160, v162\n"
"v_permlane32_swap_b32 v161, v163\n"
"s_nop 0\n"
"v_permlane16_swap_b32 v160, v162\n"
"v_permlane16_swap_b32 v161, v163\n"
"v_accvgpr_write_b32 a144, v160\n"
"v_accvgpr_write_b32 a145, v161\n"
"v_accvgpr_write_b32 a146, v162\n"
"v_accvgpr_write_b32 a147, v163\n"
"s_waitcnt lgkmcnt(4)\n"
"v_mfma_f32_16x16x32_bf16 v[64:67], a[12:15], a[140:143], v[64:67]\n"
"v_pk_mul_f32 v[168:169], v[168:169], s[68:69]\n"
"v_pk_mul_f32 v[170:171], v[170:171], s[68:69]\n"
"v_pk_mul_f32 v[172:173], v[172:173], s[68:69]\n"
"v_pk_mul_f32 v[174:175], v[174:175], s[68:69]\n"
"v_cvt_pk_bf16_f32 v168, v168, v169\n"
"v_cvt_pk_bf16_f32 v169, v170, v171\n"
"v_cvt_pk_bf16_f32 v170, v172, v173\n"
"v_cvt_pk_bf16_f32 v171, v174, v175\n"
"s_nop 0\n"
"v_permlane32_swap_b32 v168, v170\n"
"v_permlane32_swap_b32 v169, v171\n"
"s_nop 0\n"
"v_permlane16_swap_b32 v168, v170\n"
"v_permlane16_swap_b32 v169, v171\n"
"v_accvgpr_write_b32 a148, v168\n"
"v_accvgpr_write_b32 a149, v169\n"
"v_accvgpr_write_b32 a150, v170\n"
"v_accvgpr_write_b32 a151, v171\n"
"v_mfma_f32_16x16x32_bf16 v[80:83], a[0:3], a[144:147], 0\n"
"v_pk_mul_f32 v[176:177], v[176:177], s[68:69]\n"
"v_pk_mul_f32 v[178:179], v[178:179], s[68:69]\n"
"v_pk_mul_f32 v[180:181], v[180:181], s[68:69]\n"
"v_pk_mul_f32 v[182:183], v[182:183], s[68:69]\n"
"v_cvt_pk_bf16_f32 v176, v176, v177\n"
"v_cvt_pk_bf16_f32 v177, v178, v179\n"
"v_cvt_pk_bf16_f32 v178, v180, v181\n"
"v_cvt_pk_bf16_f32 v179, v182, v183\n"
"s_nop 0\n"
"v_permlane32_swap_b32 v176, v178\n"
"v_permlane32_swap_b32 v177, v179\n"
"s_nop 0\n"
"v_permlane16_swap_b32 v176, v178\n"
"v_permlane16_swap_b32 v177, v179\n"
"v_accvgpr_write_b32 a152, v176\n"
"v_accvgpr_write_b32 a153, v177\n"
"v_accvgpr_write_b32 a154, v178\n"
"v_accvgpr_write_b32 a155, v179\n"
"v_mfma_f32_16x16x32_bf16 v[80:83], a[4:7], a[148:151], v[80:83]\n"
"v_pk_mul_f32 v[184:185], v[184:185], s[68:69]\n"
"v_pk_mul_f32 v[186:187], v[186:187], s[68:69]\n"
"v_pk_mul_f32 v[188:189], v[188:189], s[68:69]\n"
"v_pk_mul_f32 v[190:191], v[190:191], s[68:69]\n"
"v_cvt_pk_bf16_f32 v184, v184, v185\n"
"v_cvt_pk_bf16_f32 v185, v186, v187\n"
"v_cvt_pk_bf16_f32 v186, v188, v189\n"
"v_cvt_pk_bf16_f32 v187, v190, v191\n"
"s_nop 0\n"
"v_permlane32_swap_b32 v184, v186\n"
"v_permlane32_swap_b32 v185, v187\n"
"s_nop 0\n"
"v_permlane16_swap_b32 v184, v186\n"
"v_permlane16_swap_b32 v185, v187\n"
"v_accvgpr_write_b32 a156, v184\n"
"v_accvgpr_write_b32 a157, v185\n"
"v_accvgpr_write_b32 a158, v186\n"
"v_accvgpr_write_b32 a159, v187\n"
"v_mfma_f32_16x16x32_bf16 v[80:83], a[8:11], a[152:155], v[80:83]\n"
"v_pk_mul_f32 v[192:193], v[192:193], s[68:69]\n"
"v_pk_mul_f32 v[194:195], v[194:195], s[68:69]\n"
"v_pk_mul_f32 v[196:197], v[196:197], s[68:69]\n"
"v_pk_mul_f32 v[198:199], v[198:199], s[68:69]\n"
"v_cvt_pk_bf16_f32 v192, v192, v193\n"
"v_cvt_pk_bf16_f32 v193, v194, v195\n"
"v_cvt_pk_bf16_f32 v194, v196, v197\n"
"v_cvt_pk_bf16_f32 v195, v198, v199\n"
"s_nop 0\n"
"v_permlane32_swap_b32 v192, v194\n"
"v_permlane32_swap_b32 v193, v195\n"
"s_nop 0\n"
"v_permlane16_swap_b32 v192, v194\n"
"v_permlane16_swap_b32 v193, v195\n"
"v_accvgpr_write_b32 a160, v192\n"
"v_accvgpr_write_b32 a161, v193\n"
"v_accvgpr_write_b32 a162, v194\n"
"v_accvgpr_write_b32 a163, v195\n"
"v_mfma_f32_16x16x32_bf16 v[80:83], a[12:15], a[156:159], v[80:83]\n"
"v_pk_mul_f32 v[200:201], v[200:201], s[68:69]\n"
"v_pk_mul_f32 v[202:203], v[202:203], s[68:69]\n"
"v_pk_mul_f32 v[204:205], v[204:205], s[68:69]\n"
"v_pk_mul_f32 v[206:207], v[206:207], s[68:69]\n"
"v_cvt_pk_bf16_f32 v200, v200, v201\n"
"v_cvt_pk_bf16_f32 v201, v202, v203\n"
"v_cvt_pk_bf16_f32 v202, v204, v205\n"
"v_cvt_pk_bf16_f32 v203, v206, v207\n"
"s_nop 0\n"
"v_permlane32_swap_b32 v200, v202\n"
"v_permlane32_swap_b32 v201, v203\n"
"s_nop 0\n"
"v_permlane16_swap_b32 v200, v202\n"
"v_permlane16_swap_b32 v201, v203\n"
"v_accvgpr_write_b32 a164, v200\n"
"v_accvgpr_write_b32 a165, v201\n"
"v_accvgpr_write_b32 a166, v202\n"
"v_accvgpr_write_b32 a167, v203\n"
"v_mfma_f32_16x16x32_bf16 v[96:99], a[0:3], a[160:163], 0\n"
"ds_read_b128 a[0:3], v6 offset:8192\n"
"v_pk_mul_f32 v[208:209], v[208:209], s[68:69]\n"
"v_pk_mul_f32 v[210:211], v[210:211], s[68:69]\n"
"v_pk_mul_f32 v[212:213], v[212:213], s[68:69]\n"
"v_pk_mul_f32 v[214:215], v[214:215], s[68:69]\n"
"v_cvt_pk_bf16_f32 v208, v208, v209\n"
"v_cvt_pk_bf16_f32 v209, v210, v211\n"
"v_cvt_pk_bf16_f32 v210, v212, v213\n"
"v_cvt_pk_bf16_f32 v211, v214, v215\n"
"s_nop 0\n"
"v_permlane32_swap_b32 v208, v210\n"
"v_permlane32_swap_b32 v209, v211\n"
"s_nop 0\n"
"v_permlane16_swap_b32 v208, v210\n"
"v_permlane16_swap_b32 v209, v211\n"
"v_accvgpr_write_b32 a168, v208\n"
"v_accvgpr_write_b32 a169, v209\n"
"v_accvgpr_write_b32 a170, v210\n"
"v_accvgpr_write_b32 a171, v211\n"
"v_mfma_f32_16x16x32_bf16 v[96:99], a[4:7], a[164:167], v[96:99]\n"
"ds_read_b128 a[4:7], v7 offset:8192\n"
"v_pk_mul_f32 v[216:217], v[216:217], s[68:69]\n"
"v_pk_mul_f32 v[218:219], v[218:219], s[68:69]\n"
"v_pk_mul_f32 v[220:221], v[220:221], s[68:69]\n"
"v_pk_mul_f32 v[222:223], v[222:223], s[68:69]\n"
"v_cvt_pk_bf16_f32 v216, v216, v217\n"
"v_cvt_pk_bf16_f32 v217, v218, v219\n"
"v_cvt_pk_bf16_f32 v218, v220, v221\n"
"v_cvt_pk_bf16_f32 v219, v222, v223\n"
"s_nop 0\n"
"v_permlane32_swap_b32 v216, v218\n"
"v_permlane32_swap_b32 v217, v219\n"
"s_nop 0\n"
"v_permlane16_swap_b32 v216, v218\n"
"v_permlane16_swap_b32 v217, v219\n"
"v_accvgpr_write_b32 a172, v216\n"
"v_accvgpr_write_b32 a173, v217\n"
"v_accvgpr_write_b32 a174, v218\n"
"v_accvgpr_write_b32 a175, v219\n"
"v_mfma_f32_16x16x32_bf16 v[96:99], a[8:11], a[168:171], v[96:99]\n"
"ds_read_b128 a[8:11], v8 offset:8192\n"
"v_max3_f32 v16, v64, v65, v66\n"
"v_mfma_f32_16x16x32_bf16 v[96:99], a[12:15], a[172:175], v[96:99]\n"
"buffer_load_dwordx4 v3, s[44:47], s60 offen offset:3072 nt lds\n"
"v_max_f32 v16, v16, v67\n"
"v_mov_b32 v17, v16\n"
"s_nop 1\n"
"v_permlane32_swap_b32 v16, v17\n"
"v_max_f32 v16, v16, v17\n"
"v_mov_b32 v17, v16\n"
"s_nop 1\n"
"v_permlane16_swap_b32 v16, v17\n"
"v_min_f32 v18, -v16, -v17\n"
"v_mov_b32 v19, v18\n"
"v_mov_b32 v16, v18\n"
"v_mov_b32 v17, v18\n"
"s_waitcnt lgkmcnt(6)\n"
"s_nop 0\n"
"v_mfma_f32_16x16x32_bf16 v[68:71], a[16:19], a[128:131], v[16:19]\n"
"ds_read_b128 a[12:15], v9 offset:8192\n"
"v_max3_f32 v20, v80, v81, v82\n"
"s_waitcnt lgkmcnt(6)\n"
"v_mfma_f32_16x16x32_bf16 v[68:71], a[20:23], a[132:135], v[68:71]\n"
"v_max_f32 v20, v20, v83\n"
"v_mov_b32 v21, v20\n"
"s_waitcnt lgkmcnt(5)\n"
"v_mfma_f32_16x16x32_bf16 v[68:71], a[24:27], a[136:139], v[68:71]\n"
"v_permlane32_swap_b32 v20, v21\n"
"v_max_f32 v20, v20, v21\n"
"s_waitcnt lgkmcnt(4)\n"
"v_mfma_f32_16x16x32_bf16 v[68:71], a[28:31], a[140:143], v[68:71]\n"
"v_mov_b32 v21, v20\n"
"s_nop 1\n"
"v_permlane16_swap_b32 v20, v21\n"
"v_min_f32 v22, -v20, -v21\n"
"v_mov_b32 v23, v22\n"
"v_mov_b32 v20, v22\n"
"v_mov_b32 v21, v22\n"
"s_nop 1\n"
"v_mfma_f32_16x16x32_bf16 v[84:87], a[16:19], a[144:147], v[20:23]\n"
"v_max3_f32 v24, v96, v97, v98\n"
"v_max_f32 v24, v24, v99\n"
"v_mfma_f32_16x16x32_bf16 v[84:87], a[20:23], a[148:151], v[84:87]\n"
"v_mov_b32 v25, v24\n"
"s_nop 1\n"
"v_permlane32_swap_b32 v24, v25\n"
"v_mfma_f32_16x16x32_bf16 v[84:87], a[24:27], a[152:155], v[84:87]\n"
"v_max_f32 v24, v24, v25\n"
"v_mov_b32 v25, v24\n"
"v_mfma_f32_16x16x32_bf16 v[84:87], a[28:31], a[156:159], v[84:87]\n"
"s_mov_b32 m0, s63\n"
"v_permlane16_swap_b32 v24, v25\n"
"v_min_f32 v26, -v24, -v25\n"
"v_mov_b32 v27, v26\n"
"v_mov_b32 v24, v26\n"
"v_mov_b32 v25, v26\n"
"s_nop 1\n"
"v_mfma_f32_16x16x32_bf16 v[100:103], a[16:19], a[160:163], v[24:27]\n"
"buffer_load_dwordx4 v4, s[48:51], s61 offen offset:0 nt lds\n"
"v_mfma_f32_16x16x32_bf16 v[100:103], a[20:23], a[164:167], v[100:103]\n"
"ds_read_b128 a[16:19], v6 offset:12288\n"
"ds_read_b128 a[20:23], v7 offset:12288\n"
"v_mfma_f32_16x16x32_bf16 v[100:103], a[24:27], a[168:171], v[100:103]\n"
"ds_read_b128 a[24:27], v8 offset:12288\n"
"v_add_f32 v64, v64, v18\n"
"v_mfma_f32_16x16x32_bf16 v[100:103], a[28:31], a[172:175], v[100:103]\n"
"ds_read_b128 a[28:31], v9 offset:12288\n"
"v_add_f32 v65, v65, v18\n"
"s_waitcnt lgkmcnt(7)\n"
"v_mfma_f32_16x16x32_bf16 v[72:75], a[0:3], a[128:131], v[16:19]\n"
"v_exp_f32 v64, v64\n"
"s_waitcnt lgkmcnt(6)\n"
"v_mfma_f32_16x16x32_bf16 v[72:75], a[4:7], a[132:135], v[72:75]\n"
"v_exp_f32 v65, v65\n"
"s_waitcnt lgkmcnt(5)\n"
"v_mfma_f32_16x16x32_bf16 v[72:75], a[8:11], a[136:139], v[72:75]\n"
"v_add_f32 v66, v66, v18\n"
"v_add_f32 v67, v67, v18\n"
"s_waitcnt lgkmcnt(4)\n"
"v_mfma_f32_16x16x32_bf16 v[72:75], a[12:15], a[140:143], v[72:75]\n"
"v_exp_f32 v66, v66\n"
"v_mfma_f32_16x16x32_bf16 v[88:91], a[0:3], a[144:147], v[20:23]\n"
"v_exp_f32 v67, v67\n"
"v_mfma_f32_16x16x32_bf16 v[88:91], a[4:7], a[148:151], v[88:91]\n"
"buffer_load_dwordx4 v5, s[48:51], s61 offen offset:1024 nt lds\n"
"v_mfma_f32_16x16x32_bf16 v[88:91], a[8:11], a[152:155], v[88:91]\n"
"v_add_f32 v80, v80, v22\n"
"v_add_f32 v81, v81, v22\n"
"v_mfma_f32_16x16x32_bf16 v[88:91], a[12:15], a[156:159], v[88:91]\n"
"v_exp_f32 v80, v80\n"
"v_mfma_f32_16x16x32_bf16 v[104:107], a[0:3], a[160:163], v[24:27]\n"
"v_exp_f32 v81, v81\n"
"v_mfma_f32_16x16x32_bf16 v[104:107], a[4:7], a[164:167], v[104:107]\n"
"v_add_f32 v82, v82, v22\n"
"v_add_f32 v83, v83, v22\n"
"v_mfma_f32_16x16x32_bf16 v[104:107], a[8:11], a[168:171], v[104:107]\n"
"v_exp_f32 v82, v82\n"
"v_mfma_f32_16x16x32_bf16 v[104:107], a[12:15], a[172:175], v[104:107]\n"
"v_exp_f32 v83, v83\n"
"s_waitcnt lgkmcnt(3)\n"
"v_mfma_f32_16x16x32_bf16 v[76:79], a[16:19], a[128:131], v[16:19]\n"
"v_add_f32 v96, v96, v26\n"
"v_add_f32 v97, v97, v26\n"
"s_waitcnt lgkmcnt(2)\n"
"v_mfma_f32_16x16x32_bf16 v[76:79], a[20:23], a[132:135], v[76:79]\n"
"v_exp_f32 v68, v68\n"
"s_waitcnt lgkmcnt(1)\n"
"v_mfma_f32_16x16x32_bf16 v[76:79], a[24:27], a[136:139], v[76:79]\n"
"buffer_load_dwordx4 v4, s[48:51], s61 offen offset:2048 nt lds\n"
"s_waitcnt lgkmcnt(0)\n"
"v_mfma_f32_16x16x32_bf16 v[76:79], a[28:31], a[140:143], v[76:79]\n"
"v_exp_f32 v69, v69\n"
"v_mfma_f32_16x16x32_bf16 v[92:95], a[16:19], a[144:147], v[20:23]\n"
"v_exp_f32 v70, v70\n"
"v_mfma_f32_16x16x32_bf16 v[92:95], a[20:23], a[148:151], v[92:95]\n"
"v_exp_f32 v71, v71\n"
"v_mfma_f32_16x16x32_bf16 v[92:95], a[24:27], a[152:155], v[92:95]\n"
"v_exp_f32 v84, v84\n"
"v_mfma_f32_16x16x32_bf16 v[92:95], a[28:31], a[156:159], v[92:95]\n"
"v_exp_f32 v85, v85\n"
"v_mfma_f32_16x16x32_bf16 v[108:111], a[16:19], a[160:163], v[24:27]\n"
"v_exp_f32 v86, v86\n"
"v_mfma_f32_16x16x32_bf16 v[108:111], a[20:23], a[164:167], v[108:111]\n"
"v_exp_f32 v87, v87\n"
"v_mfma_f32_16x16x32_bf16 v[108:111], a[24:27], a[168:171], v[108:111]\n"
"v_exp_f32 v100, v100\n"
"v_mfma_f32_16x16x32_bf16 v[108:111], a[28:31], a[172:175], v[108:111]\n"
"buffer_load_dwordx4 v5, s[48:51], s61 offen offset:3072 nt lds\n"
"v_xor_b32 v6, s64, v6\n"
"v_xor_b32 v7, s64, v7\n"
"v_xor_b32 v8, s64, v8\n"
"v_xor_b32 v9, s64, v9\n"
"v_xor_b32 v10, s65, v10\n"
"v_xor_b32 v11, s65, v11\n"
"s_xor_b32 s62, s62, s65\n"
"s_xor_b32 s63, s63, s64\n"
"s_add_u32 s60, s60, 0x4000\n"
"s_add_u32 s61, s61, 0x4000\n"
"s_xor_b32 s64, s64, 0xc000\n"
"s_xor_b32 s65, s65, 0xc000\n"
"v_exp_f32 v101, v101\n"
"v_exp_f32 v102, v102\n"
"v_exp_f32 v103, v103\n"
"v_exp_f32 v72, v72\n"
"v_exp_f32 v73, v73\n"
"v_exp_f32 v74, v74\n"
"v_exp_f32 v75, v75\n"
"v_exp_f32 v88, v88\n"
"v_exp_f32 v89, v89\n"
"v_exp_f32 v90, v90\n"
"v_cvt_pk_bf16_f32 v32, v64, v65\n"
"v_cvt_pk_bf16_f32 v33, v66, v67\n"
"v_cvt_pk_bf16_f32 v34, v68, v69\n"
"v_cvt_pk_bf16_f32 v35, v70, v71\n"
"v_cvt_pk_bf16_f32 v36, v72, v73\n"
"v_cvt_pk_bf16_f32 v37, v74, v75\n"
"v_exp_f32 v91, v91\n"
"v_exp_f32 v104, v104\n"
"v_exp_f32 v105, v105\n"
"v_exp_f32 v106, v106\n"
"v_exp_f32 v107, v107\n"
"v_exp_f32 v76, v76\n"
"v_exp_f32 v77, v77\n"
"v_exp_f32 v78, v78\n"
"v_exp_f32 v79, v79\n"
"v_cvt_pk_bf16_f32 v38, v76, v77\n"
"v_cvt_pk_bf16_f32 v39, v78, v79\n"
"v_exp_f32 v92, v92\n"
"v_exp_f32 v93, v93\n"
"v_exp_f32 v94, v94\n"
"v_exp_f32 v95, v95\n"
"v_exp_f32 v108, v108\n"
"s_waitcnt vmcnt(63)\n"
"s_barrier\n"
"ds_read_b128 a[0:3], v6 offset:0\n"
"ds_read_b128 a[4:7], v7 offset:0\n"
"ds_read_b128 a[8:11], v8 offset:0\n"
"ds_read_b128 a[12:15], v9 offset:0\n"
"s_mov_b32 m0, s62\n"
"v_cvt_pk_bf16_f32 v40, v80, v81\n"
"buffer_load_dwordx4 v0, s[44:47], s60 offen offset:0 nt lds\n"
"v_cvt_pk_bf16_f32 v41, v82, v83\n"
"ds_read_b128 a[16:19], v6 offset:4096\n"
"ds_read_b128 a[20:23], v7 offset:4096\n"
"ds_read_b128 a[24:27], v8 offset:4096\n"
"ds_read_b128 a[28:31], v9 offset:4096\n"
"v_cvt_pk_bf16_f32 v42, v84, v85\n"
"v_cvt_pk_bf16_f32 v43, v86, v87\n"
"v_cvt_pk_bf16_f32 v44, v88, v89\n"
"v_cvt_pk_bf16_f32 v45, v90, v91\n"
"v_cvt_pk_bf16_f32 v46, v92, v93\n"
"v_cvt_pk_bf16_f32 v47, v94, v95\n"
"ds_read_b64_tr_b16 a[32:33], v10 offset:0\n"
"ds_read_b64_tr_b16 a[34:35], v10 offset:4096\n"
"ds_read_b64_tr_b16 a[36:37], v10 offset:8192\n"
"v_exp_f32 v109, v109\n"
"buffer_load_dwordx4 v1, s[44:47], s60 offen offset:1024 nt lds\n"
"v_exp_f32 v110, v110\n"
"v_exp_f32 v111, v111\n"
"ds_read_b64_tr_b16 a[38:39], v10 offset:12288\n"
"ds_read_b64_tr_b16 a[40:41], v11 offset:0\n"
"ds_read_b64_tr_b16 a[42:43], v11 offset:4096\n"
"ds_read_b64_tr_b16 a[44:45], v11 offset:8192\n"
"v_exp_f32 v96, v96\n"
"v_exp_f32 v97, v97\n"
"s_nop 0\n"
"v_cvt_pk_bf16_f32 v48, v96, v97\n"
"v_add_f32 v98, v98, v26\n"
"v_exp_f32 v98, v98\n"
"v_add_f32 v99, v99, v26\n"
"v_exp_f32 v99, v99\n"
"s_nop 0\n"
"v_cvt_pk_bf16_f32 v49, v98, v99\n"
"v_mov_b32 v14, 0x3f803f80\n"
"v_accvgpr_write_b32 a112, v14\n"
"s_waitcnt lgkmcnt(14)\n"
"v_mfma_f32_16x16x32_bf16 v[64:67], a[0:3], a[128:131], v[16:19]\n"
"v_cvt_pk_bf16_f32 v50, v100, v101\n"
"v_cvt_pk_bf16_f32 v51, v102, v103\n"
"v_accvgpr_write_b32 a113, v14\n"
"v_accvgpr_write_b32 a114, v14\n"
"v_accvgpr_write_b32 a115, v14\n"
"s_waitcnt lgkmcnt(13)\n"
"v_mfma_f32_16x16x32_bf16 v[64:67], a[4:7], a[132:135], v[64:67]\n"
"v_cvt_pk_bf16_f32 v52, v104, v105\n"
"v_cvt_pk_bf16_f32 v53, v106, v107\n"
"s_waitcnt lgkmcnt(12)\n"
"v_mfma_f32_16x16x32_bf16 v[64:67], a[8:11], a[136:139], v[64:67]\n"
"buffer_load_dwordx4 v2, s[44:47], s60 offen offset:2048 nt lds\n"
"s_waitcnt lgkmcnt(11)\n"
"v_mfma_f32_16x16x32_bf16 v[64:67], a[12:15], a[140:143], v[64:67]\n"
"v_cvt_pk_bf16_f32 v54, v108, v109\n"
"v_cvt_pk_bf16_f32 v55, v110, v111\n"
"v_mfma_f32_16x16x32_bf16 v[80:83], a[0:3], a[144:147], v[20:23]\n"
"ds_read_b64_tr_b16 a[46:47], v11 offset:12288\n"
"ds_read_b64_tr_b16 a[48:49], v10 offset:256\n"
"v_mfma_f32_16x16x32_bf16 v[80:83], a[4:7], a[148:151], v[80:83]\n"
"ds_read_b64_tr_b16 a[50:51], v10 offset:4352\n"
"ds_read_b64_tr_b16 a[52:53], v10 offset:8448\n"
"v_mfma_f32_16x16x32_bf16 v[80:83], a[8:11], a[152:155], v[80:83]\n"
"ds_read_b64_tr_b16 a[54:55], v10 offset:12544\n"
"ds_read_b64_tr_b16 a[56:57], v11 offset:256\n"
"v_mfma_f32_16x16x32_bf16 v[80:83], a[12:15], a[156:159], v[80:83]\n"
"ds_read_b64_tr_b16 a[58:59], v11 offset:4352\n"
"ds_read_b64_tr_b16 a[60:61], v11 offset:8448\n"
"v_mfma_f32_16x16x32_bf16 v[96:99], a[0:3], a[160:163], v[24:27]\n"
"ds_read_b128 a[0:3], v6 offset:8192\n"
"ds_read_b64_tr_b16 a[62:63], v11 offset:12544\n"
"v_mfma_f32_16x16x32_bf16 v[96:99], a[4:7], a[164:167], v[96:99]\n"
"ds_read_b128 a[4:7], v7 offset:8192\n"
"ds_read_b64_tr_b16 a[64:65], v10 offset:512\n"
"v_mfma_f32_16x16x32_bf16 v[96:99], a[8:11], a[168:171], v[96:99]\n"
"ds_read_b128 a[8:11], v8 offset:8192\n"
"ds_read_b64_tr_b16 a[66:67], v10 offset:4608\n"
"v_mfma_f32_16x16x32_bf16 v[96:99], a[12:15], a[172:175], v[96:99]\n"
"buffer_load_dwordx4 v3, s[44:47], s60 offen offset:3072 nt lds\n"
"s_waitcnt lgkmcnt(15)\n"
"v_mfma_f32_16x16x32_bf16 v[68:71], a[16:19], a[128:131], v[16:19]\n"
"ds_read_b128 a[12:15], v9 offset:8192\n"
"ds_read_b64_tr_b16 a[68:69], v10 offset:8704\n"
"v_mfma_f32_16x16x32_bf16 v[68:71], a[20:23], a[132:135], v[68:71]\n"
"ds_read_b64_tr_b16 a[70:71], v10 offset:12800\n"
"ds_read_b64_tr_b16 a[72:73], v11 offset:512\n"
"v_mfma_f32_16x16x32_bf16 v[68:71], a[24:27], a[136:139], v[68:71]\n"
"ds_read_b64_tr_b16 a[74:75], v11 offset:4608\n"
"ds_read_b64_tr_b16 a[76:77], v11 offset:8704\n"
"v_mfma_f32_16x16x32_bf16 v[68:71], a[28:31], a[140:143], v[68:71]\n"
"ds_read_b64_tr_b16 a[78:79], v11 offset:12800\n"
"ds_read_b64_tr_b16 a[80:81], v10 offset:768\n"
"v_mfma_f32_16x16x32_bf16 v[84:87], a[16:19], a[144:147], v[20:23]\n"
"ds_read_b64_tr_b16 a[82:83], v10 offset:4864\n"
"ds_read_b64_tr_b16 a[84:85], v10 offset:8960\n"
"v_mfma_f32_16x16x32_bf16 v[84:87], a[20:23], a[148:151], v[84:87]\n"
"ds_read_b64_tr_b16 a[86:87], v10 offset:13056\n"
"ds_read_b64_tr_b16 a[88:89], v11 offset:768\n"
"v_mfma_f32_16x16x32_bf16 v[84:87], a[24:27], a[152:155], v[84:87]\n"
"ds_read_b64_tr_b16 a[90:91], v11 offset:4864\n"
"ds_read_b64_tr_b16 a[92:93], v11 offset:8960\n"
"v_mfma_f32_16x16x32_bf16 v[84:87], a[28:31], a[156:159], v[84:87]\n"
"s_mov_b32 m0, s63\n"
"ds_read_b64_tr_b16 a[94:95], v11 offset:13056\n"
"v_mfma_f32_16x16x32_bf16 v[100:103], a[16:19], a[160:163], v[24:27]\n"
"buffer_load_dwordx4 v4, s[48:51], s61 offen offset:0 nt lds\n"
"v_mfma_f32_16x16x32_bf16 v[100:103], a[20:23], a[164:167], v[100:103]\n"
"ds_read_b128 a[16:19], v6 offset:12288\n"
"ds_read_b128 a[20:23], v7 offset:12288\n"
"v_mfma_f32_16x16x32_bf16 v[100:103], a[24:27], a[168:171], v[100:103]\n"
"ds_read_b128 a[24:27], v8 offset:12288\n"
"v_mfma_f32_16x16x32_bf16 v[100:103], a[28:31], a[172:175], v[100:103]\n"
"ds_read_b128 a[28:31], v9 offset:12288\n"
"s_waitcnt lgkmcnt(15)\n"
"v_mfma_f32_16x16x32_bf16 v[72:75], a[0:3], a[128:131], v[16:19]\n"
"v_exp_f32 v64, v64\n"
"v_mfma_f32_16x16x32_bf16 v[72:75], a[4:7], a[132:135], v[72:75]\n"
"v_exp_f32 v65, v65\n"
"v_mfma_f32_16x16x32_bf16 v[72:75], a[8:11], a[136:139], v[72:75]\n"
"v_exp_f32 v66, v66\n"
"v_mfma_f32_16x16x32_bf16 v[72:75], a[12:15], a[140:143], v[72:75]\n"
"v_exp_f32 v67, v67\n"
"v_mfma_f32_16x16x32_bf16 v[88:91], a[0:3], a[144:147], v[20:23]\n"
"v_exp_f32 v80, v80\n"
"v_mfma_f32_16x16x32_bf16 v[88:91], a[4:7], a[148:151], v[88:91]\n"
"buffer_load_dwordx4 v5, s[48:51], s61 offen offset:1024 nt lds\n"
"v_mfma_f32_16x16x32_bf16 v[88:91], a[8:11], a[152:155], v[88:91]\n"
"v_exp_f32 v81, v81\n"
"v_mfma_f32_16x16x32_bf16 v[88:91], a[12:15], a[156:159], v[88:91]\n"
"v_exp_f32 v82, v82\n"
"v_mfma_f32_16x16x32_bf16 v[104:107], a[0:3], a[160:163], v[24:27]\n"
"v_exp_f32 v83, v83\n"
"v_mfma_f32_16x16x32_bf16 v[104:107], a[4:7], a[164:167], v[104:107]\n"
"v_exp_f32 v96, v96\n"
"v_mfma_f32_16x16x32_bf16 v[104:107], a[8:11], a[168:171], v[104:107]\n"
"v_exp_f32 v97, v97\n"
"v_mfma_f32_16x16x32_bf16 v[104:107], a[12:15], a[172:175], v[104:107]\n"
"v_exp_f32 v98, v98\n"
"s_waitcnt lgkmcnt(3)\n"
"v_mfma_f32_16x16x32_bf16 v[76:79], a[16:19], a[128:131], v[16:19]\n"
"v_exp_f32 v99, v99\n"
"s_waitcnt lgkmcnt(2)\n"
"v_mfma_f32_16x16x32_bf16 v[76:79], a[20:23], a[132:135], v[76:79]\n"
"v_exp_f32 v68, v68\n"
"s_waitcnt lgkmcnt(1)\n"
"v_mfma_f32_16x16x32_bf16 v[76:79], a[24:27], a[136:139], v[76:79]\n"
"buffer_load_dwordx4 v4, s[48:51], s61 offen offset:2048 nt lds\n"
"s_waitcnt lgkmcnt(0)\n"
"v_mfma_f32_16x16x32_bf16 v[76:79], a[28:31], a[140:143], v[76:79]\n"
"v_exp_f32 v69, v69\n"
"v_mfma_f32_16x16x32_bf16 v[92:95], a[16:19], a[144:147], v[20:23]\n"
"v_exp_f32 v70, v70\n"
"v_mfma_f32_16x16x32_bf16 v[92:95], a[20:23], a[148:151], v[92:95]\n"
"v_exp_f32 v71, v71\n"
"v_mfma_f32_16x16x32_bf16 v[92:95], a[24:27], a[152:155], v[92:95]\n"
"v_exp_f32 v84, v84\n"
"v_mfma_f32_16x16x32_bf16 v[92:95], a[28:31], a[156:159], v[92:95]\n"
"v_exp_f32 v85, v85\n"
"v_mfma_f32_16x16x32_bf16 v[108:111], a[16:19], a[160:163], v[24:27]\n"
"v_exp_f32 v86, v86\n"
"v_mfma_f32_16x16x32_bf16 v[108:111], a[20:23], a[164:167], v[108:111]\n"
"v_exp_f32 v87, v87\n"
"s_waitcnt vmcnt(16)\n"
"v_accvgpr_read_b32 v14, a96\n"
"v_add_u32 v14, 1, v14\n"
"v_accvgpr_write_b32 a116, v14\n"
"s_waitcnt vmcnt(15)\n"
"v_accvgpr_read_b32 v14, a97\n"
"v_add_u32 v14, 1, v14\n"
"v_accvgpr_write_b32 a118, v14\n"
"v_lshrrev_b32 v15, 2, v13\n"
"s_mov_b64 exec, 15\n"
"buffer_store_dword a116, v15, s[36:39], %[mw] offen offset:0 sc1\n"
"buffer_store_dword a118, v15, s[36:39], %[mw] offen offset:128 sc1\n"
"s_mov_b64 exec, -1\n"
"v_mfma_f32_16x16x32_bf16 v[108:111], a[24:27], a[168:171], v[108:111]\n"
"v_exp_f32 v100, v100\n"
"v_mfma_f32_16x16x32_bf16 v[108:111], a[28:31], a[172:175], v[108:111]\n"
"buffer_load_dwordx4 v5, s[48:51], s61 offen offset:3072 nt lds\n"
"v_mfma_f32_16x16x32_bf16 a[96:99], a[112:115], v[32:35], 0\n"
"v_xor_b32 v6, s64, v6\n"
"v_xor_b32 v7, s64, v7\n"
"v_mfma_f32_16x16x32_bf16 a[96:99], a[112:115], v[36:39], a[96:99]\n"
"v_xor_b32 v8, s64, v8\n"
"v_xor_b32 v9, s64, v9\n"
"v_mfma_f32_16x16x32_bf16 v[128:131], a[32:35], v[32:35], 0\n"
"v_xor_b32 v10, s65, v10\n"
"v_xor_b32 v11, s65, v11\n"
"v_mfma_f32_16x16x32_bf16 v[128:131], a[36:39], v[36:39], v[128:131]\n"
"s_xor_b32 s62, s62, s65\n"
"s_xor_b32 s63, s63, s64\n"
"v_mfma_f32_16x16x32_bf16 v[132:135], a[40:43], v[32:35], 0\n"
"s_add_u32 s60, s60, 0x4000\n"
"s_add_u32 s61, s61, 0x4000\n"
"v_mfma_f32_16x16x32_bf16 v[132:135], a[44:47], v[36:39], v[132:135]\n"
"s_xor_b32 s64, s64, 0xc000\n"
"s_xor_b32 s65, s65, 0xc000\n"
"v_mfma_f32_16x16x32_bf16 v[136:139], a[48:51], v[32:35], 0\n"
"v_exp_f32 v101, v101\n"
"v_mfma_f32_16x16x32_bf16 v[136:139], a[52:55], v[36:39], v[136:139]\n"
"v_exp_f32 v102, v102\n"
"v_mfma_f32_16x16x32_bf16 v[140:143], a[56:59], v[32:35], 0\n"
"v_exp_f32 v103, v103\n"
"v_mfma_f32_16x16x32_bf16 v[140:143], a[60:63], v[36:39], v[140:143]\n"
"v_exp_f32 v72, v72\n"
"v_mfma_f32_16x16x32_bf16 v[144:147], a[64:67], v[32:35], 0\n"
"v_exp_f32 v73, v73\n"
"v_mfma_f32_16x16x32_bf16 v[144:147], a[68:71], v[36:39], v[144:147]\n"
"v_exp_f32 v74, v74\n"
"v_mfma_f32_16x16x32_bf16 v[148:151], a[72:75], v[32:35], 0\n"
"v_exp_f32 v75, v75\n"
"v_mfma_f32_16x16x32_bf16 v[148:151], a[76:79], v[36:39], v[148:151]\n"
"v_exp_f32 v88, v88\n"
"v_mfma_f32_16x16x32_bf16 v[152:155], a[80:83], v[32:35], 0\n"
"v_exp_f32 v89, v89\n"
"v_mfma_f32_16x16x32_bf16 v[152:155], a[84:87], v[36:39], v[152:155]\n"
"v_exp_f32 v90, v90\n"
"v_mfma_f32_16x16x32_bf16 v[156:159], a[88:91], v[32:35], 0\n"
"v_cvt_pk_bf16_f32 v32, v64, v65\n"
"v_cvt_pk_bf16_f32 v33, v66, v67\n"
"v_mfma_f32_16x16x32_bf16 v[156:159], a[92:95], v[36:39], v[156:159]\n"
"v_cvt_pk_bf16_f32 v34, v68, v69\n"
"v_cvt_pk_bf16_f32 v35, v70, v71\n"
"v_mfma_f32_16x16x32_bf16 a[100:103], a[112:115], v[40:43], 0\n"
"v_cvt_pk_bf16_f32 v36, v72, v73\n"
"v_cvt_pk_bf16_f32 v37, v74, v75\n"
"v_mfma_f32_16x16x32_bf16 a[100:103], a[112:115], v[44:47], a[100:103]\n"
"v_exp_f32 v91, v91\n"
"v_mfma_f32_16x16x32_bf16 v[160:163], a[32:35], v[40:43], 0\n"
"v_exp_f32 v104, v104\n"
"v_mfma_f32_16x16x32_bf16 v[160:163], a[36:39], v[44:47], v[160:163]\n"
"v_exp_f32 v105, v105\n"
"v_mfma_f32_16x16x32_bf16 v[164:167], a[40:43], v[40:43], 0\n"
"v_exp_f32 v106, v106\n"
"v_mfma_f32_16x16x32_bf16 v[164:167], a[44:47], v[44:47], v[164:167]\n"
"v_exp_f32 v107, v107\n"
"v_mfma_f32_16x16x32_bf16 v[168:171], a[48:51], v[40:43], 0\n"
"v_exp_f32 v76, v76\n"
"v_mfma_f32_16x16x32_bf16 v[168:171], a[52:55], v[44:47], v[168:171]\n"
"v_exp_f32 v77, v77\n"
"v_mfma_f32_16x16x32_bf16 v[172:175], a[56:59], v[40:43], 0\n"
"v_exp_f32 v78, v78\n"
"v_mfma_f32_16x16x32_bf16 v[172:175], a[60:63], v[44:47], v[172:175]\n"
"v_exp_f32 v79, v79\n"
"v_mfma_f32_16x16x32_bf16 v[176:179], a[64:67], v[40:43], 0\n"
"v_cvt_pk_bf16_f32 v38, v76, v77\n"
"v_cvt_pk_bf16_f32 v39, v78, v79\n"
"v_mfma_f32_16x16x32_bf16 v[176:179], a[68:71], v[44:47], v[176:179]\n"
"v_exp_f32 v92, v92\n"
"v_mfma_f32_16x16x32_bf16 v[180:183], a[72:75], v[40:43], 0\n"
"v_exp_f32 v93, v93\n"
"v_mfma_f32_16x16x32_bf16 v[180:183], a[76:79], v[44:47], v[180:183]\n"
"v_exp_f32 v94, v94\n"
"v_mfma_f32_16x16x32_bf16 v[184:187], a[80:83], v[40:43], 0\n"
"v_exp_f32 v95, v95\n"
"v_mfma_f32_16x16x32_bf16 v[184:187], a[84:87], v[44:47], v[184:187]\n"
"v_exp_f32 v108, v108\n"
"v_mfma_f32_16x16x32_bf16 v[188:191], a[88:91], v[40:43], 0\n"
"s_waitcnt vmcnt(63)\n"
"s_barrier\n"
"v_mfma_f32_16x16x32_bf16 v[188:191], a[92:95], v[44:47], v[188:191]\n"
"ds_read_b128 a[0:3], v6 offset:0\n"
"ds_read_b128 a[4:7], v7 offset:0\n"
"v_mfma_f32_16x16x32_bf16 a[104:107], a[112:115], v[48:51], 0\n"
"ds_read_b128 a[8:11], v8 offset:0\n"
"ds_read_b128 a[12:15], v9 offset:0\n"
"v_mfma_f32_16x16x32_bf16 a[104:107], a[112:115], v[52:55], a[104:107]\n"
"s_mov_b32 m0, s62\n"
"v_cvt_pk_bf16_f32 v40, v80, v81\n"
"v_mfma_f32_16x16x32_bf16 v[192:195], a[32:35], v[48:51], 0\n"
"buffer_load_dwordx4 v0, s[44:47], s60 offen offset:0 nt lds\n"
"v_mfma_f32_16x16x32_bf16 v[192:195], a[36:39], v[52:55], v[192:195]\n"
"v_cvt_pk_bf16_f32 v41, v82, v83\n"
"ds_read_b128 a[16:19], v6 offset:4096\n"
"v_mfma_f32_16x16x32_bf16 v[196:199], a[40:43], v[48:51], 0\n"
"ds_read_b128 a[20:23], v7 offset:4096\n"
"ds_read_b128 a[24:27], v8 offset:4096\n"
"v_mfma_f32_16x16x32_bf16 v[196:199], a[44:47], v[52:55], v[196:199]\n"
"ds_read_b128 a[28:31], v9 offset:4096\n"
"v_cvt_pk_bf16_f32 v42, v84, v85\n"
"v_mfma_f32_16x16x32_bf16 v[200:203], a[48:51], v[48:51], 0\n"
"v_cvt_pk_bf16_f32 v43, v86, v87\n"
"v_cvt_pk_bf16_f32 v44, v88, v89\n"
"v_mfma_f32_16x16x32_bf16 v[200:203], a[52:55], v[52:55], v[200:203]\n"
"v_cvt_pk_bf16_f32 v45, v90, v91\n"
"v_cvt_pk_bf16_f32 v46, v92, v93\n"
"v_mfma_f32_16x16x32_bf16 v[204:207], a[56:59], v[48:51], 0\n"
"v_cvt_pk_bf16_f32 v47, v94, v95\n"
"ds_read_b64_tr_b16 a[32:33], v10 offset:0\n"
"v_mfma_f32_16x16x32_bf16 v[204:207], a[60:63], v[52:55], v[204:207]\n"
"ds_read_b64_tr_b16 a[34:35], v10 offset:4096\n"
"ds_read_b64_tr_b16 a[36:37], v10 offset:8192\n"
"v_mfma_f32_16x16x32_bf16 v[208:211], a[64:67], v[48:51], 0\n"
"v_exp_f32 v109, v109\n"
"v_mfma_f32_16x16x32_bf16 v[208:211], a[68:71], v[52:55], v[208:211]\n"
"buffer_load_dwordx4 v1, s[44:47], s60 offen offset:1024 nt lds\n"
"v_mfma_f32_16x16x32_bf16 v[212:215], a[72:75], v[48:51], 0\n"
"v_exp_f32 v110, v110\n"
"v_mfma_f32_16x16x32_bf16 v[212:215], a[76:79], v[52:55], v[212:215]\n"
"v_exp_f32 v111, v111\n"
"v_mfma_f32_16x16x32_bf16 v[216:219], a[80:83], v[48:51], 0\n"
"ds_read_b64_tr_b16 a[38:39], v10 offset:12288\n"
"ds_read_b64_tr_b16 a[40:41], v11 offset:0\n"
"v_mfma_f32_16x16x32_bf16 v[216:219], a[84:87], v[52:55], v[216:219]\n"
"ds_read_b64_tr_b16 a[42:43], v11 offset:4096\n"
"ds_read_b64_tr_b16 a[44:45], v11 offset:8192\n"
"v_mfma_f32_16x16x32_bf16 v[220:223], a[88:91], v[48:51], 0\n"
"v_cvt_pk_bf16_f32 v48, v96, v97\n"
"v_cvt_pk_bf16_f32 v49, v98, v99\n"
"v_mfma_f32_16x16x32_bf16 v[220:223], a[92:95], v[52:55], v[220:223]\n"
".LG0b_%=:\n"
"s_waitcnt lgkmcnt(14)\n"
"v_mfma_f32_16x16x32_bf16 v[64:67], a[0:3], a[128:131], v[16:19]\n"
"v_cvt_pk_bf16_f32 v50, v100, v101\n"
"v_cvt_pk_bf16_f32 v51, v102, v103\n"
"s_waitcnt lgkmcnt(13)\n"
"v_mfma_f32_16x16x32_bf16 v[64:67], a[4:7], a[132:135], v[64:67]\n"
"v_cvt_pk_bf16_f32 v52, v104, v105\n"
"v_cvt_pk_bf16_f32 v53, v106, v107\n"
"s_waitcnt lgkmcnt(12)\n"
"v_mfma_f32_16x16x32_bf16 v[64:67], a[8:11], a[136:139], v[64:67]\n"
"buffer_load_dwordx4 v2, s[44:47], s60 offen offset:2048 nt lds\n"
"s_waitcnt lgkmcnt(11)\n"
"v_mfma_f32_16x16x32_bf16 v[64:67], a[12:15], a[140:143], v[64:67]\n"
"v_cvt_pk_bf16_f32 v54, v108, v109\n"
"v_cvt_pk_bf16_f32 v55, v110, v111\n"
"v_mfma_f32_16x16x32_bf16 v[80:83], a[0:3], a[144:147], v[20:23]\n"
"ds_read_b64_tr_b16 a[46:47], v11 offset:12288\n"
"ds_read_b64_tr_b16 a[48:49], v10 offset:256\n"
"v_mfma_f32_16x16x32_bf16 v[80:83], a[4:7], a[148:151], v[80:83]\n"
"ds_read_b64_tr_b16 a[50:51], v10 offset:4352\n"
"ds_read_b64_tr_b16 a[52:53], v10 offset:8448\n"
"v_mfma_f32_16x16x32_bf16 v[80:83], a[8:11], a[152:155], v[80:83]\n"
"ds_read_b64_tr_b16 a[54:55], v10 offset:12544\n"
"ds_read_b64_tr_b16 a[56:57], v11 offset:256\n"
"v_mfma_f32_16x16x32_bf16 v[80:83], a[12:15], a[156:159], v[80:83]\n"
"ds_read_b64_tr_b16 a[58:59], v11 offset:4352\n"
"ds_read_b64_tr_b16 a[60:61], v11 offset:8448\n"
"v_mfma_f32_16x16x32_bf16 v[96:99], a[0:3], a[160:163], v[24:27]\n"
"ds_read_b128 a[0:3], v6 offset:8192\n"
"ds_read_b64_tr_b16 a[62:63], v11 offset:12544\n"
"v_mfma_f32_16x16x32_bf16 v[96:99], a[4:7], a[164:167], v[96:99]\n"
"ds_read_b128 a[4:7], v7 offset:8192\n"
"ds_read_b64_tr_b16 a[64:65], v10 offset:512\n"
"v_mfma_f32_16x16x32_bf16 v[96:99], a[8:11], a[168:171], v[96:99]\n"
"ds_read_b128 a[8:11], v8 offset:8192\n"
"ds_read_b64_tr_b16 a[66:67], v10 offset:4608\n"
"v_mfma_f32_16x16x32_bf16 v[96:99], a[12:15], a[172:175], v[96:99]\n"
"buffer_load_dwordx4 v3, s[44:47], s60 offen offset:3072 nt lds\n"
"s_waitcnt lgkmcnt(15)\n"
"v_mfma_f32_16x16x32_bf16 v[68:71], a[16:19], a[128:131], v[16:19]\n"
"ds_read_b128 a[12:15], v9 offset:8192\n"
"ds_read_b64_tr_b16 a[68:69], v10 offset:8704\n"
"v_mfma_f32_16x16x32_bf16 v[68:71], a[20:23], a[132:135], v[68:71]\n"
"ds_read_b64_tr_b16 a[70:71], v10 offset:12800\n"
"ds_read_b64_tr_b16 a[72:73], v11 offset:512\n"
"v_mfma_f32_16x16x32_bf16 v[68:71], a[24:27], a[136:139], v[68:71]\n"
"ds_read_b64_tr_b16 a[74:75], v11 offset:4608\n"
"ds_read_b64_tr_b16 a[76:77], v11 offset:8704\n"
"v_mfma_f32_16x16x32_bf16 v[68:71], a[28:31], a[140:143], v[68:71]\n"
"ds_read_b64_tr_b16 a[78:79], v11 offset:12800\n"
"ds_read_b64_tr_b16 a[80:81], v10 offset:768\n"
"v_mfma_f32_16x16x32_bf16 v[84:87], a[16:19], a[144:147], v[20:23]\n"
"ds_read_b64_tr_b16 a[82:83], v10 offset:4864\n"
"ds_read_b64_tr_b16 a[84:85], v10 offset:8960\n"
"v_mfma_f32_16x16x32_bf16 v[84:87], a[20:23], a[148:151], v[84:87]\n"
"ds_read_b64_tr_b16 a[86:87], v10 offset:13056\n"
"ds_read_b64_tr_b16 a[88:89], v11 offset:768\n"
"v_mfma_f32_16x16x32_bf16 v[84:87], a[24:27], a[152:155], v[84:87]\n"
"ds_read_b64_tr_b16 a[90:91], v11 offset:4864\n"
"ds_read_b64_tr_b16 a[92:93], v11 offset:8960\n"
"v_mfma_f32_16x16x32_bf16 v[84:87], a[28:31], a[156:159], v[84:87]\n"
"s_mov_b32 m0, s63\n"
"ds_read_b64_tr_b16 a[94:95], v11 offset:13056\n"
"v_mfma_f32_16x16x32_bf16 v[100:103], a[16:19], a[160:163], v[24:27]\n"
"buffer_load_dwordx4 v4, s[48:51], s61 offen offset:0 nt lds\n"
"v_mfma_f32_16x16x32_bf16 v[100:103], a[20:23], a[164:167], v[100:103]\n"
"ds_read_b128 a[16:19], v6 offset:12288\n"
"ds_read_b128 a[20:23], v7 offset:12288\n"
"v_mfma_f32_16x16x32_bf16 v[100:103], a[24:27], a[168:171], v[100:103]\n"
"ds_read_b128 a[24:27], v8 offset:12288\n"
"v_mfma_f32_16x16x32_bf16 v[100:103], a[28:31], a[172:175], v[100:103]\n"
"ds_read_b128 a[28:31], v9 offset:12288\n"
"s_waitcnt lgkmcnt(15)\n"
"v_mfma_f32_16x16x32_bf16 v[72:75], a[0:3], a[128:131], v[16:19]\n"
"v_exp_f32 v64, v64\n"
"v_mfma_f32_16x16x32_bf16 v[72:75], a[4:7], a[132:135], v[72:75]\n"
"v_exp_f32 v65, v65\n"
"v_mfma_f32_16x16x32_bf16 v[72:75], a[8:11], a[136:139], v[72:75]\n"
"v_exp_f32 v66, v66\n"
"v_mfma_f32_16x16x32_bf16 v[72:75], a[12:15], a[140:143], v[72:75]\n"
"v_exp_f32 v67, v67\n"
"v_mfma_f32_16x16x32_bf16 v[88:91], a[0:3], a[144:147], v[20:23]\n"
"v_exp_f32 v80, v80\n"
"v_mfma_f32_16x16x32_bf16 v[88:91], a[4:7], a[148:151], v[88:91]\n"
"buffer_load_dwordx4 v5, s[48:51], s61 offen offset:1024 nt lds\n"
"v_mfma_f32_16x16x32_bf16 v[88:91], a[8:11], a[152:155], v[88:91]\n"
"v_exp_f32 v81, v81\n"
"v_mfma_f32_16x16x32_bf16 v[88:91], a[12:15], a[156:159], v[88:91]\n"
"v_exp_f32 v82, v82\n"
"v_mfma_f32_16x16x32_bf16 v[104:107], a[0:3], a[160:163], v[24:27]\n"
"v_exp_f32 v83, v83\n"
"v_mfma_f32_16x16x32_bf16 v[104:107], a[4:7], a[164:167], v[104:107]\n"
"v_exp_f32 v96, v96\n"
"v_mfma_f32_16x16x32_bf16 v[104:107], a[8:11], a[168:171], v[104:107]\n"
"v_exp_f32 v97, v97\n"
"v_mfma_f32_16x16x32_bf16 v[104:107], a[12:15], a[172:175], v[104:107]\n"
"v_exp_f32 v98, v98\n"
"s_waitcnt lgkmcnt(3)\n"
"v_mfma_f32_16x16x32_bf16 v[76:79], a[16:19], a[128:131], v[16:19]\n"
"v_exp_f32 v99, v99\n"
"s_waitcnt lgkmcnt(2)\n"
"v_mfma_f32_16x16x32_bf16 v[76:79], a[20:23], a[132:135], v[76:79]\n"
"v_exp_f32 v68, v68\n"
"s_waitcnt lgkmcnt(1)\n"
"v_mfma_f32_16x16x32_bf16 v[76:79], a[24:27], a[136:139], v[76:79]\n"
"buffer_load_dwordx4 v4, s[48:51], s61 offen offset:2048 nt lds\n"
"s_waitcnt lgkmcnt(0)\n"
"v_mfma_f32_16x16x32_bf16 v[76:79], a[28:31], a[140:143], v[76:79]\n"
"v_exp_f32 v69, v69\n"
"v_mfma_f32_16x16x32_bf16 v[92:95], a[16:19], a[144:147], v[20:23]\n"
"v_exp_f32 v70, v70\n"
"v_mfma_f32_16x16x32_bf16 v[92:95], a[20:23], a[148:151], v[92:95]\n"
"v_exp_f32 v71, v71\n"
"v_mfma_f32_16x16x32_bf16 v[92:95], a[24:27], a[152:155], v[92:95]\n"
"v_exp_f32 v84, v84\n"
"v_mfma_f32_16x16x32_bf16 v[92:95], a[28:31], a[156:159], v[92:95]\n"
"v_exp_f32 v85, v85\n"
"v_mfma_f32_16x16x32_bf16 v[108:111], a[16:19], a[160:163], v[24:27]\n"
"v_exp_f32 v86, v86\n"
"v_mfma_f32_16x16x32_bf16 v[108:111], a[20:23], a[164:167], v[108:111]\n"
"v_exp_f32 v87, v87\n"
"v_mfma_f32_16x16x32_bf16 v[108:111], a[24:27], a[168:171], v[108:111]\n"
"v_exp_f32 v100, v100\n"
"v_mfma_f32_16x16x32_bf16 v[108:111], a[28:31], a[172:175], v[108:111]\n"
"buffer_load_dwordx4 v5, s[48:51], s61 offen offset:3072 nt lds\n"
"v_mfma_f32_16x16x32_bf16 a[96:99], a[112:115], v[32:35], a[96:99]\n"
"v_xor_b32 v6, s64, v6\n"
"v_xor_b32 v7, s64, v7\n"
"v_mfma_f32_16x16x32_bf16 a[96:99], a[112:115], v[36:39], a[96:99]\n"
"v_xor_b32 v8, s64, v8\n"
"v_xor_b32 v9, s64, v9\n"
"v_mfma_f32_16x16x32_bf16 v[128:131], a[32:35], v[32:35], v[128:131]\n"
"v_xor_b32 v10, s65, v10\n"
"v_xor_b32 v11, s65, v11\n"
"v_mfma_f32_16x16x32_bf16 v[128:131], a[36:39], v[36:39], v[128:131]\n"
"s_xor_b32 s62, s62, s65\n"
"s_xor_b32 s63, s63, s64\n"
"v_mfma_f32_16x16x32_bf16 v[132:135], a[40:43], v[32:35], v[132:135]\n"
"s_add_u32 s60, s60, 0x4000\n"
"s_add_u32 s61, s61, 0x4000\n"
"v_mfma_f32_16x16x32_bf16 v[132:135], a[44:47], v[36:39], v[132:135]\n"
"s_xor_b32 s64, s64, 0xc000\n"
"s_xor_b32 s65, s65, 0xc000\n"
"v_mfma_f32_16x16x32_bf16 v[136:139], a[48:51], v[32:35], v[136:139]\n"
"v_exp_f32 v101, v101\n"
"v_mfma_f32_16x16x32_bf16 v[136:139], a[52:55], v[36:39], v[136:139]\n"
"v_exp_f32 v102, v102\n"
"v_mfma_f32_16x16x32_bf16 v[140:143], a[56:59], v[32:35], v[140:143]\n"
"v_exp_f32 v103, v103\n"
"v_mfma_f32_16x16x32_bf16 v[140:143], a[60:63], v[36:39], v[140:143]\n"
"v_exp_f32 v72, v72\n"
"v_mfma_f32_16x16x32_bf16 v[144:147], a[64:67], v[32:35], v[144:147]\n"
"v_exp_f32 v73, v73\n"
"v_mfma_f32_16x16x32_bf16 v[144:147], a[68:71], v[36:39], v[144:147]\n"
"v_exp_f32 v74, v74\n"
"v_mfma_f32_16x16x32_bf16 v[148:151], a[72:75], v[32:35], v[148:151]\n"
"v_exp_f32 v75, v75\n"
"v_mfma_f32_16x16x32_bf16 v[148:151], a[76:79], v[36:39], v[148:151]\n"
"v_exp_f32 v88, v88\n"
"v_mfma_f32_16x16x32_bf16 v[152:155], a[80:83], v[32:35], v[152:155]\n"
"v_exp_f32 v89, v89\n"
"v_mfma_f32_16x16x32_bf16 v[152:155], a[84:87], v[36:39], v[152:155]\n"
"v_exp_f32 v90, v90\n"
"v_mfma_f32_16x16x32_bf16 v[156:159], a[88:91], v[32:35], v[156:159]\n"
"v_cvt_pk_bf16_f32 v32, v64, v65\n"
"v_cvt_pk_bf16_f32 v33, v66, v67\n"
"v_mfma_f32_16x16x32_bf16 v[156:159], a[92:95], v[36:39], v[156:159]\n"
"v_cvt_pk_bf16_f32 v34, v68, v69\n"
"v_cvt_pk_bf16_f32 v35, v70, v71\n"
"v_mfma_f32_16x16x32_bf16 a[100:103], a[112:115], v[40:43], a[100:103]\n"
"v_cvt_pk_bf16_f32 v36, v72, v73\n"
"v_cvt_pk_bf16_f32 v37, v74, v75\n"
"v_mfma_f32_16x16x32_bf16 a[100:103], a[112:115], v[44:47], a[100:103]\n"
"v_exp_f32 v91, v91\n"
"v_mfma_f32_16x16x32_bf16 v[160:163], a[32:35], v[40:43], v[160:163]\n"
"v_exp_f32 v104, v104\n"
"v_mfma_f32_16x16x32_bf16 v[160:163], a[36:39], v[44:47], v[160:163]\n"
"v_exp_f32 v105, v105\n"
"v_mfma_f32_16x16x32_bf16 v[164:167], a[40:43], v[40:43], v[164:167]\n"
"v_exp_f32 v106, v106\n"
"v_mfma_f32_16x16x32_bf16 v[164:167], a[44:47], v[44:47], v[164:167]\n"
"v_exp_f32 v107, v107\n"
"v_mfma_f32_16x16x32_bf16 v[168:171], a[48:51], v[40:43], v[168:171]\n"
"v_exp_f32 v76, v76\n"
"v_mfma_f32_16x16x32_bf16 v[168:171], a[52:55], v[44:47], v[168:171]\n"
"v_exp_f32 v77, v77\n"
"v_mfma_f32_16x16x32_bf16 v[172:175], a[56:59], v[40:43], v[172:175]\n"
"v_exp_f32 v78, v78\n"
"v_mfma_f32_16x16x32_bf16 v[172:175], a[60:63], v[44:47], v[172:175]\n"
"v_exp_f32 v79, v79\n"
"v_mfma_f32_16x16x32_bf16 v[176:179], a[64:67], v[40:43], v[176:179]\n"
"v_cvt_pk_bf16_f32 v38, v76, v77\n"
"v_cvt_pk_bf16_f32 v39, v78, v79\n"
"v_mfma_f32_16x16x32_bf16 v[176:179], a[68:71], v[44:47], v[176:179]\n"
"v_exp_f32 v92, v92\n"
"v_mfma_f32_16x16x32_bf16 v[180:183], a[72:75], v[40:43], v[180:183]\n"
"v_exp_f32 v93, v93\n"
"v_mfma_f32_16x16x32_bf16 v[180:183], a[76:79], v[44:47], v[180:183]\n"
"v_exp_f32 v94, v94\n"
"v_mfma_f32_16x16x32_bf16 v[184:187], a[80:83], v[40:43], v[184:187]\n"
"v_exp_f32 v95, v95\n"
"v_mfma_f32_16x16x32_bf16 v[184:187], a[84:87], v[44:47], v[184:187]\n"
"v_exp_f32 v108, v108\n"
"v_mfma_f32_16x16x32_bf16 v[188:191], a[88:91], v[40:43], v[188:191]\n"
"s_waitcnt vmcnt(16)\n"
"s_barrier\n"
"v_mfma_f32_16x16x32_bf16 v[188:191], a[92:95], v[44:47], v[188:191]\n"
"ds_read_b128 a[0:3], v6 offset:0\n"
"ds_read_b128 a[4:7], v7 offset:0\n"
"v_mfma_f32_16x16x32_bf16 a[104:107], a[112:115], v[48:51], a[104:107]\n"
"ds_read_b128 a[8:11], v8 offset:0\n"
"ds_read_b128 a[12:15], v9 offset:0\n"
"v_mfma_f32_16x16x32_bf16 a[104:107], a[112:115], v[52:55], a[104:107]\n"
"s_mov_b32 m0, s62\n"
"v_cvt_pk_bf16_f32 v40, v80, v81\n"
"v_mfma_f32_16x16x32_bf16 v[192:195], a[32:35], v[48:51], v[192:195]\n"
"buffer_load_dwordx4 v0, s[44:47], s60 offen offset:0 nt lds\n"
"v_mfma_f32_16x16x32_bf16 v[192:195], a[36:39], v[52:55], v[192:195]\n"
"v_cvt_pk_bf16_f32 v41, v82, v83\n"
"ds_read_b128 a[16:19], v6 offset:4096\n"
"v_mfma_f32_16x16x32_bf16 v[196:199], a[40:43], v[48:51], v[196:199]\n"
"ds_read_b128 a[20:23], v7 offset:4096\n"
"ds_read_b128 a[24:27], v8 offset:4096\n"
"v_mfma_f32_16x16x32_bf16 v[196:199], a[44:47], v[52:55], v[196:199]\n"
"ds_read_b128 a[28:31], v9 offset:4096\n"
"v_cvt_pk_bf16_f32 v42, v84, v85\n"
"v_mfma_f32_16x16x32_bf16 v[200:203], a[48:51], v[48:51], v[200:203]\n"
"v_cvt_pk_bf16_f32 v43, v86, v87\n"
"v_cvt_pk_bf16_f32 v44, v88, v89\n"
"v_mfma_f32_16x16x32_bf16 v[200:203], a[52:55], v[52:55], v[200:203]\n"
"v_cvt_pk_bf16_f32 v45, v90, v91\n"
"v_cvt_pk_bf16_f32 v46, v92, v93\n"
"v_mfma_f32_16x16x32_bf16 v[204:207], a[56:59], v[48:51], v[204:207]\n"
"v_cvt_pk_bf16_f32 v47, v94, v95\n"
"ds_read_b64_tr_b16 a[32:33], v10 offset:0\n"
"v_mfma_f32_16x16x32_bf16 v[204:207], a[60:63], v[52:55], v[204:207]\n"
"ds_read_b64_tr_b16 a[34:35], v10 offset:4096\n"
"ds_read_b64_tr_b16 a[36:37], v10 offset:8192\n"
"v_mfma_f32_16x16x32_bf16 v[208:211], a[64:67], v[48:51], v[208:211]\n"
"v_exp_f32 v109, v109\n"
"v_mfma_f32_16x16x32_bf16 v[208:211], a[68:71], v[52:55], v[208:211]\n"
"buffer_load_dwordx4 v1, s[44:47], s60 offen offset:1024 nt lds\n"
"v_mfma_f32_16x16x32_bf16 v[212:215], a[72:75], v[48:51], v[212:215]\n"
"v_exp_f32 v110, v110\n"
"v_mfma_f32_16x16x32_bf16 v[212:215], a[76:79], v[52:55], v[212:215]\n"
"v_exp_f32 v111, v111\n"
"v_mfma_f32_16x16x32_bf16 v[216:219], a[80:83], v[48:51], v[216:219]\n"
"ds_read_b64_tr_b16 a[38:39], v10 offset:12288\n"
"ds_read_b64_tr_b16 a[40:41], v11 offset:0\n"
"v_mfma_f32_16x16x32_bf16 v[216:219], a[84:87], v[52:55], v[216:219]\n"
"ds_read_b64_tr_b16 a[42:43], v11 offset:4096\n"
"ds_read_b64_tr_b16 a[44:45], v11 offset:8192\n"
"v_mfma_f32_16x16x32_bf16 v[220:223], a[88:91], v[48:51], v[220:223]\n"
"v_cvt_pk_bf16_f32 v48, v96, v97\n"
"v_cvt_pk_bf16_f32 v49, v98, v99\n"
"v_mfma_f32_16x16x32_bf16 v[220:223], a[92:95], v[52:55], v[220:223]\n"
"s_sub_u32 s66, s66, 1\n"
"s_cmp_lg_u32 s66, 0\n"
"s_cbranch_scc1 .LG0b_%=\n"
"s_waitcnt lgkmcnt(14)\n"
"v_mfma_f32_16x16x32_bf16 v[64:67], a[0:3], a[192:195], 0\n"
"v_cvt_pk_bf16_f32 v50, v100, v101\n"
"v_cvt_pk_bf16_f32 v51, v102, v103\n"
"s_waitcnt lgkmcnt(13)\n"
"v_mfma_f32_16x16x32_bf16 v[64:67], a[4:7], a[196:199], v[64:67]\n"
"v_cvt_pk_bf16_f32 v52, v104, v105\n"
"v_cvt_pk_bf16_f32 v53, v106, v107\n"
"s_waitcnt lgkmcnt(12)\n"
"v_mfma_f32_16x16x32_bf16 v[64:67], a[8:11], a[200:203], v[64:67]\n"
"buffer_load_dwordx4 v2, s[44:47], s60 offen offset:2048 nt lds\n"
"s_waitcnt lgkmcnt(11)\n"
"v_mfma_f32_16x16x32_bf16 v[64:67], a[12:15], a[204:207], v[64:67]\n"
"v_cvt_pk_bf16_f32 v54, v108, v109\n"
"v_cvt_pk_bf16_f32 v55, v110, v111\n"
"v_mfma_f32_16x16x32_bf16 v[80:83], a[0:3], a[208:211], 0\n"
"ds_read_b64_tr_b16 a[46:47], v11 offset:12288\n"
"ds_read_b64_tr_b16 a[48:49], v10 offset:256\n"
"v_mfma_f32_16x16x32_bf16 v[80:83], a[4:7], a[212:215], v[80:83]\n"
"ds_read_b64_tr_b16 a[50:51], v10 offset:4352\n"
"ds_read_b64_tr_b16 a[52:53], v10 offset:8448\n"
"v_mfma_f32_16x16x32_bf16 v[80:83], a[8:11], a[216:219], v[80:83]\n"
"ds_read_b64_tr_b16 a[54:55], v10 offset:12544\n"
"ds_read_b64_tr_b16 a[56:57], v11 offset:256\n"
"v_mfma_f32_16x16x32_bf16 v[80:83], a[12:15], a[220:223], v[80:83]\n"
"ds_read_b64_tr_b16 a[58:59], v11 offset:4352\n"
"ds_read_b64_tr_b16 a[60:61], v11 offset:8448\n"
"v_mfma_f32_16x16x32_bf16 v[96:99], a[0:3], a[224:227], 0\n"
"ds_read_b128 a[0:3], v6 offset:8192\n"
"ds_read_b64_tr_b16 a[62:63], v11 offset:12544\n"
"v_mfma_f32_16x16x32_bf16 v[96:99], a[4:7], a[228:231], v[96:99]\n"
"ds_read_b128 a[4:7], v7 offset:8192\n"
"ds_read_b64_tr_b16 a[64:65], v10 offset:512\n"
"v_mfma_f32_16x16x32_bf16 v[96:99], a[8:11], a[232:235], v[96:99]\n"
"ds_read_b128 a[8:11], v8 offset:8192\n"
"ds_read_b64_tr_b16 a[66:67], v10 offset:4608\n"
"buffer_load_dword a117, v13, s[36:39], %[ma] offen sc1\n"
"v_mfma_f32_16x16x32_bf16 v[96:99], a[12:15], a[236:239], v[96:99]\n"
"buffer_load_dwordx4 v3, s[44:47], s60 offen offset:3072 nt lds\n"
"v_max3_f32 v16, v64, v65, v66\n"
"v_max_f32 v16, v16, v67\n"
"v_mov_b32 v17, v16\n"
"s_nop 1\n"
"v_permlane32_swap_b32 v16, v17\n"
"v_max_f32 v16, v16, v17\n"
"v_mov_b32 v17, v16\n"
"s_nop 1\n"
"v_permlane16_swap_b32 v16, v17\n"
"v_min_f32 v18, -v16, -v17\n"
"v_mov_b32 v19, v18\n"
"v_mov_b32 v16, v18\n"
"v_mov_b32 v17, v18\n"
"s_waitcnt lgkmcnt(15)\n"
"s_nop 0\n"
"v_mfma_f32_16x16x32_bf16 v[68:71], a[16:19], a[192:195], v[16:19]\n"
"ds_read_b128 a[12:15], v9 offset:8192\n"
"ds_read_b64_tr_b16 a[68:69], v10 offset:8704\n"
"v_mfma_f32_16x16x32_bf16 v[68:71], a[20:23], a[196:199], v[68:71]\n"
"ds_read_b64_tr_b16 a[70:71], v10 offset:12800\n"
"ds_read_b64_tr_b16 a[72:73], v11 offset:512\n"
"v_mfma_f32_16x16x32_bf16 v[68:71], a[24:27], a[200:203], v[68:71]\n"
"ds_read_b64_tr_b16 a[74:75], v11 offset:4608\n"
"ds_read_b64_tr_b16 a[76:77], v11 offset:8704\n"
"v_mfma_f32_16x16x32_bf16 v[68:71], a[28:31], a[204:207], v[68:71]\n"
"ds_read_b64_tr_b16 a[78:79], v11 offset:12800\n"
"ds_read_b64_tr_b16 a[80:81], v10 offset:768\n"
"v_max3_f32 v20, v80, v81, v82\n"
"v_max_f32 v20, v20, v83\n"
"v_mov_b32 v21, v20\n"
"s_nop 1\n"
"v_permlane32_swap_b32 v20, v21\n"
"v_max_f32 v20, v20, v21\n"
"v_mov_b32 v21, v20\n"
"s_nop 1\n"
"v_permlane16_swap_b32 v20, v21\n"
"v_min_f32 v22, -v20, -v21\n"
"v_mov_b32 v23, v22\n"
"v_mov_b32 v20, v22\n"
"v_mov_b32 v21, v22\n"
"s_nop 1\n"
"v_mfma_f32_16x16x32_bf16 v[84:87], a[16:19], a[208:211], v[20:23]\n"
"ds_read_b64_tr_b16 a[82:83], v10 offset:4864\n"
"ds_read_b64_tr_b16 a[84:85], v10 offset:8960\n"
"v_mfma_f32_16x16x32_bf16 v[84:87], a[20:23], a[212:215], v[84:87]\n"
"ds_read_b64_tr_b16 a[86:87], v10 offset:13056\n"
"ds_read_b64_tr_b16 a[88:89], v11 offset:768\n"
"v_mfma_f32_16x16x32_bf16 v[84:87], a[24:27], a[216:219], v[84:87]\n"
"ds_read_b64_tr_b16 a[90:91], v11 offset:4864\n"
"ds_read_b64_tr_b16 a[92:93], v11 offset:8960\n"
"v_mfma_f32_16x16x32_bf16 v[84:87], a[28:31], a[220:223], v[84:87]\n"
"s_mov_b32 m0, s63\n"
"ds_read_b64_tr_b16 a[94:95], v11 offset:13056\n"
"v_max3_f32 v24, v96, v97, v98\n"
"v_max_f32 v24, v24, v99\n"
"v_mov_b32 v25, v24\n"
"s_nop 1\n"
"v_permlane32_swap_b32 v24, v25\n"
"v_max_f32 v24, v24, v25\n"
"v_mov_b32 v25, v24\n"
"s_nop 1\n"
"v_permlane16_swap_b32 v24, v25\n"
"v_min_f32 v26, -v24, -v25\n"
"v_mov_b32 v27, v26\n"
"v_mov_b32 v24, v26\n"
"v_mov_b32 v25, v26\n"
"s_nop 1\n"
"v_mfma_f32_16x16x32_bf16 v[100:103], a[16:19], a[224:227], v[24:27]\n"
"buffer_load_dwordx4 v4, s[48:51], s61 offen offset:0 nt lds\n"
"v_mfma_f32_16x16x32_bf16 v[100:103], a[20:23], a[228:231], v[100:103]\n"
"ds_read_b128 a[16:19], v6 offset:12288\n"
"ds_read_b128 a[20:23], v7 offset:12288\n"
"v_mfma_f32_16x16x32_bf16 v[100:103], a[24:27], a[232:235], v[100:103]\n"
"ds_read_b128 a[24:27], v8 offset:12288\n"
"v_add_f32 v64, v64, v18\n"
"v_mfma_f32_16x16x32_bf16 v[100:103], a[28:31], a[236:239], v[100:103]\n"
"ds_read_b128 a[28:31], v9 offset:12288\n"
"v_add_f32 v65, v65, v18\n"
"s_waitcnt lgkmcnt(15)\n"
"v_mfma_f32_16x16x32_bf16 v[72:75], a[0:3], a[192:195], v[16:19]\n"
"v_exp_f32 v64, v64\n"
"v_mfma_f32_16x16x32_bf16 v[72:75], a[4:7], a[196:199], v[72:75]\n"
"v_exp_f32 v65, v65\n"
"v_mfma_f32_16x16x32_bf16 v[72:75], a[8:11], a[200:203], v[72:75]\n"
"v_add_f32 v66, v66, v18\n"
"v_add_f32 v67, v67, v18\n"
"v_mfma_f32_16x16x32_bf16 v[72:75], a[12:15], a[204:207], v[72:75]\n"
"v_exp_f32 v66, v66\n"
"v_mfma_f32_16x16x32_bf16 v[88:91], a[0:3], a[208:211], v[20:23]\n"
"v_exp_f32 v67, v67\n"
"v_mfma_f32_16x16x32_bf16 v[88:91], a[4:7], a[212:215], v[88:91]\n"
"buffer_load_dwordx4 v5, s[48:51], s61 offen offset:1024 nt lds\n"
"v_mfma_f32_16x16x32_bf16 v[88:91], a[8:11], a[216:219], v[88:91]\n"
"v_add_f32 v80, v80, v22\n"
"v_add_f32 v81, v81, v22\n"
"v_mfma_f32_16x16x32_bf16 v[88:91], a[12:15], a[220:223], v[88:91]\n"
"v_exp_f32 v80, v80\n"
"v_mfma_f32_16x16x32_bf16 v[104:107], a[0:3], a[224:227], v[24:27]\n"
"v_exp_f32 v81, v81\n"
"v_mfma_f32_16x16x32_bf16 v[104:107], a[4:7], a[228:231], v[104:107]\n"
"v_add_f32 v82, v82, v22\n"
"v_add_f32 v83, v83, v22\n"
"v_mfma_f32_16x16x32_bf16 v[104:107], a[8:11], a[232:235], v[104:107]\n"
"v_exp_f32 v82, v82\n"
"v_mfma_f32_16x16x32_bf16 v[104:107], a[12:15], a[236:239], v[104:107]\n"
"v_exp_f32 v83, v83\n"
"s_waitcnt lgkmcnt(3)\n"
"v_mfma_f32_16x16x32_bf16 v[76:79], a[16:19], a[192:195], v[16:19]\n"
"v_add_f32 v96, v96, v26\n"
"v_add_f32 v97, v97, v26\n"
"s_waitcnt lgkmcnt(2)\n"
"v_mfma_f32_16x16x32_bf16 v[76:79], a[20:23], a[196:199], v[76:79]\n"
"v_exp_f32 v68, v68\n"
"s_waitcnt lgkmcnt(1)\n"
"v_mfma_f32_16x16x32_bf16 v[76:79], a[24:27], a[200:203], v[76:79]\n"
"buffer_load_dwordx4 v4, s[48:51], s61 offen offset:2048 nt lds\n"
"s_waitcnt lgkmcnt(0)\n"
"v_mfma_f32_16x16x32_bf16 v[76:79], a[28:31], a[204:207], v[76:79]\n"
"v_exp_f32 v69, v69\n"
"v_mfma_f32_16x16x32_bf16 v[92:95], a[16:19], a[208:211], v[20:23]\n"
"v_exp_f32 v70, v70\n"
"v_mfma_f32_16x16x32_bf16 v[92:95], a[20:23], a[212:215], v[92:95]\n"
"v_exp_f32 v71, v71\n"
"v_mfma_f32_16x16x32_bf16 v[92:95], a[24:27], a[216:219], v[92:95]\n"
"v_exp_f32 v84, v84\n"
"v_mfma_f32_16x16x32_bf16 v[92:95], a[28:31], a[220:223], v[92:95]\n"
"v_exp_f32 v85, v85\n"
"v_mfma_f32_16x16x32_bf16 v[108:111], a[16:19], a[224:227], v[24:27]\n"
"v_exp_f32 v86, v86\n"
"v_mfma_f32_16x16x32_bf16 v[108:111], a[20:23], a[228:231], v[108:111]\n"
"v_exp_f32 v87, v87\n"
"v_mfma_f32_16x16x32_bf16 v[108:111], a[24:27], a[232:235], v[108:111]\n"
"v_exp_f32 v100, v100\n"
"v_mfma_f32_16x16x32_bf16 v[108:111], a[28:31], a[236:239], v[108:111]\n"
"buffer_load_dwordx4 v5, s[48:51], s61 offen offset:3072 nt lds\n"
"v_mfma_f32_16x16x32_bf16 a[96:99], a[112:115], v[32:35], a[96:99]\n"
"v_xor_b32 v6, s64, v6\n"
"v_xor_b32 v7, s64, v7\n"
"v_mfma_f32_16x16x32_bf16 a[96:99], a[112:115], v[36:39], a[96:99]\n"
"v_xor_b32 v8, s64, v8\n"
"v_xor_b32 v9, s64, v9\n"
"v_mfma_f32_16x16x32_bf16 v[128:131], a[32:35], v[32:35], v[128:131]\n"
"v_xor_b32 v10, s65, v10\n"
"v_xor_b32 v11, s65, v11\n"
"v_mfma_f32_16x16x32_bf16 v[128:131], a[36:39], v[36:39], v[128:131]\n"
"s_xor_b32 s62, s62, s65\n"
"s_xor_b32 s63, s63, s64\n"
"v_mfma_f32_16x16x32_bf16 v[132:135], a[40:43], v[32:35], v[132:135]\n"
"s_add_u32 s60, s60, 0x4000\n"
"s_add_u32 s61, s61, 0x4000\n"
"v_mfma_f32_16x16x32_bf16 v[132:135], a[44:47], v[36:39], v[132:135]\n"
"s_xor_b32 s64, s64, 0xc000\n"
"s_xor_b32 s65, s65, 0xc000\n"
"v_mfma_f32_16x16x32_bf16 v[136:139], a[48:51], v[32:35], v[136:139]\n"
"v_exp_f32 v101, v101\n"
"v_mfma_f32_16x16x32_bf16 v[136:139], a[52:55], v[36:39], v[136:139]\n"
"v_exp_f32 v102, v102\n"
"v_mfma_f32_16x16x32_bf16 v[140:143], a[56:59], v[32:35], v[140:143]\n"
"v_exp_f32 v103, v103\n"
"v_mfma_f32_16x16x32_bf16 v[140:143], a[60:63], v[36:39], v[140:143]\n"
"v_exp_f32 v72, v72\n"
"v_mfma_f32_16x16x32_bf16 v[144:147], a[64:67], v[32:35], v[144:147]\n"
"v_exp_f32 v73, v73\n"
"v_mfma_f32_16x16x32_bf16 v[144:147], a[68:71], v[36:39], v[144:147]\n"
"v_exp_f32 v74, v74\n"
"v_mfma_f32_16x16x32_bf16 v[148:151], a[72:75], v[32:35], v[148:151]\n"
"v_exp_f32 v75, v75\n"
"v_mfma_f32_16x16x32_bf16 v[148:151], a[76:79], v[36:39], v[148:151]\n"
"v_exp_f32 v88, v88\n"
"v_mfma_f32_16x16x32_bf16 v[152:155], a[80:83], v[32:35], v[152:155]\n"
"v_exp_f32 v89, v89\n"
"v_mfma_f32_16x16x32_bf16 v[152:155], a[84:87], v[36:39], v[152:155]\n"
"v_exp_f32 v90, v90\n"
"v_mfma_f32_16x16x32_bf16 v[156:159], a[88:91], v[32:35], v[156:159]\n"
"v_cvt_pk_bf16_f32 v32, v64, v65\n"
"v_cvt_pk_bf16_f32 v33, v66, v67\n"
"v_mfma_f32_16x16x32_bf16 v[156:159], a[92:95], v[36:39], v[156:159]\n"
"v_cvt_pk_bf16_f32 v34, v68, v69\n"
"v_cvt_pk_bf16_f32 v35, v70, v71\n"
"v_mfma_f32_16x16x32_bf16 a[100:103], a[112:115], v[40:43], a[100:103]\n"
"v_cvt_pk_bf16_f32 v36, v72, v73\n"
"v_cvt_pk_bf16_f32 v37, v74, v75\n"
"v_mfma_f32_16x16x32_bf16 a[100:103], a[112:115], v[44:47], a[100:103]\n"
"v_exp_f32 v91, v91\n"
"v_mfma_f32_16x16x32_bf16 v[160:163], a[32:35], v[40:43], v[160:163]\n"
"v_exp_f32 v104, v104\n"
"v_mfma_f32_16x16x32_bf16 v[160:163], a[36:39], v[44:47], v[160:163]\n"
"v_exp_f32 v105, v105\n"
"v_mfma_f32_16x16x32_bf16 v[164:167], a[40:43], v[40:43], v[164:167]\n"
"v_exp_f32 v106, v106\n"
"v_mfma_f32_16x16x32_bf16 v[164:167], a[44:47], v[44:47], v[164:167]\n"
"v_exp_f32 v107, v107\n"
"v_mfma_f32_16x16x32_bf16 v[168:171], a[48:51], v[40:43], v[168:171]\n"
"v_exp_f32 v76, v76\n"
"v_mfma_f32_16x16x32_bf16 v[168:171], a[52:55], v[44:47], v[168:171]\n"
"v_exp_f32 v77, v77\n"
"v_mfma_f32_16x16x32_bf16 v[172:175], a[56:59], v[40:43], v[172:175]\n"
"v_exp_f32 v78, v78\n"
"v_mfma_f32_16x16x32_bf16 v[172:175], a[60:63], v[44:47], v[172:175]\n"
"v_exp_f32 v79, v79\n"
"v_mfma_f32_16x16x32_bf16 v[176:179], a[64:67], v[40:43], v[176:179]\n"
"v_cvt_pk_bf16_f32 v38, v76, v77\n"
"v_cvt_pk_bf16_f32 v39, v78, v79\n"
"v_mfma_f32_16x16x32_bf16 v[176:179], a[68:71], v[44:47], v[176:179]\n"
"v_exp_f32 v92, v92\n"
"v_mfma_f32_16x16x32_bf16 v[180:183], a[72:75], v[40:43], v[180:183]\n"
"v_exp_f32 v93, v93\n"
"v_mfma_f32_16x16x32_bf16 v[180:183], a[76:79], v[44:47], v[180:183]\n"
"v_exp_f32 v94, v94\n"
"v_mfma_f32_16x16x32_bf16 v[184:187], a[80:83], v[40:43], v[184:187]\n"
"v_exp_f32 v95, v95\n"
"v_mfma_f32_16x16x32_bf16 v[184:187], a[84:87], v[44:47], v[184:187]\n"
"v_exp_f32 v108, v108\n"
"v_mfma_f32_16x16x32_bf16 v[188:191], a[88:91], v[40:43], v[188:191]\n"
"s_waitcnt vmcnt(17)\n"
"s_barrier\n"
"v_mfma_f32_16x16x32_bf16 v[188:191], a[92:95], v[44:47], v[188:191]\n"
"ds_read_b128 a[0:3], v6 offset:0\n"
"ds_read_b128 a[4:7], v7 offset:0\n"
"v_mfma_f32_16x16x32_bf16 a[104:107], a[112:115], v[48:51], a[104:107]\n"
"ds_read_b128 a[8:11], v8 offset:0\n"
"ds_read_b128 a[12:15], v9 offset:0\n"
"v_mfma_f32_16x16x32_bf16 a[104:107], a[112:115], v[52:55], a[104:107]\n"
"s_mov_b32 m0, s62\n"
"v_cvt_pk_bf16_f32 v40, v80, v81\n"
"v_mfma_f32_16x16x32_bf16 v[192:195], a[32:35], v[48:51], v[192:195]\n"
"buffer_load_dwordx4 v0, s[44:47], s60 offen offset:0 nt lds\n"
"v_mfma_f32_16x16x32_bf16 v[192:195], a[36:39], v[52:55], v[192:195]\n"
"v_cvt_pk_bf16_f32 v41, v82, v83\n"
"ds_read_b128 a[16:19], v6 offset:4096\n"
"v_mfma_f32_16x16x32_bf16 v[196:199], a[40:43], v[48:51], v[196:199]\n"
"ds_read_b128 a[20:23], v7 offset:4096\n"
"ds_read_b128 a[24:27], v8 offset:4096\n"
"v_mfma_f32_16x16x32_bf16 v[196:199], a[44:47], v[52:55], v[196:199]\n"
"ds_read_b128 a[28:31], v9 offset:4096\n"
"v_cvt_pk_bf16_f32 v42, v84, v85\n"
"v_mfma_f32_16x16x32_bf16 v[200:203], a[48:51], v[48:51], v[200:203]\n"
"v_cvt_pk_bf16_f32 v43, v86, v87\n"
"v_cvt_pk_bf16_f32 v44, v88, v89\n"
"v_mfma_f32_16x16x32_bf16 v[200:203], a[52:55], v[52:55], v[200:203]\n"
"v_cvt_pk_bf16_f32 v45, v90, v91\n"
"v_cvt_pk_bf16_f32 v46, v92, v93\n"
"v_mfma_f32_16x16x32_bf16 v[204:207], a[56:59], v[48:51], v[204:207]\n"
"v_cvt_pk_bf16_f32 v47, v94, v95\n"
"ds_read_b64_tr_b16 a[32:33], v10 offset:0\n"
"v_mfma_f32_16x16x32_bf16 v[204:207], a[60:63], v[52:55], v[204:207]\n"
"ds_read_b64_tr_b16 a[34:35], v10 offset:4096\n"
"ds_read_b64_tr_b16 a[36:37], v10 offset:8192\n"
"v_mfma_f32_16x16x32_bf16 v[208:211], a[64:67], v[48:51], v[208:211]\n"
"v_exp_f32 v109, v109\n"
"v_mfma_f32_16x16x32_bf16 v[208:211], a[68:71], v[52:55], v[208:211]\n"
"buffer_load_dwordx4 v1, s[44:47], s60 offen offset:1024 nt lds\n"
"v_mfma_f32_16x16x32_bf16 v[212:215], a[72:75], v[48:51], v[212:215]\n"
"v_exp_f32 v110, v110\n"
"v_mfma_f32_16x16x32_bf16 v[212:215], a[76:79], v[52:55], v[212:215]\n"
"v_exp_f32 v111, v111\n"
"v_mfma_f32_16x16x32_bf16 v[216:219], a[80:83], v[48:51], v[216:219]\n"
"ds_read_b64_tr_b16 a[38:39], v10 offset:12288\n"
"ds_read_b64_tr_b16 a[40:41], v11 offset:0\n"
"v_mfma_f32_16x16x32_bf16 v[216:219], a[84:87], v[52:55], v[216:219]\n"
"ds_read_b64_tr_b16 a[42:43], v11 offset:4096\n"
"ds_read_b64_tr_b16 a[44:45], v11 offset:8192\n"
"v_mfma_f32_16x16x32_bf16 v[220:223], a[88:91], v[48:51], v[220:223]\n"
"v_exp_f32 v96, v96\n"
"v_exp_f32 v97, v97\n"
"s_nop 0\n"
"v_cvt_pk_bf16_f32 v48, v96, v97\n"
"v_add_f32 v98, v98, v26\n"
"v_exp_f32 v98, v98\n"
"v_add_f32 v99, v99, v26\n"
"v_exp_f32 v99, v99\n"
"s_nop 0\n"
"v_cvt_pk_bf16_f32 v49, v98, v99\n"
"v_mfma_f32_16x16x32_bf16 v[220:223], a[92:95], v[52:55], v[220:223]\n"
"s_mov_b32 s66, 4\n"
"s_mov_b32 s56, 0\n"
"s_mov_b32 s57, 131072\n"
"s_mov_b32 s58, 262144\n"
"v_accvgpr_read_b32 v14, a96\n"
"s_waitcnt lgkmcnt(14)\n"
"v_mfma_f32_16x16x32_bf16 v[64:67], a[0:3], a[192:195], v[16:19]\n"
"v_cvt_pk_bf16_f32 v50, v100, v101\n"
"v_cvt_pk_bf16_f32 v51, v102, v103\n"
"s_waitcnt lgkmcnt(13)\n"
"v_mfma_f32_16x16x32_bf16 v[64:67], a[4:7], a[196:199], v[64:67]\n"
"v_cvt_pk_bf16_f32 v52, v104, v105\n"
"v_cvt_pk_bf16_f32 v53, v106, v107\n"
"s_waitcnt lgkmcnt(12)\n"
"v_mfma_f32_16x16x32_bf16 v[64:67], a[8:11], a[200:203], v[64:67]\n"
"buffer_load_dwordx4 v2, s[44:47], s60 offen offset:2048 nt lds\n"
"s_waitcnt lgkmcnt(11)\n"
"v_mfma_f32_16x16x32_bf16 v[64:67], a[12:15], a[204:207], v[64:67]\n"
"v_cvt_pk_bf16_f32 v54, v108, v109\n"
"v_cvt_pk_bf16_f32 v55, v110, v111\n"
"v_mfma_f32_16x16x32_bf16 v[80:83], a[0:3], a[208:211], v[20:23]\n"
"ds_read_b64_tr_b16 a[46:47], v11 offset:12288\n"
"ds_read_b64_tr_b16 a[48:49], v10 offset:256\n"
"v_mfma_f32_16x16x32_bf16 v[80:83], a[4:7], a[212:215], v[80:83]\n"
"ds_read_b64_tr_b16 a[50:51], v10 offset:4352\n"
"ds_read_b64_tr_b16 a[52:53], v10 offset:8448\n"
"v_mfma_f32_16x16x32_bf16 v[80:83], a[8:11], a[216:219], v[80:83]\n"
"ds_read_b64_tr_b16 a[54:55], v10 offset:12544\n"
"ds_read_b64_tr_b16 a[56:57], v11 offset:256\n"
"v_mfma_f32_16x16x32_bf16 v[80:83], a[12:15], a[220:223], v[80:83]\n"
"ds_read_b64_tr_b16 a[58:59], v11 offset:4352\n"
"ds_read_b64_tr_b16 a[60:61], v11 offset:8448\n"
"v_mfma_f32_16x16x32_bf16 v[96:99], a[0:3], a[224:227], v[24:27]\n"
"ds_read_b128 a[0:3], v6 offset:8192\n"
"ds_read_b64_tr_b16 a[62:63], v11 offset:12544\n"
"v_mfma_f32_16x16x32_bf16 v[96:99], a[4:7], a[228:231], v[96:99]\n"
"ds_read_b128 a[4:7], v7 offset:8192\n"
"ds_read_b64_tr_b16 a[64:65], v10 offset:512\n"
"v_mfma_f32_16x16x32_bf16 v[96:99], a[8:11], a[232:235], v[96:99]\n"
"ds_read_b128 a[8:11], v8 offset:8192\n"
"ds_read_b64_tr_b16 a[66:67], v10 offset:4608\n"
"v_mfma_f32_16x16x32_bf16 v[96:99], a[12:15], a[236:239], v[96:99]\n"
"buffer_load_dwordx4 v3, s[44:47], s60 offen offset:3072 nt lds\n"
"s_waitcnt lgkmcnt(15)\n"
"v_mfma_f32_16x16x32_bf16 v[68:71], a[16:19], a[192:195], v[16:19]\n"
"ds_read_b128 a[12:15], v9 offset:8192\n"
"ds_read_b64_tr_b16 a[68:69], v10 offset:8704\n"
"v_mfma_f32_16x16x32_bf16 v[68:71], a[20:23], a[196:199], v[68:71]\n"
"ds_read_b64_tr_b16 a[70:71], v10 offset:12800\n"
"ds_read_b64_tr_b16 a[72:73], v11 offset:512\n"
"v_mfma_f32_16x16x32_bf16 v[68:71], a[24:27], a[200:203], v[68:71]\n"
"ds_read_b64_tr_b16 a[74:75], v11 offset:4608\n"
"ds_read_b64_tr_b16 a[76:77], v11 offset:8704\n"
"v_mfma_f32_16x16x32_bf16 v[68:71], a[28:31], a[204:207], v[68:71]\n"
"ds_read_b64_tr_b16 a[78:79], v11 offset:12800\n"
"ds_read_b64_tr_b16 a[80:81], v10 offset:768\n"
"v_mfma_f32_16x16x32_bf16 v[84:87], a[16:19], a[208:211], v[20:23]\n"
"ds_read_b64_tr_b16 a[82:83], v10 offset:4864\n"
"ds_read_b64_tr_b16 a[84:85], v10 offset:8960\n"
"v_mfma_f32_16x16x32_bf16 v[84:87], a[20:23], a[212:215], v[84:87]\n"
"ds_read_b64_tr_b16 a[86:87], v10 offset:13056\n"
"ds_read_b64_tr_b16 a[88:89], v11 offset:768\n"
"v_mfma_f32_16x16x32_bf16 v[84:87], a[24:27], a[216:219], v[84:87]\n"
"ds_read_b64_tr_b16 a[90:91], v11 offset:4864\n"
"ds_read_b64_tr_b16 a[92:93], v11 offset:8960\n"
"v_mfma_f32_16x16x32_bf16 v[84:87], a[28:31], a[220:223], v[84:87]\n"
"s_mov_b32 m0, s63\n"
"ds_read_b64_tr_b16 a[94:95], v11 offset:13056\n"
"v_mfma_f32_16x16x32_bf16 v[100:103], a[16:19], a[224:227], v[24:27]\n"
"buffer_load_dwordx4 v4, s[48:51], s61 offen offset:0 nt lds\n"
"v_mfma_f32_16x16x32_bf16 v[100:103], a[20:23], a[228:231], v[100:103]\n"
"ds_read_b128 a[16:19], v6 offset:12288\n"
"ds_read_b128 a[20:23], v7 offset:12288\n"
"v_mfma_f32_16x16x32_bf16 v[100:103], a[24:27], a[232:235], v[100:103]\n"
"ds_read_b128 a[24:27], v8 offset:12288\n"
"v_accvgpr_read_b32 v15, a100\n"
"v_mfma_f32_16x16x32_bf16 v[100:103], a[28:31], a[236:239], v[100:103]\n"
"ds_read_b128 a[28:31], v9 offset:12288\n"
"s_waitcnt lgkmcnt(15)\n"
"v_mfma_f32_16x16x32_bf16 v[72:75], a[0:3], a[192:195], v[16:19]\n"
"v_exp_f32 v64, v64\n"
"v_mfma_f32_16x16x32_bf16 v[72:75], a[4:7], a[196:199], v[72:75]\n"
"v_exp_f32 v65, v65\n"
"v_mfma_f32_16x16x32_bf16 v[72:75], a[8:11], a[200:203], v[72:75]\n"
"v_exp_f32 v66, v66\n"
"v_mfma_f32_16x16x32_bf16 v[72:75], a[12:15], a[204:207], v[72:75]\n"
"v_exp_f32 v67, v67\n"
"v_mfma_f32_16x16x32_bf16 v[88:91], a[0:3], a[208:211], v[20:23]\n"
"v_exp_f32 v80, v80\n"
"v_mfma_f32_16x16x32_bf16 v[88:91], a[4:7], a[212:215], v[88:91]\n"
"buffer_load_dwordx4 v5, s[48:51], s61 offen offset:1024 nt lds\n"
"v_mfma_f32_16x16x32_bf16 v[88:91], a[8:11], a[216:219], v[88:91]\n"
"v_exp_f32 v81, v81\n"
"v_mfma_f32_16x16x32_bf16 v[88:91], a[12:15], a[220:223], v[88:91]\n"
"v_exp_f32 v82, v82\n"
"v_mfma_f32_16x16x32_bf16 v[104:107], a[0:3], a[224:227], v[24:27]\n"
"v_exp_f32 v83, v83\n"
"v_mfma_f32_16x16x32_bf16 v[104:107], a[4:7], a[228:231], v[104:107]\n"
"v_exp_f32 v96, v96\n"
"v_mfma_f32_16x16x32_bf16 v[104:107], a[8:11], a[232:235], v[104:107]\n"
"v_exp_f32 v97, v97\n"
"v_mfma_f32_16x16x32_bf16 v[104:107], a[12:15], a[236:239], v[104:107]\n"
"v_exp_f32 v98, v98\n"
"s_waitcnt lgkmcnt(3)\n"
"v_mfma_f32_16x16x32_bf16 v[76:79], a[16:19], a[192:195], v[16:19]\n"
"v_exp_f32 v99, v99\n"
"s_waitcnt lgkmcnt(2)\n"
"v_mfma_f32_16x16x32_bf16 v[76:79], a[20:23], a[196:199], v[76:79]\n"
"v_exp_f32 v68, v68\n"
"s_waitcnt lgkmcnt(1)\n"
"v_mfma_f32_16x16x32_bf16 v[76:79], a[24:27], a[200:203], v[76:79]\n"
"buffer_load_dwordx4 v4, s[48:51], s61 offen offset:2048 nt lds\n"
"s_waitcnt lgkmcnt(0)\n"
"v_mfma_f32_16x16x32_bf16 v[76:79], a[28:31], a[204:207], v[76:79]\n"
"v_exp_f32 v69, v69\n"
"v_mfma_f32_16x16x32_bf16 v[92:95], a[16:19], a[208:211], v[20:23]\n"
"v_exp_f32 v70, v70\n"
"v_mfma_f32_16x16x32_bf16 v[92:95], a[20:23], a[212:215], v[92:95]\n"
"v_exp_f32 v71, v71\n"
"v_mfma_f32_16x16x32_bf16 v[92:95], a[24:27], a[216:219], v[92:95]\n"
"v_exp_f32 v84, v84\n"
"v_mfma_f32_16x16x32_bf16 v[92:95], a[28:31], a[220:223], v[92:95]\n"
"v_exp_f32 v85, v85\n"
"v_mfma_f32_16x16x32_bf16 v[108:111], a[16:19], a[224:227], v[24:27]\n"
"v_exp_f32 v86, v86\n"
"v_mfma_f32_16x16x32_bf16 v[108:111], a[20:23], a[228:231], v[108:111]\n"
"v_exp_f32 v87, v87\n"
"v_mfma_f32_16x16x32_bf16 v[108:111], a[24:27], a[232:235], v[108:111]\n"
"v_exp_f32 v100, v100\n"
"v_mfma_f32_16x16x32_bf16 v[108:111], a[28:31], a[236:239], v[108:111]\n"
"buffer_load_dwordx4 v5, s[48:51], s61 offen offset:3072 nt lds\n"
"v_mfma_f32_16x16x32_bf16 a[96:99], a[112:115], v[32:35], 0\n"
"v_xor_b32 v6, s64, v6\n"
"v_xor_b32 v7, s64, v7\n"
"v_cmp_ngt_f32 vcc, s67, v14\n"
"s_mov_b32 s72, 0\n"
"s_or_b64 s[70:71], s[70:71], vcc\n"
"v_rcp_f32 v14, v14\n"
"s_nop 0\n"
"v_mul_f32 v128, v128, v14\n"
"v_mul_f32 v129, v129, v14\n"
"v_mul_f32 v130, v130, v14\n"
"v_mul_f32 v131, v131, v14\n"
"v_mul_f32 v132, v132, v14\n"
"v_mul_f32 v133, v133, v14\n"
"v_mul_f32 v134, v134, v14\n"
"v_mul_f32 v135, v135, v14\n"
"v_cvt_pk_bf16_f32 v128, v128, v129\n"
"v_cvt_pk_bf16_f32 v129, v130, v131\n"
"v_cvt_pk_bf16_f32 v130, v132, v133\n"
"v_cvt_pk_bf16_f32 v131, v134, v135\n"
"v_accvgpr_write_b32 a128, v128\n"
"v_accvgpr_write_b32 a129, v129\n"
"v_accvgpr_write_b32 a130, v130\n"
"v_accvgpr_write_b32 a131, v131\n"
"v_mfma_f32_16x16x32_bf16 a[96:99], a[112:115], v[36:39], a[96:99]\n"
"v_xor_b32 v8, s64, v8\n"
"v_xor_b32 v9, s64, v9\n"
"v_mfma_f32_16x16x32_bf16 v[128:131], a[32:35], v[32:35], 0\n"
"v_xor_b32 v10, s65, v10\n"
"v_xor_b32 v11, s65, v11\n"
"v_mfma_f32_16x16x32_bf16 v[128:131], a[36:39], v[36:39], v[128:131]\n"
"s_xor_b32 s62, s62, s65\n"
"s_xor_b32 s63, s63, s64\n"
"v_mfma_f32_16x16x32_bf16 v[132:135], a[40:43], v[32:35], 0\n"
"s_add_u32 s60, s60, 0x4000\n"
"s_add_u32 s61, s61, 0x4000\n"
"v_mul_f32 v136, v136, v14\n"
"v_mul_f32 v137, v137, v14\n"
"v_mul_f32 v138, v138, v14\n"
"v_mul_f32 v139, v139, v14\n"
"v_mul_f32 v140, v140, v14\n"
"v_mul_f32 v141, v141, v14\n"
"v_mul_f32 v142, v142, v14\n"
"v_mul_f32 v143, v143, v14\n"
"v_cvt_pk_bf16_f32 v136, v136, v137\n"
"v_cvt_pk_bf16_f32 v137, v138, v139\n"
"v_cvt_pk_bf16_f32 v138, v140, v141\n"
"v_cvt_pk_bf16_f32 v139, v142, v143\n"
"v_accvgpr_write_b32 a144, v136\n"
"v_accvgpr_write_b32 a145, v137\n"
"v_accvgpr_write_b32 a146, v138\n"
"v_accvgpr_write_b32 a147, v139\n"
"v_mfma_f32_16x16x32_bf16 v[132:135], a[44:47], v[36:39], v[132:135]\n"
"s_xor_b32 s64, s64, 0xc000\n"
"s_xor_b32 s65, s65, 0xc000\n"
"v_mfma_f32_16x16x32_bf16 v[136:139], a[48:51], v[32:35], 0\n"
"v_exp_f32 v101, v101\n"
"v_mfma_f32_16x16x32_bf16 v[136:139], a[52:55], v[36:39], v[136:139]\n"
"v_exp_f32 v102, v102\n"
"v_mfma_f32_16x16x32_bf16 v[140:143], a[56:59], v[32:35], 0\n"
"v_exp_f32 v103, v103\n"
"v_mul_f32 v144, v144, v14\n"
"v_mul_f32 v145, v145, v14\n"
"v_mul_f32 v146, v146, v14\n"
"v_mul_f32 v147, v147, v14\n"
"v_mul_f32 v148, v148, v14\n"
"v_mul_f32 v149, v149, v14\n"
"v_mul_f32 v150, v150, v14\n"
"v_mul_f32 v151, v151, v14\n"
"v_cvt_pk_bf16_f32 v144, v144, v145\n"
"v_cvt_pk_bf16_f32 v145, v146, v147\n"
"v_cvt_pk_bf16_f32 v146, v148, v149\n"
"v_cvt_pk_bf16_f32 v147, v150, v151\n"
"v_accvgpr_write_b32 a160, v144\n"
"v_accvgpr_write_b32 a161, v145\n"
"v_accvgpr_write_b32 a162, v146\n"
"v_accvgpr_write_b32 a163, v147\n"
"v_mfma_f32_16x16x32_bf16 v[140:143], a[60:63], v[36:39], v[140:143]\n"
"v_exp_f32 v72, v72\n"
"v_mfma_f32_16x16x32_bf16 v[144:147], a[64:67], v[32:35], 0\n"
"v_exp_f32 v73, v73\n"
"v_mfma_f32_16x16x32_bf16 v[144:147], a[68:71], v[36:39], v[144:147]\n"
"v_exp_f32 v74, v74\n"
"v_mfma_f32_16x16x32_bf16 v[148:151], a[72:75], v[32:35], 0\n"
"v_exp_f32 v75, v75\n"
"v_mul_f32 v152, v152, v14\n"
"v_mul_f32 v153, v153, v14\n"
"v_mul_f32 v154, v154, v14\n"
"v_mul_f32 v155, v155, v14\n"
"v_mul_f32 v156, v156, v14\n"
"v_mul_f32 v157, v157, v14\n"
"v_mul_f32 v158, v158, v14\n"
"v_mul_f32 v159, v159, v14\n"
"v_cvt_pk_bf16_f32 v152, v152, v153\n"
"v_cvt_pk_bf16_f32 v153, v154, v155\n"
"v_cvt_pk_bf16_f32 v154, v156, v157\n"
"v_cvt_pk_bf16_f32 v155, v158, v159\n"
"v_accvgpr_write_b32 a176, v152\n"
"v_accvgpr_write_b32 a177, v153\n"
"v_accvgpr_write_b32 a178, v154\n"
"v_accvgpr_write_b32 a179, v155\n"
"v_mfma_f32_16x16x32_bf16 v[148:151], a[76:79], v[36:39], v[148:151]\n"
"v_exp_f32 v88, v88\n"
"v_mfma_f32_16x16x32_bf16 v[152:155], a[80:83], v[32:35], 0\n"
"v_exp_f32 v89, v89\n"
"v_mfma_f32_16x16x32_bf16 v[152:155], a[84:87], v[36:39], v[152:155]\n"
"v_exp_f32 v90, v90\n"
"v_mfma_f32_16x16x32_bf16 v[156:159], a[88:91], v[32:35], 0\n"
"v_cvt_pk_bf16_f32 v32, v64, v65\n"
"v_cvt_pk_bf16_f32 v33, v66, v67\n"
"v_mfma_f32_16x16x32_bf16 v[156:159], a[92:95], v[36:39], v[156:159]\n"
"v_cvt_pk_bf16_f32 v34, v68, v69\n"
"v_cvt_pk_bf16_f32 v35, v70, v71\n"
"v_mfma_f32_16x16x32_bf16 a[100:103], a[112:115], v[40:43], 0\n"
"v_cvt_pk_bf16_f32 v36, v72, v73\n"
"v_cvt_pk_bf16_f32 v37, v74, v75\n"
"v_cmp_ngt_f32 vcc, s67, v15\n"
"s_mov_b32 s73, 131072\n"
"s_or_b64 s[70:71], s[70:71], vcc\n"
"v_rcp_f32 v15, v15\n"
"s_nop 0\n"
"v_mul_f32 v160, v160, v15\n"
"v_mul_f32 v161, v161, v15\n"
"v_mul_f32 v162, v162, v15\n"
"v_mul_f32 v163, v163, v15\n"
"v_mul_f32 v164, v164, v15\n"
"v_mul_f32 v165, v165, v15\n"
"v_mul_f32 v166, v166, v15\n"
"v_mul_f32 v167, v167, v15\n"
"v_cvt_pk_bf16_f32 v160, v160, v161\n"
"v_cvt_pk_bf16_f32 v161, v162, v163\n"
"v_cvt_pk_bf16_f32 v162, v164, v165\n"
"v_cvt_pk_bf16_f32 v163, v166, v167\n"
"v_accvgpr_write_b32 a132, v160\n"
"v_accvgpr_write_b32 a133, v161\n"
"v_accvgpr_write_b32 a134, v162\n"
"v_accvgpr_write_b32 a135, v163\n"
"v_mfma_f32_16x16x32_bf16 a[100:103], a[112:115], v[44:47], a[100:103]\n"
"v_exp_f32 v91, v91\n"
"v_mfma_f32_16x16x32_bf16 v[160:163], a[32:35], v[40:43], 0\n"
"v_exp_f32 v104, v104\n"
"v_mfma_f32_16x16x32_bf16 v[160:163], a[36:39], v[44:47], v[160:163]\n"
"v_exp_f32 v105, v105\n"
"v_mfma_f32_16x16x32_bf16 v[164:167], a[40:43], v[40:43], 0\n"
"v_exp_f32 v106, v106\n"
"v_mul_f32 v168, v168, v15\n"
"v_mul_f32 v169, v169, v15\n"
"v_mul_f32 v170, v170, v15\n"
"v_mul_f32 v171, v171, v15\n"
"v_mul_f32 v172, v172, v15\n"
"v_mul_f32 v173, v173, v15\n"
"v_mul_f32 v174, v174, v15\n"
"v_mul_f32 v175, v175, v15\n"
"v_cvt_pk_bf16_f32 v168, v168, v169\n"
"v_cvt_pk_bf16_f32 v169, v170, v171\n"
"v_cvt_pk_bf16_f32 v170, v172, v173\n"
"v_cvt_pk_bf16_f32 v171, v174, v175\n"
"v_accvgpr_write_b32 a148, v168\n"
"v_accvgpr_write_b32 a149, v169\n"
"v_accvgpr_write_b32 a150, v170\n"
"v_accvgpr_write_b32 a151, v171\n"
"v_mfma_f32_16x16x32_bf16 v[164:167], a[44:47], v[44:47], v[164:167]\n"
"v_exp_f32 v107, v107\n"
"v_mfma_f32_16x16x32_bf16 v[168:171], a[48:51], v[40:43], 0\n"
"v_exp_f32 v76, v76\n"
"v_mfma_f32_16x16x32_bf16 v[168:171], a[52:55], v[44:47], v[168:171]\n"
"v_exp_f32 v77, v77\n"
"v_mfma_f32_16x16x32_bf16 v[172:175], a[56:59], v[40:43], 0\n"
"v_exp_f32 v78, v78\n"
"v_mul_f32 v176, v176, v15\n"
"v_mul_f32 v177, v177, v15\n"
"v_mul_f32 v178, v178, v15\n"
"v_mul_f32 v179, v179, v15\n"
"v_mul_f32 v180, v180, v15\n"
"v_mul_f32 v181, v181, v15\n"
"v_mul_f32 v182, v182, v15\n"
"v_mul_f32 v183, v183, v15\n"
"v_cvt_pk_bf16_f32 v176, v176, v177\n"
"v_cvt_pk_bf16_f32 v177, v178, v179\n"
"v_cvt_pk_bf16_f32 v178, v180, v181\n"
"v_cvt_pk_bf16_f32 v179, v182, v183\n"
"v_accvgpr_write_b32 a164, v176\n"
"v_accvgpr_write_b32 a165, v177\n"
"v_accvgpr_write_b32 a166, v178\n"
"v_accvgpr_write_b32 a167, v179\n"
"v_mfma_f32_16x16x32_bf16 v[172:175], a[60:63], v[44:47], v[172:175]\n"
"v_exp_f32 v79, v79\n"
"v_mfma_f32_16x16x32_bf16 v[176:179], a[64:67], v[40:43], 0\n"
"v_cvt_pk_bf16_f32 v38, v76, v77\n"
"v_cvt_pk_bf16_f32 v39, v78, v79\n"
"v_mfma_f32_16x16x32_bf16 v[176:179], a[68:71], v[44:47], v[176:179]\n"
"v_exp_f32 v92, v92\n"
"v_mfma_f32_16x16x32_bf16 v[180:183], a[72:75], v[40:43], 0\n"
"v_exp_f32 v93, v93\n"
"v_mul_f32 v184, v184, v15\n"
"v_mul_f32 v185, v185, v15\n"
"v_mul_f32 v186, v186, v15\n"
"v_mul_f32 v187, v187, v15\n"
"v_mul_f32 v188, v188, v15\n"
"v_mul_f32 v189, v189, v15\n"
"v_mul_f32 v190, v190, v15\n"
"v_mul_f32 v191, v191, v15\n"
"v_cvt_pk_bf16_f32 v184, v184, v185\n"
"v_cvt_pk_bf16_f32 v185, v186, v187\n"
"v_cvt_pk_bf16_f32 v186, v188, v189\n"
"v_cvt_pk_bf16_f32 v187, v190, v191\n"
"v_accvgpr_write_b32 a180, v184\n"
"v_accvgpr_write_b32 a181, v185\n"
"v_accvgpr_write_b32 a182, v186\n"
"v_accvgpr_write_b32 a183, v187\n"
"v_mfma_f32_16x16x32_bf16 v[180:183], a[76:79], v[44:47], v[180:183]\n"
"v_exp_f32 v94, v94\n"
"v_mfma_f32_16x16x32_bf16 v[184:187], a[80:83], v[40:43], 0\n"
"v_exp_f32 v95, v95\n"
"v_mfma_f32_16x16x32_bf16 v[184:187], a[84:87], v[44:47], v[184:187]\n"
"v_exp_f32 v108, v108\n"
"v_mfma_f32_16x16x32_bf16 v[188:191], a[88:91], v[40:43], 0\n"
"s_waitcnt vmcnt(17)\n"
"s_barrier\n"
"v_mfma_f32_16x16x32_bf16 v[188:191], a[92:95], v[44:47], v[188:191]\n"
"ds_read_b128 a[0:3], v6 offset:0\n"
"ds_read_b128 a[4:7], v7 offset:0\n"
"v_accvgpr_read_b32 v14, a104\n"
"v_mfma_f32_16x16x32_bf16 a[104:107], a[112:115], v[48:51], 0\n"
"ds_read_b128 a[8:11], v8 offset:0\n"
"ds_read_b128 a[12:15], v9 offset:0\n"
"v_cmp_ngt_f32 vcc, s67, v14\n"
"s_mov_b32 s72, 262144\n"
"s_or_b64 s[70:71], s[70:71], vcc\n"
"v_rcp_f32 v14, v14\n"
"s_nop 0\n"
"v_mul_f32 v192, v192, v14\n"
"v_mul_f32 v193, v193, v14\n"
"v_mul_f32 v194, v194, v14\n"
"v_mul_f32 v195, v195, v14\n"
"v_mul_f32 v196, v196, v14\n"
"v_mul_f32 v197, v197, v14\n"
"v_mul_f32 v198, v198, v14\n"
"v_mul_f32 v199, v199, v14\n"
"v_cvt_pk_bf16_f32 v192, v192, v193\n"
"v_cvt_pk_bf16_f32 v193, v194, v195\n"
"v_cvt_pk_bf16_f32 v194, v196, v197\n"
"v_cvt_pk_bf16_f32 v195, v198, v199\n"
"v_accvgpr_write_b32 a136, v192\n"
"v_accvgpr_write_b32 a137, v193\n"
"v_accvgpr_write_b32 a138, v194\n"
"v_accvgpr_write_b32 a139, v195\n"
"v_mfma_f32_16x16x32_bf16 a[104:107], a[112:115], v[52:55], a[104:107]\n"
"s_mov_b32 m0, s62\n"
"v_cvt_pk_bf16_f32 v40, v80, v81\n"
"v_mfma_f32_16x16x32_bf16 v[192:195], a[32:35], v[48:51], 0\n"
"buffer_load_dwordx4 v0, s[44:47], s60 offen offset:0 nt lds\n"
"v_mfma_f32_16x16x32_bf16 v[192:195], a[36:39], v[52:55], v[192:195]\n"
"v_cvt_pk_bf16_f32 v41, v82, v83\n"
"ds_read_b128 a[16:19], v6 offset:4096\n"
"v_mfma_f32_16x16x32_bf16 v[196:199], a[40:43], v[48:51], 0\n"
"ds_read_b128 a[20:23], v7 offset:4096\n"
"ds_read_b128 a[24:27], v8 offset:4096\n"
"v_mul_f32 v200, v200, v14\n"
"v_mul_f32 v201, v201, v14\n"
"v_mul_f32 v202, v202, v14\n"
"v_mul_f32 v203, v203, v14\n"
"v_mul_f32 v204, v204, v14\n"
"v_mul_f32 v205, v205, v14\n"
"v_mul_f32 v206, v206, v14\n"
"v_mul_f32 v207, v207, v14\n"
"v_cvt_pk_bf16_f32 v200, v200, v201\n"
"v_cvt_pk_bf16_f32 v201, v202, v203\n"
"v_cvt_pk_bf16_f32 v202, v204, v205\n"
"v_cvt_pk_bf16_f32 v203, v206, v207\n"
"v_accvgpr_write_b32 a152, v200\n"
"v_accvgpr_write_b32 a153, v201\n"
"v_accvgpr_write_b32 a154, v202\n"
"v_accvgpr_write_b32 a155, v203\n"
"v_mfma_f32_16x16x32_bf16 v[196:199], a[44:47], v[52:55], v[196:199]\n"
"ds_read_b128 a[28:31], v9 offset:4096\n"
"v_cvt_pk_bf16_f32 v42, v84, v85\n"
"v_mfma_f32_16x16x32_bf16 v[200:203], a[48:51], v[48:51], 0\n"
"v_cvt_pk_bf16_f32 v43, v86, v87\n"
"v_cvt_pk_bf16_f32 v44, v88, v89\n"
"v_mfma_f32_16x16x32_bf16 v[200:203], a[52:55], v[52:55], v[200:203]\n"
"v_cvt_pk_bf16_f32 v45, v90, v91\n"
"v_cvt_pk_bf16_f32 v46, v92, v93\n"
"v_mfma_f32_16x16x32_bf16 v[204:207], a[56:59], v[48:51], 0\n"
"v_cvt_pk_bf16_f32 v47, v94, v95\n"
"ds_read_b64_tr_b16 a[32:33], v10 offset:0\n"
"v_mul_f32 v208, v208, v14\n"
"v_mul_f32 v209, v209, v14\n"
"v_mul_f32 v210, v210, v14\n"
"v_mul_f32 v211, v211, v14\n"
"v_mul_f32 v212, v212, v14\n"
"v_mul_f32 v213, v213, v14\n"
"v_mul_f32 v214, v214, v14\n"
"v_mul_f32 v215, v215, v14\n"
"v_cvt_pk_bf16_f32 v208, v208, v209\n"
"v_cvt_pk_bf16_f32 v209, v210, v211\n"
"v_cvt_pk_bf16_f32 v210, v212, v213\n"
"v_cvt_pk_bf16_f32 v211, v214, v215\n"
"v_accvgpr_write_b32 a168, v208\n"
"v_accvgpr_write_b32 a169, v209\n"
"v_accvgpr_write_b32 a170, v210\n"
"v_accvgpr_write_b32 a171, v211\n"
"v_mfma_f32_16x16x32_bf16 v[204:207], a[60:63], v[52:55], v[204:207]\n"
"ds_read_b64_tr_b16 a[34:35], v10 offset:4096\n"
"ds_read_b64_tr_b16 a[36:37], v10 offset:8192\n"
"v_mfma_f32_16x16x32_bf16 v[208:211], a[64:67], v[48:51], 0\n"
"v_exp_f32 v109, v109\n"
"v_mfma_f32_16x16x32_bf16 v[208:211], a[68:71], v[52:55], v[208:211]\n"
"buffer_load_dwordx4 v1, s[44:47], s60 offen offset:1024 nt lds\n"
"v_mfma_f32_16x16x32_bf16 v[212:215], a[72:75], v[48:51], 0\n"
"v_exp_f32 v110, v110\n"
"v_mul_f32 v216, v216, v14\n"
"v_mul_f32 v217, v217, v14\n"
"v_mul_f32 v218, v218, v14\n"
"v_mul_f32 v219, v219, v14\n"
"v_mul_f32 v220, v220, v14\n"
"v_mul_f32 v221, v221, v14\n"
"v_mul_f32 v222, v222, v14\n"
"v_mul_f32 v223, v223, v14\n"
"v_cvt_pk_bf16_f32 v216, v216, v217\n"
"v_cvt_pk_bf16_f32 v217, v218, v219\n"
"v_cvt_pk_bf16_f32 v218, v220, v221\n"
"v_cvt_pk_bf16_f32 v219, v222, v223\n"
"v_accvgpr_write_b32 a184, v216\n"
"v_accvgpr_write_b32 a185, v217\n"
"v_accvgpr_write_b32 a186, v218\n"
"v_accvgpr_write_b32 a187, v219\n"
"v_mfma_f32_16x16x32_bf16 v[212:215], a[76:79], v[52:55], v[212:215]\n"
"v_exp_f32 v111, v111\n"
"v_mfma_f32_16x16x32_bf16 v[216:219], a[80:83], v[48:51], 0\n"
"ds_read_b64_tr_b16 a[38:39], v10 offset:12288\n"
"ds_read_b64_tr_b16 a[40:41], v11 offset:0\n"
"v_mfma_f32_16x16x32_bf16 v[216:219], a[84:87], v[52:55], v[216:219]\n"
"ds_read_b64_tr_b16 a[42:43], v11 offset:4096\n"
"ds_read_b64_tr_b16 a[44:45], v11 offset:8192\n"
"v_mfma_f32_16x16x32_bf16 v[220:223], a[88:91], v[48:51], 0\n"
"v_cvt_pk_bf16_f32 v48, v96, v97\n"
"v_cvt_pk_bf16_f32 v49, v98, v99\n"
"v_lshrrev_b32 v14, 2, v13\n"
"v_mfma_f32_16x16x32_bf16 v[220:223], a[92:95], v[52:55], v[220:223]\n"
".LG1b_%=:\n"
"s_waitcnt lgkmcnt(14)\n"
"v_mfma_f32_16x16x32_bf16 v[64:67], a[0:3], a[192:195], v[16:19]\n"
"v_cvt_pk_bf16_f32 v50, v100, v101\n"
"v_cvt_pk_bf16_f32 v51, v102, v103\n"
"s_waitcnt lgkmcnt(13)\n"
"v_mfma_f32_16x16x32_bf16 v[64:67], a[4:7], a[196:199], v[64:67]\n"
"v_cvt_pk_bf16_f32 v52, v104, v105\n"
"v_cvt_pk_bf16_f32 v53, v106, v107\n"
"s_waitcnt lgkmcnt(12)\n"
"v_mfma_f32_16x16x32_bf16 v[64:67], a[8:11], a[200:203], v[64:67]\n"
"buffer_load_dwordx4 v2, s[44:47], s60 offen offset:2048 nt lds\n"
"s_waitcnt lgkmcnt(11)\n"
"v_mfma_f32_16x16x32_bf16 v[64:67], a[12:15], a[204:207], v[64:67]\n"
"v_cvt_pk_bf16_f32 v54, v108, v109\n"
"v_cvt_pk_bf16_f32 v55, v110, v111\n"
"v_mfma_f32_16x16x32_bf16 v[80:83], a[0:3], a[208:211], v[20:23]\n"
"ds_read_b64_tr_b16 a[46:47], v11 offset:12288\n"
"ds_read_b64_tr_b16 a[48:49], v10 offset:256\n"
"v_mfma_f32_16x16x32_bf16 v[80:83], a[4:7], a[212:215], v[80:83]\n"
"ds_read_b64_tr_b16 a[50:51], v10 offset:4352\n"
"ds_read_b64_tr_b16 a[52:53], v10 offset:8448\n"
"v_mfma_f32_16x16x32_bf16 v[80:83], a[8:11], a[216:219], v[80:83]\n"
"ds_read_b64_tr_b16 a[54:55], v10 offset:12544\n"
"ds_read_b64_tr_b16 a[56:57], v11 offset:256\n"
"v_mfma_f32_16x16x32_bf16 v[80:83], a[12:15], a[220:223], v[80:83]\n"
"ds_read_b64_tr_b16 a[58:59], v11 offset:4352\n"
"ds_read_b64_tr_b16 a[60:61], v11 offset:8448\n"
"v_mfma_f32_16x16x32_bf16 v[96:99], a[0:3], a[224:227], v[24:27]\n"
"ds_read_b128 a[0:3], v6 offset:8192\n"
"ds_read_b64_tr_b16 a[62:63], v11 offset:12544\n"
"v_mfma_f32_16x16x32_bf16 v[96:99], a[4:7], a[228:231], v[96:99]\n"
"ds_read_b128 a[4:7], v7 offset:8192\n"
"ds_read_b64_tr_b16 a[64:65], v10 offset:512\n"
"v_mfma_f32_16x16x32_bf16 v[96:99], a[8:11], a[232:235], v[96:99]\n"
"ds_read_b128 a[8:11], v8 offset:8192\n"
"ds_read_b64_tr_b16 a[66:67], v10 offset:4608\n"
"v_mfma_f32_16x16x32_bf16 v[96:99], a[12:15], a[236:239], v[96:99]\n"
"buffer_load_dwordx4 v3, s[44:47], s60 offen offset:3072 nt lds\n"
"s_waitcnt lgkmcnt(15)\n"
"v_mfma_f32_16x16x32_bf16 v[68:71], a[16:19], a[192:195], v[16:19]\n"
"ds_read_b128 a[12:15], v9 offset:8192\n"
"ds_read_b64_tr_b16 a[68:69], v10 offset:8704\n"
"v_mfma_f32_16x16x32_bf16 v[68:71], a[20:23], a[196:199], v[68:71]\n"
"ds_read_b64_tr_b16 a[70:71], v10 offset:12800\n"
"ds_read_b64_tr_b16 a[72:73], v11 offset:512\n"
"v_mfma_f32_16x16x32_bf16 v[68:71], a[24:27], a[200:203], v[68:71]\n"
"ds_read_b64_tr_b16 a[74:75], v11 offset:4608\n"
"ds_read_b64_tr_b16 a[76:77], v11 offset:8704\n"
"v_mfma_f32_16x16x32_bf16 v[68:71], a[28:31], a[204:207], v[68:71]\n"
"ds_read_b64_tr_b16 a[78:79], v11 offset:12800\n"
"ds_read_b64_tr_b16 a[80:81], v10 offset:768\n"
"v_mfma_f32_16x16x32_bf16 v[84:87], a[16:19], a[208:211], v[20:23]\n"
"ds_read_b64_tr_b16 a[82:83], v10 offset:4864\n"
"ds_read_b64_tr_b16 a[84:85], v10 offset:8960\n"
"v_mfma_f32_16x16x32_bf16 v[84:87], a[20:23], a[212:215], v[84:87]\n"
"ds_read_b64_tr_b16 a[86:87], v10 offset:13056\n"
"ds_read_b64_tr_b16 a[88:89], v11 offset:768\n"
"v_mfma_f32_16x16x32_bf16 v[84:87], a[24:27], a[216:219], v[84:87]\n"
"ds_read_b64_tr_b16 a[90:91], v11 offset:4864\n"
"ds_read_b64_tr_b16 a[92:93], v11 offset:8960\n"
"v_mfma_f32_16x16x32_bf16 v[84:87], a[28:31], a[220:223], v[84:87]\n"
"s_mov_b32 m0, s63\n"
"ds_read_b64_tr_b16 a[94:95], v11 offset:13056\n"
"v_mfma_f32_16x16x32_bf16 v[100:103], a[16:19], a[224:227], v[24:27]\n"
"buffer_load_dwordx4 v4, s[48:51], s61 offen offset:0 nt lds\n"
"v_mfma_f32_16x16x32_bf16 v[100:103], a[20:23], a[228:231], v[100:103]\n"
"ds_read_b128 a[16:19], v6 offset:12288\n"
"ds_read_b128 a[20:23], v7 offset:12288\n"
"v_mfma_f32_16x16x32_bf16 v[100:103], a[24:27], a[232:235], v[100:103]\n"
"ds_read_b128 a[24:27], v8 offset:12288\n"
"v_mfma_f32_16x16x32_bf16 v[100:103], a[28:31], a[236:239], v[100:103]\n"
"ds_read_b128 a[28:31], v9 offset:12288\n"
"s_waitcnt lgkmcnt(15)\n"
"v_mfma_f32_16x16x32_bf16 v[72:75], a[0:3], a[192:195], v[16:19]\n"
"v_exp_f32 v64, v64\n"
"v_mfma_f32_16x16x32_bf16 v[72:75], a[4:7], a[196:199], v[72:75]\n"
"v_exp_f32 v65, v65\n"
"v_mfma_f32_16x16x32_bf16 v[72:75], a[8:11], a[200:203], v[72:75]\n"
"v_exp_f32 v66, v66\n"
"v_mfma_f32_16x16x32_bf16 v[72:75], a[12:15], a[204:207], v[72:75]\n"
"v_exp_f32 v67, v67\n"
"v_mfma_f32_16x16x32_bf16 v[88:91], a[0:3], a[208:211], v[20:23]\n"
"v_exp_f32 v80, v80\n"
"v_mfma_f32_16x16x32_bf16 v[88:91], a[4:7], a[212:215], v[88:91]\n"
"buffer_load_dwordx4 v5, s[48:51], s61 offen offset:1024 nt lds\n"
"v_mfma_f32_16x16x32_bf16 v[88:91], a[8:11], a[216:219], v[88:91]\n"
"v_exp_f32 v81, v81\n"
"v_mfma_f32_16x16x32_bf16 v[88:91], a[12:15], a[220:223], v[88:91]\n"
"v_exp_f32 v82, v82\n"
"v_mfma_f32_16x16x32_bf16 v[104:107], a[0:3], a[224:227], v[24:27]\n"
"v_exp_f32 v83, v83\n"
"v_mfma_f32_16x16x32_bf16 v[104:107], a[4:7], a[228:231], v[104:107]\n"
"v_exp_f32 v96, v96\n"
"v_mfma_f32_16x16x32_bf16 v[104:107], a[8:11], a[232:235], v[104:107]\n"
"v_exp_f32 v97, v97\n"
"v_mfma_f32_16x16x32_bf16 v[104:107], a[12:15], a[236:239], v[104:107]\n"
"v_exp_f32 v98, v98\n"
"s_waitcnt lgkmcnt(3)\n"
"v_mfma_f32_16x16x32_bf16 v[76:79], a[16:19], a[192:195], v[16:19]\n"
"v_exp_f32 v99, v99\n"
"s_waitcnt lgkmcnt(2)\n"
"v_mfma_f32_16x16x32_bf16 v[76:79], a[20:23], a[196:199], v[76:79]\n"
"v_exp_f32 v68, v68\n"
"s_waitcnt lgkmcnt(1)\n"
"v_mfma_f32_16x16x32_bf16 v[76:79], a[24:27], a[200:203], v[76:79]\n"
"buffer_load_dwordx4 v4, s[48:51], s61 offen offset:2048 nt lds\n"
"s_waitcnt lgkmcnt(0)\n"
"v_mfma_f32_16x16x32_bf16 v[76:79], a[28:31], a[204:207], v[76:79]\n"
"v_exp_f32 v69, v69\n"
"v_mfma_f32_16x16x32_bf16 v[92:95], a[16:19], a[208:211], v[20:23]\n"
"v_exp_f32 v70, v70\n"
"v_mfma_f32_16x16x32_bf16 v[92:95], a[20:23], a[212:215], v[92:95]\n"
"v_exp_f32 v71, v71\n"
"v_mfma_f32_16x16x32_bf16 v[92:95], a[24:27], a[216:219], v[92:95]\n"
"v_exp_f32 v84, v84\n"
"v_mfma_f32_16x16x32_bf16 v[92:95], a[28:31], a[220:223], v[92:95]\n"
"v_exp_f32 v85, v85\n"
"v_mfma_f32_16x16x32_bf16 v[108:111], a[16:19], a[224:227], v[24:27]\n"
"v_exp_f32 v86, v86\n"
"v_mfma_f32_16x16x32_bf16 v[108:111], a[20:23], a[228:231], v[108:111]\n"
"v_exp_f32 v87, v87\n"
"v_mfma_f32_16x16x32_bf16 v[108:111], a[24:27], a[232:235], v[108:111]\n"
"v_exp_f32 v100, v100\n"
"v_mfma_f32_16x16x32_bf16 v[108:111], a[28:31], a[236:239], v[108:111]\n"
"buffer_load_dwordx4 v5, s[48:51], s61 offen offset:3072 nt lds\n"
"v_mfma_f32_16x16x32_bf16 a[96:99], a[112:115], v[32:35], a[96:99]\n"
"v_xor_b32 v6, s64, v6\n"
"v_xor_b32 v7, s64, v7\n"
"v_mfma_f32_16x16x32_bf16 a[96:99], a[112:115], v[36:39], a[96:99]\n"
"v_xor_b32 v8, s64, v8\n"
"v_xor_b32 v9, s64, v9\n"
"v_mfma_f32_16x16x32_bf16 v[128:131], a[32:35], v[32:35], v[128:131]\n"
"v_xor_b32 v10, s65, v10\n"
"v_xor_b32 v11, s65, v11\n"
"v_mfma_f32_16x16x32_bf16 v[128:131], a[36:39], v[36:39], v[128:131]\n"
"s_xor_b32 s62, s62, s65\n"
"s_xor_b32 s63, s63, s64\n"
"v_mfma_f32_16x16x32_bf16 v[132:135], a[40:43], v[32:35], v[132:135]\n"
"s_add_u32 s60, s60, 0x4000\n"
"s_add_u32 s61, s61, 0x4000\n"
"v_mfma_f32_16x16x32_bf16 v[132:135], a[44:47], v[36:39], v[132:135]\n"
"s_xor_b32 s64, s64, 0xc000\n"
"s_xor_b32 s65, s65, 0xc000\n"
"v_mfma_f32_16x16x32_bf16 v[136:139], a[48:51], v[32:35], v[136:139]\n"
"v_exp_f32 v101, v101\n"
"v_mfma_f32_16x16x32_bf16 v[136:139], a[52:55], v[36:39], v[136:139]\n"
"v_exp_f32 v102, v102\n"
"v_mfma_f32_16x16x32_bf16 v[140:143], a[56:59], v[32:35], v[140:143]\n"
"v_exp_f32 v103, v103\n"
"v_mfma_f32_16x16x32_bf16 v[140:143], a[60:63], v[36:39], v[140:143]\n"
"v_exp_f32 v72, v72\n"
"v_mfma_f32_16x16x32_bf16 v[144:147], a[64:67], v[32:35], v[144:147]\n"
"v_exp_f32 v73, v73\n"
"v_mfma_f32_16x16x32_bf16 v[144:147], a[68:71], v[36:39], v[144:147]\n"
"v_exp_f32 v74, v74\n"
"v_mfma_f32_16x16x32_bf16 v[148:151], a[72:75], v[32:35], v[148:151]\n"
"v_exp_f32 v75, v75\n"
"v_mfma_f32_16x16x32_bf16 v[148:151], a[76:79], v[36:39], v[148:151]\n"
"v_exp_f32 v88, v88\n"
"v_mfma_f32_16x16x32_bf16 v[152:155], a[80:83], v[32:35], v[152:155]\n"
"v_exp_f32 v89, v89\n"
"v_mfma_f32_16x16x32_bf16 v[152:155], a[84:87], v[36:39], v[152:155]\n"
"v_exp_f32 v90, v90\n"
"v_mfma_f32_16x16x32_bf16 v[156:159], a[88:91], v[32:35], v[156:159]\n"
"v_cvt_pk_bf16_f32 v32, v64, v65\n"
"v_cvt_pk_bf16_f32 v33, v66, v67\n"
"v_mfma_f32_16x16x32_bf16 v[156:159], a[92:95], v[36:39], v[156:159]\n"
"v_cvt_pk_bf16_f32 v34, v68, v69\n"
"v_cvt_pk_bf16_f32 v35, v70, v71\n"
"v_mfma_f32_16x16x32_bf16 a[100:103], a[112:115], v[40:43], a[100:103]\n"
"v_cvt_pk_bf16_f32 v36, v72, v73\n"
"v_cvt_pk_bf16_f32 v37, v74, v75\n"
"v_mfma_f32_16x16x32_bf16 a[100:103], a[112:115], v[44:47], a[100:103]\n"
"v_exp_f32 v91, v91\n"
"v_mfma_f32_16x16x32_bf16 v[160:163], a[32:35], v[40:43], v[160:163]\n"
"v_exp_f32 v104, v104\n"
"v_mfma_f32_16x16x32_bf16 v[160:163], a[36:39], v[44:47], v[160:163]\n"
"v_exp_f32 v105, v105\n"
"v_mfma_f32_16x16x32_bf16 v[164:167], a[40:43], v[40:43], v[164:167]\n"
"v_exp_f32 v106, v106\n"
"v_mfma_f32_16x16x32_bf16 v[164:167], a[44:47], v[44:47], v[164:167]\n"
"v_exp_f32 v107, v107\n"
"v_mfma_f32_16x16x32_bf16 v[168:171], a[48:51], v[40:43], v[168:171]\n"
"v_exp_f32 v76, v76\n"
"v_mfma_f32_16x16x32_bf16 v[168:171], a[52:55], v[44:47], v[168:171]\n"
"v_exp_f32 v77, v77\n"
"v_mfma_f32_16x16x32_bf16 v[172:175], a[56:59], v[40:43], v[172:175]\n"
"v_exp_f32 v78, v78\n"
"v_mfma_f32_16x16x32_bf16 v[172:175], a[60:63], v[44:47], v[172:175]\n"
"v_exp_f32 v79, v79\n"
"v_mfma_f32_16x16x32_bf16 v[176:179], a[64:67], v[40:43], v[176:179]\n"
"v_cvt_pk_bf16_f32 v38, v76, v77\n"
"v_cvt_pk_bf16_f32 v39, v78, v79\n"
"v_mfma_f32_16x16x32_bf16 v[176:179], a[68:71], v[44:47], v[176:179]\n"
"v_exp_f32 v92, v92\n"
"v_mfma_f32_16x16x32_bf16 v[180:183], a[72:75], v[40:43], v[180:183]\n"
"v_exp_f32 v93, v93\n"
"v_mfma_f32_16x16x32_bf16 v[180:183], a[76:79], v[44:47], v[180:183]\n"
"v_exp_f32 v94, v94\n"
"v_mfma_f32_16x16x32_bf16 v[184:187], a[80:83], v[40:43], v[184:187]\n"
"v_exp_f32 v95, v95\n"
"v_mfma_f32_16x16x32_bf16 v[184:187], a[84:87], v[44:47], v[184:187]\n"
"v_exp_f32 v108, v108\n"
"v_mfma_f32_16x16x32_bf16 v[188:191], a[88:91], v[40:43], v[188:191]\n"
"s_waitcnt vmcnt(16)\n"
"s_barrier\n"
"v_mfma_f32_16x16x32_bf16 v[188:191], a[92:95], v[44:47], v[188:191]\n"
"ds_read_b128 a[0:3], v6 offset:0\n"
"ds_read_b128 a[4:7], v7 offset:0\n"
"v_mfma_f32_16x16x32_bf16 a[104:107], a[112:115], v[48:51], a[104:107]\n"
"ds_read_b128 a[8:11], v8 offset:0\n"
"ds_read_b128 a[12:15], v9 offset:0\n"
"v_mfma_f32_16x16x32_bf16 a[104:107], a[112:115], v[52:55], a[104:107]\n"
"s_mov_b32 m0, s62\n"
"v_cvt_pk_bf16_f32 v40, v80, v81\n"
"v_mfma_f32_16x16x32_bf16 v[192:195], a[32:35], v[48:51], v[192:195]\n"
"buffer_load_dwordx4 v0, s[44:47], s60 offen offset:0 nt lds\n"
"v_mfma_f32_16x16x32_bf16 v[192:195], a[36:39], v[52:55], v[192:195]\n"
"v_cvt_pk_bf16_f32 v41, v82, v83\n"
"ds_read_b128 a[16:19], v6 offset:4096\n"
"v_mfma_f32_16x16x32_bf16 v[196:199], a[40:43], v[48:51], v[196:199]\n"
"ds_read_b128 a[20:23], v7 offset:4096\n"
"ds_read_b128 a[24:27], v8 offset:4096\n"
"v_mfma_f32_16x16x32_bf16 v[196:199], a[44:47], v[52:55], v[196:199]\n"
"ds_read_b128 a[28:31], v9 offset:4096\n"
"v_cvt_pk_bf16_f32 v42, v84, v85\n"
"v_mfma_f32_16x16x32_bf16 v[200:203], a[48:51], v[48:51], v[200:203]\n"
"v_cvt_pk_bf16_f32 v43, v86, v87\n"
"v_cvt_pk_bf16_f32 v44, v88, v89\n"
"v_mfma_f32_16x16x32_bf16 v[200:203], a[52:55], v[52:55], v[200:203]\n"
"v_cvt_pk_bf16_f32 v45, v90, v91\n"
"v_cvt_pk_bf16_f32 v46, v92, v93\n"
"v_mfma_f32_16x16x32_bf16 v[204:207], a[56:59], v[48:51], v[204:207]\n"
"v_cvt_pk_bf16_f32 v47, v94, v95\n"
"ds_read_b64_tr_b16 a[32:33], v10 offset:0\n"
"v_mfma_f32_16x16x32_bf16 v[204:207], a[60:63], v[52:55], v[204:207]\n"
"ds_read_b64_tr_b16 a[34:35], v10 offset:4096\n"
"ds_read_b64_tr_b16 a[36:37], v10 offset:8192\n"
"v_mfma_f32_16x16x32_bf16 v[208:211], a[64:67], v[48:51], v[208:211]\n"
"v_exp_f32 v109, v109\n"
"v_mfma_f32_16x16x32_bf16 v[208:211], a[68:71], v[52:55], v[208:211]\n"
"buffer_load_dwordx4 v1, s[44:47], s60 offen offset:1024 nt lds\n"
"v_mfma_f32_16x16x32_bf16 v[212:215], a[72:75], v[48:51], v[212:215]\n"
"v_exp_f32 v110, v110\n"
"v_mfma_f32_16x16x32_bf16 v[212:215], a[76:79], v[52:55], v[212:215]\n"
"v_exp_f32 v111, v111\n"
"v_mfma_f32_16x16x32_bf16 v[216:219], a[80:83], v[48:51], v[216:219]\n"
"ds_read_b64_tr_b16 a[38:39], v10 offset:12288\n"
"ds_read_b64_tr_b16 a[40:41], v11 offset:0\n"
"v_mfma_f32_16x16x32_bf16 v[216:219], a[84:87], v[52:55], v[216:219]\n"
"ds_read_b64_tr_b16 a[42:43], v11 offset:4096\n"
"ds_read_b64_tr_b16 a[44:45], v11 offset:8192\n"
"v_mfma_f32_16x16x32_bf16 v[220:223], a[88:91], v[48:51], v[220:223]\n"
"v_cvt_pk_bf16_f32 v48, v96, v97\n"
"v_cvt_pk_bf16_f32 v49, v98, v99\n"
"v_mfma_f32_16x16x32_bf16 v[220:223], a[92:95], v[52:55], v[220:223]\n"
"s_sub_u32 s66, s66, 1\n"
"s_cmp_lg_u32 s66, 0\n"
"s_cbranch_scc1 .LG1b_%=\n"
"s_waitcnt lgkmcnt(14)\n"
"v_mfma_f32_16x16x32_bf16 v[64:67], a[0:3], a[192:195], v[16:19]\n"
"v_cvt_pk_bf16_f32 v50, v100, v101\n"
"v_cvt_pk_bf16_f32 v51, v102, v103\n"
"s_mov_b32 s40, %[wol]\n"
"s_and_b32 s41, %[woh], 0xffff\n"
"s_mov_b32 s42, 1047808\n"
"s_lshl_b32 s74, %[ksp], 6\n"
"v_add_u32 v2, s74, v4\n"
"v_and_b32 v2, 0xff, v2\n"
"v_and_b32 v3, 0xffffff00, v4\n"
"v_lshl_add_u32 v0, v3, 2, v2\n"
"v_add_u32 v2, s74, v5\n"
"v_and_b32 v2, 0xff, v2\n"
"v_and_b32 v3, 0xffffff00, v5\n"
"v_lshl_add_u32 v1, v3, 2, v2\n"
"s_mul_i32 s74, %[ntp], 0x22222222\n"
"s_xor_b32 s68, s74, 0x75364210\n"
"s_mov_b32 s69, -1\n"
"s_waitcnt lgkmcnt(13)\n"
"v_mfma_f32_16x16x32_bf16 v[64:67], a[4:7], a[196:199], v[64:67]\n"
"v_cvt_pk_bf16_f32 v52, v104, v105\n"
"v_cvt_pk_bf16_f32 v53, v106, v107\n"
"s_waitcnt lgkmcnt(12)\n"
"v_mfma_f32_16x16x32_bf16 v[64:67], a[8:11], a[200:203], v[64:67]\n"
"buffer_store_dwordx4 a[128:131], v12, s[52:55], s56 offen offset:0 sc1\n"
"s_waitcnt lgkmcnt(11)\n"
"v_mfma_f32_16x16x32_bf16 v[64:67], a[12:15], a[204:207], v[64:67]\n"
"v_cvt_pk_bf16_f32 v54, v108, v109\n"
"v_cvt_pk_bf16_f32 v55, v110, v111\n"
"v_mfma_f32_16x16x32_bf16 v[80:83], a[0:3], a[208:211], v[20:23]\n"
"ds_read_b64_tr_b16 a[46:47], v11 offset:12288\n"
"ds_read_b64_tr_b16 a[48:49], v10 offset:256\n"
"v_mfma_f32_16x16x32_bf16 v[80:83], a[4:7], a[212:215], v[80:83]\n"
"ds_read_b64_tr_b16 a[50:51], v10 offset:4352\n"
"ds_read_b64_tr_b16 a[52:53], v10 offset:8448\n"
"v_mfma_f32_16x16x32_bf16 v[80:83], a[8:11], a[216:219], v[80:83]\n"
"ds_read_b64_tr_b16 a[54:55], v10 offset:12544\n"
"ds_read_b64_tr_b16 a[56:57], v11 offset:256\n"
"v_mfma_f32_16x16x32_bf16 v[80:83], a[12:15], a[220:223], v[80:83]\n"
"ds_read_b64_tr_b16 a[58:59], v11 offset:4352\n"
"ds_read_b64_tr_b16 a[60:61], v11 offset:8448\n"
"v_mfma_f32_16x16x32_bf16 v[96:99], a[0:3], a[224:227], v[24:27]\n"
"ds_read_b128 a[0:3], v6 offset:8192\n"
"ds_read_b64_tr_b16 a[62:63], v11 offset:12544\n"
"v_mfma_f32_16x16x32_bf16 v[96:99], a[4:7], a[228:231], v[96:99]\n"
"ds_read_b128 a[4:7], v7 offset:8192\n"
"ds_read_b64_tr_b16 a[64:65], v10 offset:512\n"
"v_mfma_f32_16x16x32_bf16 v[96:99], a[8:11], a[232:235], v[96:99]\n"
"ds_read_b128 a[8:11], v8 offset:8192\n"
"ds_read_b64_tr_b16 a[66:67], v10 offset:4608\n"
"v_mfma_f32_16x16x32_bf16 v[96:99], a[12:15], a[236:239], v[96:99]\n"
"buffer_store_dwordx4 a[144:147], v12, s[52:55], s56 offen offset:1024 sc1\n"
"s_waitcnt lgkmcnt(15)\n"
"v_mfma_f32_16x16x32_bf16 v[68:71], a[16:19], a[192:195], v[16:19]\n"
"ds_read_b128 a[12:15], v9 offset:8192\n"
"ds_read_b64_tr_b16 a[68:69], v10 offset:8704\n"
"v_mfma_f32_16x16x32_bf16 v[68:71], a[20:23], a[196:199], v[68:71]\n"
"ds_read_b64_tr_b16 a[70:71], v10 offset:12800\n"
"ds_read_b64_tr_b16 a[72:73], v11 offset:512\n"
"v_mfma_f32_16x16x32_bf16 v[68:71], a[24:27], a[200:203], v[68:71]\n"
"ds_read_b64_tr_b16 a[74:75], v11 offset:4608\n"
"ds_read_b64_tr_b16 a[76:77], v11 offset:8704\n"
"s_add_u32 s75, s77, 0\n"
"s_add_u32 m0, s75, 0\n"
"s_bfe_u32 s76, s68, 0x40004\n"
"s_lshl_b32 s76, s76, 17\n"
"buffer_load_dwordx4 v0, s[40:43], s76 offen offset:0 lds\n"
"v_mfma_f32_16x16x32_bf16 v[68:71], a[28:31], a[204:207], v[68:71]\n"
"ds_read_b64_tr_b16 a[78:79], v11 offset:12800\n"
"ds_read_b64_tr_b16 a[80:81], v10 offset:768\n"
"v_mfma_f32_16x16x32_bf16 v[84:87], a[16:19], a[208:211], v[20:23]\n"
"ds_read_b64_tr_b16 a[82:83], v10 offset:4864\n"
"ds_read_b64_tr_b16 a[84:85], v10 offset:8960\n"
"v_mfma_f32_16x16x32_bf16 v[84:87], a[20:23], a[212:215], v[84:87]\n"
"ds_read_b64_tr_b16 a[86:87], v10 offset:13056\n"
"ds_read_b64_tr_b16 a[88:89], v11 offset:768\n"
"v_mfma_f32_16x16x32_bf16 v[84:87], a[24:27], a[216:219], v[84:87]\n"
"ds_read_b64_tr_b16 a[90:91], v11 offset:4864\n"
"ds_read_b64_tr_b16 a[92:93], v11 offset:8960\n"
"buffer_store_dwordx4 a[160:163], v12, s[52:55], s56 offen offset:2048 sc1\n"
"v_mfma_f32_16x16x32_bf16 v[84:87], a[28:31], a[220:223], v[84:87]\n"
"ds_read_b64_tr_b16 a[94:95], v11 offset:13056\n"
"v_mfma_f32_16x16x32_bf16 v[100:103], a[16:19], a[224:227], v[24:27]\n"
"s_add_u32 m0, s75, 0\n"
"s_add_u32 s76, s76, 3072\n"
"buffer_load_dwordx4 v1, s[40:43], s76 offen offset:1024 lds\n"
"v_mfma_f32_16x16x32_bf16 v[100:103], a[20:23], a[228:231], v[100:103]\n"
"ds_read_b128 a[16:19], v6 offset:12288\n"
"ds_read_b128 a[20:23], v7 offset:12288\n"
"v_mfma_f32_16x16x32_bf16 v[100:103], a[24:27], a[232:235], v[100:103]\n"
"ds_read_b128 a[24:27], v8 offset:12288\n"
"v_mfma_f32_16x16x32_bf16 v[100:103], a[28:31], a[236:239], v[100:103]\n"
"ds_read_b128 a[28:31], v9 offset:12288\n"
"buffer_store_dwordx4 a[176:179], v12, s[52:55], s56 offen offset:3072 sc1\n"
"s_waitcnt lgkmcnt(15)\n"
"v_mfma_f32_16x16x32_bf16 v[72:75], a[0:3], a[192:195], v[16:19]\n"
"v_exp_f32 v64, v64\n"
"v_mfma_f32_16x16x32_bf16 v[72:75], a[4:7], a[196:199], v[72:75]\n"
"v_exp_f32 v65, v65\n"
"v_mfma_f32_16x16x32_bf16 v[72:75], a[8:11], a[200:203], v[72:75]\n"
"v_exp_f32 v66, v66\n"
"v_mfma_f32_16x16x32_bf16 v[72:75], a[12:15], a[204:207], v[72:75]\n"
"v_exp_f32 v67, v67\n"
"v_mfma_f32_16x16x32_bf16 v[88:91], a[0:3], a[208:211], v[20:23]\n"
"v_exp_f32 v80, v80\n"
"buffer_store_dwordx4 a[132:135], v12, s[52:55], s57 offen offset:0 sc1\n"
"v_mfma_f32_16x16x32_bf16 v[88:91], a[4:7], a[212:215], v[88:91]\n"
"s_add_u32 m0, s75, 0\n"
"s_add_u32 s76, s76, 3072\n"
"buffer_load_dwordx4 v0, s[40:43], s76 offen offset:2048 lds\n"
"v_mfma_f32_16x16x32_bf16 v[88:91], a[8:11], a[216:219], v[88:91]\n"
"v_exp_f32 v81, v81\n"
"v_mfma_f32_16x16x32_bf16 v[88:91], a[12:15], a[220:223], v[88:91]\n"
"v_exp_f32 v82, v82\n"
"v_mfma_f32_16x16x32_bf16 v[104:107], a[0:3], a[224:227], v[24:27]\n"
"v_exp_f32 v83, v83\n"
"v_mfma_f32_16x16x32_bf16 v[104:107], a[4:7], a[228:231], v[104:107]\n"
"v_exp_f32 v96, v96\n"
"buffer_store_dwordx4 a[148:151], v12, s[52:55], s57 offen offset:1024 sc1\n"
"v_mfma_f32_16x16x32_bf16 v[104:107], a[8:11], a[232:235], v[104:107]\n"
"v_exp_f32 v97, v97\n"
"v_mfma_f32_16x16x32_bf16 v[104:107], a[12:15], a[236:239], v[104:107]\n"
"v_exp_f32 v98, v98\n"
"s_waitcnt lgkmcnt(3)\n"
"v_mfma_f32_16x16x32_bf16 v[76:79], a[16:19], a[192:195], v[16:19]\n"
"v_exp_f32 v99, v99\n"
"s_waitcnt lgkmcnt(2)\n"
"v_mfma_f32_16x16x32_bf16 v[76:79], a[20:23], a[196:199], v[76:79]\n"
"v_exp_f32 v68, v68\n"
"s_waitcnt lgkmcnt(1)\n"
"v_mfma_f32_16x16x32_bf16 v[76:79], a[24:27], a[200:203], v[76:79]\n"
"buffer_store_dwordx4 a[164:167], v12, s[52:55], s57 offen offset:2048 sc1\n"
"s_add_u32 m0, s75, 0\n"
"s_add_u32 s76, s76, 3072\n"
"buffer_load_dwordx4 v1, s[40:43], s76 offen offset:3072 lds\n"
"s_waitcnt lgkmcnt(0)\n"
"v_mfma_f32_16x16x32_bf16 v[76:79], a[28:31], a[204:207], v[76:79]\n"
"v_exp_f32 v69, v69\n"
"v_mfma_f32_16x16x32_bf16 v[92:95], a[16:19], a[208:211], v[20:23]\n"
"v_exp_f32 v70, v70\n"
"v_mfma_f32_16x16x32_bf16 v[92:95], a[20:23], a[212:215], v[92:95]\n"
"v_exp_f32 v71, v71\n"
"v_mfma_f32_16x16x32_bf16 v[92:95], a[24:27], a[216:219], v[92:95]\n"
"v_exp_f32 v84, v84\n"
"v_mfma_f32_16x16x32_bf16 v[92:95], a[28:31], a[220:223], v[92:95]\n"
"v_exp_f32 v85, v85\n"
"buffer_store_dwordx4 a[180:183], v12, s[52:55], s57 offen offset:3072 sc1\n"
"v_mfma_f32_16x16x32_bf16 v[108:111], a[16:19], a[224:227], v[24:27]\n"
"v_exp_f32 v86, v86\n"
"v_mfma_f32_16x16x32_bf16 v[108:111], a[20:23], a[228:231], v[108:111]\n"
"v_exp_f32 v87, v87\n"
"v_mfma_f32_16x16x32_bf16 v[108:111], a[24:27], a[232:235], v[108:111]\n"
"v_exp_f32 v100, v100\n"
"s_add_u32 m0, s75, 16384\n"
"s_add_u32 s76, s76, 56320\n"
"buffer_load_dwordx4 v0, s[40:43], s76 offen offset:0 lds\n"
"v_mfma_f32_16x16x32_bf16 v[108:111], a[28:31], a[236:239], v[108:111]\n"
"buffer_store_dwordx4 a[136:139], v12, s[52:55], s58 offen offset:0 sc1\n"
"v_mfma_f32_16x16x32_bf16 a[96:99], a[112:115], v[32:35], a[96:99]\n"
"v_xor_b32 v6, s64, v6\n"
"v_xor_b32 v7, s64, v7\n"
"v_mfma_f32_16x16x32_bf16 a[96:99], a[112:115], v[36:39], a[96:99]\n"
"v_xor_b32 v8, s64, v8\n"
"v_xor_b32 v9, s64, v9\n"
"v_mfma_f32_16x16x32_bf16 v[128:131], a[32:35], v[32:35], v[128:131]\n"
"v_xor_b32 v10, s65, v10\n"
"v_xor_b32 v11, s65, v11\n"
"v_mfma_f32_16x16x32_bf16 v[128:131], a[36:39], v[36:39], v[128:131]\n"
"s_xor_b32 s62, s62, s65\n"
"s_xor_b32 s63, s63, s64\n"
"v_mfma_f32_16x16x32_bf16 v[132:135], a[40:43], v[32:35], v[132:135]\n"
"s_add_u32 s60, s60, 0x4000\n"
"s_add_u32 s61, s61, 0x4000\n"
"v_mfma_f32_16x16x32_bf16 v[132:135], a[44:47], v[36:39], v[132:135]\n"
"s_xor_b32 s64, s64, 0xc000\n"
"s_xor_b32 s65, s65, 0xc000\n"
"buffer_store_dwordx4 a[152:155], v12, s[52:55], s58 offen offset:1024 sc1\n"
"v_mfma_f32_16x16x32_bf16 v[136:139], a[48:51], v[32:35], v[136:139]\n"
"v_exp_f32 v101, v101\n"
"s_add_u32 m0, s75, 16384\n"
"s_add_u32 s76, s76, 3072\n"
"buffer_load_dwordx4 v1, s[40:43], s76 offen offset:1024 lds\n"
"v_mfma_f32_16x16x32_bf16 v[136:139], a[52:55], v[36:39], v[136:139]\n"
"v_exp_f32 v102, v102\n"
"v_mfma_f32_16x16x32_bf16 v[140:143], a[56:59], v[32:35], v[140:143]\n"
"v_exp_f32 v103, v103\n"
"v_mfma_f32_16x16x32_bf16 v[140:143], a[60:63], v[36:39], v[140:143]\n"
"v_exp_f32 v72, v72\n"
"v_mfma_f32_16x16x32_bf16 v[144:147], a[64:67], v[32:35], v[144:147]\n"
"v_exp_f32 v73, v73\n"
"buffer_store_dwordx4 a[168:171], v12, s[52:55], s58 offen offset:2048 sc1\n"
"v_mfma_f32_16x16x32_bf16 v[144:147], a[68:71], v[36:39], v[144:147]\n"
"v_exp_f32 v74, v74\n"
"v_mfma_f32_16x16x32_bf16 v[148:151], a[72:75], v[32:35], v[148:151]\n"
"v_exp_f32 v75, v75\n"
"v_mfma_f32_16x16x32_bf16 v[148:151], a[76:79], v[36:39], v[148:151]\n"
"v_exp_f32 v88, v88\n"
"v_mfma_f32_16x16x32_bf16 v[152:155], a[80:83], v[32:35], v[152:155]\n"
"v_exp_f32 v89, v89\n"
"s_add_u32 m0, s75, 16384\n"
"s_add_u32 s76, s76, 3072\n"
"buffer_load_dwordx4 v0, s[40:43], s76 offen offset:2048 lds\n"
"v_mfma_f32_16x16x32_bf16 v[152:155], a[84:87], v[36:39], v[152:155]\n"
"v_exp_f32 v90, v90\n"
"buffer_store_dwordx4 a[184:187], v12, s[52:55], s58 offen offset:3072 sc1\n"
"v_mfma_f32_16x16x32_bf16 v[156:159], a[88:91], v[32:35], v[156:159]\n"
"v_cvt_pk_bf16_f32 v32, v64, v65\n"
"v_cvt_pk_bf16_f32 v33, v66, v67\n"
"v_mfma_f32_16x16x32_bf16 v[156:159], a[92:95], v[36:39], v[156:159]\n"
"v_cvt_pk_bf16_f32 v34, v68, v69\n"
"v_cvt_pk_bf16_f32 v35, v70, v71\n"
"v_mfma_f32_16x16x32_bf16 a[100:103], a[112:115], v[40:43], a[100:103]\n"
"v_cvt_pk_bf16_f32 v36, v72, v73\n"
"v_cvt_pk_bf16_f32 v37, v74, v75\n"
"v_mfma_f32_16x16x32_bf16 a[100:103], a[112:115], v[44:47], a[100:103]\n"
"v_exp_f32 v91, v91\n"
"v_mfma_f32_16x16x32_bf16 v[160:163], a[32:35], v[40:43], v[160:163]\n"
"v_exp_f32 v104, v104\n"
"v_mfma_f32_16x16x32_bf16 v[160:163], a[36:39], v[44:47], v[160:163]\n"
"v_exp_f32 v105, v105\n"
"v_mfma_f32_16x16x32_bf16 v[164:167], a[40:43], v[40:43], v[164:167]\n"
"v_exp_f32 v106, v106\n"
"s_add_u32 m0, s75, 16384\n"
"s_add_u32 s76, s76, 3072\n"
"buffer_load_dwordx4 v1, s[40:43], s76 offen offset:3072 lds\n"
"s_xor_b32 s75, s75, 0x8000\n"
"v_mfma_f32_16x16x32_bf16 v[164:167], a[44:47], v[44:47], v[164:167]\n"
"v_exp_f32 v107, v107\n"
"v_mfma_f32_16x16x32_bf16 v[168:171], a[48:51], v[40:43], v[168:171]\n"
"v_exp_f32 v76, v76\n"
"v_mfma_f32_16x16x32_bf16 v[168:171], a[52:55], v[44:47], v[168:171]\n"
"v_exp_f32 v77, v77\n"
"v_mfma_f32_16x16x32_bf16 v[172:175], a[56:59], v[40:43], v[172:175]\n"
"v_exp_f32 v78, v78\n"
"v_mfma_f32_16x16x32_bf16 v[172:175], a[60:63], v[44:47], v[172:175]\n"
"v_exp_f32 v79, v79\n"
"v_mfma_f32_16x16x32_bf16 v[176:179], a[64:67], v[40:43], v[176:179]\n"
"v_cvt_pk_bf16_f32 v38, v76, v77\n"
"v_cvt_pk_bf16_f32 v39, v78, v79\n"
"v_mfma_f32_16x16x32_bf16 v[176:179], a[68:71], v[44:47], v[176:179]\n"
"v_exp_f32 v92, v92\n"
"v_mfma_f32_16x16x32_bf16 v[180:183], a[72:75], v[40:43], v[180:183]\n"
"v_exp_f32 v93, v93\n"
"v_mfma_f32_16x16x32_bf16 v[180:183], a[76:79], v[44:47], v[180:183]\n"
"v_exp_f32 v94, v94\n"
"v_mfma_f32_16x16x32_bf16 v[184:187], a[80:83], v[40:43], v[184:187]\n"
"v_exp_f32 v95, v95\n"
"v_mfma_f32_16x16x32_bf16 v[184:187], a[84:87], v[44:47], v[184:187]\n"
"v_exp_f32 v108, v108\n"
"v_mfma_f32_16x16x32_bf16 v[188:191], a[88:91], v[40:43], v[188:191]\n"
"s_waitcnt vmcnt(30)\n"
"s_barrier\n"
"v_mfma_f32_16x16x32_bf16 v[188:191], a[92:95], v[44:47], v[188:191]\n"
"ds_read_b128 a[0:3], v6 offset:0\n"
"ds_read_b128 a[4:7], v7 offset:0\n"
"v_mfma_f32_16x16x32_bf16 a[104:107], a[112:115], v[48:51], a[104:107]\n"
"ds_read_b128 a[8:11], v8 offset:0\n"
"ds_read_b128 a[12:15], v9 offset:0\n"
"v_mfma_f32_16x16x32_bf16 a[104:107], a[112:115], v[52:55], a[104:107]\n"
"v_cvt_pk_bf16_f32 v40, v80, v81\n"
"v_mfma_f32_16x16x32_bf16 v[192:195], a[32:35], v[48:51], v[192:195]\n"
"s_add_u32 s75, s77, 65536\n"
"s_add_u32 m0, s75, 0\n"
"s_bfe_u32 s76, s68, 0x40000\n"
"s_lshl_b32 s76, s76, 17\n"
"buffer_load_dwordx4 v0, s[40:43], s76 offen offset:0 lds\n"
"v_mfma_f32_16x16x32_bf16 v[192:195], a[36:39], v[52:55], v[192:195]\n"
"v_cvt_pk_bf16_f32 v41, v82, v83\n"
"ds_read_b128 a[16:19], v6 offset:4096\n"
"v_mfma_f32_16x16x32_bf16 v[196:199], a[40:43], v[48:51], v[196:199]\n"
"ds_read_b128 a[20:23], v7 offset:4096\n"
"ds_read_b128 a[24:27], v8 offset:4096\n"
"v_mfma_f32_16x16x32_bf16 v[196:199], a[44:47], v[52:55], v[196:199]\n"
"ds_read_b128 a[28:31], v9 offset:4096\n"
"v_cvt_pk_bf16_f32 v42, v84, v85\n"
"v_mfma_f32_16x16x32_bf16 v[200:203], a[48:51], v[48:51], v[200:203]\n"
"v_cvt_pk_bf16_f32 v43, v86, v87\n"
"v_cvt_pk_bf16_f32 v44, v88, v89\n"
"v_mfma_f32_16x16x32_bf16 v[200:203], a[52:55], v[52:55], v[200:203]\n"
"v_cvt_pk_bf16_f32 v45, v90, v91\n"
"v_cvt_pk_bf16_f32 v46, v92, v93\n"
"v_mfma_f32_16x16x32_bf16 v[204:207], a[56:59], v[48:51], v[204:207]\n"
"v_cvt_pk_bf16_f32 v47, v94, v95\n"
"ds_read_b64_tr_b16 a[32:33], v10 offset:0\n"
"v_mfma_f32_16x16x32_bf16 v[204:207], a[60:63], v[52:55], v[204:207]\n"
"ds_read_b64_tr_b16 a[34:35], v10 offset:4096\n"
"ds_read_b64_tr_b16 a[36:37], v10 offset:8192\n"
"v_mfma_f32_16x16x32_bf16 v[208:211], a[64:67], v[48:51], v[208:211]\n"
"v_exp_f32 v109, v109\n"
"v_mfma_f32_16x16x32_bf16 v[208:211], a[68:71], v[52:55], v[208:211]\n"
"s_add_u32 m0, s75, 0\n"
"s_add_u32 s76, s76, 3072\n"
"buffer_load_dwordx4 v1, s[40:43], s76 offen offset:1024 lds\n"
"v_mfma_f32_16x16x32_bf16 v[212:215], a[72:75], v[48:51], v[212:215]\n"
"v_exp_f32 v110, v110\n"
"v_mfma_f32_16x16x32_bf16 v[212:215], a[76:79], v[52:55], v[212:215]\n"
"v_exp_f32 v111, v111\n"
"v_mfma_f32_16x16x32_bf16 v[216:219], a[80:83], v[48:51], v[216:219]\n"
"ds_read_b64_tr_b16 a[38:39], v10 offset:12288\n"
"ds_read_b64_tr_b16 a[40:41], v11 offset:0\n"
"v_mfma_f32_16x16x32_bf16 v[216:219], a[84:87], v[52:55], v[216:219]\n"
"ds_read_b64_tr_b16 a[42:43], v11 offset:4096\n"
"ds_read_b64_tr_b16 a[44:45], v11 offset:8192\n"
"v_mfma_f32_16x16x32_bf16 v[220:223], a[88:91], v[48:51], v[220:223]\n"
"v_cvt_pk_bf16_f32 v48, v96, v97\n"
"v_cvt_pk_bf16_f32 v49, v98, v99\n"
"v_mfma_f32_16x16x32_bf16 v[220:223], a[92:95], v[52:55], v[220:223]\n"
"s_waitcnt lgkmcnt(14)\n"
"v_mfma_f32_16x16x32_bf16 v[64:67], a[0:3], a[192:195], v[16:19]\n"
"v_cvt_pk_bf16_f32 v50, v100, v101\n"
"v_cvt_pk_bf16_f32 v51, v102, v103\n"
"s_waitcnt lgkmcnt(13)\n"
"v_mfma_f32_16x16x32_bf16 v[64:67], a[4:7], a[196:199], v[64:67]\n"
"v_cvt_pk_bf16_f32 v52, v104, v105\n"
"v_cvt_pk_bf16_f32 v53, v106, v107\n"
"s_waitcnt lgkmcnt(12)\n"
"v_mfma_f32_16x16x32_bf16 v[64:67], a[8:11], a[200:203], v[64:67]\n"
"s_add_u32 m0, s75, 0\n"
"s_add_u32 s76, s76, 3072\n"
"buffer_load_dwordx4 v0, s[40:43], s76 offen offset:2048 lds\n"
"s_waitcnt lgkmcnt(11)\n"
"v_mfma_f32_16x16x32_bf16 v[64:67], a[12:15], a[204:207], v[64:67]\n"
"v_cvt_pk_bf16_f32 v54, v108, v109\n"
"v_cvt_pk_bf16_f32 v55, v110, v111\n"
"v_mfma_f32_16x16x32_bf16 v[80:83], a[0:3], a[208:211], v[20:23]\n"
"ds_read_b64_tr_b16 a[46:47], v11 offset:12288\n"
"ds_read_b64_tr_b16 a[48:49], v10 offset:256\n"
"v_mfma_f32_16x16x32_bf16 v[80:83], a[4:7], a[212:215], v[80:83]\n"
"ds_read_b64_tr_b16 a[50:51], v10 offset:4352\n"
"ds_read_b64_tr_b16 a[52:53], v10 offset:8448\n"
"v_mfma_f32_16x16x32_bf16 v[80:83], a[8:11], a[216:219], v[80:83]\n"
"ds_read_b64_tr_b16 a[54:55], v10 offset:12544\n"
"ds_read_b64_tr_b16 a[56:57], v11 offset:256\n"
"v_mfma_f32_16x16x32_bf16 v[80:83], a[12:15], a[220:223], v[80:83]\n"
"ds_read_b64_tr_b16 a[58:59], v11 offset:4352\n"
"ds_read_b64_tr_b16 a[60:61], v11 offset:8448\n"
"v_mfma_f32_16x16x32_bf16 v[96:99], a[0:3], a[224:227], v[24:27]\n"
"ds_read_b128 a[0:3], v6 offset:8192\n"
"ds_read_b64_tr_b16 a[62:63], v11 offset:12544\n"
"v_mfma_f32_16x16x32_bf16 v[96:99], a[4:7], a[228:231], v[96:99]\n"
"ds_read_b128 a[4:7], v7 offset:8192\n"
"ds_read_b64_tr_b16 a[64:65], v10 offset:512\n"
"v_mfma_f32_16x16x32_bf16 v[96:99], a[8:11], a[232:235], v[96:99]\n"
"ds_read_b128 a[8:11], v8 offset:8192\n"
"ds_read_b64_tr_b16 a[66:67], v10 offset:4608\n"
"v_mfma_f32_16x16x32_bf16 v[96:99], a[12:15], a[236:239], v[96:99]\n"
"s_add_u32 m0, s75, 0\n"
"s_add_u32 s76, s76, 3072\n"
"buffer_load_dwordx4 v1, s[40:43], s76 offen offset:3072 lds\n"
"s_waitcnt lgkmcnt(15)\n"
"v_mfma_f32_16x16x32_bf16 v[68:71], a[16:19], a[192:195], v[16:19]\n"
"ds_read_b128 a[12:15], v9 offset:8192\n"
"ds_read_b64_tr_b16 a[68:69], v10 offset:8704\n"
"v_mfma_f32_16x16x32_bf16 v[68:71], a[20:23], a[196:199], v[68:71]\n"
"ds_read_b64_tr_b16 a[70:71], v10 offset:12800\n"
"ds_read_b64_tr_b16 a[72:73], v11 offset:512\n"
"v_mfma_f32_16x16x32_bf16 v[68:71], a[24:27], a[200:203], v[68:71]\n"
"ds_read_b64_tr_b16 a[74:75], v11 offset:4608\n"
"ds_read_b64_tr_b16 a[76:77], v11 offset:8704\n"
"v_mfma_f32_16x16x32_bf16 v[68:71], a[28:31], a[204:207], v[68:71]\n"
"ds_read_b64_tr_b16 a[78:79], v11 offset:12800\n"
"ds_read_b64_tr_b16 a[80:81], v10 offset:768\n"
"v_mfma_f32_16x16x32_bf16 v[84:87], a[16:19], a[208:211], v[20:23]\n"
"ds_read_b64_tr_b16 a[82:83], v10 offset:4864\n"
"ds_read_b64_tr_b16 a[84:85], v10 offset:8960\n"
"v_mfma_f32_16x16x32_bf16 v[84:87], a[20:23], a[212:215], v[84:87]\n"
"ds_read_b64_tr_b16 a[86:87], v10 offset:13056\n"
"ds_read_b64_tr_b16 a[88:89], v11 offset:768\n"
"v_mfma_f32_16x16x32_bf16 v[84:87], a[24:27], a[216:219], v[84:87]\n"
"ds_read_b64_tr_b16 a[90:91], v11 offset:4864\n"
"ds_read_b64_tr_b16 a[92:93], v11 offset:8960\n"
"v_mfma_f32_16x16x32_bf16 v[84:87], a[28:31], a[220:223], v[84:87]\n"
"ds_read_b64_tr_b16 a[94:95], v11 offset:13056\n"
"v_mfma_f32_16x16x32_bf16 v[100:103], a[16:19], a[224:227], v[24:27]\n"
"s_add_u32 m0, s75, 16384\n"
"s_add_u32 s76, s76, 56320\n"
"buffer_load_dwordx4 v0, s[40:43], s76 offen offset:0 lds\n"
"v_mfma_f32_16x16x32_bf16 v[100:103], a[20:23], a[228:231], v[100:103]\n"
"ds_read_b128 a[16:19], v6 offset:12288\n"
"ds_read_b128 a[20:23], v7 offset:12288\n"
"v_mfma_f32_16x16x32_bf16 v[100:103], a[24:27], a[232:235], v[100:103]\n"
"ds_read_b128 a[24:27], v8 offset:12288\n"
"v_mfma_f32_16x16x32_bf16 v[100:103], a[28:31], a[236:239], v[100:103]\n"
"ds_read_b128 a[28:31], v9 offset:12288\n"
"s_waitcnt lgkmcnt(15)\n"
"v_mfma_f32_16x16x32_bf16 v[72:75], a[0:3], a[192:195], v[16:19]\n"
"v_exp_f32 v64, v64\n"
"v_mfma_f32_16x16x32_bf16 v[72:75], a[4:7], a[196:199], v[72:75]\n"
"v_exp_f32 v65, v65\n"
"v_mfma_f32_16x16x32_bf16 v[72:75], a[8:11], a[200:203], v[72:75]\n"
"v_exp_f32 v66, v66\n"
"v_mfma_f32_16x16x32_bf16 v[72:75], a[12:15], a[204:207], v[72:75]\n"
"v_exp_f32 v67, v67\n"
"v_mfma_f32_16x16x32_bf16 v[88:91], a[0:3], a[208:211], v[20:23]\n"
"v_exp_f32 v80, v80\n"
"v_mfma_f32_16x16x32_bf16 v[88:91], a[4:7], a[212:215], v[88:91]\n"
"s_add_u32 m0, s75, 16384\n"
"s_add_u32 s76, s76, 3072\n"
"buffer_load_dwordx4 v1, s[40:43], s76 offen offset:1024 lds\n"
"v_mfma_f32_16x16x32_bf16 v[88:91], a[8:11], a[216:219], v[88:91]\n"
"v_exp_f32 v81, v81\n"
"v_mfma_f32_16x16x32_bf16 v[88:91], a[12:15], a[220:223], v[88:91]\n"
"v_exp_f32 v82, v82\n"
"v_mfma_f32_16x16x32_bf16 v[104:107], a[0:3], a[224:227], v[24:27]\n"
"v_exp_f32 v83, v83\n"
"s_mov_b32 s54, 1843200\n"
"buffer_load_dword a119, v13, s[36:39], %[ma] offen offset:128 sc1\n"
"s_waitcnt vmcnt(8)\n"
"s_cmp_lg_u64 s[70:71], 0\n"
"s_cbranch_scc1 .Lsgb_%=\n"
"s_mov_b64 exec, 15\n"
"buffer_store_dwordx2 a[116:117], v14, s[36:39], %[mw] offen sc1\n"
"s_mov_b64 exec, -1\n"
".Lsgb_%=:\n"
"v_mfma_f32_16x16x32_bf16 v[104:107], a[4:7], a[228:231], v[104:107]\n"
"v_exp_f32 v96, v96\n"
"v_mfma_f32_16x16x32_bf16 v[104:107], a[8:11], a[232:235], v[104:107]\n"
"v_exp_f32 v97, v97\n"
"v_mfma_f32_16x16x32_bf16 v[104:107], a[12:15], a[236:239], v[104:107]\n"
"v_exp_f32 v98, v98\n"
"s_waitcnt lgkmcnt(3)\n"
"v_mfma_f32_16x16x32_bf16 v[76:79], a[16:19], a[192:195], v[16:19]\n"
"v_exp_f32 v99, v99\n"
"s_waitcnt lgkmcnt(2)\n"
"v_mfma_f32_16x16x32_bf16 v[76:79], a[20:23], a[196:199], v[76:79]\n"
"v_exp_f32 v68, v68\n"
"s_waitcnt lgkmcnt(1)\n"
"v_mfma_f32_16x16x32_bf16 v[76:79], a[24:27], a[200:203], v[76:79]\n"
"s_add_u32 m0, s75, 16384\n"
"s_add_u32 s76, s76, 3072\n"
"buffer_load_dwordx4 v0, s[40:43], s76 offen offset:2048 lds\n"
"s_waitcnt lgkmcnt(0)\n"
"v_mfma_f32_16x16x32_bf16 v[76:79], a[28:31], a[204:207], v[76:79]\n"
"v_exp_f32 v69, v69\n"
"v_mfma_f32_16x16x32_bf16 v[92:95], a[16:19], a[208:211], v[20:23]\n"
"v_exp_f32 v70, v70\n"
"v_mfma_f32_16x16x32_bf16 v[92:95], a[20:23], a[212:215], v[92:95]\n"
"v_exp_f32 v71, v71\n"
"v_mfma_f32_16x16x32_bf16 v[92:95], a[24:27], a[216:219], v[92:95]\n"
"v_exp_f32 v84, v84\n"
"v_mfma_f32_16x16x32_bf16 v[92:95], a[28:31], a[220:223], v[92:95]\n"
"v_exp_f32 v85, v85\n"
"v_mfma_f32_16x16x32_bf16 v[108:111], a[16:19], a[224:227], v[24:27]\n"
"v_exp_f32 v86, v86\n"
"v_mfma_f32_16x16x32_bf16 v[108:111], a[20:23], a[228:231], v[108:111]\n"
"v_exp_f32 v87, v87\n"
"v_mfma_f32_16x16x32_bf16 v[108:111], a[24:27], a[232:235], v[108:111]\n"
"v_exp_f32 v100, v100\n"
"v_mfma_f32_16x16x32_bf16 v[108:111], a[28:31], a[236:239], v[108:111]\n"
"s_add_u32 m0, s75, 16384\n"
"s_add_u32 s76, s76, 3072\n"
"buffer_load_dwordx4 v1, s[40:43], s76 offen offset:3072 lds\n"
"s_xor_b32 s75, s75, 0x10000\n"
"v_mfma_f32_16x16x32_bf16 a[96:99], a[112:115], v[32:35], a[96:99]\n"
"v_xor_b32 v6, s64, v6\n"
"v_xor_b32 v7, s64, v7\n"
"v_mfma_f32_16x16x32_bf16 a[96:99], a[112:115], v[36:39], a[96:99]\n"
"v_xor_b32 v8, s64, v8\n"
"v_xor_b32 v9, s64, v9\n"
"v_mfma_f32_16x16x32_bf16 v[128:131], a[32:35], v[32:35], v[128:131]\n"
"v_xor_b32 v10, s65, v10\n"
"v_xor_b32 v11, s65, v11\n"
"v_mfma_f32_16x16x32_bf16 v[128:131], a[36:39], v[36:39], v[128:131]\n"
"s_xor_b32 s62, s62, s65\n"
"s_xor_b32 s63, s63, s64\n"
"v_mfma_f32_16x16x32_bf16 v[132:135], a[40:43], v[32:35], v[132:135]\n"
"s_add_u32 s60, s60, 0x4000\n"
"s_add_u32 s61, s61, 0x4000\n"
"v_mfma_f32_16x16x32_bf16 v[132:135], a[44:47], v[36:39], v[132:135]\n"
"s_xor_b32 s64, s64, 0xc000\n"
"s_xor_b32 s65, s65, 0xc000\n"
"v_mfma_f32_16x16x32_bf16 v[136:139], a[48:51], v[32:35], v[136:139]\n"
"v_exp_f32 v101, v101\n"
"v_mfma_f32_16x16x32_bf16 v[136:139], a[52:55], v[36:39], v[136:139]\n"
"v_exp_f32 v102, v102\n"
"v_mfma_f32_16x16x32_bf16 v[140:143], a[56:59], v[32:35], v[140:143]\n"
"v_exp_f32 v103, v103\n"
"v_mfma_f32_16x16x32_bf16 v[140:143], a[60:63], v[36:39], v[140:143]\n"
"v_exp_f32 v72, v72\n"
"v_mfma_f32_16x16x32_bf16 v[144:147], a[64:67], v[32:35], v[144:147]\n"
"v_exp_f32 v73, v73\n"
"v_mfma_f32_16x16x32_bf16 v[144:147], a[68:71], v[36:39], v[144:147]\n"
"v_exp_f32 v74, v74\n"
"v_mfma_f32_16x16x32_bf16 v[148:151], a[72:75], v[32:35], v[148:151]\n"
"v_exp_f32 v75, v75\n"
"v_mfma_f32_16x16x32_bf16 v[148:151], a[76:79], v[36:39], v[148:151]\n"
"v_exp_f32 v88, v88\n"
"v_mfma_f32_16x16x32_bf16 v[152:155], a[80:83], v[32:35], v[152:155]\n"
"v_exp_f32 v89, v89\n"
"v_mfma_f32_16x16x32_bf16 v[152:155], a[84:87], v[36:39], v[152:155]\n"
"v_exp_f32 v90, v90\n"
"v_mfma_f32_16x16x32_bf16 v[156:159], a[88:91], v[32:35], v[156:159]\n"
"v_cvt_pk_bf16_f32 v32, v64, v65\n"
"v_cvt_pk_bf16_f32 v33, v66, v67\n"
"v_mfma_f32_16x16x32_bf16 v[156:159], a[92:95], v[36:39], v[156:159]\n"
"v_cvt_pk_bf16_f32 v34, v68, v69\n"
"v_cvt_pk_bf16_f32 v35, v70, v71\n"
"v_mfma_f32_16x16x32_bf16 a[100:103], a[112:115], v[40:43], a[100:103]\n"
"v_cvt_pk_bf16_f32 v36, v72, v73\n"
"v_cvt_pk_bf16_f32 v37, v74, v75\n"
"v_mfma_f32_16x16x32_bf16 a[100:103], a[112:115], v[44:47], a[100:103]\n"
"v_exp_f32 v91, v91\n"
"v_mfma_f32_16x16x32_bf16 v[160:163], a[32:35], v[40:43], v[160:163]\n"
"v_exp_f32 v104, v104\n"
"v_mfma_f32_16x16x32_bf16 v[160:163], a[36:39], v[44:47], v[160:163]\n"
"v_exp_f32 v105, v105\n"
"v_mfma_f32_16x16x32_bf16 v[164:167], a[40:43], v[40:43], v[164:167]\n"
"v_exp_f32 v106, v106\n"
"v_mfma_f32_16x16x32_bf16 v[164:167], a[44:47], v[44:47], v[164:167]\n"
"v_exp_f32 v107, v107\n"
"v_mfma_f32_16x16x32_bf16 v[168:171], a[48:51], v[40:43], v[168:171]\n"
"v_exp_f32 v76, v76\n"
"v_mfma_f32_16x16x32_bf16 v[168:171], a[52:55], v[44:47], v[168:171]\n"
"v_exp_f32 v77, v77\n"
"v_mfma_f32_16x16x32_bf16 v[172:175], a[56:59], v[40:43], v[172:175]\n"
"v_exp_f32 v78, v78\n"
"v_mfma_f32_16x16x32_bf16 v[172:175], a[60:63], v[44:47], v[172:175]\n"
"v_exp_f32 v79, v79\n"
"v_mfma_f32_16x16x32_bf16 v[176:179], a[64:67], v[40:43], v[176:179]\n"
"v_cvt_pk_bf16_f32 v38, v76, v77\n"
"v_cvt_pk_bf16_f32 v39, v78, v79\n"
"v_mfma_f32_16x16x32_bf16 v[176:179], a[68:71], v[44:47], v[176:179]\n"
"v_exp_f32 v92, v92\n"
"v_mfma_f32_16x16x32_bf16 v[180:183], a[72:75], v[40:43], v[180:183]\n"
"v_exp_f32 v93, v93\n"
"v_mfma_f32_16x16x32_bf16 v[180:183], a[76:79], v[44:47], v[180:183]\n"
"v_exp_f32 v94, v94\n"
"v_mfma_f32_16x16x32_bf16 v[184:187], a[80:83], v[40:43], v[184:187]\n"
"v_exp_f32 v95, v95\n"
"v_mfma_f32_16x16x32_bf16 v[184:187], a[84:87], v[44:47], v[184:187]\n"
"v_exp_f32 v108, v108\n"
"v_mfma_f32_16x16x32_bf16 v[188:191], a[88:91], v[40:43], v[188:191]\n"
"s_waitcnt vmcnt(63)\n"
"s_barrier\n"
"v_mfma_f32_16x16x32_bf16 v[188:191], a[92:95], v[44:47], v[188:191]\n"
"ds_read_b128 a[0:3], v6 offset:0\n"
"ds_read_b128 a[4:7], v7 offset:0\n"
"v_mfma_f32_16x16x32_bf16 a[104:107], a[112:115], v[48:51], a[104:107]\n"
"ds_read_b128 a[8:11], v8 offset:0\n"
"ds_read_b128 a[12:15], v9 offset:0\n"
"v_mfma_f32_16x16x32_bf16 a[104:107], a[112:115], v[52:55], a[104:107]\n"
"v_cvt_pk_bf16_f32 v40, v80, v81\n"
"v_mfma_f32_16x16x32_bf16 v[192:195], a[32:35], v[48:51], v[192:195]\n"
"s_add_u32 s75, s77, 32768\n"
"s_add_u32 m0, s75, 0\n"
"s_bfe_u32 s76, s68, 0x40008\n"
"s_lshl_b32 s76, s76, 17\n"
"buffer_load_dwordx4 v0, s[40:43], s76 offen offset:0 lds\n"
"v_mfma_f32_16x16x32_bf16 v[192:195], a[36:39], v[52:55], v[192:195]\n"
"v_cvt_pk_bf16_f32 v41, v82, v83\n"
"ds_read_b128 a[16:19], v6 offset:4096\n"
"v_mfma_f32_16x16x32_bf16 v[196:199], a[40:43], v[48:51], v[196:199]\n"
"ds_read_b128 a[20:23], v7 offset:4096\n"
"ds_read_b128 a[24:27], v8 offset:4096\n"
"v_mfma_f32_16x16x32_bf16 v[196:199], a[44:47], v[52:55], v[196:199]\n"
"ds_read_b128 a[28:31], v9 offset:4096\n"
"v_cvt_pk_bf16_f32 v42, v84, v85\n"
"v_mfma_f32_16x16x32_bf16 v[200:203], a[48:51], v[48:51], v[200:203]\n"
"v_cvt_pk_bf16_f32 v43, v86, v87\n"
"v_cvt_pk_bf16_f32 v44, v88, v89\n"
"v_mfma_f32_16x16x32_bf16 v[200:203], a[52:55], v[52:55], v[200:203]\n"
"v_cvt_pk_bf16_f32 v45, v90, v91\n"
"v_cvt_pk_bf16_f32 v46, v92, v93\n"
"v_mfma_f32_16x16x32_bf16 v[204:207], a[56:59], v[48:51], v[204:207]\n"
"v_cvt_pk_bf16_f32 v47, v94, v95\n"
"ds_read_b64_tr_b16 a[32:33], v10 offset:0\n"
"v_mfma_f32_16x16x32_bf16 v[204:207], a[60:63], v[52:55], v[204:207]\n"
"ds_read_b64_tr_b16 a[34:35], v10 offset:4096\n"
"ds_read_b64_tr_b16 a[36:37], v10 offset:8192\n"
"v_mfma_f32_16x16x32_bf16 v[208:211], a[64:67], v[48:51], v[208:211]\n"
"v_exp_f32 v109, v109\n"
"v_mfma_f32_16x16x32_bf16 v[208:211], a[68:71], v[52:55], v[208:211]\n"
"s_add_u32 m0, s75, 0\n"
"s_add_u32 s76, s76, 3072\n"
"buffer_load_dwordx4 v1, s[40:43], s76 offen offset:1024 lds\n"
"v_mfma_f32_16x16x32_bf16 v[212:215], a[72:75], v[48:51], v[212:215]\n"
"v_exp_f32 v110, v110\n"
"v_mfma_f32_16x16x32_bf16 v[212:215], a[76:79], v[52:55], v[212:215]\n"
"v_exp_f32 v111, v111\n"
"v_mfma_f32_16x16x32_bf16 v[216:219], a[80:83], v[48:51], v[216:219]\n"
"ds_read_b64_tr_b16 a[38:39], v10 offset:12288\n"
"ds_read_b64_tr_b16 a[40:41], v11 offset:0\n"
"v_mfma_f32_16x16x32_bf16 v[216:219], a[84:87], v[52:55], v[216:219]\n"
"ds_read_b64_tr_b16 a[42:43], v11 offset:4096\n"
"ds_read_b64_tr_b16 a[44:45], v11 offset:8192\n"
"v_mfma_f32_16x16x32_bf16 v[220:223], a[88:91], v[48:51], v[220:223]\n"
"v_cvt_pk_bf16_f32 v48, v96, v97\n"
"v_cvt_pk_bf16_f32 v49, v98, v99\n"
"v_mfma_f32_16x16x32_bf16 v[220:223], a[92:95], v[52:55], v[220:223]\n"
"s_add_u32 m0, s75, 0\n"
"s_add_u32 s76, s76, 3072\n"
"buffer_load_dwordx4 v0, s[40:43], s76 offen offset:2048 lds\n"
"v_cvt_pk_bf16_f32 v50, v100, v101\n"
"v_cvt_pk_bf16_f32 v51, v102, v103\n"
"buffer_load_dword v20, v13, s[36:39], %[xa] offen offset:4 sc1\n"
"v_cvt_pk_bf16_f32 v52, v104, v105\n"
"v_cvt_pk_bf16_f32 v53, v106, v107\n"
"s_add_u32 m0, s75, 0\n"
"s_add_u32 s76, s76, 3072\n"
"buffer_load_dwordx4 v1, s[40:43], s76 offen offset:3072 lds\n"
"v_cvt_pk_bf16_f32 v54, v108, v109\n"
"v_cvt_pk_bf16_f32 v55, v110, v111\n"
"ds_read_b64_tr_b16 a[46:47], v11 offset:12288\n"
"ds_read_b64_tr_b16 a[48:49], v10 offset:256\n"
"ds_read_b64_tr_b16 a[50:51], v10 offset:4352\n"
"ds_read_b64_tr_b16 a[52:53], v10 offset:8448\n"
"ds_read_b64_tr_b16 a[54:55], v10 offset:12544\n"
"ds_read_b64_tr_b16 a[56:57], v11 offset:256\n"
"ds_read_b64_tr_b16 a[58:59], v11 offset:4352\n"
"ds_read_b64_tr_b16 a[60:61], v11 offset:8448\n"
"ds_read_b64_tr_b16 a[62:63], v11 offset:12544\n"
"ds_read_b64_tr_b16 a[64:65], v10 offset:512\n"
"ds_read_b64_tr_b16 a[66:67], v10 offset:4608\n"
"s_add_u32 m0, s75, 16384\n"
"s_add_u32 s76, s76, 56320\n"
"buffer_load_dwordx4 v0, s[40:43], s76 offen offset:0 lds\n"
"ds_read_b64_tr_b16 a[68:69], v10 offset:8704\n"
"ds_read_b64_tr_b16 a[70:71], v10 offset:12800\n"
"ds_read_b64_tr_b16 a[72:73], v11 offset:512\n"
"ds_read_b64_tr_b16 a[74:75], v11 offset:4608\n"
"ds_read_b64_tr_b16 a[76:77], v11 offset:8704\n"
"ds_read_b64_tr_b16 a[78:79], v11 offset:12800\n"
"ds_read_b64_tr_b16 a[80:81], v10 offset:768\n"
"ds_read_b64_tr_b16 a[82:83], v10 offset:4864\n"
"ds_read_b64_tr_b16 a[84:85], v10 offset:8960\n"
"ds_read_b64_tr_b16 a[86:87], v10 offset:13056\n"
"ds_read_b64_tr_b16 a[88:89], v11 offset:768\n"
"ds_read_b64_tr_b16 a[90:91], v11 offset:4864\n"
"ds_read_b64_tr_b16 a[92:93], v11 offset:8960\n"
"ds_read_b64_tr_b16 a[94:95], v11 offset:13056\n"
"s_add_u32 m0, s75, 16384\n"
"s_add_u32 s76, s76, 3072\n"
"buffer_load_dwordx4 v1, s[40:43], s76 offen offset:1024 lds\n"
"s_add_u32 m0, s75, 16384\n"
"s_add_u32 s76, s76, 3072\n"
"buffer_load_dwordx4 v0, s[40:43], s76 offen offset:2048 lds\n"
"v_mbcnt_lo_u32_b32 v26, -1, 0\n"
"v_mbcnt_hi_u32_b32 v26, -1, v26\n"
"v_and_b32 v27, 15, v26\n"
"v_lshrrev_b32 v28, 4, v26\n"
"s_add_u32 m0, s75, 16384\n"
"s_add_u32 s76, s76, 3072\n"
"buffer_load_dwordx4 v1, s[40:43], s76 offen offset:3072 lds\n"
"s_xor_b32 s75, s75, 0x10000\n"
"s_lshr_b32 s65, s68, 8\n"
"s_lshr_b64 s[68:69], s[68:69], 12\n"
"s_add_u32 s75, s77, 98304\n"
"v_lshrrev_b32 v29, 2, v27\n"
"v_and_b32 v30, 1, v28\n"
"v_lshl_add_u32 v29, v30, 2, v29\n"
"v_lshlrev_b32 v29, 5, v29\n"
"v_and_b32 v30, 3, v26\n"
"v_lshl_add_u32 v29, v30, 3, v29\n"
"v_lshl_add_u32 v2, v28, 10, v29\n"
"v_add_u32 v2, 0x10000, v2\n"
"v_xor_b32 v3, 0x80, v2\n"
"v_mov_b32 v4, v12\n"
"v_lshlrev_b32 v5, 11, v27\n"
"v_lshl_add_u32 v5, v28, 3, v5\n"
"s_lshl_b32 s63, %[wv], 17\n"
"v_add_u32 v5, s63, v5\n"
"v_lshlrev_b32 v6, 4, v26\n"
"v_add_u32 v6, s77, v6\n"
"v_lshlrev_b32 v8, 3, v30\n"
"s_lshl_b32 s63, %[ntp], 13\n"
"s_sub_u32 s44, s52, s63\n"
"s_subb_u32 s45, s53, 0\n"
"s_lshl_b32 s63, %[ksp], 15\n"
"s_sub_u32 s46, 1966080, s63\n"
"s_mov_b32 s48, %[yl]\n"
"s_and_b32 s49, %[yh], 0xffff\n"
"s_mov_b32 s50, 489536\n"
"s_mov_b32 s62, 3\n"
"v_mfma_f32_16x16x32_bf16 a[96:99], a[112:115], v[32:35], a[96:99]\n"
"v_mfma_f32_16x16x32_bf16 a[96:99], a[112:115], v[36:39], a[96:99]\n"
"s_waitcnt lgkmcnt(15)\n"
"v_mfma_f32_16x16x32_bf16 v[128:131], a[32:35], v[32:35], v[128:131]\n"
"v_mfma_f32_16x16x32_bf16 v[128:131], a[36:39], v[36:39], v[128:131]\n"
"v_mfma_f32_16x16x32_bf16 v[132:135], a[40:43], v[32:35], v[132:135]\n"
"s_nop 3\n"
"v_accvgpr_read_b32 v14, a96\n"
"v_mfma_f32_16x16x32_bf16 v[132:135], a[44:47], v[36:39], v[132:135]\n"
"v_cmp_ngt_f32 vcc, s67, v14\n"
"s_mov_b32 s72, 4096\n"
"s_or_b64 s[70:71], s[70:71], vcc\n"
"v_mfma_f32_16x16x32_bf16 v[136:139], a[48:51], v[32:35], v[136:139]\n"
"v_rcp_f32 v14, v14\n"
"v_mfma_f32_16x16x32_bf16 v[136:139], a[52:55], v[36:39], v[136:139]\n"
"v_mfma_f32_16x16x32_bf16 v[140:143], a[56:59], v[32:35], v[140:143]\n"
"v_mul_f32 v128, v128, v14\n"
"v_mul_f32 v129, v129, v14\n"
"v_mfma_f32_16x16x32_bf16 v[140:143], a[60:63], v[36:39], v[140:143]\n"
"v_mul_f32 v130, v130, v14\n"
"v_mul_f32 v131, v131, v14\n"
"s_waitcnt lgkmcnt(14)\n"
"v_mfma_f32_16x16x32_bf16 v[144:147], a[64:67], v[32:35], v[144:147]\n"
"v_mul_f32 v132, v132, v14\n"
"v_mul_f32 v133, v133, v14\n"
"s_waitcnt lgkmcnt(12)\n"
"v_mfma_f32_16x16x32_bf16 v[144:147], a[68:71], v[36:39], v[144:147]\n"
"v_mul_f32 v134, v134, v14\n"
"v_mul_f32 v135, v135, v14\n"
"s_waitcnt lgkmcnt(10)\n"
"v_mfma_f32_16x16x32_bf16 v[148:151], a[72:75], v[32:35], v[148:151]\n"
"v_cvt_pk_bf16_f32 v128, v128, v129\n"
"v_cvt_pk_bf16_f32 v129, v130, v131\n"
"s_waitcnt lgkmcnt(8)\n"
"v_mfma_f32_16x16x32_bf16 v[148:151], a[76:79], v[36:39], v[148:151]\n"
"v_cvt_pk_bf16_f32 v130, v132, v133\n"
"v_cvt_pk_bf16_f32 v131, v134, v135\n"
"s_waitcnt lgkmcnt(6)\n"
"v_mfma_f32_16x16x32_bf16 v[152:155], a[80:83], v[32:35], v[152:155]\n"
"buffer_store_dwordx4 v[128:131], v12, s[52:55], s72 offen offset:0 sc1\n"
"s_waitcnt lgkmcnt(4)\n"
"v_mfma_f32_16x16x32_bf16 v[152:155], a[84:87], v[36:39], v[152:155]\n"
"v_accvgpr_write_b32 a192, v128\n"
"v_accvgpr_write_b32 a193, v129\n"
"s_waitcnt lgkmcnt(2)\n"
"v_mfma_f32_16x16x32_bf16 v[156:159], a[88:91], v[32:35], v[156:159]\n"
"v_accvgpr_write_b32 a194, v130\n"
"v_accvgpr_write_b32 a195, v131\n"
"s_waitcnt lgkmcnt(0)\n"
"v_mfma_f32_16x16x32_bf16 v[156:159], a[92:95], v[36:39], v[156:159]\n"
"v_mul_f32 v136, v136, v14\n"
"v_mul_f32 v137, v137, v14\n"
"v_mfma_f32_16x16x32_bf16 a[100:103], a[112:115], v[40:43], a[100:103]\n"
"buffer_load_dwordx2 v[24:25], v13, s[36:39], %[ma] offen sc1\n"
"v_mfma_f32_16x16x32_bf16 a[100:103], a[112:115], v[44:47], a[100:103]\n"
"v_mul_f32 v138, v138, v14\n"
"v_mul_f32 v139, v139, v14\n"
"v_mfma_f32_16x16x32_bf16 v[160:163], a[32:35], v[40:43], v[160:163]\n"
"v_mul_f32 v140, v140, v14\n"
"v_mul_f32 v141, v141, v14\n"
"v_mfma_f32_16x16x32_bf16 v[160:163], a[36:39], v[44:47], v[160:163]\n"
"v_mul_f32 v142, v142, v14\n"
"v_mul_f32 v143, v143, v14\n"
"v_mfma_f32_16x16x32_bf16 v[164:167], a[40:43], v[40:43], v[164:167]\n"
"v_cvt_pk_bf16_f32 v136, v136, v137\n"
"v_cvt_pk_bf16_f32 v137, v138, v139\n"
"v_mfma_f32_16x16x32_bf16 v[164:167], a[44:47], v[44:47], v[164:167]\n"
"v_cvt_pk_bf16_f32 v138, v140, v141\n"
"v_cvt_pk_bf16_f32 v139, v142, v143\n"
"v_mfma_f32_16x16x32_bf16 v[168:171], a[48:51], v[40:43], v[168:171]\n"
"buffer_store_dwordx4 v[136:139], v12, s[52:55], s72 offen offset:1024 sc1\n"
"v_mfma_f32_16x16x32_bf16 v[168:171], a[52:55], v[44:47], v[168:171]\n"
"v_accvgpr_write_b32 a208, v136\n"
"v_accvgpr_write_b32 a209, v137\n"
"v_mfma_f32_16x16x32_bf16 v[172:175], a[56:59], v[40:43], v[172:175]\n"
"v_accvgpr_write_b32 a210, v138\n"
"v_accvgpr_write_b32 a211, v139\n"
"v_mfma_f32_16x16x32_bf16 v[172:175], a[60:63], v[44:47], v[172:175]\n"
"v_mul_f32 v144, v144, v14\n"
"v_mul_f32 v145, v145, v14\n"
"v_mfma_f32_16x16x32_bf16 v[176:179], a[64:67], v[40:43], v[176:179]\n"
"v_mul_f32 v146, v146, v14\n"
"v_mul_f32 v147, v147, v14\n"
"v_mfma_f32_16x16x32_bf16 v[176:179], a[68:71], v[44:47], v[176:179]\n"
"v_mul_f32 v148, v148, v14\n"
"v_mul_f32 v149, v149, v14\n"
"v_mfma_f32_16x16x32_bf16 v[180:183], a[72:75], v[40:43], v[180:183]\n"
"v_mul_f32 v150, v150, v14\n"
"v_mul_f32 v151, v151, v14\n"
"v_mfma_f32_16x16x32_bf16 v[180:183], a[76:79], v[44:47], v[180:183]\n"
"v_cvt_pk_bf16_f32 v144, v144, v145\n"
"v_cvt_pk_bf16_f32 v145, v146, v147\n"
"v_mfma_f32_16x16x32_bf16 v[184:187], a[80:83], v[40:43], v[184:187]\n"
"v_cvt_pk_bf16_f32 v146, v148, v149\n"
"v_cvt_pk_bf16_f32 v147, v150, v151\n"
"v_mfma_f32_16x16x32_bf16 v[184:187], a[84:87], v[44:47], v[184:187]\n"
"buffer_store_dwordx4 v[144:147], v12, s[52:55], s72 offen offset:2048 sc1\n"
"v_mfma_f32_16x16x32_bf16 v[188:191], a[88:91], v[40:43], v[188:191]\n"
"v_accvgpr_write_b32 a224, v144\n"
"v_accvgpr_write_b32 a225, v145\n"
"v_mfma_f32_16x16x32_bf16 v[188:191], a[92:95], v[44:47], v[188:191]\n"
"v_accvgpr_write_b32 a226, v146\n"
"v_accvgpr_write_b32 a227, v147\n"
"v_mfma_f32_16x16x32_bf16 a[104:107], a[112:115], v[48:51], a[104:107]\n"
"v_mul_f32 v152, v152, v14\n"
"v_mul_f32 v153, v153, v14\n"
"v_mfma_f32_16x16x32_bf16 a[104:107], a[112:115], v[52:55], a[104:107]\n"
"v_mul_f32 v154, v154, v14\n"
"v_mul_f32 v155, v155, v14\n"
"v_mfma_f32_16x16x32_bf16 v[192:195], a[32:35], v[48:51], v[192:195]\n"
"v_mul_f32 v156, v156, v14\n"
"v_mul_f32 v157, v157, v14\n"
"v_mfma_f32_16x16x32_bf16 v[192:195], a[36:39], v[52:55], v[192:195]\n"
"v_mul_f32 v158, v158, v14\n"
"v_mul_f32 v159, v159, v14\n"
"v_mfma_f32_16x16x32_bf16 v[196:199], a[40:43], v[48:51], v[196:199]\n"
"v_cvt_pk_bf16_f32 v152, v152, v153\n"
"v_cvt_pk_bf16_f32 v153, v154, v155\n"
"v_mfma_f32_16x16x32_bf16 v[196:199], a[44:47], v[52:55], v[196:199]\n"
"v_cvt_pk_bf16_f32 v154, v156, v157\n"
"v_cvt_pk_bf16_f32 v155, v158, v159\n"
"v_mfma_f32_16x16x32_bf16 v[200:203], a[48:51], v[48:51], v[200:203]\n"
"buffer_store_dwordx4 v[152:155], v12, s[52:55], s72 offen offset:3072 sc1\n"
"v_mfma_f32_16x16x32_bf16 v[200:203], a[52:55], v[52:55], v[200:203]\n"
"v_accvgpr_write_b32 a240, v152\n"
"v_accvgpr_write_b32 a241, v153\n"
"v_mfma_f32_16x16x32_bf16 v[204:207], a[56:59], v[48:51], v[204:207]\n"
"v_accvgpr_write_b32 a242, v154\n"
"v_accvgpr_write_b32 a243, v155\n"
"v_mfma_f32_16x16x32_bf16 v[204:207], a[60:63], v[52:55], v[204:207]\n"
"v_accvgpr_read_b32 v15, a100\n"
"v_accvgpr_read_b32 v14, a104\n"
"v_mfma_f32_16x16x32_bf16 v[208:211], a[64:67], v[48:51], v[208:211]\n"
"v_accvgpr_read_b32 v16, a116\n"
"v_accvgpr_read_b32 v17, a117\n"
"v_accvgpr_read_b32 v18, a118\n"
"s_waitcnt vmcnt(16)\n"
"v_accvgpr_read_b32 v19, a119\n"
"v_mfma_f32_16x16x32_bf16 v[208:211], a[68:71], v[52:55], v[208:211]\n"
"v_cmp_ngt_f32 vcc, s67, v15\n"
"s_mov_b32 s73, 135168\n"
"s_or_b64 s[70:71], s[70:71], vcc\n"
"v_mfma_f32_16x16x32_bf16 v[212:215], a[72:75], v[48:51], v[212:215]\n"
"v_rcp_f32 v15, v15\n"
"v_mfma_f32_16x16x32_bf16 v[212:215], a[76:79], v[52:55], v[212:215]\n"
"v_mul_f32 v160, v160, v15\n"
"v_mul_f32 v161, v161, v15\n"
"v_mfma_f32_16x16x32_bf16 v[216:219], a[80:83], v[48:51], v[216:219]\n"
"v_mul_f32 v162, v162, v15\n"
"v_mul_f32 v163, v163, v15\n"
"v_mfma_f32_16x16x32_bf16 v[216:219], a[84:87], v[52:55], v[216:219]\n"
"v_mul_f32 v164, v164, v15\n"
"v_mul_f32 v165, v165, v15\n"
"v_mfma_f32_16x16x32_bf16 v[220:223], a[88:91], v[48:51], v[220:223]\n"
"v_mul_f32 v166, v166, v15\n"
"v_mul_f32 v167, v167, v15\n"
"v_mfma_f32_16x16x32_bf16 v[220:223], a[92:95], v[52:55], v[220:223]\n"
"v_cvt_pk_bf16_f32 v160, v160, v161\n"
"v_cvt_pk_bf16_f32 v161, v162, v163\n"
"v_cvt_pk_bf16_f32 v162, v164, v165\n"
"v_cvt_pk_bf16_f32 v163, v166, v167\n"
"buffer_store_dwordx4 v[160:163], v12, s[52:55], s73 offen offset:0 sc1\n"
"v_accvgpr_write_b32 a196, v160\n"
"v_accvgpr_write_b32 a197, v161\n"
"v_accvgpr_write_b32 a198, v162\n"
"v_accvgpr_write_b32 a199, v163\n"
"v_mul_f32 v168, v168, v15\n"
"v_mul_f32 v169, v169, v15\n"
"v_mul_f32 v170, v170, v15\n"
"v_mul_f32 v171, v171, v15\n"
"v_mul_f32 v172, v172, v15\n"
"v_mul_f32 v173, v173, v15\n"
"v_mul_f32 v174, v174, v15\n"
"v_mul_f32 v175, v175, v15\n"
"v_cvt_pk_bf16_f32 v168, v168, v169\n"
"v_cvt_pk_bf16_f32 v169, v170, v171\n"
"v_cvt_pk_bf16_f32 v170, v172, v173\n"
"v_cvt_pk_bf16_f32 v171, v174, v175\n"
"buffer_store_dwordx4 v[168:171], v12, s[52:55], s73 offen offset:1024 sc1\n"
"v_accvgpr_write_b32 a212, v168\n"
"v_accvgpr_write_b32 a213, v169\n"
"v_accvgpr_write_b32 a214, v170\n"
"v_accvgpr_write_b32 a215, v171\n"
"v_mul_f32 v176, v176, v15\n"
"v_mul_f32 v177, v177, v15\n"
"v_mul_f32 v178, v178, v15\n"
"v_mul_f32 v179, v179, v15\n"
"v_mul_f32 v180, v180, v15\n"
"v_mul_f32 v181, v181, v15\n"
"v_mul_f32 v182, v182, v15\n"
"v_mul_f32 v183, v183, v15\n"
"v_cvt_pk_bf16_f32 v176, v176, v177\n"
"v_cvt_pk_bf16_f32 v177, v178, v179\n"
"v_cvt_pk_bf16_f32 v178, v180, v181\n"
"v_cvt_pk_bf16_f32 v179, v182, v183\n"
"buffer_store_dwordx4 v[176:179], v12, s[52:55], s73 offen offset:2048 sc1\n"
"v_accvgpr_write_b32 a228, v176\n"
"v_accvgpr_write_b32 a229, v177\n"
"v_accvgpr_write_b32 a230, v178\n"
"v_accvgpr_write_b32 a231, v179\n"
"v_mul_f32 v184, v184, v15\n"
"v_mul_f32 v185, v185, v15\n"
"v_mul_f32 v186, v186, v15\n"
"v_mul_f32 v187, v187, v15\n"
"v_mul_f32 v188, v188, v15\n"
"v_mul_f32 v189, v189, v15\n"
"v_mul_f32 v190, v190, v15\n"
"v_mul_f32 v191, v191, v15\n"
"v_cvt_pk_bf16_f32 v184, v184, v185\n"
"v_cvt_pk_bf16_f32 v185, v186, v187\n"
"v_cvt_pk_bf16_f32 v186, v188, v189\n"
"v_cvt_pk_bf16_f32 v187, v190, v191\n"
"buffer_store_dwordx4 v[184:187], v12, s[52:55], s73 offen offset:3072 sc1\n"
"v_accvgpr_write_b32 a244, v184\n"
"v_accvgpr_write_b32 a245, v185\n"
"v_accvgpr_write_b32 a246, v186\n"
"v_accvgpr_write_b32 a247, v187\n"
"v_cmp_ngt_f32 vcc, s67, v14\n"
"s_mov_b32 s72, 266240\n"
"s_or_b64 s[70:71], s[70:71], vcc\n"
"v_rcp_f32 v14, v14\n"
"s_nop 0\n"
"v_mul_f32 v192, v192, v14\n"
"v_mul_f32 v193, v193, v14\n"
"v_mul_f32 v194, v194, v14\n"
"v_mul_f32 v195, v195, v14\n"
"v_mul_f32 v196, v196, v14\n"
"v_mul_f32 v197, v197, v14\n"
"v_mul_f32 v198, v198, v14\n"
"v_mul_f32 v199, v199, v14\n"
"v_cvt_pk_bf16_f32 v192, v192, v193\n"
"v_cvt_pk_bf16_f32 v193, v194, v195\n"
"v_cvt_pk_bf16_f32 v194, v196, v197\n"
"v_cvt_pk_bf16_f32 v195, v198, v199\n"
"buffer_store_dwordx4 v[192:195], v12, s[52:55], s72 offen offset:0 sc1\n"
"v_accvgpr_write_b32 a200, v192\n"
"v_accvgpr_write_b32 a201, v193\n"
"v_accvgpr_write_b32 a202, v194\n"
"v_accvgpr_write_b32 a203, v195\n"
"v_mul_f32 v200, v200, v14\n"
"v_mul_f32 v201, v201, v14\n"
"v_mul_f32 v202, v202, v14\n"
"v_mul_f32 v203, v203, v14\n"
"v_mul_f32 v204, v204, v14\n"
"v_mul_f32 v205, v205, v14\n"
"v_mul_f32 v206, v206, v14\n"
"v_mul_f32 v207, v207, v14\n"
"v_cvt_pk_bf16_f32 v200, v200, v201\n"
"v_cvt_pk_bf16_f32 v201, v202, v203\n"
"v_cvt_pk_bf16_f32 v202, v204, v205\n"
"v_cvt_pk_bf16_f32 v203, v206, v207\n"
"buffer_store_dwordx4 v[200:203], v12, s[52:55], s72 offen offset:1024 sc1\n"
"v_accvgpr_write_b32 a216, v200\n"
"v_accvgpr_write_b32 a217, v201\n"
"v_accvgpr_write_b32 a218, v202\n"
"v_accvgpr_write_b32 a219, v203\n"
"v_mul_f32 v208, v208, v14\n"
"v_mul_f32 v209, v209, v14\n"
"v_mul_f32 v210, v210, v14\n"
"v_mul_f32 v211, v211, v14\n"
"v_mul_f32 v212, v212, v14\n"
"v_mul_f32 v213, v213, v14\n"
"v_mul_f32 v214, v214, v14\n"
"v_mul_f32 v215, v215, v14\n"
"v_cvt_pk_bf16_f32 v208, v208, v209\n"
"v_cvt_pk_bf16_f32 v209, v210, v211\n"
"v_cvt_pk_bf16_f32 v210, v212, v213\n"
"v_cvt_pk_bf16_f32 v211, v214, v215\n"
"buffer_store_dwordx4 v[208:211], v12, s[52:55], s72 offen offset:2048 sc1\n"
"v_accvgpr_write_b32 a232, v208\n"
"v_accvgpr_write_b32 a233, v209\n"
"v_accvgpr_write_b32 a234, v210\n"
"v_accvgpr_write_b32 a235, v211\n"
"v_mul_f32 v216, v216, v14\n"
"v_mul_f32 v217, v217, v14\n"
"v_mul_f32 v218, v218, v14\n"
"v_mul_f32 v219, v219, v14\n"
"v_mul_f32 v220, v220, v14\n"
"v_mul_f32 v221, v221, v14\n"
"v_mul_f32 v222, v222, v14\n"
"v_mul_f32 v223, v223, v14\n"
"v_cvt_pk_bf16_f32 v216, v216, v217\n"
"v_cvt_pk_bf16_f32 v217, v218, v219\n"
"v_cvt_pk_bf16_f32 v218, v220, v221\n"
"v_cvt_pk_bf16_f32 v219, v222, v223\n"
"buffer_store_dwordx4 v[216:219], v12, s[52:55], s72 offen offset:3072 sc1\n"
"v_accvgpr_write_b32 a248, v216\n"
"v_accvgpr_write_b32 a249, v217\n"
"v_accvgpr_write_b32 a250, v218\n"
"v_accvgpr_write_b32 a251, v219\n"
"s_or_b32 %[bad], s70, s71\n"
"s_lshl_b32 s63, %[wv], 2\n"
"s_add_u32 s63, s63, 131072\n"
"v_mov_b32 v64, s63\n"
"v_mov_b32 v65, %[bad]\n"
"v_mov_b32 v66, 131072\n"
"ds_write_b32 v64, v65\n"
"s_waitcnt lgkmcnt(0)\n"
"s_waitcnt vmcnt(22)\n"
"s_barrier\n"
"ds_read_b128 v[68:71], v66\n"
"ds_read_b64_tr_b16 a[0:1], v2 offset:0\n"
"ds_read_b64_tr_b16 a[2:3], v2 offset:4096\n"
"ds_read_b64_tr_b16 a[4:5], v2 offset:8192\n"
"ds_read_b64_tr_b16 a[6:7], v2 offset:12288\n"
"ds_read_b64_tr_b16 a[8:9], v3 offset:0\n"
"ds_read_b64_tr_b16 a[10:11], v3 offset:4096\n"
"ds_read_b64_tr_b16 a[12:13], v3 offset:8192\n"
"ds_read_b64_tr_b16 a[14:15], v3 offset:12288\n"
"ds_read_b64_tr_b16 a[16:17], v2 offset:256\n"
"ds_read_b64_tr_b16 a[18:19], v2 offset:4352\n"
"ds_read_b64_tr_b16 a[20:21], v2 offset:8448\n"
"ds_read_b64_tr_b16 a[22:23], v2 offset:12544\n"
"ds_read_b64_tr_b16 a[24:25], v3 offset:256\n"
"ds_read_b64_tr_b16 a[26:27], v3 offset:4352\n"
"ds_read_b64_tr_b16 a[28:29], v3 offset:8448\n"
"ds_read_b64_tr_b16 a[30:31], v3 offset:12544\n"
"ds_read_b64_tr_b16 a[32:33], v2 offset:512\n"
"ds_read_b64_tr_b16 a[34:35], v2 offset:4608\n"
"ds_read_b64_tr_b16 a[36:37], v2 offset:8704\n"
"ds_read_b64_tr_b16 a[38:39], v2 offset:12800\n"
"ds_read_b64_tr_b16 a[40:41], v3 offset:512\n"
"ds_read_b64_tr_b16 a[42:43], v3 offset:4608\n"
"ds_read_b64_tr_b16 a[44:45], v3 offset:8704\n"
"ds_read_b64_tr_b16 a[46:47], v3 offset:12800\n"
"ds_read_b64_tr_b16 a[48:49], v2 offset:768\n"
"ds_read_b64_tr_b16 a[50:51], v2 offset:4864\n"
"ds_read_b64_tr_b16 a[52:53], v2 offset:8960\n"
"ds_read_b64_tr_b16 a[54:55], v2 offset:13056\n"
"ds_read_b64_tr_b16 a[56:57], v3 offset:768\n"
"ds_read_b64_tr_b16 a[58:59], v3 offset:4864\n"
"s_waitcnt lgkmcnt(15)\n"
"v_or3_b32 v68, v68, v69, v70\n"
"v_or_b32 v68, v68, v71\n"
"s_nop 1\n"
"v_readfirstlane_b32 %[any], v68\n"
"s_nop 3\n"
"s_cmp_lg_u32 %[any], 0\n"
"s_cbranch_scc1 .Lzb_%=\n"
"v_mfma_f32_16x16x32_bf16 v[128:131], a[0:3], a[128:131], 0\n"
"ds_read_b64_tr_b16 a[60:61], v3 offset:8960\n"
"ds_read_b64_tr_b16 a[62:63], v3 offset:13056\n"
"v_mfma_f32_16x16x32_bf16 v[128:131], a[4:7], a[144:147], v[128:131]\n"
"v_mfma_f32_16x16x32_bf16 v[160:163], a[0:3], a[132:135], 0\n"
"s_add_u32 s63, %[ksp], 1\n"
"s_and_b32 s63, s63, 3\n"
"s_add_u32 s63, s63, %[gr4]\n"
"s_mul_i32 s63, s63, 3\n"
"s_add_u32 s63, s63, 0\n"
"s_mul_i32 s52, s63, 15360\n"
"s_add_u32 s63, %[ksp], 2\n"
"s_and_b32 s63, s63, 3\n"
"s_add_u32 s63, s63, %[gr4]\n"
"s_mul_i32 s63, s63, 3\n"
"s_add_u32 s63, s63, 1\n"
"s_mul_i32 s53, s63, 15360\n"
"s_add_u32 s63, %[ksp], 3\n"
"s_and_b32 s63, s63, 3\n"
"s_add_u32 s63, s63, %[gr4]\n"
"s_mul_i32 s63, s63, 3\n"
"s_add_u32 s63, s63, 2\n"
"s_mul_i32 s54, s63, 15360\n"
"s_add_u32 s63, %[gr4], %[ksp]\n"
"s_mul_i32 s55, s63, 46080\n"
"s_add_u32 s66, s55, 15360\n"
"s_add_u32 s67, s55, 30720\n"
"v_mfma_f32_16x16x32_bf16 v[160:163], a[4:7], a[148:151], v[160:163]\n"
"v_mfma_f32_16x16x32_bf16 v[192:195], a[0:3], a[136:139], 0\n"
"ds_read_b64_tr_b16 a[0:1], v2 offset:16384\n"
"ds_read_b64_tr_b16 a[2:3], v2 offset:20480\n"
"v_mfma_f32_16x16x32_bf16 v[192:195], a[4:7], a[152:155], v[192:195]\n"
"ds_read_b64_tr_b16 a[4:5], v2 offset:24576\n"
"ds_read_b64_tr_b16 a[6:7], v2 offset:28672\n"
"v_mfma_f32_16x16x32_bf16 v[132:135], a[8:11], a[128:131], 0\n"
"v_mfma_f32_16x16x32_bf16 v[132:135], a[12:15], a[144:147], v[132:135]\n"
"v_mfma_f32_16x16x32_bf16 v[164:167], a[8:11], a[132:135], 0\n"
"v_mfma_f32_16x16x32_bf16 v[164:167], a[12:15], a[148:151], v[164:167]\n"
"v_mfma_f32_16x16x32_bf16 v[196:199], a[8:11], a[136:139], 0\n"
"ds_read_b64_tr_b16 a[8:9], v3 offset:16384\n"
"ds_read_b64_tr_b16 a[10:11], v3 offset:20480\n"
"v_mfma_f32_16x16x32_bf16 v[196:199], a[12:15], a[152:155], v[196:199]\n"
"ds_read_b64_tr_b16 a[12:13], v3 offset:24576\n"
"ds_read_b64_tr_b16 a[14:15], v3 offset:28672\n"
"v_mfma_f32_16x16x32_bf16 v[136:139], a[16:19], a[128:131], 0\n"
"s_waitcnt vmcnt(18)\n"
"v_add_u32 v20, 1, v20\n"
"s_mov_b64 exec, 15\n"
"buffer_store_dword v20, v8, s[36:39], %[xw] offen sc1\n"
"s_mov_b64 exec, -1\n"
"v_mfma_f32_16x16x32_bf16 v[136:139], a[20:23], a[144:147], v[136:139]\n"
"v_mfma_f32_16x16x32_bf16 v[168:171], a[16:19], a[132:135], 0\n"
"v_mfma_f32_16x16x32_bf16 v[168:171], a[20:23], a[148:151], v[168:171]\n"
"v_mfma_f32_16x16x32_bf16 v[200:203], a[16:19], a[136:139], 0\n"
"ds_read_b64_tr_b16 a[16:17], v2 offset:16640\n"
"ds_read_b64_tr_b16 a[18:19], v2 offset:20736\n"
"v_mfma_f32_16x16x32_bf16 v[200:203], a[20:23], a[152:155], v[200:203]\n"
"ds_read_b64_tr_b16 a[20:21], v2 offset:24832\n"
"ds_read_b64_tr_b16 a[22:23], v2 offset:28928\n"
"v_mfma_f32_16x16x32_bf16 v[140:143], a[24:27], a[128:131], 0\n"
"s_waitcnt lgkmcnt(15)\n"
"v_mfma_f32_16x16x32_bf16 v[140:143], a[28:31], a[144:147], v[140:143]\n"
"v_mfma_f32_16x16x32_bf16 v[172:175], a[24:27], a[132:135], 0\n"
"v_mfma_f32_16x16x32_bf16 v[172:175], a[28:31], a[148:151], v[172:175]\n"
"v_mfma_f32_16x16x32_bf16 v[204:207], a[24:27], a[136:139], 0\n"
"ds_read_b64_tr_b16 a[24:25], v3 offset:16640\n"
"ds_read_b64_tr_b16 a[26:27], v3 offset:20736\n"
"v_mfma_f32_16x16x32_bf16 v[204:207], a[28:31], a[152:155], v[204:207]\n"
"ds_read_b64_tr_b16 a[28:29], v3 offset:24832\n"
"ds_read_b64_tr_b16 a[30:31], v3 offset:28928\n"
"v_mfma_f32_16x16x32_bf16 v[144:147], a[32:35], a[128:131], 0\n"
"v_mfma_f32_16x16x32_bf16 v[144:147], a[36:39], a[144:147], v[144:147]\n"
"v_mfma_f32_16x16x32_bf16 v[176:179], a[32:35], a[132:135], 0\n"
"v_mfma_f32_16x16x32_bf16 v[176:179], a[36:39], a[148:151], v[176:179]\n"
"v_mfma_f32_16x16x32_bf16 v[208:211], a[32:35], a[136:139], 0\n"
"ds_read_b64_tr_b16 a[32:33], v2 offset:16896\n"
"ds_read_b64_tr_b16 a[34:35], v2 offset:20992\n"
"v_mfma_f32_16x16x32_bf16 v[208:211], a[36:39], a[152:155], v[208:211]\n"
"ds_read_b64_tr_b16 a[36:37], v2 offset:25088\n"
"ds_read_b64_tr_b16 a[38:39], v2 offset:29184\n"
"v_mfma_f32_16x16x32_bf16 v[148:151], a[40:43], a[128:131], 0\n"
"v_mfma_f32_16x16x32_bf16 v[148:151], a[44:47], a[144:147], v[148:151]\n"
"v_mfma_f32_16x16x32_bf16 v[180:183], a[40:43], a[132:135], 0\n"
"v_mfma_f32_16x16x32_bf16 v[180:183], a[44:47], a[148:151], v[180:183]\n"
"v_mfma_f32_16x16x32_bf16 v[212:215], a[40:43], a[136:139], 0\n"
"ds_read_b64_tr_b16 a[40:41], v3 offset:16896\n"
"ds_read_b64_tr_b16 a[42:43], v3 offset:20992\n"
"v_mfma_f32_16x16x32_bf16 v[212:215], a[44:47], a[152:155], v[212:215]\n"
"ds_read_b64_tr_b16 a[44:45], v3 offset:25088\n"
"ds_read_b64_tr_b16 a[46:47], v3 offset:29184\n"
"v_mfma_f32_16x16x32_bf16 v[152:155], a[48:51], a[128:131], 0\n"
"v_mfma_f32_16x16x32_bf16 v[152:155], a[52:55], a[144:147], v[152:155]\n"
"s_and_b32 s63, s65, 15\n"
"s_lshl_b32 s58, s63, 12\n"
"s_lshr_b32 s65, s65, 4\n"
"s_add_u32 s59, s58, 131072\n"
"s_add_u32 s60, s58, 262144\n"
"s_waitcnt vmcnt(12)\n"
"v_cmp_ne_u32 vcc, v24, v17\n"
"s_nop 1\n"
"s_mov_b64 s[78:79], vcc\n"
"v_cmp_ne_u32 vcc, v25, v16\n"
"s_nop 1\n"
"s_or_b64 s[78:79], s[78:79], vcc\n"
"s_cbranch_scc1 .Ls0b_%=\n"
".Lr0b_%=:\n"
"v_mfma_f32_16x16x32_bf16 v[184:187], a[48:51], a[132:135], 0\n"
"v_mfma_f32_16x16x32_bf16 v[184:187], a[52:55], a[148:151], v[184:187]\n"
"v_mfma_f32_16x16x32_bf16 v[216:219], a[48:51], a[136:139], 0\n"
"ds_read_b64_tr_b16 a[48:49], v2 offset:17152\n"
"ds_read_b64_tr_b16 a[50:51], v2 offset:21248\n"
"v_mfma_f32_16x16x32_bf16 v[216:219], a[52:55], a[152:155], v[216:219]\n"
"ds_read_b64_tr_b16 a[52:53], v2 offset:25344\n"
"ds_read_b64_tr_b16 a[54:55], v2 offset:29440\n"
"s_waitcnt lgkmcnt(15)\n"
"v_mfma_f32_16x16x32_bf16 v[156:159], a[56:59], a[128:131], 0\n"
"v_mfma_f32_16x16x32_bf16 v[156:159], a[60:63], a[144:147], v[156:159]\n"
"v_mfma_f32_16x16x32_bf16 v[188:191], a[56:59], a[132:135], 0\n"
"v_mfma_f32_16x16x32_bf16 v[188:191], a[60:63], a[148:151], v[188:191]\n"
"v_mfma_f32_16x16x32_bf16 v[220:223], a[56:59], a[136:139], 0\n"
"ds_read_b64_tr_b16 a[56:57], v3 offset:17152\n"
"ds_read_b64_tr_b16 a[58:59], v3 offset:21248\n"
"v_mfma_f32_16x16x32_bf16 v[220:223], a[60:63], a[152:155], v[220:223]\n"
"ds_read_b64_tr_b16 a[60:61], v3 offset:25344\n"
"ds_read_b64_tr_b16 a[62:63], v3 offset:29440\n"
"v_mfma_f32_16x16x32_bf16 v[128:131], a[0:3], a[160:163], v[128:131]\n"
"buffer_load_dwordx4 a[128:131], v4, s[44:47], s58 offen offset:0 sc1\n"
"v_mfma_f32_16x16x32_bf16 v[128:131], a[4:7], a[176:179], v[128:131]\n"
"v_mfma_f32_16x16x32_bf16 v[160:163], a[0:3], a[164:167], v[160:163]\n"
"v_mfma_f32_16x16x32_bf16 v[160:163], a[4:7], a[180:183], v[160:163]\n"
"s_waitcnt vmcnt(63)\n"
"s_barrier\n"
"v_xor_b32 v2, 0x10000, v2\n"
"v_xor_b32 v3, 0x10000, v3\n"
"v_mfma_f32_16x16x32_bf16 v[192:195], a[0:3], a[168:171], v[192:195]\n"
"s_add_u32 m0, s75, 0\n"
"s_and_b32 s76, s68, 15\n"
"s_lshl_b32 s76, s76, 17\n"
"s_lshr_b64 s[68:69], s[68:69], 4\n"
"buffer_load_dwordx4 v0, s[40:43], s76 offen offset:0 lds\n"
"v_mfma_f32_16x16x32_bf16 v[192:195], a[4:7], a[184:187], v[192:195]\n"
"buffer_load_dwordx4 a[132:135], v4, s[44:47], s59 offen offset:0 sc1\n"
"v_mfma_f32_16x16x32_bf16 v[132:135], a[8:11], a[160:163], v[132:135]\n"
"ds_read_b64_tr_b16 a[0:1], v2 offset:0\n"
"ds_read_b64_tr_b16 a[2:3], v2 offset:4096\n"
"v_mfma_f32_16x16x32_bf16 v[132:135], a[12:15], a[176:179], v[132:135]\n"
"ds_read_b64_tr_b16 a[4:5], v2 offset:8192\n"
"ds_read_b64_tr_b16 a[6:7], v2 offset:12288\n"
"v_mfma_f32_16x16x32_bf16 v[164:167], a[8:11], a[164:167], v[164:167]\n"
"v_mfma_f32_16x16x32_bf16 v[164:167], a[12:15], a[180:183], v[164:167]\n"
"s_add_u32 m0, s75, 0\n"
"s_add_u32 s76, s76, 3072\n"
"buffer_load_dwordx4 v1, s[40:43], s76 offen offset:1024 lds\n"
"v_mfma_f32_16x16x32_bf16 v[196:199], a[8:11], a[168:171], v[196:199]\n"
"ds_read_b64_tr_b16 a[8:9], v3 offset:0\n"
"ds_read_b64_tr_b16 a[10:11], v3 offset:4096\n"
"v_mfma_f32_16x16x32_bf16 v[196:199], a[12:15], a[184:187], v[196:199]\n"
"ds_read_b64_tr_b16 a[12:13], v3 offset:8192\n"
"ds_read_b64_tr_b16 a[14:15], v3 offset:12288\n"
"v_mfma_f32_16x16x32_bf16 v[136:139], a[16:19], a[160:163], v[136:139]\n"
"buffer_load_dwordx4 a[136:139], v4, s[44:47], s60 offen offset:0 sc1\n"
"v_mfma_f32_16x16x32_bf16 v[136:139], a[20:23], a[176:179], v[136:139]\n"
"v_mfma_f32_16x16x32_bf16 v[168:171], a[16:19], a[164:167], v[168:171]\n"
"s_add_u32 m0, s75, 0\n"
"s_add_u32 s76, s76, 3072\n"
"buffer_load_dwordx4 v0, s[40:43], s76 offen offset:2048 lds\n"
"v_mfma_f32_16x16x32_bf16 v[168:171], a[20:23], a[180:183], v[168:171]\n"
"v_mfma_f32_16x16x32_bf16 v[200:203], a[16:19], a[168:171], v[200:203]\n"
"ds_read_b64_tr_b16 a[16:17], v2 offset:256\n"
"ds_read_b64_tr_b16 a[18:19], v2 offset:4352\n"
"v_mfma_f32_16x16x32_bf16 v[200:203], a[20:23], a[184:187], v[200:203]\n"
"ds_read_b64_tr_b16 a[20:21], v2 offset:8448\n"
"ds_read_b64_tr_b16 a[22:23], v2 offset:12544\n"
"s_waitcnt lgkmcnt(15)\n"
"v_mfma_f32_16x16x32_bf16 v[140:143], a[24:27], a[160:163], v[140:143]\n"
"v_mfma_f32_16x16x32_bf16 v[140:143], a[28:31], a[176:179], v[140:143]\n"
"s_add_u32 m0, s75, 0\n"
"s_add_u32 s76, s76, 3072\n"
"buffer_load_dwordx4 v1, s[40:43], s76 offen offset:3072 lds\n"
"v_mfma_f32_16x16x32_bf16 v[172:175], a[24:27], a[164:167], v[172:175]\n"
"buffer_load_dwordx4 a[144:147], v4, s[44:47], s58 offen offset:1024 sc1\n"
"v_mfma_f32_16x16x32_bf16 v[172:175], a[28:31], a[180:183], v[172:175]\n"
"v_mfma_f32_16x16x32_bf16 v[204:207], a[24:27], a[168:171], v[204:207]\n"
"ds_read_b64_tr_b16 a[24:25], v3 offset:256\n"
"ds_read_b64_tr_b16 a[26:27], v3 offset:4352\n"
"v_mfma_f32_16x16x32_bf16 v[204:207], a[28:31], a[184:187], v[204:207]\n"
"ds_read_b64_tr_b16 a[28:29], v3 offset:8448\n"
"ds_read_b64_tr_b16 a[30:31], v3 offset:12544\n"
"v_mfma_f32_16x16x32_bf16 v[144:147], a[32:35], a[160:163], v[144:147]\n"
"s_add_u32 m0, s75, 16384\n"
"s_add_u32 s76, s76, 56320\n"
"buffer_load_dwordx4 v0, s[40:43], s76 offen offset:0 lds\n"
"v_mfma_f32_16x16x32_bf16 v[144:147], a[36:39], a[176:179], v[144:147]\n"
"v_mfma_f32_16x16x32_bf16 v[176:179], a[32:35], a[164:167], v[176:179]\n"
"buffer_load_dwordx4 a[148:151], v4, s[44:47], s59 offen offset:1024 sc1\n"
"v_mfma_f32_16x16x32_bf16 v[176:179], a[36:39], a[180:183], v[176:179]\n"
"v_mfma_f32_16x16x32_bf16 v[208:211], a[32:35], a[168:171], v[208:211]\n"
"ds_read_b64_tr_b16 a[32:33], v2 offset:512\n"
"ds_read_b64_tr_b16 a[34:35], v2 offset:4608\n"
"v_mfma_f32_16x16x32_bf16 v[208:211], a[36:39], a[184:187], v[208:211]\n"
"s_add_u32 m0, s75, 16384\n"
"s_add_u32 s76, s76, 3072\n"
"buffer_load_dwordx4 v1, s[40:43], s76 offen offset:1024 lds\n"
"v_mfma_f32_16x16x32_bf16 v[148:151], a[40:43], a[160:163], v[148:151]\n"
"ds_read_b64_tr_b16 a[36:37], v2 offset:8704\n"
"ds_read_b64_tr_b16 a[38:39], v2 offset:12800\n"
"v_mfma_f32_16x16x32_bf16 v[148:151], a[44:47], a[176:179], v[148:151]\n"
"v_mfma_f32_16x16x32_bf16 v[180:183], a[40:43], a[164:167], v[180:183]\n"
"v_mfma_f32_16x16x32_bf16 v[180:183], a[44:47], a[180:183], v[180:183]\n"
"buffer_load_dwordx4 a[152:155], v4, s[44:47], s60 offen offset:1024 sc1\n"
"v_mfma_f32_16x16x32_bf16 v[212:215], a[40:43], a[168:171], v[212:215]\n"
"s_add_u32 m0, s75, 16384\n"
"s_add_u32 s76, s76, 3072\n"
"buffer_load_dwordx4 v0, s[40:43], s76 offen offset:2048 lds\n"
"v_mfma_f32_16x16x32_bf16 v[212:215], a[44:47], a[184:187], v[212:215]\n"
"ds_read_b64_tr_b16 a[40:41], v3 offset:512\n"
"ds_read_b64_tr_b16 a[42:43], v3 offset:4608\n"
"v_mfma_f32_16x16x32_bf16 v[152:155], a[48:51], a[160:163], v[152:155]\n"
"ds_read_b64_tr_b16 a[44:45], v3 offset:8704\n"
"ds_read_b64_tr_b16 a[46:47], v3 offset:12800\n"
"v_mfma_f32_16x16x32_bf16 v[152:155], a[52:55], a[176:179], v[152:155]\n"
"v_mfma_f32_16x16x32_bf16 v[184:187], a[48:51], a[164:167], v[184:187]\n"
"v_mfma_f32_16x16x32_bf16 v[184:187], a[52:55], a[180:183], v[184:187]\n"
"s_add_u32 m0, s75, 16384\n"
"s_add_u32 s76, s76, 3072\n"
"buffer_load_dwordx4 v1, s[40:43], s76 offen offset:3072 lds\n"
"s_xor_b32 s75, s75, 0x8000\n"
"v_mfma_f32_16x16x32_bf16 v[216:219], a[48:51], a[168:171], v[216:219]\n"
"ds_read_b64_tr_b16 a[48:49], v2 offset:768\n"
"ds_read_b64_tr_b16 a[50:51], v2 offset:4864\n"
"v_mfma_f32_16x16x32_bf16 v[216:219], a[52:55], a[184:187], v[216:219]\n"
"ds_read_b64_tr_b16 a[52:53], v2 offset:8960\n"
"ds_read_b64_tr_b16 a[54:55], v2 offset:13056\n"
"s_waitcnt lgkmcnt(15)\n"
"v_mfma_f32_16x16x32_bf16 v[156:159], a[56:59], a[160:163], v[156:159]\n"
"v_mfma_f32_16x16x32_bf16 v[156:159], a[60:63], a[176:179], v[156:159]\n"
"v_mfma_f32_16x16x32_bf16 v[188:191], a[56:59], a[164:167], v[188:191]\n"
"v_mfma_f32_16x16x32_bf16 v[188:191], a[60:63], a[180:183], v[188:191]\n"
"v_mfma_f32_16x16x32_bf16 v[220:223], a[56:59], a[168:171], v[220:223]\n"
"ds_read_b64_tr_b16 a[56:57], v3 offset:768\n"
"ds_read_b64_tr_b16 a[58:59], v3 offset:4864\n"
"v_mfma_f32_16x16x32_bf16 v[220:223], a[60:63], a[184:187], v[220:223]\n"
"v_mfma_f32_16x16x32_bf16 v[128:131], a[0:3], a[192:195], v[128:131]\n"
"ds_read_b64_tr_b16 a[60:61], v3 offset:8960\n"
"ds_read_b64_tr_b16 a[62:63], v3 offset:13056\n"
"v_mfma_f32_16x16x32_bf16 v[128:131], a[4:7], a[208:211], v[128:131]\n"
"buffer_load_dwordx4 a[160:163], v4, s[44:47], s58 offen offset:2048 sc1\n"
"v_mfma_f32_16x16x32_bf16 v[160:163], a[0:3], a[196:199], v[160:163]\n"
"v_mfma_f32_16x16x32_bf16 v[160:163], a[4:7], a[212:215], v[160:163]\n"
"v_mfma_f32_16x16x32_bf16 v[192:195], a[0:3], a[200:203], v[192:195]\n"
"ds_read_b64_tr_b16 a[0:1], v2 offset:16384\n"
"ds_read_b64_tr_b16 a[2:3], v2 offset:20480\n"
"v_mfma_f32_16x16x32_bf16 v[192:195], a[4:7], a[216:219], v[192:195]\n"
"ds_read_b64_tr_b16 a[4:5], v2 offset:24576\n"
"ds_read_b64_tr_b16 a[6:7], v2 offset:28672\n"
"v_mfma_f32_16x16x32_bf16 v[132:135], a[8:11], a[192:195], v[132:135]\n"
"v_mfma_f32_16x16x32_bf16 v[132:135], a[12:15], a[208:211], v[132:135]\n"
"buffer_load_dwordx4 a[164:167], v4, s[44:47], s59 offen offset:2048 sc1\n"
"v_mfma_f32_16x16x32_bf16 v[164:167], a[8:11], a[196:199], v[164:167]\n"
"s_waitcnt vmcnt(17)\n"
"s_mov_b64 exec, 15\n"
"buffer_store_dwordx2 v[18:19], v8, s[36:39], %[mw] offen offset:128 sc1\n"
"s_mov_b64 exec, -1\n"
"v_mfma_f32_16x16x32_bf16 v[164:167], a[12:15], a[212:215], v[164:167]\n"
"v_mfma_f32_16x16x32_bf16 v[196:199], a[8:11], a[200:203], v[196:199]\n"
"ds_read_b64_tr_b16 a[8:9], v3 offset:16384\n"
"ds_read_b64_tr_b16 a[10:11], v3 offset:20480\n"
"v_mfma_f32_16x16x32_bf16 v[196:199], a[12:15], a[216:219], v[196:199]\n"
"ds_read_b64_tr_b16 a[12:13], v3 offset:24576\n"
"ds_read_b64_tr_b16 a[14:15], v3 offset:28672\n"
"v_mfma_f32_16x16x32_bf16 v[136:139], a[16:19], a[192:195], v[136:139]\n"
"v_mfma_f32_16x16x32_bf16 v[136:139], a[20:23], a[208:211], v[136:139]\n"
"buffer_load_dwordx4 a[168:171], v4, s[44:47], s60 offen offset:2048 sc1\n"
"v_mfma_f32_16x16x32_bf16 v[168:171], a[16:19], a[196:199], v[168:171]\n"
"v_mfma_f32_16x16x32_bf16 v[168:171], a[20:23], a[212:215], v[168:171]\n"
"v_mfma_f32_16x16x32_bf16 v[200:203], a[16:19], a[200:203], v[200:203]\n"
"ds_read_b64_tr_b16 a[16:17], v2 offset:16640\n"
"ds_read_b64_tr_b16 a[18:19], v2 offset:20736\n"
"v_mfma_f32_16x16x32_bf16 v[200:203], a[20:23], a[216:219], v[200:203]\n"
"ds_read_b64_tr_b16 a[20:21], v2 offset:24832\n"
"ds_read_b64_tr_b16 a[22:23], v2 offset:28928\n"
"s_waitcnt lgkmcnt(15)\n"
"v_mfma_f32_16x16x32_bf16 v[140:143], a[24:27], a[192:195], v[140:143]\n"
"v_mfma_f32_16x16x32_bf16 v[140:143], a[28:31], a[208:211], v[140:143]\n"
"buffer_load_dwordx4 a[176:179], v4, s[44:47], s58 offen offset:3072 sc1\n"
"v_mfma_f32_16x16x32_bf16 v[172:175], a[24:27], a[196:199], v[172:175]\n"
"v_mfma_f32_16x16x32_bf16 v[172:175], a[28:31], a[212:215], v[172:175]\n"
"v_mfma_f32_16x16x32_bf16 v[204:207], a[24:27], a[200:203], v[204:207]\n"
"ds_read_b64_tr_b16 a[24:25], v3 offset:16640\n"
"ds_read_b64_tr_b16 a[26:27], v3 offset:20736\n"
"v_mfma_f32_16x16x32_bf16 v[204:207], a[28:31], a[216:219], v[204:207]\n"
"ds_read_b64_tr_b16 a[28:29], v3 offset:24832\n"
"ds_read_b64_tr_b16 a[30:31], v3 offset:28928\n"
"v_mfma_f32_16x16x32_bf16 v[144:147], a[32:35], a[192:195], v[144:147]\n"
"v_mfma_f32_16x16x32_bf16 v[144:147], a[36:39], a[208:211], v[144:147]\n"
"buffer_load_dwordx4 a[180:183], v4, s[44:47], s59 offen offset:3072 sc1\n"
"v_mfma_f32_16x16x32_bf16 v[176:179], a[32:35], a[196:199], v[176:179]\n"
"v_mfma_f32_16x16x32_bf16 v[176:179], a[36:39], a[212:215], v[176:179]\n"
"v_mfma_f32_16x16x32_bf16 v[208:211], a[32:35], a[200:203], v[208:211]\n"
"ds_read_b64_tr_b16 a[32:33], v2 offset:16896\n"
"ds_read_b64_tr_b16 a[34:35], v2 offset:20992\n"
"v_mfma_f32_16x16x32_bf16 v[208:211], a[36:39], a[216:219], v[208:211]\n"
"ds_read_b64_tr_b16 a[36:37], v2 offset:25088\n"
"ds_read_b64_tr_b16 a[38:39], v2 offset:29184\n"
"v_mfma_f32_16x16x32_bf16 v[148:151], a[40:43], a[192:195], v[148:151]\n"
"v_mfma_f32_16x16x32_bf16 v[148:151], a[44:47], a[208:211], v[148:151]\n"
"buffer_load_dwordx4 a[184:187], v4, s[44:47], s60 offen offset:3072 sc1\n"
"v_mfma_f32_16x16x32_bf16 v[180:183], a[40:43], a[196:199], v[180:183]\n"
"v_mfma_f32_16x16x32_bf16 v[180:183], a[44:47], a[212:215], v[180:183]\n"
"v_mfma_f32_16x16x32_bf16 v[212:215], a[40:43], a[200:203], v[212:215]\n"
"ds_read_b64_tr_b16 a[40:41], v3 offset:16896\n"
"ds_read_b64_tr_b16 a[42:43], v3 offset:20992\n"
"v_mfma_f32_16x16x32_bf16 v[212:215], a[44:47], a[216:219], v[212:215]\n"
"ds_read_b64_tr_b16 a[44:45], v3 offset:25088\n"
"ds_read_b64_tr_b16 a[46:47], v3 offset:29184\n"
"v_mfma_f32_16x16x32_bf16 v[152:155], a[48:51], a[192:195], v[152:155]\n"
"v_mfma_f32_16x16x32_bf16 v[152:155], a[52:55], a[208:211], v[152:155]\n"
"s_and_b32 s63, s65, 15\n"
"s_lshl_b32 s58, s63, 12\n"
"s_lshr_b32 s65, s65, 4\n"
"s_add_u32 s59, s58, 131072\n"
"s_add_u32 s60, s58, 262144\n"
"v_mfma_f32_16x16x32_bf16 v[184:187], a[48:51], a[196:199], v[184:187]\n"
"v_mfma_f32_16x16x32_bf16 v[184:187], a[52:55], a[212:215], v[184:187]\n"
"v_mfma_f32_16x16x32_bf16 v[216:219], a[48:51], a[200:203], v[216:219]\n"
"ds_read_b64_tr_b16 a[48:49], v2 offset:17152\n"
"ds_read_b64_tr_b16 a[50:51], v2 offset:21248\n"
"v_mfma_f32_16x16x32_bf16 v[216:219], a[52:55], a[216:219], v[216:219]\n"
"ds_read_b64_tr_b16 a[52:53], v2 offset:25344\n"
"ds_read_b64_tr_b16 a[54:55], v2 offset:29440\n"
"s_waitcnt lgkmcnt(15)\n"
"v_mfma_f32_16x16x32_bf16 v[156:159], a[56:59], a[192:195], v[156:159]\n"
"v_mfma_f32_16x16x32_bf16 v[156:159], a[60:63], a[208:211], v[156:159]\n"
"v_mfma_f32_16x16x32_bf16 v[188:191], a[56:59], a[196:199], v[188:191]\n"
"v_mfma_f32_16x16x32_bf16 v[188:191], a[60:63], a[212:215], v[188:191]\n"
"v_mfma_f32_16x16x32_bf16 v[220:223], a[56:59], a[200:203], v[220:223]\n"
"ds_read_b64_tr_b16 a[56:57], v3 offset:17152\n"
"ds_read_b64_tr_b16 a[58:59], v3 offset:21248\n"
"v_mfma_f32_16x16x32_bf16 v[220:223], a[60:63], a[216:219], v[220:223]\n"
"ds_read_b64_tr_b16 a[60:61], v3 offset:25344\n"
"ds_read_b64_tr_b16 a[62:63], v3 offset:29440\n"
"v_mfma_f32_16x16x32_bf16 v[128:131], a[0:3], a[224:227], v[128:131]\n"
"buffer_load_dwordx4 a[192:195], v4, s[44:47], s58 offen offset:0 sc1\n"
"v_mfma_f32_16x16x32_bf16 v[128:131], a[4:7], a[240:243], v[128:131]\n"
"v_mfma_f32_16x16x32_bf16 v[160:163], a[0:3], a[228:231], v[160:163]\n"
"v_mfma_f32_16x16x32_bf16 v[160:163], a[4:7], a[244:247], v[160:163]\n"
"s_waitcnt vmcnt(63)\n"
"s_barrier\n"
"v_xor_b32 v2, 0x8000, v2\n"
"v_xor_b32 v3, 0x8000, v3\n"
"v_mfma_f32_16x16x32_bf16 v[192:195], a[0:3], a[232:235], v[192:195]\n"
"s_add_u32 m0, s75, 0\n"
"s_and_b32 s76, s68, 15\n"
"s_lshl_b32 s76, s76, 17\n"
"s_lshr_b64 s[68:69], s[68:69], 4\n"
"buffer_load_dwordx4 v0, s[40:43], s76 offen offset:0 lds\n"
"v_mfma_f32_16x16x32_bf16 v[192:195], a[4:7], a[248:251], v[192:195]\n"
"buffer_load_dwordx4 a[196:199], v4, s[44:47], s59 offen offset:0 sc1\n"
"v_mfma_f32_16x16x32_bf16 v[132:135], a[8:11], a[224:227], v[132:135]\n"
"ds_read_b64_tr_b16 a[0:1], v2 offset:0\n"
"ds_read_b64_tr_b16 a[2:3], v2 offset:4096\n"
"v_mfma_f32_16x16x32_bf16 v[132:135], a[12:15], a[240:243], v[132:135]\n"
"ds_read_b64_tr_b16 a[4:5], v2 offset:8192\n"
"ds_read_b64_tr_b16 a[6:7], v2 offset:12288\n"
"v_mfma_f32_16x16x32_bf16 v[164:167], a[8:11], a[228:231], v[164:167]\n"
"v_mfma_f32_16x16x32_bf16 v[164:167], a[12:15], a[244:247], v[164:167]\n"
"s_add_u32 m0, s75, 0\n"
"s_add_u32 s76, s76, 3072\n"
"buffer_load_dwordx4 v1, s[40:43], s76 offen offset:1024 lds\n"
"v_mfma_f32_16x16x32_bf16 v[196:199], a[8:11], a[232:235], v[196:199]\n"
"ds_read_b64_tr_b16 a[8:9], v3 offset:0\n"
"ds_read_b64_tr_b16 a[10:11], v3 offset:4096\n"
"v_mfma_f32_16x16x32_bf16 v[196:199], a[12:15], a[248:251], v[196:199]\n"
"ds_read_b64_tr_b16 a[12:13], v3 offset:8192\n"
"ds_read_b64_tr_b16 a[14:15], v3 offset:12288\n"
"v_mfma_f32_16x16x32_bf16 v[136:139], a[16:19], a[224:227], v[136:139]\n"
"buffer_load_dwordx4 a[200:203], v4, s[44:47], s60 offen offset:0 sc1\n"
"v_mfma_f32_16x16x32_bf16 v[136:139], a[20:23], a[240:243], v[136:139]\n"
"v_mfma_f32_16x16x32_bf16 v[168:171], a[16:19], a[228:231], v[168:171]\n"
"s_add_u32 m0, s75, 0\n"
"s_add_u32 s76, s76, 3072\n"
"buffer_load_dwordx4 v0, s[40:43], s76 offen offset:2048 lds\n"
"v_mfma_f32_16x16x32_bf16 v[168:171], a[20:23], a[244:247], v[168:171]\n"
"v_mfma_f32_16x16x32_bf16 v[200:203], a[16:19], a[232:235], v[200:203]\n"
"ds_read_b64_tr_b16 a[16:17], v2 offset:256\n"
"ds_read_b64_tr_b16 a[18:19], v2 offset:4352\n"
"v_mfma_f32_16x16x32_bf16 v[200:203], a[20:23], a[248:251], v[200:203]\n"
"ds_read_b64_tr_b16 a[20:21], v2 offset:8448\n"
"ds_read_b64_tr_b16 a[22:23], v2 offset:12544\n"
"s_waitcnt lgkmcnt(15)\n"
"v_mfma_f32_16x16x32_bf16 v[140:143], a[24:27], a[224:227], v[140:143]\n"
"v_mfma_f32_16x16x32_bf16 v[140:143], a[28:31], a[240:243], v[140:143]\n"
"s_add_u32 m0, s75, 0\n"
"s_add_u32 s76, s76, 3072\n"
"buffer_load_dwordx4 v1, s[40:43], s76 offen offset:3072 lds\n"
"v_mfma_f32_16x16x32_bf16 v[172:175], a[24:27], a[228:231], v[172:175]\n"
"buffer_load_dwordx4 a[208:211], v4, s[44:47], s58 offen offset:1024 sc1\n"
"v_mfma_f32_16x16x32_bf16 v[172:175], a[28:31], a[244:247], v[172:175]\n"
"v_mfma_f32_16x16x32_bf16 v[204:207], a[24:27], a[232:235], v[204:207]\n"
"ds_read_b64_tr_b16 a[24:25], v3 offset:256\n"
"ds_read_b64_tr_b16 a[26:27], v3 offset:4352\n"
"v_mfma_f32_16x16x32_bf16 v[204:207], a[28:31], a[248:251], v[204:207]\n"
"ds_read_b64_tr_b16 a[28:29], v3 offset:8448\n"
"ds_read_b64_tr_b16 a[30:31], v3 offset:12544\n"
"v_mfma_f32_16x16x32_bf16 v[144:147], a[32:35], a[224:227], v[144:147]\n"
"s_add_u32 m0, s75, 16384\n"
"s_add_u32 s76, s76, 56320\n"
"buffer_load_dwordx4 v0, s[40:43], s76 offen offset:0 lds\n"
"v_mfma_f32_16x16x32_bf16 v[144:147], a[36:39], a[240:243], v[144:147]\n"
"v_mfma_f32_16x16x32_bf16 v[176:179], a[32:35], a[228:231], v[176:179]\n"
"buffer_load_dwordx4 a[212:215], v4, s[44:47], s59 offen offset:1024 sc1\n"
"v_mfma_f32_16x16x32_bf16 v[176:179], a[36:39], a[244:247], v[176:179]\n"
"v_mfma_f32_16x16x32_bf16 v[208:211], a[32:35], a[232:235], v[208:211]\n"
"ds_read_b64_tr_b16 a[32:33], v2 offset:512\n"
"ds_read_b64_tr_b16 a[34:35], v2 offset:4608\n"
"v_mfma_f32_16x16x32_bf16 v[208:211], a[36:39], a[248:251], v[208:211]\n"
"s_add_u32 m0, s75, 16384\n"
"s_add_u32 s76, s76, 3072\n"
"buffer_load_dwordx4 v1, s[40:43], s76 offen offset:1024 lds\n"
"v_mfma_f32_16x16x32_bf16 v[148:151], a[40:43], a[224:227], v[148:151]\n"
"ds_read_b64_tr_b16 a[36:37], v2 offset:8704\n"
"ds_read_b64_tr_b16 a[38:39], v2 offset:12800\n"
"v_mfma_f32_16x16x32_bf16 v[148:151], a[44:47], a[240:243], v[148:151]\n"
"v_mfma_f32_16x16x32_bf16 v[180:183], a[40:43], a[228:231], v[180:183]\n"
"v_mfma_f32_16x16x32_bf16 v[180:183], a[44:47], a[244:247], v[180:183]\n"
"buffer_load_dwordx4 a[216:219], v4, s[44:47], s60 offen offset:1024 sc1\n"
"v_mfma_f32_16x16x32_bf16 v[212:215], a[40:43], a[232:235], v[212:215]\n"
"s_add_u32 m0, s75, 16384\n"
"s_add_u32 s76, s76, 3072\n"
"buffer_load_dwordx4 v0, s[40:43], s76 offen offset:2048 lds\n"
"v_mfma_f32_16x16x32_bf16 v[212:215], a[44:47], a[248:251], v[212:215]\n"
"ds_read_b64_tr_b16 a[40:41], v3 offset:512\n"
"ds_read_b64_tr_b16 a[42:43], v3 offset:4608\n"
"v_mfma_f32_16x16x32_bf16 v[152:155], a[48:51], a[224:227], v[152:155]\n"
"ds_read_b64_tr_b16 a[44:45], v3 offset:8704\n"
"ds_read_b64_tr_b16 a[46:47], v3 offset:12800\n"
"v_mfma_f32_16x16x32_bf16 v[152:155], a[52:55], a[240:243], v[152:155]\n"
"v_mfma_f32_16x16x32_bf16 v[184:187], a[48:51], a[228:231], v[184:187]\n"
"v_mfma_f32_16x16x32_bf16 v[184:187], a[52:55], a[244:247], v[184:187]\n"
"s_add_u32 m0, s75, 16384\n"
"s_add_u32 s76, s76, 3072\n"
"buffer_load_dwordx4 v1, s[40:43], s76 offen offset:3072 lds\n"
"s_xor_b32 s75, s75, 0x10000\n"
"v_mfma_f32_16x16x32_bf16 v[216:219], a[48:51], a[232:235], v[216:219]\n"
"ds_read_b64_tr_b16 a[48:49], v2 offset:768\n"
"ds_read_b64_tr_b16 a[50:51], v2 offset:4864\n"
"v_mfma_f32_16x16x32_bf16 v[216:219], a[52:55], a[248:251], v[216:219]\n"
"ds_read_b64_tr_b16 a[52:53], v2 offset:8960\n"
"ds_read_b64_tr_b16 a[54:55], v2 offset:13056\n"
"s_waitcnt lgkmcnt(15)\n"
"v_mfma_f32_16x16x32_bf16 v[156:159], a[56:59], a[224:227], v[156:159]\n"
"v_mfma_f32_16x16x32_bf16 v[156:159], a[60:63], a[240:243], v[156:159]\n"
"v_mfma_f32_16x16x32_bf16 v[188:191], a[56:59], a[228:231], v[188:191]\n"
"v_mfma_f32_16x16x32_bf16 v[188:191], a[60:63], a[244:247], v[188:191]\n"
"v_mfma_f32_16x16x32_bf16 v[220:223], a[56:59], a[232:235], v[220:223]\n"
"ds_read_b64_tr_b16 a[56:57], v3 offset:768\n"
"ds_read_b64_tr_b16 a[58:59], v3 offset:4864\n"
"v_mfma_f32_16x16x32_bf16 v[220:223], a[60:63], a[248:251], v[220:223]\n"
".LLb_%=:\n"
"s_waitcnt vmcnt(33)\n"
"v_mfma_f32_16x16x32_bf16 v[128:131], a[0:3], a[128:131], v[128:131]\n"
"ds_read_b64_tr_b16 a[60:61], v3 offset:8960\n"
"ds_read_b64_tr_b16 a[62:63], v3 offset:13056\n"
"s_waitcnt vmcnt(26)\n"
"v_mfma_f32_16x16x32_bf16 v[128:131], a[4:7], a[144:147], v[128:131]\n"
"buffer_load_dwordx4 a[224:227], v4, s[44:47], s58 offen offset:2048 sc1\n"
"v_mfma_f32_16x16x32_bf16 v[160:163], a[0:3], a[132:135], v[160:163]\n"
"s_waitcnt vmcnt(25)\n"
"v_mfma_f32_16x16x32_bf16 v[160:163], a[4:7], a[148:151], v[160:163]\n"
"v_mfma_f32_16x16x32_bf16 v[192:195], a[0:3], a[136:139], v[192:195]\n"
"ds_read_b64_tr_b16 a[0:1], v2 offset:16384\n"
"ds_read_b64_tr_b16 a[2:3], v2 offset:20480\n"
"s_waitcnt vmcnt(23)\n"
"v_mfma_f32_16x16x32_bf16 v[192:195], a[4:7], a[152:155], v[192:195]\n"
"ds_read_b64_tr_b16 a[4:5], v2 offset:24576\n"
"ds_read_b64_tr_b16 a[6:7], v2 offset:28672\n"
"v_mfma_f32_16x16x32_bf16 v[132:135], a[8:11], a[128:131], v[132:135]\n"
"v_mfma_f32_16x16x32_bf16 v[132:135], a[12:15], a[144:147], v[132:135]\n"
"buffer_load_dwordx4 a[228:231], v4, s[44:47], s59 offen offset:2048 sc1\n"
"v_mfma_f32_16x16x32_bf16 v[164:167], a[8:11], a[132:135], v[164:167]\n"
"v_mfma_f32_16x16x32_bf16 v[164:167], a[12:15], a[148:151], v[164:167]\n"
"v_mfma_f32_16x16x32_bf16 v[196:199], a[8:11], a[136:139], v[196:199]\n"
"ds_read_b64_tr_b16 a[8:9], v3 offset:16384\n"
"ds_read_b64_tr_b16 a[10:11], v3 offset:20480\n"
"v_mfma_f32_16x16x32_bf16 v[196:199], a[12:15], a[152:155], v[196:199]\n"
"ds_read_b64_tr_b16 a[12:13], v3 offset:24576\n"
"ds_read_b64_tr_b16 a[14:15], v3 offset:28672\n"
"v_mfma_f32_16x16x32_bf16 v[136:139], a[16:19], a[128:131], v[136:139]\n"
"v_mfma_f32_16x16x32_bf16 v[136:139], a[20:23], a[144:147], v[136:139]\n"
"buffer_load_dwordx4 a[232:235], v4, s[44:47], s60 offen offset:2048 sc1\n"
"v_mfma_f32_16x16x32_bf16 v[168:171], a[16:19], a[132:135], v[168:171]\n"
"v_mfma_f32_16x16x32_bf16 v[168:171], a[20:23], a[148:151], v[168:171]\n"
"v_mfma_f32_16x16x32_bf16 v[200:203], a[16:19], a[136:139], v[200:203]\n"
"ds_read_b64_tr_b16 a[16:17], v2 offset:16640\n"
"ds_read_b64_tr_b16 a[18:19], v2 offset:20736\n"
"v_mfma_f32_16x16x32_bf16 v[200:203], a[20:23], a[152:155], v[200:203]\n"
"ds_read_b64_tr_b16 a[20:21], v2 offset:24832\n"
"ds_read_b64_tr_b16 a[22:23], v2 offset:28928\n"
"s_waitcnt lgkmcnt(15)\n"
"v_mfma_f32_16x16x32_bf16 v[140:143], a[24:27], a[128:131], v[140:143]\n"
"v_mfma_f32_16x16x32_bf16 v[140:143], a[28:31], a[144:147], v[140:143]\n"
"buffer_load_dwordx4 a[240:243], v4, s[44:47], s58 offen offset:3072 sc1\n"
"v_mfma_f32_16x16x32_bf16 v[172:175], a[24:27], a[132:135], v[172:175]\n"
"v_mfma_f32_16x16x32_bf16 v[172:175], a[28:31], a[148:151], v[172:175]\n"
"v_mfma_f32_16x16x32_bf16 v[204:207], a[24:27], a[136:139], v[204:207]\n"
"ds_read_b64_tr_b16 a[24:25], v3 offset:16640\n"
"ds_read_b64_tr_b16 a[26:27], v3 offset:20736\n"
"v_mfma_f32_16x16x32_bf16 v[204:207], a[28:31], a[152:155], v[204:207]\n"
"ds_read_b64_tr_b16 a[28:29], v3 offset:24832\n"
"ds_read_b64_tr_b16 a[30:31], v3 offset:28928\n"
"v_mfma_f32_16x16x32_bf16 v[144:147], a[32:35], a[128:131], v[144:147]\n"
"v_mfma_f32_16x16x32_bf16 v[144:147], a[36:39], a[144:147], v[144:147]\n"
"buffer_load_dwordx4 a[244:247], v4, s[44:47], s59 offen offset:3072 sc1\n"
"v_mfma_f32_16x16x32_bf16 v[176:179], a[32:35], a[132:135], v[176:179]\n"
"v_mfma_f32_16x16x32_bf16 v[176:179], a[36:39], a[148:151], v[176:179]\n"
"v_mfma_f32_16x16x32_bf16 v[208:211], a[32:35], a[136:139], v[208:211]\n"
"ds_read_b64_tr_b16 a[32:33], v2 offset:16896\n"
"ds_read_b64_tr_b16 a[34:35], v2 offset:20992\n"
"v_mfma_f32_16x16x32_bf16 v[208:211], a[36:39], a[152:155], v[208:211]\n"
"ds_read_b64_tr_b16 a[36:37], v2 offset:25088\n"
"ds_read_b64_tr_b16 a[38:39], v2 offset:29184\n"
"v_mfma_f32_16x16x32_bf16 v[148:151], a[40:43], a[128:131], v[148:151]\n"
"v_mfma_f32_16x16x32_bf16 v[148:151], a[44:47], a[144:147], v[148:151]\n"
"buffer_load_dwordx4 a[248:251], v4, s[44:47], s60 offen offset:3072 sc1\n"
"v_mfma_f32_16x16x32_bf16 v[180:183], a[40:43], a[132:135], v[180:183]\n"
"v_mfma_f32_16x16x32_bf16 v[180:183], a[44:47], a[148:151], v[180:183]\n"
"v_mfma_f32_16x16x32_bf16 v[212:215], a[40:43], a[136:139], v[212:215]\n"
"ds_read_b64_tr_b16 a[40:41], v3 offset:16896\n"
"ds_read_b64_tr_b16 a[42:43], v3 offset:20992\n"
"v_mfma_f32_16x16x32_bf16 v[212:215], a[44:47], a[152:155], v[212:215]\n"
"ds_read_b64_tr_b16 a[44:45], v3 offset:25088\n"
"ds_read_b64_tr_b16 a[46:47], v3 offset:29184\n"
"v_mfma_f32_16x16x32_bf16 v[152:155], a[48:51], a[128:131], v[152:155]\n"
"v_mfma_f32_16x16x32_bf16 v[152:155], a[52:55], a[144:147], v[152:155]\n"
"s_and_b32 s63, s65, 15\n"
"s_lshl_b32 s58, s63, 12\n"
"s_lshr_b32 s65, s65, 4\n"
"s_add_u32 s59, s58, 131072\n"
"s_add_u32 s60, s58, 262144\n"
"s_cmp_eq_u32 s62, 1\n"
"s_cselect_b32 s46, 0, s46\n"
"v_mfma_f32_16x16x32_bf16 v[184:187], a[48:51], a[132:135], v[184:187]\n"
"v_mfma_f32_16x16x32_bf16 v[184:187], a[52:55], a[148:151], v[184:187]\n"
"v_mfma_f32_16x16x32_bf16 v[216:219], a[48:51], a[136:139], v[216:219]\n"
"ds_read_b64_tr_b16 a[48:49], v2 offset:17152\n"
"ds_read_b64_tr_b16 a[50:51], v2 offset:21248\n"
"v_mfma_f32_16x16x32_bf16 v[216:219], a[52:55], a[152:155], v[216:219]\n"
"ds_read_b64_tr_b16 a[52:53], v2 offset:25344\n"
"ds_read_b64_tr_b16 a[54:55], v2 offset:29440\n"
"s_waitcnt lgkmcnt(15)\n"
"v_mfma_f32_16x16x32_bf16 v[156:159], a[56:59], a[128:131], v[156:159]\n"
"v_mfma_f32_16x16x32_bf16 v[156:159], a[60:63], a[144:147], v[156:159]\n"
"v_mfma_f32_16x16x32_bf16 v[188:191], a[56:59], a[132:135], v[188:191]\n"
"v_mfma_f32_16x16x32_bf16 v[188:191], a[60:63], a[148:151], v[188:191]\n"
"buffer_load_dwordx2 v[24:25], v13, s[36:39], %[ma] offen offset:128 sc1\n"
"buffer_load_dword v21, v13, s[36:39], %[xa] offen sc1\n"
"v_mfma_f32_16x16x32_bf16 v[220:223], a[56:59], a[136:139], v[220:223]\n"
"ds_read_b64_tr_b16 a[56:57], v3 offset:17152\n"
"ds_read_b64_tr_b16 a[58:59], v3 offset:21248\n"
"v_mfma_f32_16x16x32_bf16 v[220:223], a[60:63], a[152:155], v[220:223]\n"
"ds_read_b64_tr_b16 a[60:61], v3 offset:25344\n"
"ds_read_b64_tr_b16 a[62:63], v3 offset:29440\n"
"s_waitcnt vmcnt(27)\n"
"v_mfma_f32_16x16x32_bf16 v[128:131], a[0:3], a[160:163], v[128:131]\n"
"buffer_load_dwordx4 a[128:131], v4, s[44:47], s58 offen offset:0 sc1\n"
"s_waitcnt vmcnt(25)\n"
"v_mfma_f32_16x16x32_bf16 v[128:131], a[4:7], a[176:179], v[128:131]\n"
"v_mfma_f32_16x16x32_bf16 v[160:163], a[0:3], a[164:167], v[160:163]\n"
"s_waitcnt vmcnt(24)\n"
"v_mfma_f32_16x16x32_bf16 v[160:163], a[4:7], a[180:183], v[160:163]\n"
"s_waitcnt vmcnt(63)\n"
"s_barrier\n"
"v_xor_b32 v2, 0x10000, v2\n"
"v_xor_b32 v3, 0x10000, v3\n"
"v_mfma_f32_16x16x32_bf16 v[192:195], a[0:3], a[168:171], v[192:195]\n"
"s_add_u32 m0, s75, 0\n"
"s_and_b32 s76, s68, 15\n"
"s_lshl_b32 s76, s76, 17\n"
"s_lshr_b64 s[68:69], s[68:69], 4\n"
"buffer_load_dwordx4 v0, s[40:43], s76 offen offset:0 lds\n"
"s_waitcnt vmcnt(24)\n"
"v_mfma_f32_16x16x32_bf16 v[192:195], a[4:7], a[184:187], v[192:195]\n"
"buffer_load_dwordx4 a[132:135], v4, s[44:47], s59 offen offset:0 sc1\n"
"v_mfma_f32_16x16x32_bf16 v[132:135], a[8:11], a[160:163], v[132:135]\n"
"ds_read_b64_tr_b16 a[0:1], v2 offset:0\n"
"ds_read_b64_tr_b16 a[2:3], v2 offset:4096\n"
"v_mfma_f32_16x16x32_bf16 v[132:135], a[12:15], a[176:179], v[132:135]\n"
"ds_read_b64_tr_b16 a[4:5], v2 offset:8192\n"
"ds_read_b64_tr_b16 a[6:7], v2 offset:12288\n"
"v_mfma_f32_16x16x32_bf16 v[164:167], a[8:11], a[164:167], v[164:167]\n"
"v_mfma_f32_16x16x32_bf16 v[164:167], a[12:15], a[180:183], v[164:167]\n"
"s_add_u32 m0, s75, 0\n"
"s_add_u32 s76, s76, 3072\n"
"buffer_load_dwordx4 v1, s[40:43], s76 offen offset:1024 lds\n"
"v_mfma_f32_16x16x32_bf16 v[196:199], a[8:11], a[168:171], v[196:199]\n"
"ds_read_b64_tr_b16 a[8:9], v3 offset:0\n"
"ds_read_b64_tr_b16 a[10:11], v3 offset:4096\n"
"v_mfma_f32_16x16x32_bf16 v[196:199], a[12:15], a[184:187], v[196:199]\n"
"ds_read_b64_tr_b16 a[12:13], v3 offset:8192\n"
"ds_read_b64_tr_b16 a[14:15], v3 offset:12288\n"
"v_mfma_f32_16x16x32_bf16 v[136:139], a[16:19], a[160:163], v[136:139]\n"
"buffer_load_dwordx4 a[136:139], v4, s[44:47], s60 offen offset:0 sc1\n"
"v_mfma_f32_16x16x32_bf16 v[136:139], a[20:23], a[176:179], v[136:139]\n"
"v_mfma_f32_16x16x32_bf16 v[168:171], a[16:19], a[164:167], v[168:171]\n"
"s_add_u32 m0, s75, 0\n"
"s_add_u32 s76, s76, 3072\n"
"buffer_load_dwordx4 v0, s[40:43], s76 offen offset:2048 lds\n"
"v_mfma_f32_16x16x32_bf16 v[168:171], a[20:23], a[180:183], v[168:171]\n"
"v_mfma_f32_16x16x32_bf16 v[200:203], a[16:19], a[168:171], v[200:203]\n"
"ds_read_b64_tr_b16 a[16:17], v2 offset:256\n"
"ds_read_b64_tr_b16 a[18:19], v2 offset:4352\n"
"v_mfma_f32_16x16x32_bf16 v[200:203], a[20:23], a[184:187], v[200:203]\n"
"ds_read_b64_tr_b16 a[20:21], v2 offset:8448\n"
"ds_read_b64_tr_b16 a[22:23], v2 offset:12544\n"
"s_waitcnt lgkmcnt(15)\n"
"v_mfma_f32_16x16x32_bf16 v[140:143], a[24:27], a[160:163], v[140:143]\n"
"v_mfma_f32_16x16x32_bf16 v[140:143], a[28:31], a[176:179], v[140:143]\n"
"s_add_u32 m0, s75, 0\n"
"s_add_u32 s76, s76, 3072\n"
"buffer_load_dwordx4 v1, s[40:43], s76 offen offset:3072 lds\n"
"v_mfma_f32_16x16x32_bf16 v[172:175], a[24:27], a[164:167], v[172:175]\n"
"buffer_load_dwordx4 a[144:147], v4, s[44:47], s58 offen offset:1024 sc1\n"
"v_mfma_f32_16x16x32_bf16 v[172:175], a[28:31], a[180:183], v[172:175]\n"
"v_mfma_f32_16x16x32_bf16 v[204:207], a[24:27], a[168:171], v[204:207]\n"
"ds_read_b64_tr_b16 a[24:25], v3 offset:256\n"
"ds_read_b64_tr_b16 a[26:27], v3 offset:4352\n"
"v_mfma_f32_16x16x32_bf16 v[204:207], a[28:31], a[184:187], v[204:207]\n"
"ds_read_b64_tr_b16 a[28:29], v3 offset:8448\n"
"ds_read_b64_tr_b16 a[30:31], v3 offset:12544\n"
"v_mfma_f32_16x16x32_bf16 v[144:147], a[32:35], a[160:163], v[144:147]\n"
"s_add_u32 m0, s75, 16384\n"
"s_add_u32 s76, s76, 56320\n"
"buffer_load_dwordx4 v0, s[40:43], s76 offen offset:0 lds\n"
"v_mfma_f32_16x16x32_bf16 v[144:147], a[36:39], a[176:179], v[144:147]\n"
"v_mfma_f32_16x16x32_bf16 v[176:179], a[32:35], a[164:167], v[176:179]\n"
"buffer_load_dwordx4 a[148:151], v4, s[44:47], s59 offen offset:1024 sc1\n"
"v_mfma_f32_16x16x32_bf16 v[176:179], a[36:39], a[180:183], v[176:179]\n"
"v_mfma_f32_16x16x32_bf16 v[208:211], a[32:35], a[168:171], v[208:211]\n"
"ds_read_b64_tr_b16 a[32:33], v2 offset:512\n"
"ds_read_b64_tr_b16 a[34:35], v2 offset:4608\n"
"v_mfma_f32_16x16x32_bf16 v[208:211], a[36:39], a[184:187], v[208:211]\n"
"s_add_u32 m0, s75, 16384\n"
"s_add_u32 s76, s76, 3072\n"
"buffer_load_dwordx4 v1, s[40:43], s76 offen offset:1024 lds\n"
"v_mfma_f32_16x16x32_bf16 v[148:151], a[40:43], a[160:163], v[148:151]\n"
"ds_read_b64_tr_b16 a[36:37], v2 offset:8704\n"
"ds_read_b64_tr_b16 a[38:39], v2 offset:12800\n"
"v_mfma_f32_16x16x32_bf16 v[148:151], a[44:47], a[176:179], v[148:151]\n"
"v_mfma_f32_16x16x32_bf16 v[180:183], a[40:43], a[164:167], v[180:183]\n"
"v_mfma_f32_16x16x32_bf16 v[180:183], a[44:47], a[180:183], v[180:183]\n"
"buffer_load_dwordx4 a[152:155], v4, s[44:47], s60 offen offset:1024 sc1\n"
"v_mfma_f32_16x16x32_bf16 v[212:215], a[40:43], a[168:171], v[212:215]\n"
"s_add_u32 m0, s75, 16384\n"
"s_add_u32 s76, s76, 3072\n"
"buffer_load_dwordx4 v0, s[40:43], s76 offen offset:2048 lds\n"
"v_mfma_f32_16x16x32_bf16 v[212:215], a[44:47], a[184:187], v[212:215]\n"
"ds_read_b64_tr_b16 a[40:41], v3 offset:512\n"
"ds_read_b64_tr_b16 a[42:43], v3 offset:4608\n"
"v_mfma_f32_16x16x32_bf16 v[152:155], a[48:51], a[160:163], v[152:155]\n"
"ds_read_b64_tr_b16 a[44:45], v3 offset:8704\n"
"ds_read_b64_tr_b16 a[46:47], v3 offset:12800\n"
"v_mfma_f32_16x16x32_bf16 v[152:155], a[52:55], a[176:179], v[152:155]\n"
"v_mfma_f32_16x16x32_bf16 v[184:187], a[48:51], a[164:167], v[184:187]\n"
"v_mfma_f32_16x16x32_bf16 v[184:187], a[52:55], a[180:183], v[184:187]\n"
"s_add_u32 m0, s75, 16384\n"
"s_add_u32 s76, s76, 3072\n"
"buffer_load_dwordx4 v1, s[40:43], s76 offen offset:3072 lds\n"
"s_xor_b32 s75, s75, 0x8000\n"
"v_mfma_f32_16x16x32_bf16 v[216:219], a[48:51], a[168:171], v[216:219]\n"
"ds_read_b64_tr_b16 a[48:49], v2 offset:768\n"
"ds_read_b64_tr_b16 a[50:51], v2 offset:4864\n"
"v_mfma_f32_16x16x32_bf16 v[216:219], a[52:55], a[184:187], v[216:219]\n"
"ds_read_b64_tr_b16 a[52:53], v2 offset:8960\n"
"ds_read_b64_tr_b16 a[54:55], v2 offset:13056\n"
"s_waitcnt lgkmcnt(15)\n"
"v_mfma_f32_16x16x32_bf16 v[156:159], a[56:59], a[160:163], v[156:159]\n"
"v_mfma_f32_16x16x32_bf16 v[156:159], a[60:63], a[176:179], v[156:159]\n"
"v_mfma_f32_16x16x32_bf16 v[188:191], a[56:59], a[164:167], v[188:191]\n"
"v_mfma_f32_16x16x32_bf16 v[188:191], a[60:63], a[180:183], v[188:191]\n"
"v_mfma_f32_16x16x32_bf16 v[220:223], a[56:59], a[168:171], v[220:223]\n"
"ds_read_b64_tr_b16 a[56:57], v3 offset:768\n"
"ds_read_b64_tr_b16 a[58:59], v3 offset:4864\n"
"v_mfma_f32_16x16x32_bf16 v[220:223], a[60:63], a[184:187], v[220:223]\n"
"s_waitcnt vmcnt(35)\n"
"v_mfma_f32_16x16x32_bf16 v[128:131], a[0:3], a[192:195], v[128:131]\n"
"ds_read_b64_tr_b16 a[60:61], v3 offset:8960\n"
"ds_read_b64_tr_b16 a[62:63], v3 offset:13056\n"
"s_waitcnt vmcnt(28)\n"
"v_mfma_f32_16x16x32_bf16 v[128:131], a[4:7], a[208:211], v[128:131]\n"
"buffer_load_dwordx4 a[160:163], v4, s[44:47], s58 offen offset:2048 sc1\n"
"v_mfma_f32_16x16x32_bf16 v[160:163], a[0:3], a[196:199], v[160:163]\n"
"s_waitcnt vmcnt(27)\n"
"v_mfma_f32_16x16x32_bf16 v[160:163], a[4:7], a[212:215], v[160:163]\n"
"v_mfma_f32_16x16x32_bf16 v[192:195], a[0:3], a[200:203], v[192:195]\n"
"ds_read_b64_tr_b16 a[0:1], v2 offset:16384\n"
"ds_read_b64_tr_b16 a[2:3], v2 offset:20480\n"
"s_waitcnt vmcnt(25)\n"
"v_mfma_f32_16x16x32_bf16 v[192:195], a[4:7], a[216:219], v[192:195]\n"
"ds_read_b64_tr_b16 a[4:5], v2 offset:24576\n"
"ds_read_b64_tr_b16 a[6:7], v2 offset:28672\n"
"v_mfma_f32_16x16x32_bf16 v[132:135], a[8:11], a[192:195], v[132:135]\n"
"v_mfma_f32_16x16x32_bf16 v[132:135], a[12:15], a[208:211], v[132:135]\n"
"buffer_load_dwordx4 a[164:167], v4, s[44:47], s59 offen offset:2048 sc1\n"
"v_mfma_f32_16x16x32_bf16 v[164:167], a[8:11], a[196:199], v[164:167]\n"
"v_mfma_f32_16x16x32_bf16 v[164:167], a[12:15], a[212:215], v[164:167]\n"
"v_mfma_f32_16x16x32_bf16 v[196:199], a[8:11], a[200:203], v[196:199]\n"
"ds_read_b64_tr_b16 a[8:9], v3 offset:16384\n"
"ds_read_b64_tr_b16 a[10:11], v3 offset:20480\n"
"v_mfma_f32_16x16x32_bf16 v[196:199], a[12:15], a[216:219], v[196:199]\n"
"ds_read_b64_tr_b16 a[12:13], v3 offset:24576\n"
"ds_read_b64_tr_b16 a[14:15], v3 offset:28672\n"
"v_mfma_f32_16x16x32_bf16 v[136:139], a[16:19], a[192:195], v[136:139]\n"
"v_mfma_f32_16x16x32_bf16 v[136:139], a[20:23], a[208:211], v[136:139]\n"
"buffer_load_dwordx4 a[168:171], v4, s[44:47], s60 offen offset:2048 sc1\n"
"v_mfma_f32_16x16x32_bf16 v[168:171], a[16:19], a[196:199], v[168:171]\n"
"v_mfma_f32_16x16x32_bf16 v[168:171], a[20:23], a[212:215], v[168:171]\n"
"v_mfma_f32_16x16x32_bf16 v[200:203], a[16:19], a[200:203], v[200:203]\n"
"ds_read_b64_tr_b16 a[16:17], v2 offset:16640\n"
"ds_read_b64_tr_b16 a[18:19], v2 offset:20736\n"
"v_mfma_f32_16x16x32_bf16 v[200:203], a[20:23], a[216:219], v[200:203]\n"
"ds_read_b64_tr_b16 a[20:21], v2 offset:24832\n"
"ds_read_b64_tr_b16 a[22:23], v2 offset:28928\n"
"s_waitcnt lgkmcnt(15)\n"
"v_mfma_f32_16x16x32_bf16 v[140:143], a[24:27], a[192:195], v[140:143]\n"
"v_mfma_f32_16x16x32_bf16 v[140:143], a[28:31], a[208:211], v[140:143]\n"
"buffer_load_dwordx4 a[176:179], v4, s[44:47], s58 offen offset:3072 sc1\n"
"v_mfma_f32_16x16x32_bf16 v[172:175], a[24:27], a[196:199], v[172:175]\n"
"v_mfma_f32_16x16x32_bf16 v[172:175], a[28:31], a[212:215], v[172:175]\n"
"v_mfma_f32_16x16x32_bf16 v[204:207], a[24:27], a[200:203], v[204:207]\n"
"ds_read_b64_tr_b16 a[24:25], v3 offset:16640\n"
"ds_read_b64_tr_b16 a[26:27], v3 offset:20736\n"
"v_mfma_f32_16x16x32_bf16 v[204:207], a[28:31], a[216:219], v[204:207]\n"
"ds_read_b64_tr_b16 a[28:29], v3 offset:24832\n"
"ds_read_b64_tr_b16 a[30:31], v3 offset:28928\n"
"v_mfma_f32_16x16x32_bf16 v[144:147], a[32:35], a[192:195], v[144:147]\n"
"v_mfma_f32_16x16x32_bf16 v[144:147], a[36:39], a[208:211], v[144:147]\n"
"buffer_load_dwordx4 a[180:183], v4, s[44:47], s59 offen offset:3072 sc1\n"
"v_mfma_f32_16x16x32_bf16 v[176:179], a[32:35], a[196:199], v[176:179]\n"
"v_mfma_f32_16x16x32_bf16 v[176:179], a[36:39], a[212:215], v[176:179]\n"
"v_mfma_f32_16x16x32_bf16 v[208:211], a[32:35], a[200:203], v[208:211]\n"
"ds_read_b64_tr_b16 a[32:33], v2 offset:16896\n"
"ds_read_b64_tr_b16 a[34:35], v2 offset:20992\n"
"v_mfma_f32_16x16x32_bf16 v[208:211], a[36:39], a[216:219], v[208:211]\n"
"ds_read_b64_tr_b16 a[36:37], v2 offset:25088\n"
"ds_read_b64_tr_b16 a[38:39], v2 offset:29184\n"
"v_mfma_f32_16x16x32_bf16 v[148:151], a[40:43], a[192:195], v[148:151]\n"
"v_mfma_f32_16x16x32_bf16 v[148:151], a[44:47], a[208:211], v[148:151]\n"
"buffer_load_dwordx4 a[184:187], v4, s[44:47], s60 offen offset:3072 sc1\n"
"v_mfma_f32_16x16x32_bf16 v[180:183], a[40:43], a[196:199], v[180:183]\n"
"v_mfma_f32_16x16x32_bf16 v[180:183], a[44:47], a[212:215], v[180:183]\n"
"v_mfma_f32_16x16x32_bf16 v[212:215], a[40:43], a[200:203], v[212:215]\n"
"ds_read_b64_tr_b16 a[40:41], v3 offset:16896\n"
"ds_read_b64_tr_b16 a[42:43], v3 offset:20992\n"
"v_mfma_f32_16x16x32_bf16 v[212:215], a[44:47], a[216:219], v[212:215]\n"
"ds_read_b64_tr_b16 a[44:45], v3 offset:25088\n"
"ds_read_b64_tr_b16 a[46:47], v3 offset:29184\n"
"v_mfma_f32_16x16x32_bf16 v[152:155], a[48:51], a[192:195], v[152:155]\n"
"v_mfma_f32_16x16x32_bf16 v[152:155], a[52:55], a[208:211], v[152:155]\n"
"s_and_b32 s63, s65, 15\n"
"s_lshl_b32 s58, s63, 12\n"
"s_lshr_b32 s65, s65, 4\n"
"s_add_u32 s59, s58, 131072\n"
"s_add_u32 s60, s58, 262144\n"
"s_waitcnt vmcnt(21)\n"
"v_cmp_ne_u32 vcc, v24, v19\n"
"s_nop 1\n"
"s_mov_b64 s[78:79], vcc\n"
"v_cmp_ne_u32 vcc, v25, v18\n"
"s_cmp_eq_u32 s62, 3\n"
"s_cselect_b64 s[70:71], -1, 0\n"
"s_or_b64 s[78:79], s[78:79], vcc\n"
"s_and_b64 s[78:79], s[78:79], s[70:71]\n"
"s_cbranch_scc1 .Ls1b_%=\n"
".Lr1b_%=:\n"
"v_mfma_f32_16x16x32_bf16 v[184:187], a[48:51], a[196:199], v[184:187]\n"
"v_mfma_f32_16x16x32_bf16 v[184:187], a[52:55], a[212:215], v[184:187]\n"
"v_mfma_f32_16x16x32_bf16 v[216:219], a[48:51], a[200:203], v[216:219]\n"
"ds_read_b64_tr_b16 a[48:49], v2 offset:17152\n"
"ds_read_b64_tr_b16 a[50:51], v2 offset:21248\n"
"v_mfma_f32_16x16x32_bf16 v[216:219], a[52:55], a[216:219], v[216:219]\n"
"ds_read_b64_tr_b16 a[52:53], v2 offset:25344\n"
"ds_read_b64_tr_b16 a[54:55], v2 offset:29440\n"
"s_waitcnt lgkmcnt(15)\n"
"v_mfma_f32_16x16x32_bf16 v[156:159], a[56:59], a[192:195], v[156:159]\n"
"v_mfma_f32_16x16x32_bf16 v[156:159], a[60:63], a[208:211], v[156:159]\n"
"v_mfma_f32_16x16x32_bf16 v[188:191], a[56:59], a[196:199], v[188:191]\n"
"v_mfma_f32_16x16x32_bf16 v[188:191], a[60:63], a[212:215], v[188:191]\n"
"v_mfma_f32_16x16x32_bf16 v[220:223], a[56:59], a[200:203], v[220:223]\n"
"ds_read_b64_tr_b16 a[56:57], v3 offset:17152\n"
"ds_read_b64_tr_b16 a[58:59], v3 offset:21248\n"
"v_mfma_f32_16x16x32_bf16 v[220:223], a[60:63], a[216:219], v[220:223]\n"
"ds_read_b64_tr_b16 a[60:61], v3 offset:25344\n"
"ds_read_b64_tr_b16 a[62:63], v3 offset:29440\n"
"v_mfma_f32_16x16x32_bf16 v[128:131], a[0:3], a[224:227], v[128:131]\n"
"buffer_load_dwordx4 a[192:195], v4, s[44:47], s58 offen offset:0 sc1\n"
"v_mfma_f32_16x16x32_bf16 v[128:131], a[4:7], a[240:243], v[128:131]\n"
"v_mfma_f32_16x16x32_bf16 v[160:163], a[0:3], a[228:231], v[160:163]\n"
"v_mfma_f32_16x16x32_bf16 v[160:163], a[4:7], a[244:247], v[160:163]\n"
"s_waitcnt vmcnt(63)\n"
"s_barrier\n"
"v_xor_b32 v2, 0x8000, v2\n"
"v_xor_b32 v3, 0x8000, v3\n"
"v_mfma_f32_16x16x32_bf16 v[192:195], a[0:3], a[232:235], v[192:195]\n"
"s_add_u32 m0, s75, 0\n"
"s_and_b32 s76, s68, 15\n"
"s_lshl_b32 s76, s76, 17\n"
"s_lshr_b64 s[68:69], s[68:69], 4\n"
"buffer_load_dwordx4 v0, s[40:43], s76 offen offset:0 lds\n"
"v_mfma_f32_16x16x32_bf16 v[192:195], a[4:7], a[248:251], v[192:195]\n"
"buffer_load_dwordx4 a[196:199], v4, s[44:47], s59 offen offset:0 sc1\n"
"v_mfma_f32_16x16x32_bf16 v[132:135], a[8:11], a[224:227], v[132:135]\n"
"ds_read_b64_tr_b16 a[0:1], v2 offset:0\n"
"ds_read_b64_tr_b16 a[2:3], v2 offset:4096\n"
"v_mfma_f32_16x16x32_bf16 v[132:135], a[12:15], a[240:243], v[132:135]\n"
"ds_read_b64_tr_b16 a[4:5], v2 offset:8192\n"
"ds_read_b64_tr_b16 a[6:7], v2 offset:12288\n"
"v_mfma_f32_16x16x32_bf16 v[164:167], a[8:11], a[228:231], v[164:167]\n"
"v_mfma_f32_16x16x32_bf16 v[164:167], a[12:15], a[244:247], v[164:167]\n"
"s_add_u32 m0, s75, 0\n"
"s_add_u32 s76, s76, 3072\n"
"buffer_load_dwordx4 v1, s[40:43], s76 offen offset:1024 lds\n"
"v_mfma_f32_16x16x32_bf16 v[196:199], a[8:11], a[232:235], v[196:199]\n"
"ds_read_b64_tr_b16 a[8:9], v3 offset:0\n"
"ds_read_b64_tr_b16 a[10:11], v3 offset:4096\n"
"v_mfma_f32_16x16x32_bf16 v[196:199], a[12:15], a[248:251], v[196:199]\n"
"ds_read_b64_tr_b16 a[12:13], v3 offset:8192\n"
"ds_read_b64_tr_b16 a[14:15], v3 offset:12288\n"
"v_mfma_f32_16x16x32_bf16 v[136:139], a[16:19], a[224:227], v[136:139]\n"
"buffer_load_dwordx4 a[200:203], v4, s[44:47], s60 offen offset:0 sc1\n"
"v_mfma_f32_16x16x32_bf16 v[136:139], a[20:23], a[240:243], v[136:139]\n"
"v_mfma_f32_16x16x32_bf16 v[168:171], a[16:19], a[228:231], v[168:171]\n"
"s_add_u32 m0, s75, 0\n"
"s_add_u32 s76, s76, 3072\n"
"buffer_load_dwordx4 v0, s[40:43], s76 offen offset:2048 lds\n"
"v_mfma_f32_16x16x32_bf16 v[168:171], a[20:23], a[244:247], v[168:171]\n"
"v_mfma_f32_16x16x32_bf16 v[200:203], a[16:19], a[232:235], v[200:203]\n"
"ds_read_b64_tr_b16 a[16:17], v2 offset:256\n"
"ds_read_b64_tr_b16 a[18:19], v2 offset:4352\n"
"v_mfma_f32_16x16x32_bf16 v[200:203], a[20:23], a[248:251], v[200:203]\n"
"ds_read_b64_tr_b16 a[20:21], v2 offset:8448\n"
"ds_read_b64_tr_b16 a[22:23], v2 offset:12544\n"
"s_waitcnt lgkmcnt(15)\n"
"v_mfma_f32_16x16x32_bf16 v[140:143], a[24:27], a[224:227], v[140:143]\n"
"v_mfma_f32_16x16x32_bf16 v[140:143], a[28:31], a[240:243], v[140:143]\n"
"s_add_u32 m0, s75, 0\n"
"s_add_u32 s76, s76, 3072\n"
"buffer_load_dwordx4 v1, s[40:43], s76 offen offset:3072 lds\n"
"v_mfma_f32_16x16x32_bf16 v[172:175], a[24:27], a[228:231], v[172:175]\n"
"buffer_load_dwordx4 a[208:211], v4, s[44:47], s58 offen offset:1024 sc1\n"
"v_mfma_f32_16x16x32_bf16 v[172:175], a[28:31], a[244:247], v[172:175]\n"
"v_mfma_f32_16x16x32_bf16 v[204:207], a[24:27], a[232:235], v[204:207]\n"
"ds_read_b64_tr_b16 a[24:25], v3 offset:256\n"
"ds_read_b64_tr_b16 a[26:27], v3 offset:4352\n"
"v_mfma_f32_16x16x32_bf16 v[204:207], a[28:31], a[248:251], v[204:207]\n"
"ds_read_b64_tr_b16 a[28:29], v3 offset:8448\n"
"ds_read_b64_tr_b16 a[30:31], v3 offset:12544\n"
"v_mfma_f32_16x16x32_bf16 v[144:147], a[32:35], a[224:227], v[144:147]\n"
"s_add_u32 m0, s75, 16384\n"
"s_add_u32 s76, s76, 56320\n"
"buffer_load_dwordx4 v0, s[40:43], s76 offen offset:0 lds\n"
"v_mfma_f32_16x16x32_bf16 v[144:147], a[36:39], a[240:243], v[144:147]\n"
"v_mfma_f32_16x16x32_bf16 v[176:179], a[32:35], a[228:231], v[176:179]\n"
"buffer_load_dwordx4 a[212:215], v4, s[44:47], s59 offen offset:1024 sc1\n"
"v_mfma_f32_16x16x32_bf16 v[176:179], a[36:39], a[244:247], v[176:179]\n"
"v_mfma_f32_16x16x32_bf16 v[208:211], a[32:35], a[232:235], v[208:211]\n"
"ds_read_b64_tr_b16 a[32:33], v2 offset:512\n"
"ds_read_b64_tr_b16 a[34:35], v2 offset:4608\n"
"v_mfma_f32_16x16x32_bf16 v[208:211], a[36:39], a[248:251], v[208:211]\n"
"s_add_u32 m0, s75, 16384\n"
"s_add_u32 s76, s76, 3072\n"
"buffer_load_dwordx4 v1, s[40:43], s76 offen offset:1024 lds\n"
"v_mfma_f32_16x16x32_bf16 v[148:151], a[40:43], a[224:227], v[148:151]\n"
"ds_read_b64_tr_b16 a[36:37], v2 offset:8704\n"
"ds_read_b64_tr_b16 a[38:39], v2 offset:12800\n"
"v_mfma_f32_16x16x32_bf16 v[148:151], a[44:47], a[240:243], v[148:151]\n"
"v_mfma_f32_16x16x32_bf16 v[180:183], a[40:43], a[228:231], v[180:183]\n"
"v_mfma_f32_16x16x32_bf16 v[180:183], a[44:47], a[244:247], v[180:183]\n"
"buffer_load_dwordx4 a[216:219], v4, s[44:47], s60 offen offset:1024 sc1\n"
"v_mfma_f32_16x16x32_bf16 v[212:215], a[40:43], a[232:235], v[212:215]\n"
"s_add_u32 m0, s75, 16384\n"
"s_add_u32 s76, s76, 3072\n"
"buffer_load_dwordx4 v0, s[40:43], s76 offen offset:2048 lds\n"
"v_mfma_f32_16x16x32_bf16 v[212:215], a[44:47], a[248:251], v[212:215]\n"
"ds_read_b64_tr_b16 a[40:41], v3 offset:512\n"
"ds_read_b64_tr_b16 a[42:43], v3 offset:4608\n"
"v_mfma_f32_16x16x32_bf16 v[152:155], a[48:51], a[224:227], v[152:155]\n"
"ds_read_b64_tr_b16 a[44:45], v3 offset:8704\n"
"ds_read_b64_tr_b16 a[46:47], v3 offset:12800\n"
"v_mfma_f32_16x16x32_bf16 v[152:155], a[52:55], a[240:243], v[152:155]\n"
"v_mfma_f32_16x16x32_bf16 v[184:187], a[48:51], a[228:231], v[184:187]\n"
"v_mfma_f32_16x16x32_bf16 v[184:187], a[52:55], a[244:247], v[184:187]\n"
"s_add_u32 m0, s75, 16384\n"
"s_add_u32 s76, s76, 3072\n"
"buffer_load_dwordx4 v1, s[40:43], s76 offen offset:3072 lds\n"
"s_xor_b32 s75, s75, 0x10000\n"
"v_mfma_f32_16x16x32_bf16 v[216:219], a[48:51], a[232:235], v[216:219]\n"
"ds_read_b64_tr_b16 a[48:49], v2 offset:768\n"
"ds_read_b64_tr_b16 a[50:51], v2 offset:4864\n"
"v_mfma_f32_16x16x32_bf16 v[216:219], a[52:55], a[248:251], v[216:219]\n"
"ds_read_b64_tr_b16 a[52:53], v2 offset:8960\n"
"ds_read_b64_tr_b16 a[54:55], v2 offset:13056\n"
"s_waitcnt lgkmcnt(15)\n"
"v_mfma_f32_16x16x32_bf16 v[156:159], a[56:59], a[224:227], v[156:159]\n"
"v_mfma_f32_16x16x32_bf16 v[156:159], a[60:63], a[240:243], v[156:159]\n"
"v_mfma_f32_16x16x32_bf16 v[188:191], a[56:59], a[228:231], v[188:191]\n"
"v_mfma_f32_16x16x32_bf16 v[188:191], a[60:63], a[244:247], v[188:191]\n"
"v_mfma_f32_16x16x32_bf16 v[220:223], a[56:59], a[232:235], v[220:223]\n"
"ds_read_b64_tr_b16 a[56:57], v3 offset:768\n"
"ds_read_b64_tr_b16 a[58:59], v3 offset:4864\n"
"v_mfma_f32_16x16x32_bf16 v[220:223], a[60:63], a[248:251], v[220:223]\n"
"s_sub_u32 s62, s62, 1\n"
"s_cmp_lg_u32 s62, 0\n"
"s_cbranch_scc1 .LLb_%=\n"
"v_cvt_pk_bf16_f32 v136, v136, v137\n"
"v_cvt_pk_bf16_f32 v137, v138, v139\n"
"v_cvt_pk_bf16_f32 v138, v140, v141\n"
"v_cvt_pk_bf16_f32 v139, v142, v143\n"
"buffer_store_dwordx4 v[136:139], v6, s[36:39], s52 offen offset:0 sc1\n"
"v_cvt_pk_bf16_f32 v144, v144, v145\n"
"v_cvt_pk_bf16_f32 v145, v146, v147\n"
"v_cvt_pk_bf16_f32 v146, v148, v149\n"
"v_cvt_pk_bf16_f32 v147, v150, v151\n"
"buffer_store_dwordx4 v[144:147], v6, s[36:39], s53 offen offset:0 sc1\n"
"v_cvt_pk_bf16_f32 v152, v152, v153\n"
"v_cvt_pk_bf16_f32 v153, v154, v155\n"
"v_cvt_pk_bf16_f32 v154, v156, v157\n"
"v_cvt_pk_bf16_f32 v155, v158, v159\n"
"buffer_store_dwordx4 v[152:155], v6, s[36:39], s54 offen offset:0 sc1\n"
"v_cvt_pk_bf16_f32 v168, v168, v169\n"
"v_cvt_pk_bf16_f32 v169, v170, v171\n"
"v_cvt_pk_bf16_f32 v170, v172, v173\n"
"v_cvt_pk_bf16_f32 v171, v174, v175\n"
"buffer_store_dwordx4 v[168:171], v6, s[36:39], s52 offen offset:1024 sc1\n"
"v_cvt_pk_bf16_f32 v176, v176, v177\n"
"v_cvt_pk_bf16_f32 v177, v178, v179\n"
"v_cvt_pk_bf16_f32 v178, v180, v181\n"
"v_cvt_pk_bf16_f32 v179, v182, v183\n"
"buffer_store_dwordx4 v[176:179], v6, s[36:39], s53 offen offset:1024 sc1\n"
"v_cvt_pk_bf16_f32 v184, v184, v185\n"
"v_cvt_pk_bf16_f32 v185, v186, v187\n"
"v_cvt_pk_bf16_f32 v186, v188, v189\n"
"v_cvt_pk_bf16_f32 v187, v190, v191\n"
"buffer_store_dwordx4 v[184:187], v6, s[36:39], s54 offen offset:1024 sc1\n"
"v_cvt_pk_bf16_f32 v200, v200, v201\n"
"v_cvt_pk_bf16_f32 v201, v202, v203\n"
"v_cvt_pk_bf16_f32 v202, v204, v205\n"
"v_cvt_pk_bf16_f32 v203, v206, v207\n"
"buffer_store_dwordx4 v[200:203], v6, s[36:39], s52 offen offset:2048 sc1\n"
"v_cvt_pk_bf16_f32 v208, v208, v209\n"
"v_cvt_pk_bf16_f32 v209, v210, v211\n"
"v_cvt_pk_bf16_f32 v210, v212, v213\n"
"v_cvt_pk_bf16_f32 v211, v214, v215\n"
"buffer_store_dwordx4 v[208:211], v6, s[36:39], s53 offen offset:2048 sc1\n"
"v_cvt_pk_bf16_f32 v216, v216, v217\n"
"v_cvt_pk_bf16_f32 v217, v218, v219\n"
"v_cvt_pk_bf16_f32 v218, v220, v221\n"
"v_cvt_pk_bf16_f32 v219, v222, v223\n"
"buffer_store_dwordx4 v[216:219], v6, s[36:39], s54 offen offset:2048 sc1\n"
"s_waitcnt vmcnt(0)\n"
"s_mov_b64 exec, 15\n"
"buffer_store_dwordx2 v[20:21], v8, s[36:39], %[xw] offen sc1\n"
"s_mov_b64 exec, -1\n"
"s_mov_b32 s64, 0x200000\n"
".Lpxb_%=:\n"
"buffer_load_dwordx2 v[22:23], v13, s[36:39], %[xa] offen offset:0 sc1\n"
"s_waitcnt vmcnt(0)\n"
"v_cmp_ne_u32 vcc, v22, v21\n"
"s_nop 1\n"
"s_cmp_eq_u64 vcc, 0\n"
"s_cbranch_scc1 .Lexb_%=\n"
"v_mov_b32 v21, v22\n"
"s_mov_b64 exec, 15\n"
"buffer_store_dwordx2 v[20:21], v8, s[36:39], %[xw] offen offset:0 sc1\n"
"s_mov_b64 exec, -1\n"
".Lexb_%=:\n"
"v_cmp_ne_u32 vcc, v23, v20\n"
"s_nop 1\n"
"s_cmp_eq_u64 vcc, 0\n"
"s_cbranch_scc1 .Lqxb_%=\n"
"s_sleep 1\n"
"s_sub_u32 s64, s64, 1\n"
"s_cmp_lg_u32 s64, 0\n"
"s_cbranch_scc1 .Lpxb_%=\n"
".Lqxb_%=:\n"
"s_waitcnt vmcnt(0)\n"
"buffer_load_dwordx4 v[136:139], v6, s[36:39], s55 offen offset:0 sc1\n"
"buffer_load_dwordx4 v[144:147], v6, s[36:39], s66 offen offset:0 sc1\n"
"buffer_load_dwordx4 v[152:155], v6, s[36:39], s67 offen offset:0 sc1\n"
"buffer_load_dwordx4 v[168:171], v6, s[36:39], s55 offen offset:1024 sc1\n"
"buffer_load_dwordx4 v[176:179], v6, s[36:39], s66 offen offset:1024 sc1\n"
"buffer_load_dwordx4 v[184:187], v6, s[36:39], s67 offen offset:1024 sc1\n"
"buffer_load_dwordx4 v[200:203], v6, s[36:39], s55 offen offset:2048 sc1\n"
"buffer_load_dwordx4 v[208:211], v6, s[36:39], s66 offen offset:2048 sc1\n"
"buffer_load_dwordx4 v[216:219], v6, s[36:39], s67 offen offset:2048 sc1\n"
"s_waitcnt vmcnt(8)\n"
"v_lshlrev_b32 v140, 16, v136\n"
"v_and_b32 v141, 0xffff0000, v136\n"
"v_add_f32 v128, v128, v140\n"
"v_add_f32 v129, v129, v141\n"
"v_lshlrev_b32 v142, 16, v137\n"
"v_and_b32 v143, 0xffff0000, v137\n"
"v_add_f32 v130, v130, v142\n"
"v_add_f32 v131, v131, v143\n"
"v_lshlrev_b32 v140, 16, v138\n"
"v_and_b32 v141, 0xffff0000, v138\n"
"v_add_f32 v132, v132, v140\n"
"v_add_f32 v133, v133, v141\n"
"v_lshlrev_b32 v142, 16, v139\n"
"v_and_b32 v143, 0xffff0000, v139\n"
"v_add_f32 v134, v134, v142\n"
"v_add_f32 v135, v135, v143\n"
"s_waitcnt vmcnt(7)\n"
"v_lshlrev_b32 v148, 16, v144\n"
"v_and_b32 v149, 0xffff0000, v144\n"
"v_add_f32 v128, v128, v148\n"
"v_add_f32 v129, v129, v149\n"
"v_lshlrev_b32 v150, 16, v145\n"
"v_and_b32 v151, 0xffff0000, v145\n"
"v_add_f32 v130, v130, v150\n"
"v_add_f32 v131, v131, v151\n"
"v_lshlrev_b32 v148, 16, v146\n"
"v_and_b32 v149, 0xffff0000, v146\n"
"v_add_f32 v132, v132, v148\n"
"v_add_f32 v133, v133, v149\n"
"v_lshlrev_b32 v150, 16, v147\n"
"v_and_b32 v151, 0xffff0000, v147\n"
"v_add_f32 v134, v134, v150\n"
"v_add_f32 v135, v135, v151\n"
"s_waitcnt vmcnt(6)\n"
"v_lshlrev_b32 v156, 16, v152\n"
"v_and_b32 v157, 0xffff0000, v152\n"
"v_add_f32 v128, v128, v156\n"
"v_add_f32 v129, v129, v157\n"
"v_lshlrev_b32 v158, 16, v153\n"
"v_and_b32 v159, 0xffff0000, v153\n"
"v_add_f32 v130, v130, v158\n"
"v_add_f32 v131, v131, v159\n"
"v_lshlrev_b32 v156, 16, v154\n"
"v_and_b32 v157, 0xffff0000, v154\n"
"v_add_f32 v132, v132, v156\n"
"v_add_f32 v133, v133, v157\n"
"v_lshlrev_b32 v158, 16, v155\n"
"v_and_b32 v159, 0xffff0000, v155\n"
"v_add_f32 v134, v134, v158\n"
"v_add_f32 v135, v135, v159\n"
"v_cvt_pk_bf16_f32 v128, v128, v129\n"
"v_cvt_pk_bf16_f32 v129, v130, v131\n"
"v_cvt_pk_bf16_f32 v130, v132, v133\n"
"v_cvt_pk_bf16_f32 v131, v134, v135\n"
"s_mov_b32 s63, 0\n"
"buffer_store_dwordx2 v[128:129], v5, s[48:51], s63 offen offset:0\n"
"buffer_store_dwordx2 v[130:131], v5, s[48:51], s63 offen offset:32\n"
"s_waitcnt vmcnt(7)\n"
"v_lshlrev_b32 v172, 16, v168\n"
"v_and_b32 v173, 0xffff0000, v168\n"
"v_add_f32 v160, v160, v172\n"
"v_add_f32 v161, v161, v173\n"
"v_lshlrev_b32 v174, 16, v169\n"
"v_and_b32 v175, 0xffff0000, v169\n"
"v_add_f32 v162, v162, v174\n"
"v_add_f32 v163, v163, v175\n"
"v_lshlrev_b32 v172, 16, v170\n"
"v_and_b32 v173, 0xffff0000, v170\n"
"v_add_f32 v164, v164, v172\n"
"v_add_f32 v165, v165, v173\n"
"v_lshlrev_b32 v174, 16, v171\n"
"v_and_b32 v175, 0xffff0000, v171\n"
"v_add_f32 v166, v166, v174\n"
"v_add_f32 v167, v167, v175\n"
"s_waitcnt vmcnt(6)\n"
"v_lshlrev_b32 v180, 16, v176\n"
"v_and_b32 v181, 0xffff0000, v176\n"
"v_add_f32 v160, v160, v180\n"
"v_add_f32 v161, v161, v181\n"
"v_lshlrev_b32 v182, 16, v177\n"
"v_and_b32 v183, 0xffff0000, v177\n"
"v_add_f32 v162, v162, v182\n"
"v_add_f32 v163, v163, v183\n"
"v_lshlrev_b32 v180, 16, v178\n"
"v_and_b32 v181, 0xffff0000, v178\n"
"v_add_f32 v164, v164, v180\n"
"v_add_f32 v165, v165, v181\n"
"v_lshlrev_b32 v182, 16, v179\n"
"v_and_b32 v183, 0xffff0000, v179\n"
"v_add_f32 v166, v166, v182\n"
"v_add_f32 v167, v167, v183\n"
"s_waitcnt vmcnt(5)\n"
"v_lshlrev_b32 v188, 16, v184\n"
"v_and_b32 v189, 0xffff0000, v184\n"
"v_add_f32 v160, v160, v188\n"
"v_add_f32 v161, v161, v189\n"
"v_lshlrev_b32 v190, 16, v185\n"
"v_and_b32 v191, 0xffff0000, v185\n"
"v_add_f32 v162, v162, v190\n"
"v_add_f32 v163, v163, v191\n"
"v_lshlrev_b32 v188, 16, v186\n"
"v_and_b32 v189, 0xffff0000, v186\n"
"v_add_f32 v164, v164, v188\n"
"v_add_f32 v165, v165, v189\n"
"v_lshlrev_b32 v190, 16, v187\n"
"v_and_b32 v191, 0xffff0000, v187\n"
"v_add_f32 v166, v166, v190\n"
"v_add_f32 v167, v167, v191\n"
"v_cvt_pk_bf16_f32 v160, v160, v161\n"
"v_cvt_pk_bf16_f32 v161, v162, v163\n"
"v_cvt_pk_bf16_f32 v162, v164, v165\n"
"v_cvt_pk_bf16_f32 v163, v166, v167\n"
"s_mov_b32 s63, 32768\n"
"buffer_store_dwordx2 v[160:161], v5, s[48:51], s63 offen offset:0\n"
"buffer_store_dwordx2 v[162:163], v5, s[48:51], s63 offen offset:32\n"
"s_waitcnt vmcnt(6)\n"
"v_lshlrev_b32 v204, 16, v200\n"
"v_and_b32 v205, 0xffff0000, v200\n"
"v_add_f32 v192, v192, v204\n"
"v_add_f32 v193, v193, v205\n"
"v_lshlrev_b32 v206, 16, v201\n"
"v_and_b32 v207, 0xffff0000, v201\n"
"v_add_f32 v194, v194, v206\n"
"v_add_f32 v195, v195, v207\n"
"v_lshlrev_b32 v204, 16, v202\n"
"v_and_b32 v205, 0xffff0000, v202\n"
"v_add_f32 v196, v196, v204\n"
"v_add_f32 v197, v197, v205\n"
"v_lshlrev_b32 v206, 16, v203\n"
"v_and_b32 v207, 0xffff0000, v203\n"
"v_add_f32 v198, v198, v206\n"
"v_add_f32 v199, v199, v207\n"
"s_waitcnt vmcnt(5)\n"
"v_lshlrev_b32 v212, 16, v208\n"
"v_and_b32 v213, 0xffff0000, v208\n"
"v_add_f32 v192, v192, v212\n"
"v_add_f32 v193, v193, v213\n"
"v_lshlrev_b32 v214, 16, v209\n"
"v_and_b32 v215, 0xffff0000, v209\n"
"v_add_f32 v194, v194, v214\n"
"v_add_f32 v195, v195, v215\n"
"v_lshlrev_b32 v212, 16, v210\n"
"v_and_b32 v213, 0xffff0000, v210\n"
"v_add_f32 v196, v196, v212\n"
"v_add_f32 v197, v197, v213\n"
"v_lshlrev_b32 v214, 16, v211\n"
"v_and_b32 v215, 0xffff0000, v211\n"
"v_add_f32 v198, v198, v214\n"
"v_add_f32 v199, v199, v215\n"
"s_waitcnt vmcnt(4)\n"
"v_lshlrev_b32 v220, 16, v216\n"
"v_and_b32 v221, 0xffff0000, v216\n"
"v_add_f32 v192, v192, v220\n"
"v_add_f32 v193, v193, v221\n"
"v_lshlrev_b32 v222, 16, v217\n"
"v_and_b32 v223, 0xffff0000, v217\n"
"v_add_f32 v194, v194, v222\n"
"v_add_f32 v195, v195, v223\n"
"v_lshlrev_b32 v220, 16, v218\n"
"v_and_b32 v221, 0xffff0000, v218\n"
"v_add_f32 v196, v196, v220\n"
"v_add_f32 v197, v197, v221\n"
"v_lshlrev_b32 v222, 16, v219\n"
"v_and_b32 v223, 0xffff0000, v219\n"
"v_add_f32 v198, v198, v222\n"
"v_add_f32 v199, v199, v223\n"
"v_cvt_pk_bf16_f32 v192, v192, v193\n"
"v_cvt_pk_bf16_f32 v193, v194, v195\n"
"v_cvt_pk_bf16_f32 v194, v196, v197\n"
"v_cvt_pk_bf16_f32 v195, v198, v199\n"
"s_mov_b32 s63, 65536\n"
"buffer_store_dwordx2 v[192:193], v5, s[48:51], s63 offen offset:0\n"
"buffer_store_dwordx2 v[194:195], v5, s[48:51], s63 offen offset:32\n"
"s_branch .Lzb_%=\n"
".Ls0b_%=:\n"
"s_mov_b32 s64, 0x200000\n"
".Lpab_%=:\n"
"buffer_load_dwordx2 v[22:23], v13, s[36:39], %[ma] offen offset:0 sc1\n"
"s_waitcnt vmcnt(0)\n"
"v_cmp_ne_u32 vcc, v22, v17\n"
"s_nop 1\n"
"s_cmp_eq_u64 vcc, 0\n"
"s_cbranch_scc1 .Leab_%=\n"
"v_mov_b32 v17, v22\n"
"s_mov_b64 exec, 15\n"
"buffer_store_dwordx2 v[16:17], v8, s[36:39], %[mw] offen offset:0 sc1\n"
"s_mov_b64 exec, -1\n"
".Leab_%=:\n"
"v_cmp_ne_u32 vcc, v23, v16\n"
"s_nop 1\n"
"s_cmp_eq_u64 vcc, 0\n"
"s_cbranch_scc1 .Lqab_%=\n"
"s_sleep 1\n"
"s_sub_u32 s64, s64, 1\n"
"s_cmp_lg_u32 s64, 0\n"
"s_cbranch_scc1 .Lpab_%=\n"
".Lqab_%=:\n"
"s_waitcnt vmcnt(0)\n"
"s_branch .Lr0b_%=\n"
".Ls1b_%=:\n"
"s_mov_b32 s64, 0x200000\n"
".Lpcb_%=:\n"
"buffer_load_dwordx2 v[22:23], v13, s[36:39], %[ma] offen offset:128 sc1\n"
"s_waitcnt vmcnt(0)\n"
"v_cmp_ne_u32 vcc, v22, v19\n"
"s_nop 1\n"
"s_cmp_eq_u64 vcc, 0\n"
"s_cbranch_scc1 .Lecb_%=\n"
"v_mov_b32 v19, v22\n"
"s_mov_b64 exec, 15\n"
"buffer_store_dwordx2 v[18:19], v8, s[36:39], %[mw] offen offset:128 sc1\n"
"s_mov_b64 exec, -1\n"
".Lecb_%=:\n"
"v_cmp_ne_u32 vcc, v23, v18\n"
"s_nop 1\n"
"s_cmp_eq_u64 vcc, 0\n"
"s_cbranch_scc1 .Lqcb_%=\n"
"s_sleep 1\n"
"s_sub_u32 s64, s64, 1\n"
"s_cmp_lg_u32 s64, 0\n"
"s_cbranch_scc1 .Lpcb_%=\n"
".Lqcb_%=:\n"
"s_waitcnt vmcnt(0)\n"
"s_branch .Lr1b_%=\n"
".Lzb_%=:\n"
"s_waitcnt vmcnt(0) lgkmcnt(0)\n"
".Lend_%=:\n"
".Lkf_end:\n"
      : [bad] "=&s"(bad), [any] "=&s"(any)
      : [xl] "s"((unsigned)px), [xh] "s"((unsigned)(px >> 32)), [wl] "s"((unsigned)pw), [wh] "s"((unsigned)(pw >> 32)),
        [kl] "s"((unsigned)pk), [kh] "s"((unsigned)(pk >> 32)), [vl] "s"((unsigned)pv), [vh] "s"((unsigned)(pv >> 32)),
        [wsl] "s"((unsigned)(unsigned long long)ws), [wsh] "s"((unsigned)((unsigned long long)ws >> 32)),
        [wol] "s"((unsigned)pwo), [woh] "s"((unsigned)(pwo >> 32)), [yl] "s"((unsigned)py), [yh] "s"((unsigned)(py >> 32)),
        [oof] "s"(blk * (120u * 131072u) + b * (15u * 131072u) + pair * 8192u), [wv] "s"(wv), [ma] "s"(oA + nt * 8), [mw] "s"(oA + nt * 32),
        [xa] "s"(oX + ks * 8), [xw] "s"(oX + ks * 32), [ksp] "s"(ks), [ntp] "s"(nt), [gr4] "s"(grp * 4),
        [cpo] "s"(((idx * 4 + wv) & 15) * 8192), [wto] "s"((b * 4 + wv) * 32768)
      : "v0","v1","v2","v3","v4","v5","v6","v7","v8","v9","v10","v11","v12","v13","v14","v15","v16","v17","v18","v19","v20","v21","v22","v23","v24","v25","v26","v27","v28","v29","v30","v31","v32","v33","v34","v35","v36","v37","v38","v39","v40","v41","v42","v43","v44","v45","v46","v47","v48","v49","v50","v51","v52","v53","v54","v55","v56","v57","v58","v59","v60","v61","v62","v63","v64","v65","v66","v67","v68","v69","v70","v71","v72","v73","v74","v75","v76","v77","v78","v79","v80","v81","v82","v83","v84","v85","v86","v87","v88","v89","v90","v91","v92","v93","v94","v95","v96","v97","v98","v99","v100","v101","v102","v103","v104","v105","v106","v107","v108","v109","v110","v111","v112","v113","v114","v115","v116","v117","v118","v119","v120","v121","v122","v123","v124","v125","v126","v127","v128","v129","v130","v131","v132","v133","v134","v135","v136","v137","v138","v139","v140","v141","v142","v143","v144","v145","v146","v147","v148","v149","v150","v151","v152","v153","v154","v155","v156","v157","v158","v159","v160","v161","v162","v163","v164","v165","v166","v167","v168","v169","v170","v171","v172","v173","v174","v175","v176","v177","v178","v179","v180","v181","v182","v183","v184","v185","v186","v187","v188","v189","v190","v191","v192","v193","v194","v195","v196","v197","v198","v199","v200","v201","v202","v203","v204","v205","v206","v207","v208","v209","v210","v211","v212","v213","v214","v215","v216","v217","v218","v219","v220","v221","v222","v223","v224","v225","v226","v227","v228","v229","v230","v231","v232","v233","v234","v235","v236","v237","v238","v239","v240","v241","v242","v243","v244","v245","v246","v247","v248","v249","v250","v251","v252","v253","v254","v255","a0","a1","a2","a3","a4","a5","a6","a7","a8","a9","a10","a11","a12","a13","a14","a15","a16","a17","a18","a19","a20","a21","a22","a23","a24","a25","a26","a27","a28","a29","a30","a31","a32","a33","a34","a35","a36","a37","a38","a39","a40","a41","a42","a43","a44","a45","a46","a47","a48","a49","a50","a51","a52","a53","a54","a55","a56","a57","a58","a59","a60","a61","a62","a63","a64","a65","a66","a67","a68","a69","a70","a71","a72","a73","a74","a75","a76","a77","a78","a79","a80","a81","a82","a83","a84","a85","a86","a87","a88","a89","a90","a91","a92","a93","a94","a95","a96","a97","a98","a99","a100","a101","a102","a103","a104","a105","a106","a107","a108","a109","a110","a111","a112","a113","a114","a115","a116","a117","a118","a119","a120","a121","a122","a123","a124","a125","a126","a127","a128","a129","a130","a131","a132","a133","a134","a135","a136","a137","a138","a139","a140","a141","a142","a143","a144","a145","a146","a147","a148","a149","a150","a151","a152","a153","a154","a155","a156","a157","a158","a159","a160","a161","a162","a163","a164","a165","a166","a167","a168","a169","a170","a171","a172","a173","a174","a175","a176","a177","a178","a179","a180","a181","a182","a183","a184","a185","a186","a187","a188","a189","a190","a191","a192","a193","a194","a195","a196","a197","a198","a199","a200","a201","a202","a203","a204","a205","a206","a207","a208","a209","a210","a211","a212","a213","a214","a215","a216","a217","a218","a219","a220","a221","a222","a223","a224","a225","a226","a227","a228","a229","a230","a231","a232","a233","a234","a235","a236","a237","a238","a239","a240","a241","a242","a243","a244","a245","a246","a247","a248","a249","a250","a251","a252","a253","a254","a255","s36","s37","s38","s39","s40","s41","s42","s43","s44","s45","s46","s47","s48","s49","s50","s51","s52","s53","s54","s55","s56","s57","s58","s59","s60","s61","s62","s63","s64","s65","s66","s67","s68","s69","s70","s71","s72","s73","s74","s75","s76","s77","s78","s79","vcc","scc","m0","memory");
  }
  if (any) {
    asm volatile("" : "+s"(ka), "+s"(id), "+s"(wv) :: "memory");
    char* const w2 = (char*)ka[10];
    const int lane = __builtin_amdgcn_mbcnt_hi(~0u, __builtin_amdgcn_mbcnt_lo(~0u, 0u));
    for (int hh = 0; hh < 2; ++hh)
      attn_head_safe(ka[5 * blk], ka[5 * blk + 1], ka[5 * blk + 2], ka[5 * blk + 3], w2 + (size_t)blk * (120 * 131072), b, 2 * pair + hh, lds, wv, lane, bad != 0);
    asm volatile("s_waitcnt vmcnt(0)" ::: "memory");
    __threadfence();
    asm volatile("s_waitcnt vmcnt(0)" ::: "memory");
    __syncthreads();
    asm volatile("" : "+s"(ka), "+s"(id), "+s"(wv) :: "memory");
    char* const w3 = (char*)ka[10];
    const unsigned long long pw = (unsigned long long)(ka[5 * blk + 4] + (size_t)ks * 1048576 + nt * 256);
    const unsigned long long po = (unsigned long long)(w3 + (size_t)blk * (120 * 131072) + (size_t)b * (15 * 131072) + ks * 32768);
    const unsigned long long px = (unsigned long long)(w3 + (32u << 20));
    const unsigned long long py = (unsigned long long)(ka[11] + (size_t)b * (240 * 2048) + blk * 1024 + nt * 256 + ks * 64);
    asm volatile(
"s_cmp_eq_u32 %[wv], 3\n"
"s_cbranch_scc1 .Lw3_%=\n"
"s_mov_b32 s36, %[wl]\n"
"s_and_b32 s37, %[wh], 0xffff\n"
"s_mov_b32 s38, 1047808\n"
"s_mov_b32 s39, 0x20000\n"
"s_mov_b32 s40, %[ol]\n"
"s_and_b32 s41, %[oh], 0xffff\n"
"s_mov_b32 s42, %[osz]\n"
"s_mov_b32 s43, 0x20000\n"
"s_mov_b32 s44, %[xl]\n"
"s_and_b32 s45, %[xh], 0xffff\n"
"s_mov_b32 s46, 11796480\n"
"s_mov_b32 s47, 0x20000\n"
"s_mov_b32 s48, %[yl]\n"
"s_and_b32 s49, %[yh], 0xffff\n"
"s_mov_b32 s50, 489536\n"
"s_mov_b32 s51, 0x20000\n"
"v_mbcnt_lo_u32_b32 v20, -1, 0\n"
"v_mbcnt_hi_u32_b32 v20, -1, v20\n"
"s_lshl_b32 s61, %[wv], 12\n"
"v_and_b32 v21, 15, v20\n"
"v_lshrrev_b32 v22, 4, v20\n"
"v_bfe_u32 v23, v20, 1, 2\n"
"v_bfe_u32 v24, v20, 3, 1\n"
"v_and_b32 v25, 1, v20\n"
"v_lshl_add_u32 v26, v22, 1, v24\n"
"v_lshlrev_b32 v26, 5, v26\n"
"v_lshl_add_u32 v26, v25, 4, v26\n"
"s_lshl_b32 s59, %[ksp], 6\n"
"v_add_u32 v27, s59, v26\n"
"v_and_b32 v27, 0xff, v27\n"
"v_lshl_add_u32 v0, v23, 10, v27\n"
"s_lshl_b32 s60, %[wv], 14\n"
"v_add_u32 v0, s60, v0\n"
"v_xor_b32 v26, 32, v26\n"
"v_add_u32 v27, s59, v26\n"
"v_and_b32 v27, 0xff, v27\n"
"v_lshl_add_u32 v1, v23, 10, v27\n"
"s_lshl_b32 s60, %[wv], 14\n"
"v_add_u32 v1, s60, v1\n"
"s_mov_b32 s52, 0xfffd2400\n"
"s_mov_b32 s53, s61\n"
"s_mov_b32 s70, 0xfff4dc00\n"
"s_mov_b32 s72, 0x2000\n"
"s_mov_b32 s73, 0xffffb000\n"
"s_mov_b32 s74, 0x2000\n"
"s_mov_b32 s75, 0x400000\n"
"v_and_b32 v8, 3, v20\n"
"v_lshlrev_b32 v7, 5, v8\n"
"v_lshlrev_b32 v8, 3, v8\n"
"s_lshl_b32 s59, %[wv], 19\n"
"v_lshlrev_b32 v4, 4, v20\n"
"v_add_u32 v4, s59, v4\n"
"s_mov_b32 s54, 0\n"
"s_mov_b32 s55, 131072\n"
"s_mov_b32 s56, 262144\n"
"s_mov_b32 s57, 393216\n"
"s_add_u32 m0, s53, 0\n"
"s_add_u32 s52, s52, 187392\n"
"buffer_load_dwordx4 v0, s[36:39], s52 offen offset:0 lds\n"
"s_add_u32 m0, s53, 0\n"
"s_add_u32 s52, s52, 3072\n"
"buffer_load_dwordx4 v1, s[36:39], s52 offen offset:1024 lds\n"
"s_add_u32 m0, s53, 0\n"
"s_add_u32 s52, s52, 3072\n"
"buffer_load_dwordx4 v0, s[36:39], s52 offen offset:2048 lds\n"
"s_add_u32 m0, s53, 0\n"
"s_add_u32 s52, s52, 3072\n"
"buffer_load_dwordx4 v1, s[36:39], s52 offen offset:3072 lds\n"
"s_add_u32 m0, s53, 16384\n"
"s_add_u32 s52, s52, 56320\n"
"buffer_load_dwordx4 v0, s[36:39], s52 offen offset:0 lds\n"
"s_add_u32 m0, s53, 16384\n"
"s_add_u32 s52, s52, 3072\n"
"buffer_load_dwordx4 v1, s[36:39], s52 offen offset:1024 lds\n"
"s_add_u32 m0, s53, 16384\n"
"s_add_u32 s52, s52, 3072\n"
"buffer_load_dwordx4 v0, s[36:39], s52 offen offset:2048 lds\n"
"s_add_u32 m0, s53, 16384\n"
"s_add_u32 s52, s52, 3072\n"
"buffer_load_dwordx4 v1, s[36:39], s52 offen offset:3072 lds\n"
"s_xor_b32 s53, s53, 0x8000\n"
"s_cmp_eq_u32 %[nok], 0\n"
"s_cbranch_scc1 .Lfc_%=\n"
"s_mov_b32 s60, 0x200000\n"
"s_waitcnt vmcnt(0)\n"
"global_load_dwordx2 v[10:11], v8, %[mw] offset:0 sc1\n"
".Lpac_%=:\n"
"global_load_dwordx2 v[16:17], v7, %[ma] offset:0 sc1\n"
"s_waitcnt vmcnt(0)\n"
"v_cmp_ne_u32 vcc, v16, v11\n"
"s_nop 1\n"
"s_cmp_eq_u64 vcc, 0\n"
"s_cbranch_scc1 .Leac_%=\n"
"v_mov_b32 v11, v16\n"
"s_mov_b64 exec, 15\n"
"global_store_dwordx2 v8, v[10:11], %[mw] offset:0 sc1\n"
"s_mov_b64 exec, -1\n"
".Leac_%=:\n"
"v_cmp_ne_u32 vcc, v17, v10\n"
"s_nop 1\n"
"s_cmp_eq_u64 vcc, 0\n"
"s_cbranch_scc1 .Lqac_%=\n"
"s_sleep 1\n"
"s_sub_u32 s60, s60, 1\n"
"s_cmp_lg_u32 s60, 0\n"
"s_cbranch_scc1 .Lpac_%=\n"
".Lqac_%=:\n"
"s_mov_b32 s60, 0x200000\n"
"s_waitcnt vmcnt(0)\n"
"global_load_dwordx2 v[12:13], v8, %[mw] offset:128 sc1\n"
".Lpbc_%=:\n"
"global_load_dwordx2 v[16:17], v7, %[ma] offset:128 sc1\n"
"s_waitcnt vmcnt(0)\n"
"v_cmp_ne_u32 vcc, v16, v13\n"
"s_nop 1\n"
"s_cmp_eq_u64 vcc, 0\n"
"s_cbranch_scc1 .Lebc_%=\n"
"v_mov_b32 v13, v16\n"
"s_mov_b64 exec, 15\n"
"global_store_dwordx2 v8, v[12:13], %[mw] offset:128 sc1\n"
"s_mov_b64 exec, -1\n"
".Lebc_%=:\n"
"v_cmp_ne_u32 vcc, v17, v12\n"
"s_nop 1\n"
"s_cmp_eq_u64 vcc, 0\n"
"s_cbranch_scc1 .Lqbc_%=\n"
"s_sleep 1\n"
"s_sub_u32 s60, s60, 1\n"
"s_cmp_lg_u32 s60, 0\n"
"s_cbranch_scc1 .Lpbc_%=\n"
".Lqbc_%=:\n"
".Lfc_%=:\n"
"global_load_dword v14, v7, %[xa] offset:4 sc1\n"
"buffer_load_dwordx4 a[128:131], v4, s[40:43], s54 offen offset:0 sc1\n"
"buffer_load_dwordx4 a[132:135], v4, s[40:43], s55 offen offset:0 sc1\n"
"buffer_load_dwordx4 a[136:139], v4, s[40:43], s56 offen offset:0 sc1\n"
"buffer_load_dwordx4 a[140:143], v4, s[40:43], s57 offen offset:0 sc1\n"
"buffer_load_dwordx4 a[144:147], v4, s[40:43], s54 offen offset:1024 sc1\n"
"buffer_load_dwordx4 a[148:151], v4, s[40:43], s55 offen offset:1024 sc1\n"
"buffer_load_dwordx4 a[152:155], v4, s[40:43], s56 offen offset:1024 sc1\n"
"buffer_load_dwordx4 a[156:159], v4, s[40:43], s57 offen offset:1024 sc1\n"
"buffer_load_dwordx4 a[160:163], v4, s[40:43], s54 offen offset:2048 sc1\n"
"buffer_load_dwordx4 a[164:167], v4, s[40:43], s55 offen offset:2048 sc1\n"
"buffer_load_dwordx4 a[168:171], v4, s[40:43], s56 offen offset:2048 sc1\n"
"buffer_load_dwordx4 a[172:175], v4, s[40:43], s57 offen offset:2048 sc1\n"
"buffer_load_dwordx4 a[176:179], v4, s[40:43], s54 offen offset:3072 sc1\n"
"buffer_load_dwordx4 a[180:183], v4, s[40:43], s55 offen offset:3072 sc1\n"
"buffer_load_dwordx4 a[184:187], v4, s[40:43], s56 offen offset:3072 sc1\n"
"buffer_load_dwordx4 a[188:191], v4, s[40:43], s57 offen offset:3072 sc1\n"
"s_add_u32 s54, s54, 0x2000\n"
"s_add_u32 s55, s55, 0x2000\n"
"s_add_u32 s56, s56, 0x2000\n"
"s_add_u32 s57, s57, 0x2000\n"
"s_add_u32 m0, s53, 0\n"
"s_add_u32 s52, s52, 187392\n"
"buffer_load_dwordx4 v0, s[36:39], s52 offen offset:0 lds\n"
"s_add_u32 m0, s53, 0\n"
"s_add_u32 s52, s52, 3072\n"
"buffer_load_dwordx4 v1, s[36:39], s52 offen offset:1024 lds\n"
"s_add_u32 m0, s53, 0\n"
"s_add_u32 s52, s52, 3072\n"
"buffer_load_dwordx4 v0, s[36:39], s52 offen offset:2048 lds\n"
"s_add_u32 m0, s53, 0\n"
"s_add_u32 s52, s52, 3072\n"
"buffer_load_dwordx4 v1, s[36:39], s52 offen offset:3072 lds\n"
"s_add_u32 m0, s53, 16384\n"
"s_add_u32 s52, s52, 56320\n"
"buffer_load_dwordx4 v0, s[36:39], s52 offen offset:0 lds\n"
"s_add_u32 m0, s53, 16384\n"
"s_add_u32 s52, s52, 3072\n"
"buffer_load_dwordx4 v1, s[36:39], s52 offen offset:1024 lds\n"
"s_add_u32 m0, s53, 16384\n"
"s_add_u32 s52, s52, 3072\n"
"buffer_load_dwordx4 v0, s[36:39], s52 offen offset:2048 lds\n"
"s_add_u32 m0, s53, 16384\n"
"s_add_u32 s52, s52, 3072\n"
"buffer_load_dwordx4 v1, s[36:39], s52 offen offset:3072 lds\n"
"s_xor_b32 s53, s53, 0x10000\n"
"buffer_load_dwordx4 a[192:195], v4, s[40:43], s54 offen offset:0 sc1\n"
"buffer_load_dwordx4 a[196:199], v4, s[40:43], s55 offen offset:0 sc1\n"
"buffer_load_dwordx4 a[200:203], v4, s[40:43], s56 offen offset:0 sc1\n"
"buffer_load_dwordx4 a[204:207], v4, s[40:43], s57 offen offset:0 sc1\n"
"buffer_load_dwordx4 a[208:211], v4, s[40:43], s54 offen offset:1024 sc1\n"
"buffer_load_dwordx4 a[212:215], v4, s[40:43], s55 offen offset:1024 sc1\n"
"buffer_load_dwordx4 a[216:219], v4, s[40:43], s56 offen offset:1024 sc1\n"
"buffer_load_dwordx4 a[220:223], v4, s[40:43], s57 offen offset:1024 sc1\n"
"s_add_u32 m0, s53, 0\n"
"s_add_u32 s52, s52, 187392\n"
"buffer_load_dwordx4 v0, s[36:39], s52 offen offset:0 lds\n"
"s_add_u32 m0, s53, 0\n"
"s_add_u32 s52, s52, 3072\n"
"buffer_load_dwordx4 v1, s[36:39], s52 offen offset:1024 lds\n"
"s_add_u32 m0, s53, 0\n"
"s_add_u32 s52, s52, 3072\n"
"buffer_load_dwordx4 v0, s[36:39], s52 offen offset:2048 lds\n"
"s_add_u32 m0, s53, 0\n"
"s_add_u32 s52, s52, 3072\n"
"buffer_load_dwordx4 v1, s[36:39], s52 offen offset:3072 lds\n"
"s_add_u32 m0, s53, 16384\n"
"s_add_u32 s52, s52, 56320\n"
"buffer_load_dwordx4 v0, s[36:39], s52 offen offset:0 lds\n"
"s_add_u32 m0, s53, 16384\n"
"s_add_u32 s52, s52, 3072\n"
"buffer_load_dwordx4 v1, s[36:39], s52 offen offset:1024 lds\n"
"s_add_u32 m0, s53, 16384\n"
"s_add_u32 s52, s52, 3072\n"
"buffer_load_dwordx4 v0, s[36:39], s52 offen offset:2048 lds\n"
"s_add_u32 m0, s53, 16384\n"
"s_add_u32 s52, s52, 3072\n"
"buffer_load_dwordx4 v1, s[36:39], s52 offen offset:3072 lds\n"
"s_xor_b32 s53, s53, 0x8000\n"
"s_waitcnt vmcnt(49)\n"
"s_mov_b64 exec, 15\n"
"global_store_dwordx2 v8, v[12:13], %[mw] offset:128 sc1\n"
"s_mov_b64 exec, -1\n"
"v_lshrrev_b32 v30, 2, v21\n"
"v_and_b32 v31, 1, v22\n"
"v_lshl_add_u32 v30, v31, 2, v30\n"
"v_lshlrev_b32 v30, 5, v30\n"
"v_and_b32 v31, 3, v20\n"
"v_lshl_add_u32 v30, v31, 3, v30\n"
"v_lshl_add_u32 v2, v22, 10, v30\n"
"v_xor_b32 v3, 0x80, v2\n"
"v_lshlrev_b32 v5, 11, v21\n"
"v_lshl_add_u32 v5, v22, 3, v5\n"
"s_lshl_b32 s59, %[wv], 17\n"
"v_add_u32 v5, s59, v5\n"
"v_lshlrev_b32 v6, 4, v20\n"
"v_add_u32 v6, s61, v6\n"
"s_add_u32 s59, %[ksp], 1\n"
"s_and_b32 s59, s59, 3\n"
"s_lshl_b32 s60, %[grpp], 2\n"
"s_add_u32 s59, s59, s60\n"
"s_mul_i32 s59, s59, 3\n"
"s_add_u32 s59, s59, 0\n"
"s_mul_i32 s62, s59, 15360\n"
"s_add_u32 s59, %[ksp], 2\n"
"s_and_b32 s59, s59, 3\n"
"s_lshl_b32 s60, %[grpp], 2\n"
"s_add_u32 s59, s59, s60\n"
"s_mul_i32 s59, s59, 3\n"
"s_add_u32 s59, s59, 1\n"
"s_mul_i32 s63, s59, 15360\n"
"s_add_u32 s59, %[ksp], 3\n"
"s_and_b32 s59, s59, 3\n"
"s_lshl_b32 s60, %[grpp], 2\n"
"s_add_u32 s59, s59, s60\n"
"s_mul_i32 s59, s59, 3\n"
"s_add_u32 s59, s59, 2\n"
"s_mul_i32 s64, s59, 15360\n"
"s_lshl_b32 s60, %[grpp], 2\n"
"s_add_u32 s59, s60, %[ksp]\n"
"s_mul_i32 s65, s59, 46080\n"
"s_add_u32 s66, s65, 15360\n"
"s_add_u32 s67, s65, 30720\n"
"s_mov_b32 s58, 4\n"
"v_mov_b32 v128, 0\n"
"v_mov_b32 v129, 0\n"
"v_mov_b32 v130, 0\n"
"v_mov_b32 v131, 0\n"
"v_mov_b32 v132, 0\n"
"v_mov_b32 v133, 0\n"
"v_mov_b32 v134, 0\n"
"v_mov_b32 v135, 0\n"
"v_mov_b32 v136, 0\n"
"v_mov_b32 v137, 0\n"
"v_mov_b32 v138, 0\n"
"v_mov_b32 v139, 0\n"
"v_mov_b32 v140, 0\n"
"v_mov_b32 v141, 0\n"
"v_mov_b32 v142, 0\n"
"v_mov_b32 v143, 0\n"
"v_mov_b32 v144, 0\n"
"v_mov_b32 v145, 0\n"
"v_mov_b32 v146, 0\n"
"v_mov_b32 v147, 0\n"
"v_mov_b32 v148, 0\n"
"v_mov_b32 v149, 0\n"
"v_mov_b32 v150, 0\n"
"v_mov_b32 v151, 0\n"
"v_mov_b32 v152, 0\n"
"v_mov_b32 v153, 0\n"
"v_mov_b32 v154, 0\n"
"v_mov_b32 v155, 0\n"
"v_mov_b32 v156, 0\n"
"v_mov_b32 v157, 0\n"
"v_mov_b32 v158, 0\n"
"v_mov_b32 v159, 0\n"
"v_mov_b32 v160, 0\n"
"v_mov_b32 v161, 0\n"
"v_mov_b32 v162, 0\n"
"v_mov_b32 v163, 0\n"
"v_mov_b32 v164, 0\n"
"v_mov_b32 v165, 0\n"
"v_mov_b32 v166, 0\n"
"v_mov_b32 v167, 0\n"
"v_mov_b32 v168, 0\n"
"v_mov_b32 v169, 0\n"
"v_mov_b32 v170, 0\n"
"v_mov_b32 v171, 0\n"
"v_mov_b32 v172, 0\n"
"v_mov_b32 v173, 0\n"
"v_mov_b32 v174, 0\n"
"v_mov_b32 v175, 0\n"
"v_mov_b32 v176, 0\n"
"v_mov_b32 v177, 0\n"
"v_mov_b32 v178, 0\n"
"v_mov_b32 v179, 0\n"
"v_mov_b32 v180, 0\n"
"v_mov_b32 v181, 0\n"
"v_mov_b32 v182, 0\n"
"v_mov_b32 v183, 0\n"
"v_mov_b32 v184, 0\n"
"v_mov_b32 v185, 0\n"
"v_mov_b32 v186, 0\n"
"v_mov_b32 v187, 0\n"
"v_mov_b32 v188, 0\n"
"v_mov_b32 v189, 0\n"
"v_mov_b32 v190, 0\n"
"v_mov_b32 v191, 0\n"
"v_mov_b32 v192, 0\n"
"v_mov_b32 v193, 0\n"
"v_mov_b32 v194, 0\n"
"v_mov_b32 v195, 0\n"
"v_mov_b32 v196, 0\n"
"v_mov_b32 v197, 0\n"
"v_mov_b32 v198, 0\n"
"v_mov_b32 v199, 0\n"
"v_mov_b32 v200, 0\n"
"v_mov_b32 v201, 0\n"
"v_mov_b32 v202, 0\n"
"v_mov_b32 v203, 0\n"
"v_mov_b32 v204, 0\n"
"v_mov_b32 v205, 0\n"
"v_mov_b32 v206, 0\n"
"v_mov_b32 v207, 0\n"
"v_mov_b32 v208, 0\n"
"v_mov_b32 v209, 0\n"
"v_mov_b32 v210, 0\n"
"v_mov_b32 v211, 0\n"
"v_mov_b32 v212, 0\n"
"v_mov_b32 v213, 0\n"
"v_mov_b32 v214, 0\n"
"v_mov_b32 v215, 0\n"
"v_mov_b32 v216, 0\n"
"v_mov_b32 v217, 0\n"
"v_mov_b32 v218, 0\n"
"v_mov_b32 v219, 0\n"
"v_mov_b32 v220, 0\n"
"v_mov_b32 v221, 0\n"
"v_mov_b32 v222, 0\n"
"v_mov_b32 v223, 0\n"
"v_mov_b32 v224, 0\n"
"v_mov_b32 v225, 0\n"
"v_mov_b32 v226, 0\n"
"v_mov_b32 v227, 0\n"
"v_mov_b32 v228, 0\n"
"v_mov_b32 v229, 0\n"
"v_mov_b32 v230, 0\n"
"v_mov_b32 v231, 0\n"
"v_mov_b32 v232, 0\n"
"v_mov_b32 v233, 0\n"
"v_mov_b32 v234, 0\n"
"v_mov_b32 v235, 0\n"
"v_mov_b32 v236, 0\n"
"v_mov_b32 v237, 0\n"
"v_mov_b32 v238, 0\n"
"v_mov_b32 v239, 0\n"
"v_mov_b32 v240, 0\n"
"v_mov_b32 v241, 0\n"
"v_mov_b32 v242, 0\n"
"v_mov_b32 v243, 0\n"
"v_mov_b32 v244, 0\n"
"v_mov_b32 v245, 0\n"
"v_mov_b32 v246, 0\n"
"v_mov_b32 v247, 0\n"
"v_mov_b32 v248, 0\n"
"v_mov_b32 v249, 0\n"
"v_mov_b32 v250, 0\n"
"v_mov_b32 v251, 0\n"
"v_mov_b32 v252, 0\n"
"v_mov_b32 v253, 0\n"
"v_mov_b32 v254, 0\n"
"v_mov_b32 v255, 0\n"
"s_waitcnt vmcnt(41)\n"
"v_add_u32 v14, 1, v14\n"
"s_mov_b64 exec, 15\n"
"global_store_dword v8, v14, %[xw] sc1\n"
"s_mov_b64 exec, -1\n"
"s_waitcnt vmcnt(63)\n"
"s_barrier\n"
"ds_read_b64_tr_b16 a[0:1], v2 offset:0\n"
"ds_read_b64_tr_b16 a[2:3], v2 offset:4096\n"
"ds_read_b64_tr_b16 a[4:5], v2 offset:8192\n"
"ds_read_b64_tr_b16 a[6:7], v2 offset:12288\n"
"ds_read_b64_tr_b16 a[8:9], v3 offset:0\n"
"ds_read_b64_tr_b16 a[10:11], v3 offset:4096\n"
"ds_read_b64_tr_b16 a[12:13], v3 offset:8192\n"
"ds_read_b64_tr_b16 a[14:15], v3 offset:12288\n"
"ds_read_b64_tr_b16 a[16:17], v2 offset:256\n"
"ds_read_b64_tr_b16 a[18:19], v2 offset:4352\n"
"ds_read_b64_tr_b16 a[20:21], v2 offset:8448\n"
"ds_read_b64_tr_b16 a[22:23], v2 offset:12544\n"
"ds_read_b64_tr_b16 a[24:25], v3 offset:256\n"
"ds_read_b64_tr_b16 a[26:27], v3 offset:4352\n"
"ds_read_b64_tr_b16 a[28:29], v3 offset:8448\n"
"ds_read_b64_tr_b16 a[30:31], v3 offset:12544\n"
"ds_read_b64_tr_b16 a[32:33], v2 offset:512\n"
"ds_read_b64_tr_b16 a[34:35], v2 offset:4608\n"
"ds_read_b64_tr_b16 a[36:37], v2 offset:8704\n"
"ds_read_b64_tr_b16 a[38:39], v2 offset:12800\n"
"ds_read_b64_tr_b16 a[40:41], v3 offset:512\n"
"ds_read_b64_tr_b16 a[42:43], v3 offset:4608\n"
"ds_read_b64_tr_b16 a[44:45], v3 offset:8704\n"
"ds_read_b64_tr_b16 a[46:47], v3 offset:12800\n"
"ds_read_b64_tr_b16 a[48:49], v2 offset:768\n"
"ds_read_b64_tr_b16 a[50:51], v2 offset:4864\n"
"ds_read_b64_tr_b16 a[52:53], v2 offset:8960\n"
"ds_read_b64_tr_b16 a[54:55], v2 offset:13056\n"
"ds_read_b64_tr_b16 a[56:57], v3 offset:768\n"
"ds_read_b64_tr_b16 a[58:59], v3 offset:4864\n"
".LLc_%=:\n"
"s_waitcnt lgkmcnt(15)\n"
"s_waitcnt vmcnt(41)\n"
"v_mfma_f32_16x16x32_bf16 v[128:131], a[0:3], a[128:131], v[128:131]\n"
"ds_read_b64_tr_b16 a[60:61], v3 offset:8960\n"
"ds_read_b64_tr_b16 a[62:63], v3 offset:13056\n"
"s_waitcnt vmcnt(34)\n"
"v_mfma_f32_16x16x32_bf16 v[128:131], a[4:7], a[144:147], v[128:131]\n"
"buffer_load_dwordx4 a[224:227], v4, s[40:43], s54 offen offset:2048 sc1\n"
"v_mfma_f32_16x16x32_bf16 v[160:163], a[0:3], a[132:135], v[160:163]\n"
"s_waitcnt vmcnt(33)\n"
"v_mfma_f32_16x16x32_bf16 v[160:163], a[4:7], a[148:151], v[160:163]\n"
"v_mfma_f32_16x16x32_bf16 v[192:195], a[0:3], a[136:139], v[192:195]\n"
"s_waitcnt vmcnt(31)\n"
"v_mfma_f32_16x16x32_bf16 v[192:195], a[4:7], a[152:155], v[192:195]\n"
"v_mfma_f32_16x16x32_bf16 v[224:227], a[0:3], a[140:143], v[224:227]\n"
"ds_read_b64_tr_b16 a[0:1], v2 offset:16384\n"
"ds_read_b64_tr_b16 a[2:3], v2 offset:20480\n"
"s_waitcnt vmcnt(29)\n"
"v_mfma_f32_16x16x32_bf16 v[224:227], a[4:7], a[156:159], v[224:227]\n"
"buffer_load_dwordx4 a[228:231], v4, s[40:43], s55 offen offset:2048 sc1\n"
"v_mfma_f32_16x16x32_bf16 v[132:135], a[8:11], a[128:131], v[132:135]\n"
"ds_read_b64_tr_b16 a[4:5], v2 offset:24576\n"
"ds_read_b64_tr_b16 a[6:7], v2 offset:28672\n"
"v_mfma_f32_16x16x32_bf16 v[132:135], a[12:15], a[144:147], v[132:135]\n"
"v_mfma_f32_16x16x32_bf16 v[164:167], a[8:11], a[132:135], v[164:167]\n"
"v_mfma_f32_16x16x32_bf16 v[164:167], a[12:15], a[148:151], v[164:167]\n"
"v_mfma_f32_16x16x32_bf16 v[196:199], a[8:11], a[136:139], v[196:199]\n"
"v_mfma_f32_16x16x32_bf16 v[196:199], a[12:15], a[152:155], v[196:199]\n"
"buffer_load_dwordx4 a[232:235], v4, s[40:43], s56 offen offset:2048 sc1\n"
"v_mfma_f32_16x16x32_bf16 v[228:231], a[8:11], a[140:143], v[228:231]\n"
"ds_read_b64_tr_b16 a[8:9], v3 offset:16384\n"
"ds_read_b64_tr_b16 a[10:11], v3 offset:20480\n"
"v_mfma_f32_16x16x32_bf16 v[228:231], a[12:15], a[156:159], v[228:231]\n"
"ds_read_b64_tr_b16 a[12:13], v3 offset:24576\n"
"ds_read_b64_tr_b16 a[14:15], v3 offset:28672\n"
"v_mfma_f32_16x16x32_bf16 v[136:139], a[16:19], a[128:131], v[136:139]\n"
"v_mfma_f32_16x16x32_bf16 v[136:139], a[20:23], a[144:147], v[136:139]\n"
"v_mfma_f32_16x16x32_bf16 v[168:171], a[16:19], a[132:135], v[168:171]\n"
"v_mfma_f32_16x16x32_bf16 v[168:171], a[20:23], a[148:151], v[168:171]\n"
"buffer_load_dwordx4 a[236:239], v4, s[40:43], s57 offen offset:2048 sc1\n"
"v_mfma_f32_16x16x32_bf16 v[200:203], a[16:19], a[136:139], v[200:203]\n"
"v_mfma_f32_16x16x32_bf16 v[200:203], a[20:23], a[152:155], v[200:203]\n"
"v_mfma_f32_16x16x32_bf16 v[232:235], a[16:19], a[140:143], v[232:235]\n"
"ds_read_b64_tr_b16 a[16:17], v2 offset:16640\n"
"ds_read_b64_tr_b16 a[18:19], v2 offset:20736\n"
"v_mfma_f32_16x16x32_bf16 v[232:235], a[20:23], a[156:159], v[232:235]\n"
"ds_read_b64_tr_b16 a[20:21], v2 offset:24832\n"
"ds_read_b64_tr_b16 a[22:23], v2 offset:28928\n"
"v_mfma_f32_16x16x32_bf16 v[140:143], a[24:27], a[128:131], v[140:143]\n"
"s_waitcnt lgkmcnt(15)\n"
"v_mfma_f32_16x16x32_bf16 v[140:143], a[28:31], a[144:147], v[140:143]\n"
"buffer_load_dwordx4 a[240:243], v4, s[40:43], s54 offen offset:3072 sc1\n"
"v_mfma_f32_16x16x32_bf16 v[172:175], a[24:27], a[132:135], v[172:175]\n"
"v_mfma_f32_16x16x32_bf16 v[172:175], a[28:31], a[148:151], v[172:175]\n"
"v_mfma_f32_16x16x32_bf16 v[204:207], a[24:27], a[136:139], v[204:207]\n"
"v_mfma_f32_16x16x32_bf16 v[204:207], a[28:31], a[152:155], v[204:207]\n"
"v_mfma_f32_16x16x32_bf16 v[236:239], a[24:27], a[140:143], v[236:239]\n"
"ds_read_b64_tr_b16 a[24:25], v3 offset:16640\n"
"ds_read_b64_tr_b16 a[26:27], v3 offset:20736\n"
"v_mfma_f32_16x16x32_bf16 v[236:239], a[28:31], a[156:159], v[236:239]\n"
"buffer_load_dwordx4 a[244:247], v4, s[40:43], s55 offen offset:3072 sc1\n"
"v_mfma_f32_16x16x32_bf16 v[144:147], a[32:35], a[128:131], v[144:147]\n"
"ds_read_b64_tr_b16 a[28:29], v3 offset:24832\n"
"ds_read_b64_tr_b16 a[30:31], v3 offset:28928\n"
"v_mfma_f32_16x16x32_bf16 v[144:147], a[36:39], a[144:147], v[144:147]\n"
"v_mfma_f32_16x16x32_bf16 v[176:179], a[32:35], a[132:135], v[176:179]\n"
"v_mfma_f32_16x16x32_bf16 v[176:179], a[36:39], a[148:151], v[176:179]\n"
"v_mfma_f32_16x16x32_bf16 v[208:211], a[32:35], a[136:139], v[208:211]\n"
"v_mfma_f32_16x16x32_bf16 v[208:211], a[36:39], a[152:155], v[208:211]\n"
"buffer_load_dwordx4 a[248:251], v4, s[40:43], s56 offen offset:3072 sc1\n"
"v_mfma_f32_16x16x32_bf16 v[240:243], a[32:35], a[140:143], v[240:243]\n"
"ds_read_b64_tr_b16 a[32:33], v2 offset:16896\n"
"ds_read_b64_tr_b16 a[34:35], v2 offset:20992\n"
"v_mfma_f32_16x16x32_bf16 v[240:243], a[36:39], a[156:159], v[240:243]\n"
"ds_read_b64_tr_b16 a[36:37], v2 offset:25088\n"
"ds_read_b64_tr_b16 a[38:39], v2 offset:29184\n"
"v_mfma_f32_16x16x32_bf16 v[148:151], a[40:43], a[128:131], v[148:151]\n"
"v_mfma_f32_16x16x32_bf16 v[148:151], a[44:47], a[144:147], v[148:151]\n"
"v_mfma_f32_16x16x32_bf16 v[180:183], a[40:43], a[132:135], v[180:183]\n"
"v_mfma_f32_16x16x32_bf16 v[180:183], a[44:47], a[148:151], v[180:183]\n"
"buffer_load_dwordx4 a[252:255], v4, s[40:43], s57 offen offset:3072 sc1\n"
"v_mfma_f32_16x16x32_bf16 v[212:215], a[40:43], a[136:139], v[212:215]\n"
"v_mfma_f32_16x16x32_bf16 v[212:215], a[44:47], a[152:155], v[212:215]\n"
"v_mfma_f32_16x16x32_bf16 v[244:247], a[40:43], a[140:143], v[244:247]\n"
"ds_read_b64_tr_b16 a[40:41], v3 offset:16896\n"
"ds_read_b64_tr_b16 a[42:43], v3 offset:20992\n"
"v_mfma_f32_16x16x32_bf16 v[244:247], a[44:47], a[156:159], v[244:247]\n"
"ds_read_b64_tr_b16 a[44:45], v3 offset:25088\n"
"ds_read_b64_tr_b16 a[46:47], v3 offset:29184\n"
"v_mfma_f32_16x16x32_bf16 v[152:155], a[48:51], a[128:131], v[152:155]\n"
"v_mfma_f32_16x16x32_bf16 v[152:155], a[52:55], a[144:147], v[152:155]\n"
"s_add_u32 s54, s54, s72\n"
"s_add_u32 s55, s55, s72\n"
"s_add_u32 s56, s56, s72\n"
"s_add_u32 s57, s57, s72\n"
"s_mov_b32 s72, s73\n"
"s_mov_b32 s73, s74\n"
"s_mov_b32 s74, s75\n"
"s_waitcnt vmcnt(24)\n"
"v_cmp_ne_u32 vcc, v18, v13\n"
"s_nop 1\n"
"s_mov_b64 s[78:79], vcc\n"
"v_cmp_ne_u32 vcc, v19, v12\n"
"s_cmp_eq_u32 s58, 3\n"
"s_cselect_b64 s[76:77], -1, 0\n"
"s_or_b64 s[78:79], s[78:79], vcc\n"
"s_and_b64 s[78:79], s[78:79], s[76:77]\n"
"s_cbranch_scc1 .Ls1c_%=\n"
".Lr1c_%=:\n"
"v_mfma_f32_16x16x32_bf16 v[184:187], a[48:51], a[132:135], v[184:187]\n"
"v_mfma_f32_16x16x32_bf16 v[184:187], a[52:55], a[148:151], v[184:187]\n"
"v_mfma_f32_16x16x32_bf16 v[216:219], a[48:51], a[136:139], v[216:219]\n"
"v_mfma_f32_16x16x32_bf16 v[216:219], a[52:55], a[152:155], v[216:219]\n"
"v_mfma_f32_16x16x32_bf16 v[248:251], a[48:51], a[140:143], v[248:251]\n"
"ds_read_b64_tr_b16 a[48:49], v2 offset:17152\n"
"ds_read_b64_tr_b16 a[50:51], v2 offset:21248\n"
"v_mfma_f32_16x16x32_bf16 v[248:251], a[52:55], a[156:159], v[248:251]\n"
"ds_read_b64_tr_b16 a[52:53], v2 offset:25344\n"
"ds_read_b64_tr_b16 a[54:55], v2 offset:29440\n"
"s_waitcnt lgkmcnt(15)\n"
"v_mfma_f32_16x16x32_bf16 v[156:159], a[56:59], a[128:131], v[156:159]\n"
"v_mfma_f32_16x16x32_bf16 v[156:159], a[60:63], a[144:147], v[156:159]\n"
"v_mfma_f32_16x16x32_bf16 v[188:191], a[56:59], a[132:135], v[188:191]\n"
"v_mfma_f32_16x16x32_bf16 v[188:191], a[60:63], a[148:151], v[188:191]\n"
"v_mfma_f32_16x16x32_bf16 v[220:223], a[56:59], a[136:139], v[220:223]\n"
"buffer_load_dwordx4 a[128:131], v4, s[40:43], s54 offen offset:0 sc1\n"
"v_mfma_f32_16x16x32_bf16 v[220:223], a[60:63], a[152:155], v[220:223]\n"
"v_mfma_f32_16x16x32_bf16 v[252:255], a[56:59], a[140:143], v[252:255]\n"
"ds_read_b64_tr_b16 a[56:57], v3 offset:17152\n"
"ds_read_b64_tr_b16 a[58:59], v3 offset:21248\n"
"v_mfma_f32_16x16x32_bf16 v[252:255], a[60:63], a[156:159], v[252:255]\n"
"ds_read_b64_tr_b16 a[60:61], v3 offset:25344\n"
"ds_read_b64_tr_b16 a[62:63], v3 offset:29440\n"
"s_waitcnt vmcnt(42)\n"
"v_mfma_f32_16x16x32_bf16 v[128:131], a[0:3], a[160:163], v[128:131]\n"
"s_waitcnt vmcnt(38)\n"
"v_mfma_f32_16x16x32_bf16 v[128:131], a[4:7], a[176:179], v[128:131]\n"
"v_mfma_f32_16x16x32_bf16 v[160:163], a[0:3], a[164:167], v[160:163]\n"
"s_waitcnt vmcnt(37)\n"
"v_mfma_f32_16x16x32_bf16 v[160:163], a[4:7], a[180:183], v[160:163]\n"
"buffer_load_dwordx4 a[132:135], v4, s[40:43], s55 offen offset:0 sc1\n"
"v_mfma_f32_16x16x32_bf16 v[192:195], a[0:3], a[168:171], v[192:195]\n"
"s_waitcnt vmcnt(28)\n"
"s_barrier\n"
"v_xor_b32 v2, 0x8000, v2\n"
"v_xor_b32 v3, 0x8000, v3\n"
"v_mfma_f32_16x16x32_bf16 v[192:195], a[4:7], a[184:187], v[192:195]\n"
"s_add_u32 m0, s53, 0\n"
"s_add_u32 s52, s52, 187392\n"
"buffer_load_dwordx4 v0, s[36:39], s52 offen offset:0 lds\n"
"v_mfma_f32_16x16x32_bf16 v[224:227], a[0:3], a[172:175], v[224:227]\n"
"ds_read_b64_tr_b16 a[0:1], v2 offset:0\n"
"ds_read_b64_tr_b16 a[2:3], v2 offset:4096\n"
"v_mfma_f32_16x16x32_bf16 v[224:227], a[4:7], a[188:191], v[224:227]\n"
"ds_read_b64_tr_b16 a[4:5], v2 offset:8192\n"
"ds_read_b64_tr_b16 a[6:7], v2 offset:12288\n"
"v_mfma_f32_16x16x32_bf16 v[132:135], a[8:11], a[160:163], v[132:135]\n"
"v_mfma_f32_16x16x32_bf16 v[132:135], a[12:15], a[176:179], v[132:135]\n"
"v_mfma_f32_16x16x32_bf16 v[164:167], a[8:11], a[164:167], v[164:167]\n"
"buffer_load_dwordx4 a[136:139], v4, s[40:43], s56 offen offset:0 sc1\n"
"v_mfma_f32_16x16x32_bf16 v[164:167], a[12:15], a[180:183], v[164:167]\n"
"v_mfma_f32_16x16x32_bf16 v[196:199], a[8:11], a[168:171], v[196:199]\n"
"s_add_u32 m0, s53, 0\n"
"s_add_u32 s52, s52, 3072\n"
"buffer_load_dwordx4 v1, s[36:39], s52 offen offset:1024 lds\n"
"v_mfma_f32_16x16x32_bf16 v[196:199], a[12:15], a[184:187], v[196:199]\n"
"v_mfma_f32_16x16x32_bf16 v[228:231], a[8:11], a[172:175], v[228:231]\n"
"ds_read_b64_tr_b16 a[8:9], v3 offset:0\n"
"ds_read_b64_tr_b16 a[10:11], v3 offset:4096\n"
"v_mfma_f32_16x16x32_bf16 v[228:231], a[12:15], a[188:191], v[228:231]\n"
"ds_read_b64_tr_b16 a[12:13], v3 offset:8192\n"
"ds_read_b64_tr_b16 a[14:15], v3 offset:12288\n"
"v_mfma_f32_16x16x32_bf16 v[136:139], a[16:19], a[160:163], v[136:139]\n"
"v_mfma_f32_16x16x32_bf16 v[136:139], a[20:23], a[176:179], v[136:139]\n"
"buffer_load_dwordx4 a[140:143], v4, s[40:43], s57 offen offset:0 sc1\n"
"v_mfma_f32_16x16x32_bf16 v[168:171], a[16:19], a[164:167], v[168:171]\n"
"v_mfma_f32_16x16x32_bf16 v[168:171], a[20:23], a[180:183], v[168:171]\n"
"s_add_u32 m0, s53, 0\n"
"s_add_u32 s52, s52, 3072\n"
"buffer_load_dwordx4 v0, s[36:39], s52 offen offset:2048 lds\n"
"v_mfma_f32_16x16x32_bf16 v[200:203], a[16:19], a[168:171], v[200:203]\n"
"v_mfma_f32_16x16x32_bf16 v[200:203], a[20:23], a[184:187], v[200:203]\n"
"v_mfma_f32_16x16x32_bf16 v[232:235], a[16:19], a[172:175], v[232:235]\n"
"ds_read_b64_tr_b16 a[16:17], v2 offset:256\n"
"ds_read_b64_tr_b16 a[18:19], v2 offset:4352\n"
"v_mfma_f32_16x16x32_bf16 v[232:235], a[20:23], a[188:191], v[232:235]\n"
"ds_read_b64_tr_b16 a[20:21], v2 offset:8448\n"
"ds_read_b64_tr_b16 a[22:23], v2 offset:12544\n"
"s_waitcnt lgkmcnt(15)\n"
"v_mfma_f32_16x16x32_bf16 v[140:143], a[24:27], a[160:163], v[140:143]\n"
"buffer_load_dwordx4 a[144:147], v4, s[40:43], s54 offen offset:1024 sc1\n"
"v_mfma_f32_16x16x32_bf16 v[140:143], a[28:31], a[176:179], v[140:143]\n"
"v_mfma_f32_16x16x32_bf16 v[172:175], a[24:27], a[164:167], v[172:175]\n"
"s_add_u32 m0, s53, 0\n"
"s_add_u32 s52, s52, 3072\n"
"buffer_load_dwordx4 v1, s[36:39], s52 offen offset:3072 lds\n"
"v_mfma_f32_16x16x32_bf16 v[172:175], a[28:31], a[180:183], v[172:175]\n"
"v_mfma_f32_16x16x32_bf16 v[204:207], a[24:27], a[168:171], v[204:207]\n"
"v_mfma_f32_16x16x32_bf16 v[204:207], a[28:31], a[184:187], v[204:207]\n"
"v_mfma_f32_16x16x32_bf16 v[236:239], a[24:27], a[172:175], v[236:239]\n"
"ds_read_b64_tr_b16 a[24:25], v3 offset:256\n"
"ds_read_b64_tr_b16 a[26:27], v3 offset:4352\n"
"v_mfma_f32_16x16x32_bf16 v[236:239], a[28:31], a[188:191], v[236:239]\n"
"buffer_load_dwordx4 a[148:151], v4, s[40:43], s55 offen offset:1024 sc1\n"
"v_mfma_f32_16x16x32_bf16 v[144:147], a[32:35], a[160:163], v[144:147]\n"
"ds_read_b64_tr_b16 a[28:29], v3 offset:8448\n"
"ds_read_b64_tr_b16 a[30:31], v3 offset:12544\n"
"v_mfma_f32_16x16x32_bf16 v[144:147], a[36:39], a[176:179], v[144:147]\n"
"s_add_u32 m0, s53, 16384\n"
"s_add_u32 s52, s52, 56320\n"
"buffer_load_dwordx4 v0, s[36:39], s52 offen offset:0 lds\n"
"v_mfma_f32_16x16x32_bf16 v[176:179], a[32:35], a[164:167], v[176:179]\n"
"v_mfma_f32_16x16x32_bf16 v[176:179], a[36:39], a[180:183], v[176:179]\n"
"v_mfma_f32_16x16x32_bf16 v[208:211], a[32:35], a[168:171], v[208:211]\n"
"v_mfma_f32_16x16x32_bf16 v[208:211], a[36:39], a[184:187], v[208:211]\n"
"v_mfma_f32_16x16x32_bf16 v[240:243], a[32:35], a[172:175], v[240:243]\n"
"buffer_load_dwordx4 a[152:155], v4, s[40:43], s56 offen offset:1024 sc1\n"
"v_mfma_f32_16x16x32_bf16 v[240:243], a[36:39], a[188:191], v[240:243]\n"
"ds_read_b64_tr_b16 a[32:33], v2 offset:512\n"
"ds_read_b64_tr_b16 a[34:35], v2 offset:4608\n"
"v_mfma_f32_16x16x32_bf16 v[148:151], a[40:43], a[160:163], v[148:151]\n"
"s_add_u32 m0, s53, 16384\n"
"s_add_u32 s52, s52, 3072\n"
"buffer_load_dwordx4 v1, s[36:39], s52 offen offset:1024 lds\n"
"v_mfma_f32_16x16x32_bf16 v[148:151], a[44:47], a[176:179], v[148:151]\n"
"ds_read_b64_tr_b16 a[36:37], v2 offset:8704\n"
"ds_read_b64_tr_b16 a[38:39], v2 offset:12800\n"
"v_mfma_f32_16x16x32_bf16 v[180:183], a[40:43], a[164:167], v[180:183]\n"
"v_mfma_f32_16x16x32_bf16 v[180:183], a[44:47], a[180:183], v[180:183]\n"
"v_mfma_f32_16x16x32_bf16 v[212:215], a[40:43], a[168:171], v[212:215]\n"
"v_mfma_f32_16x16x32_bf16 v[212:215], a[44:47], a[184:187], v[212:215]\n"
"buffer_load_dwordx4 a[156:159], v4, s[40:43], s57 offen offset:1024 sc1\n"
"v_mfma_f32_16x16x32_bf16 v[244:247], a[40:43], a[172:175], v[244:247]\n"
"ds_read_b64_tr_b16 a[40:41], v3 offset:512\n"
"ds_read_b64_tr_b16 a[42:43], v3 offset:4608\n"
"v_mfma_f32_16x16x32_bf16 v[244:247], a[44:47], a[188:191], v[244:247]\n"
"s_add_u32 m0, s53, 16384\n"
"s_add_u32 s52, s52, 3072\n"
"buffer_load_dwordx4 v0, s[36:39], s52 offen offset:2048 lds\n"
"v_mfma_f32_16x16x32_bf16 v[152:155], a[48:51], a[160:163], v[152:155]\n"
"ds_read_b64_tr_b16 a[44:45], v3 offset:8704\n"
"ds_read_b64_tr_b16 a[46:47], v3 offset:12800\n"
"v_mfma_f32_16x16x32_bf16 v[152:155], a[52:55], a[176:179], v[152:155]\n"
"v_mfma_f32_16x16x32_bf16 v[184:187], a[48:51], a[164:167], v[184:187]\n"
"v_mfma_f32_16x16x32_bf16 v[184:187], a[52:55], a[180:183], v[184:187]\n"
"v_mfma_f32_16x16x32_bf16 v[216:219], a[48:51], a[168:171], v[216:219]\n"
"v_mfma_f32_16x16x32_bf16 v[216:219], a[52:55], a[184:187], v[216:219]\n"
"v_mfma_f32_16x16x32_bf16 v[248:251], a[48:51], a[172:175], v[248:251]\n"
"s_add_u32 m0, s53, 16384\n"
"s_add_u32 s52, s52, 3072\n"
"buffer_load_dwordx4 v1, s[36:39], s52 offen offset:3072 lds\n"
"s_xor_b32 s53, s53, 0x10000\n"
"v_mfma_f32_16x16x32_bf16 v[248:251], a[52:55], a[188:191], v[248:251]\n"
"ds_read_b64_tr_b16 a[48:49], v2 offset:768\n"
"ds_read_b64_tr_b16 a[50:51], v2 offset:4864\n"
"s_waitcnt lgkmcnt(15)\n"
"v_mfma_f32_16x16x32_bf16 v[156:159], a[56:59], a[160:163], v[156:159]\n"
"ds_read_b64_tr_b16 a[52:53], v2 offset:8960\n"
"ds_read_b64_tr_b16 a[54:55], v2 offset:13056\n"
"v_mfma_f32_16x16x32_bf16 v[156:159], a[60:63], a[176:179], v[156:159]\n"
"v_mfma_f32_16x16x32_bf16 v[188:191], a[56:59], a[164:167], v[188:191]\n"
"v_mfma_f32_16x16x32_bf16 v[188:191], a[60:63], a[180:183], v[188:191]\n"
"v_mfma_f32_16x16x32_bf16 v[220:223], a[56:59], a[168:171], v[220:223]\n"
"v_mfma_f32_16x16x32_bf16 v[220:223], a[60:63], a[184:187], v[220:223]\n"
"v_mfma_f32_16x16x32_bf16 v[252:255], a[56:59], a[172:175], v[252:255]\n"
"ds_read_b64_tr_b16 a[56:57], v3 offset:768\n"
"ds_read_b64_tr_b16 a[58:59], v3 offset:4864\n"
"v_mfma_f32_16x16x32_bf16 v[252:255], a[60:63], a[188:191], v[252:255]\n"
"s_waitcnt vmcnt(41)\n"
"v_mfma_f32_16x16x32_bf16 v[128:131], a[0:3], a[192:195], v[128:131]\n"
"ds_read_b64_tr_b16 a[60:61], v3 offset:8960\n"
"ds_read_b64_tr_b16 a[62:63], v3 offset:13056\n"
"s_waitcnt vmcnt(32)\n"
"v_mfma_f32_16x16x32_bf16 v[128:131], a[4:7], a[208:211], v[128:131]\n"
"buffer_load_dwordx4 a[160:163], v4, s[40:43], s54 offen offset:2048 sc1\n"
"v_mfma_f32_16x16x32_bf16 v[160:163], a[0:3], a[196:199], v[160:163]\n"
"s_waitcnt vmcnt(31)\n"
"v_mfma_f32_16x16x32_bf16 v[160:163], a[4:7], a[212:215], v[160:163]\n"
"v_mfma_f32_16x16x32_bf16 v[192:195], a[0:3], a[200:203], v[192:195]\n"
"s_waitcnt vmcnt(29)\n"
"v_mfma_f32_16x16x32_bf16 v[192:195], a[4:7], a[216:219], v[192:195]\n"
"v_mfma_f32_16x16x32_bf16 v[224:227], a[0:3], a[204:207], v[224:227]\n"
"ds_read_b64_tr_b16 a[0:1], v2 offset:16384\n"
"ds_read_b64_tr_b16 a[2:3], v2 offset:20480\n"
"s_waitcnt vmcnt(27)\n"
"v_mfma_f32_16x16x32_bf16 v[224:227], a[4:7], a[220:223], v[224:227]\n"
"buffer_load_dwordx4 a[164:167], v4, s[40:43], s55 offen offset:2048 sc1\n"
"v_mfma_f32_16x16x32_bf16 v[132:135], a[8:11], a[192:195], v[132:135]\n"
"ds_read_b64_tr_b16 a[4:5], v2 offset:24576\n"
"ds_read_b64_tr_b16 a[6:7], v2 offset:28672\n"
"v_mfma_f32_16x16x32_bf16 v[132:135], a[12:15], a[208:211], v[132:135]\n"
"v_mfma_f32_16x16x32_bf16 v[164:167], a[8:11], a[196:199], v[164:167]\n"
"v_mfma_f32_16x16x32_bf16 v[164:167], a[12:15], a[212:215], v[164:167]\n"
"v_mfma_f32_16x16x32_bf16 v[196:199], a[8:11], a[200:203], v[196:199]\n"
"v_mfma_f32_16x16x32_bf16 v[196:199], a[12:15], a[216:219], v[196:199]\n"
"buffer_load_dwordx4 a[168:171], v4, s[40:43], s56 offen offset:2048 sc1\n"
"v_mfma_f32_16x16x32_bf16 v[228:231], a[8:11], a[204:207], v[228:231]\n"
"ds_read_b64_tr_b16 a[8:9], v3 offset:16384\n"
"ds_read_b64_tr_b16 a[10:11], v3 offset:20480\n"
"v_mfma_f32_16x16x32_bf16 v[228:231], a[12:15], a[220:223], v[228:231]\n"
"ds_read_b64_tr_b16 a[12:13], v3 offset:24576\n"
"ds_read_b64_tr_b16 a[14:15], v3 offset:28672\n"
"v_mfma_f32_16x16x32_bf16 v[136:139], a[16:19], a[192:195], v[136:139]\n"
"s_waitcnt lgkmcnt(15)\n"
"v_mfma_f32_16x16x32_bf16 v[136:139], a[20:23], a[208:211], v[136:139]\n"
"v_mfma_f32_16x16x32_bf16 v[168:171], a[16:19], a[196:199], v[168:171]\n"
"v_mfma_f32_16x16x32_bf16 v[168:171], a[20:23], a[212:215], v[168:171]\n"
"buffer_load_dwordx4 a[172:175], v4, s[40:43], s57 offen offset:2048 sc1\n"
"v_mfma_f32_16x16x32_bf16 v[200:203], a[16:19], a[200:203], v[200:203]\n"
"v_mfma_f32_16x16x32_bf16 v[200:203], a[20:23], a[216:219], v[200:203]\n"
"v_mfma_f32_16x16x32_bf16 v[232:235], a[16:19], a[204:207], v[232:235]\n"
"ds_read_b64_tr_b16 a[16:17], v2 offset:16640\n"
"ds_read_b64_tr_b16 a[18:19], v2 offset:20736\n"
"v_mfma_f32_16x16x32_bf16 v[232:235], a[20:23], a[220:223], v[232:235]\n"
"ds_read_b64_tr_b16 a[20:21], v2 offset:24832\n"
"ds_read_b64_tr_b16 a[22:23], v2 offset:28928\n"
"v_mfma_f32_16x16x32_bf16 v[140:143], a[24:27], a[192:195], v[140:143]\n"
"v_mfma_f32_16x16x32_bf16 v[140:143], a[28:31], a[208:211], v[140:143]\n"
"buffer_load_dwordx4 a[176:179], v4, s[40:43], s54 offen offset:3072 sc1\n"
"v_mfma_f32_16x16x32_bf16 v[172:175], a[24:27], a[196:199], v[172:175]\n"
"v_mfma_f32_16x16x32_bf16 v[172:175], a[28:31], a[212:215], v[172:175]\n"
"v_mfma_f32_16x16x32_bf16 v[204:207], a[24:27], a[200:203], v[204:207]\n"
"v_mfma_f32_16x16x32_bf16 v[204:207], a[28:31], a[216:219], v[204:207]\n"
"v_mfma_f32_16x16x32_bf16 v[236:239], a[24:27], a[204:207], v[236:239]\n"
"ds_read_b64_tr_b16 a[24:25], v3 offset:16640\n"
"ds_read_b64_tr_b16 a[26:27], v3 offset:20736\n"
"v_mfma_f32_16x16x32_bf16 v[236:239], a[28:31], a[220:223], v[236:239]\n"
"buffer_load_dwordx4 a[180:183], v4, s[40:43], s55 offen offset:3072 sc1\n"
"v_mfma_f32_16x16x32_bf16 v[144:147], a[32:35], a[192:195], v[144:147]\n"
"ds_read_b64_tr_b16 a[28:29], v3 offset:24832\n"
"ds_read_b64_tr_b16 a[30:31], v3 offset:28928\n"
"v_mfma_f32_16x16x32_bf16 v[144:147], a[36:39], a[208:211], v[144:147]\n"
"v_mfma_f32_16x16x32_bf16 v[176:179], a[32:35], a[196:199], v[176:179]\n"
"v_mfma_f32_16x16x32_bf16 v[176:179], a[36:39], a[212:215], v[176:179]\n"
"v_mfma_f32_16x16x32_bf16 v[208:211], a[32:35], a[200:203], v[208:211]\n"
"v_mfma_f32_16x16x32_bf16 v[208:211], a[36:39], a[216:219], v[208:211]\n"
"buffer_load_dwordx4 a[184:187], v4, s[40:43], s56 offen offset:3072 sc1\n"
"v_mfma_f32_16x16x32_bf16 v[240:243], a[32:35], a[204:207], v[240:243]\n"
"ds_read_b64_tr_b16 a[32:33], v2 offset:16896\n"
"ds_read_b64_tr_b16 a[34:35], v2 offset:20992\n"
"v_mfma_f32_16x16x32_bf16 v[240:243], a[36:39], a[220:223], v[240:243]\n"
"ds_read_b64_tr_b16 a[36:37], v2 offset:25088\n"
"ds_read_b64_tr_b16 a[38:39], v2 offset:29184\n"
"v_mfma_f32_16x16x32_bf16 v[148:151], a[40:43], a[192:195], v[148:151]\n"
"v_mfma_f32_16x16x32_bf16 v[148:151], a[44:47], a[208:211], v[148:151]\n"
"v_mfma_f32_16x16x32_bf16 v[180:183], a[40:43], a[196:199], v[180:183]\n"
"v_mfma_f32_16x16x32_bf16 v[180:183], a[44:47], a[212:215], v[180:183]\n"
"buffer_load_dwordx4 a[188:191], v4, s[40:43], s57 offen offset:3072 sc1\n"
"v_mfma_f32_16x16x32_bf16 v[212:215], a[40:43], a[200:203], v[212:215]\n"
"v_mfma_f32_16x16x32_bf16 v[212:215], a[44:47], a[216:219], v[212:215]\n"
"v_mfma_f32_16x16x32_bf16 v[244:247], a[40:43], a[204:207], v[244:247]\n"
"ds_read_b64_tr_b16 a[40:41], v3 offset:16896\n"
"ds_read_b64_tr_b16 a[42:43], v3 offset:20992\n"
"v_mfma_f32_16x16x32_bf16 v[244:247], a[44:47], a[220:223], v[244:247]\n"
"ds_read_b64_tr_b16 a[44:45], v3 offset:25088\n"
"ds_read_b64_tr_b16 a[46:47], v3 offset:29184\n"
"s_waitcnt lgkmcnt(15)\n"
"v_mfma_f32_16x16x32_bf16 v[152:155], a[48:51], a[192:195], v[152:155]\n"
"v_mfma_f32_16x16x32_bf16 v[152:155], a[52:55], a[208:211], v[152:155]\n"
"s_add_u32 s54, s54, 0x2000\n"
"s_add_u32 s55, s55, 0x2000\n"
"s_add_u32 s56, s56, 0x2000\n"
"s_add_u32 s57, s57, 0x2000\n"
"v_mfma_f32_16x16x32_bf16 v[184:187], a[48:51], a[196:199], v[184:187]\n"
"v_mfma_f32_16x16x32_bf16 v[184:187], a[52:55], a[212:215], v[184:187]\n"
"v_mfma_f32_16x16x32_bf16 v[216:219], a[48:51], a[200:203], v[216:219]\n"
"v_mfma_f32_16x16x32_bf16 v[216:219], a[52:55], a[216:219], v[216:219]\n"
"v_mfma_f32_16x16x32_bf16 v[248:251], a[48:51], a[204:207], v[248:251]\n"
"ds_read_b64_tr_b16 a[48:49], v2 offset:17152\n"
"ds_read_b64_tr_b16 a[50:51], v2 offset:21248\n"
"v_mfma_f32_16x16x32_bf16 v[248:251], a[52:55], a[220:223], v[248:251]\n"
"ds_read_b64_tr_b16 a[52:53], v2 offset:25344\n"
"ds_read_b64_tr_b16 a[54:55], v2 offset:29440\n"
"v_mfma_f32_16x16x32_bf16 v[156:159], a[56:59], a[192:195], v[156:159]\n"
"v_mfma_f32_16x16x32_bf16 v[156:159], a[60:63], a[208:211], v[156:159]\n"
"v_mfma_f32_16x16x32_bf16 v[188:191], a[56:59], a[196:199], v[188:191]\n"
"v_mfma_f32_16x16x32_bf16 v[188:191], a[60:63], a[212:215], v[188:191]\n"
"v_mfma_f32_16x16x32_bf16 v[220:223], a[56:59], a[200:203], v[220:223]\n"
"buffer_load_dwordx4 a[192:195], v4, s[40:43], s54 offen offset:0 sc1\n"
"v_mfma_f32_16x16x32_bf16 v[220:223], a[60:63], a[216:219], v[220:223]\n"
"v_mfma_f32_16x16x32_bf16 v[252:255], a[56:59], a[204:207], v[252:255]\n"
"ds_read_b64_tr_b16 a[56:57], v3 offset:17152\n"
"ds_read_b64_tr_b16 a[58:59], v3 offset:21248\n"
"v_mfma_f32_16x16x32_bf16 v[252:255], a[60:63], a[220:223], v[252:255]\n"
"ds_read_b64_tr_b16 a[60:61], v3 offset:25344\n"
"ds_read_b64_tr_b16 a[62:63], v3 offset:29440\n"
"s_waitcnt vmcnt(32)\n"
"v_mfma_f32_16x16x32_bf16 v[128:131], a[0:3], a[224:227], v[128:131]\n"
"global_load_dwordx2 v[18:19], v7, %[ma] offset:128 sc1\n"
"global_load_dword v15, v7, %[xa] sc1\n"
"s_waitcnt vmcnt(30)\n"
"v_mfma_f32_16x16x32_bf16 v[128:131], a[4:7], a[240:243], v[128:131]\n"
"v_mfma_f32_16x16x32_bf16 v[160:163], a[0:3], a[228:231], v[160:163]\n"
"s_waitcnt vmcnt(29)\n"
"v_mfma_f32_16x16x32_bf16 v[160:163], a[4:7], a[244:247], v[160:163]\n"
"buffer_load_dwordx4 a[196:199], v4, s[40:43], s55 offen offset:0 sc1\n"
"v_mfma_f32_16x16x32_bf16 v[192:195], a[0:3], a[232:235], v[192:195]\n"
"s_waitcnt vmcnt(63)\n"
"s_barrier\n"
"v_xor_b32 v2, 0x10000, v2\n"
"v_xor_b32 v3, 0x10000, v3\n"
"s_waitcnt vmcnt(29)\n"
"v_mfma_f32_16x16x32_bf16 v[192:195], a[4:7], a[248:251], v[192:195]\n"
"s_add_u32 m0, s53, 0\n"
"s_add_u32 s52, s52, s70\n"
"buffer_load_dwordx4 v0, s[36:39], s52 offen offset:0 lds\n"
"s_mov_b32 s70, 187392\n"
"v_mfma_f32_16x16x32_bf16 v[224:227], a[0:3], a[236:239], v[224:227]\n"
"ds_read_b64_tr_b16 a[0:1], v2 offset:0\n"
"ds_read_b64_tr_b16 a[2:3], v2 offset:4096\n"
"s_waitcnt vmcnt(29)\n"
"v_mfma_f32_16x16x32_bf16 v[224:227], a[4:7], a[252:255], v[224:227]\n"
"ds_read_b64_tr_b16 a[4:5], v2 offset:8192\n"
"ds_read_b64_tr_b16 a[6:7], v2 offset:12288\n"
"v_mfma_f32_16x16x32_bf16 v[132:135], a[8:11], a[224:227], v[132:135]\n"
"v_mfma_f32_16x16x32_bf16 v[132:135], a[12:15], a[240:243], v[132:135]\n"
"v_mfma_f32_16x16x32_bf16 v[164:167], a[8:11], a[228:231], v[164:167]\n"
"buffer_load_dwordx4 a[200:203], v4, s[40:43], s56 offen offset:0 sc1\n"
"v_mfma_f32_16x16x32_bf16 v[164:167], a[12:15], a[244:247], v[164:167]\n"
"v_mfma_f32_16x16x32_bf16 v[196:199], a[8:11], a[232:235], v[196:199]\n"
"s_add_u32 m0, s53, 0\n"
"s_add_u32 s52, s52, 3072\n"
"buffer_load_dwordx4 v1, s[36:39], s52 offen offset:1024 lds\n"
"v_mfma_f32_16x16x32_bf16 v[196:199], a[12:15], a[248:251], v[196:199]\n"
"v_mfma_f32_16x16x32_bf16 v[228:231], a[8:11], a[236:239], v[228:231]\n"
"ds_read_b64_tr_b16 a[8:9], v3 offset:0\n"
"ds_read_b64_tr_b16 a[10:11], v3 offset:4096\n"
"v_mfma_f32_16x16x32_bf16 v[228:231], a[12:15], a[252:255], v[228:231]\n"
"ds_read_b64_tr_b16 a[12:13], v3 offset:8192\n"
"ds_read_b64_tr_b16 a[14:15], v3 offset:12288\n"
"s_waitcnt lgkmcnt(15)\n"
"v_mfma_f32_16x16x32_bf16 v[136:139], a[16:19], a[224:227], v[136:139]\n"
"v_mfma_f32_16x16x32_bf16 v[136:139], a[20:23], a[240:243], v[136:139]\n"
"buffer_load_dwordx4 a[204:207], v4, s[40:43], s57 offen offset:0 sc1\n"
"v_mfma_f32_16x16x32_bf16 v[168:171], a[16:19], a[228:231], v[168:171]\n"
"v_mfma_f32_16x16x32_bf16 v[168:171], a[20:23], a[244:247], v[168:171]\n"
"s_add_u32 m0, s53, 0\n"
"s_add_u32 s52, s52, 3072\n"
"buffer_load_dwordx4 v0, s[36:39], s52 offen offset:2048 lds\n"
"v_mfma_f32_16x16x32_bf16 v[200:203], a[16:19], a[232:235], v[200:203]\n"
"v_mfma_f32_16x16x32_bf16 v[200:203], a[20:23], a[248:251], v[200:203]\n"
"v_mfma_f32_16x16x32_bf16 v[232:235], a[16:19], a[236:239], v[232:235]\n"
"ds_read_b64_tr_b16 a[16:17], v2 offset:256\n"
"ds_read_b64_tr_b16 a[18:19], v2 offset:4352\n"
"v_mfma_f32_16x16x32_bf16 v[232:235], a[20:23], a[252:255], v[232:235]\n"
"ds_read_b64_tr_b16 a[20:21], v2 offset:8448\n"
"ds_read_b64_tr_b16 a[22:23], v2 offset:12544\n"
"v_mfma_f32_16x16x32_bf16 v[140:143], a[24:27], a[224:227], v[140:143]\n"
"buffer_load_dwordx4 a[208:211], v4, s[40:43], s54 offen offset:1024 sc1\n"
"v_mfma_f32_16x16x32_bf16 v[140:143], a[28:31], a[240:243], v[140:143]\n"
"v_mfma_f32_16x16x32_bf16 v[172:175], a[24:27], a[228:231], v[172:175]\n"
"s_add_u32 m0, s53, 0\n"
"s_add_u32 s52, s52, 3072\n"
"buffer_load_dwordx4 v1, s[36:39], s52 offen offset:3072 lds\n"
"v_mfma_f32_16x16x32_bf16 v[172:175], a[28:31], a[244:247], v[172:175]\n"
"v_mfma_f32_16x16x32_bf16 v[204:207], a[24:27], a[232:235], v[204:207]\n"
"v_mfma_f32_16x16x32_bf16 v[204:207], a[28:31], a[248:251], v[204:207]\n"
"v_mfma_f32_16x16x32_bf16 v[236:239], a[24:27], a[236:239], v[236:239]\n"
"ds_read_b64_tr_b16 a[24:25], v3 offset:256\n"
"ds_read_b64_tr_b16 a[26:27], v3 offset:4352\n"
"v_mfma_f32_16x16x32_bf16 v[236:239], a[28:31], a[252:255], v[236:239]\n"
"buffer_load_dwordx4 a[212:215], v4, s[40:43], s55 offen offset:1024 sc1\n"
"v_mfma_f32_16x16x32_bf16 v[144:147], a[32:35], a[224:227], v[144:147]\n"
"ds_read_b64_tr_b16 a[28:29], v3 offset:8448\n"
"ds_read_b64_tr_b16 a[30:31], v3 offset:12544\n"
"v_mfma_f32_16x16x32_bf16 v[144:147], a[36:39], a[240:243], v[144:147]\n"
"s_add_u32 m0, s53, 16384\n"
"s_add_u32 s52, s52, 56320\n"
"buffer_load_dwordx4 v0, s[36:39], s52 offen offset:0 lds\n"
"v_mfma_f32_16x16x32_bf16 v[176:179], a[32:35], a[228:231], v[176:179]\n"
"v_mfma_f32_16x16x32_bf16 v[176:179], a[36:39], a[244:247], v[176:179]\n"
"v_mfma_f32_16x16x32_bf16 v[208:211], a[32:35], a[232:235], v[208:211]\n"
"v_mfma_f32_16x16x32_bf16 v[208:211], a[36:39], a[248:251], v[208:211]\n"
"v_mfma_f32_16x16x32_bf16 v[240:243], a[32:35], a[236:239], v[240:243]\n"
"buffer_load_dwordx4 a[216:219], v4, s[40:43], s56 offen offset:1024 sc1\n"
"v_mfma_f32_16x16x32_bf16 v[240:243], a[36:39], a[252:255], v[240:243]\n"
"ds_read_b64_tr_b16 a[32:33], v2 offset:512\n"
"ds_read_b64_tr_b16 a[34:35], v2 offset:4608\n"
"v_mfma_f32_16x16x32_bf16 v[148:151], a[40:43], a[224:227], v[148:151]\n"
"s_add_u32 m0, s53, 16384\n"
"s_add_u32 s52, s52, 3072\n"
"buffer_load_dwordx4 v1, s[36:39], s52 offen offset:1024 lds\n"
"v_mfma_f32_16x16x32_bf16 v[148:151], a[44:47], a[240:243], v[148:151]\n"
"ds_read_b64_tr_b16 a[36:37], v2 offset:8704\n"
"ds_read_b64_tr_b16 a[38:39], v2 offset:12800\n"
"v_mfma_f32_16x16x32_bf16 v[180:183], a[40:43], a[228:231], v[180:183]\n"
"v_mfma_f32_16x16x32_bf16 v[180:183], a[44:47], a[244:247], v[180:183]\n"
"v_mfma_f32_16x16x32_bf16 v[212:215], a[40:43], a[232:235], v[212:215]\n"
"v_mfma_f32_16x16x32_bf16 v[212:215], a[44:47], a[248:251], v[212:215]\n"
"buffer_load_dwordx4 a[220:223], v4, s[40:43], s57 offen offset:1024 sc1\n"
"v_mfma_f32_16x16x32_bf16 v[244:247], a[40:43], a[236:239], v[244:247]\n"
"ds_read_b64_tr_b16 a[40:41], v3 offset:512\n"
"ds_read_b64_tr_b16 a[42:43], v3 offset:4608\n"
"v_mfma_f32_16x16x32_bf16 v[244:247], a[44:47], a[252:255], v[244:247]\n"
"s_add_u32 m0, s53, 16384\n"
"s_add_u32 s52, s52, 3072\n"
"buffer_load_dwordx4 v0, s[36:39], s52 offen offset:2048 lds\n"
"s_waitcnt lgkmcnt(15)\n"
"v_mfma_f32_16x16x32_bf16 v[152:155], a[48:51], a[224:227], v[152:155]\n"
"ds_read_b64_tr_b16 a[44:45], v3 offset:8704\n"
"ds_read_b64_tr_b16 a[46:47], v3 offset:12800\n"
"v_mfma_f32_16x16x32_bf16 v[152:155], a[52:55], a[240:243], v[152:155]\n"
"v_mfma_f32_16x16x32_bf16 v[184:187], a[48:51], a[228:231], v[184:187]\n"
"v_mfma_f32_16x16x32_bf16 v[184:187], a[52:55], a[244:247], v[184:187]\n"
"v_mfma_f32_16x16x32_bf16 v[216:219], a[48:51], a[232:235], v[216:219]\n"
"v_mfma_f32_16x16x32_bf16 v[216:219], a[52:55], a[248:251], v[216:219]\n"
"v_mfma_f32_16x16x32_bf16 v[248:251], a[48:51], a[236:239], v[248:251]\n"
"s_add_u32 m0, s53, 16384\n"
"s_add_u32 s52, s52, 3072\n"
"buffer_load_dwordx4 v1, s[36:39], s52 offen offset:3072 lds\n"
"s_xor_b32 s53, s53, 0x8000\n"
"v_mfma_f32_16x16x32_bf16 v[248:251], a[52:55], a[252:255], v[248:251]\n"
"ds_read_b64_tr_b16 a[48:49], v2 offset:768\n"
"ds_read_b64_tr_b16 a[50:51], v2 offset:4864\n"
"v_mfma_f32_16x16x32_bf16 v[156:159], a[56:59], a[224:227], v[156:159]\n"
"ds_read_b64_tr_b16 a[52:53], v2 offset:8960\n"
"ds_read_b64_tr_b16 a[54:55], v2 offset:13056\n"
"v_mfma_f32_16x16x32_bf16 v[156:159], a[60:63], a[240:243], v[156:159]\n"
"v_mfma_f32_16x16x32_bf16 v[188:191], a[56:59], a[228:231], v[188:191]\n"
"v_mfma_f32_16x16x32_bf16 v[188:191], a[60:63], a[244:247], v[188:191]\n"
"v_mfma_f32_16x16x32_bf16 v[220:223], a[56:59], a[232:235], v[220:223]\n"
"v_mfma_f32_16x16x32_bf16 v[220:223], a[60:63], a[248:251], v[220:223]\n"
"v_mfma_f32_16x16x32_bf16 v[252:255], a[56:59], a[236:239], v[252:255]\n"
"ds_read_b64_tr_b16 a[56:57], v3 offset:768\n"
"ds_read_b64_tr_b16 a[58:59], v3 offset:4864\n"
"v_mfma_f32_16x16x32_bf16 v[252:255], a[60:63], a[252:255], v[252:255]\n"
"s_sub_u32 s58, s58, 1\n"
"s_cmp_lg_u32 s58, 0\n"
"s_cbranch_scc1 .LLc_%=\n"
"v_cvt_pk_bf16_f32 v136, v136, v137\n"
"v_cvt_pk_bf16_f32 v137, v138, v139\n"
"v_cvt_pk_bf16_f32 v138, v140, v141\n"
"v_cvt_pk_bf16_f32 v139, v142, v143\n"
"buffer_store_dwordx4 v[136:139], v6, s[44:47], s62 offen offset:0 sc1\n"
"v_cvt_pk_bf16_f32 v144, v144, v145\n"
"v_cvt_pk_bf16_f32 v145, v146, v147\n"
"v_cvt_pk_bf16_f32 v146, v148, v149\n"
"v_cvt_pk_bf16_f32 v147, v150, v151\n"
"buffer_store_dwordx4 v[144:147], v6, s[44:47], s63 offen offset:0 sc1\n"
"v_cvt_pk_bf16_f32 v152, v152, v153\n"
"v_cvt_pk_bf16_f32 v153, v154, v155\n"
"v_cvt_pk_bf16_f32 v154, v156, v157\n"
"v_cvt_pk_bf16_f32 v155, v158, v159\n"
"buffer_store_dwordx4 v[152:155], v6, s[44:47], s64 offen offset:0 sc1\n"
"v_cvt_pk_bf16_f32 v168, v168, v169\n"
"v_cvt_pk_bf16_f32 v169, v170, v171\n"
"v_cvt_pk_bf16_f32 v170, v172, v173\n"
"v_cvt_pk_bf16_f32 v171, v174, v175\n"
"buffer_store_dwordx4 v[168:171], v6, s[44:47], s62 offen offset:1024 sc1\n"
"v_cvt_pk_bf16_f32 v176, v176, v177\n"
"v_cvt_pk_bf16_f32 v177, v178, v179\n"
"v_cvt_pk_bf16_f32 v178, v180, v181\n"
"v_cvt_pk_bf16_f32 v179, v182, v183\n"
"buffer_store_dwordx4 v[176:179], v6, s[44:47], s63 offen offset:1024 sc1\n"
"v_cvt_pk_bf16_f32 v184, v184, v185\n"
"v_cvt_pk_bf16_f32 v185, v186, v187\n"
"v_cvt_pk_bf16_f32 v186, v188, v189\n"
"v_cvt_pk_bf16_f32 v187, v190, v191\n"
"buffer_store_dwordx4 v[184:187], v6, s[44:47], s64 offen offset:1024 sc1\n"
"v_cvt_pk_bf16_f32 v200, v200, v201\n"
"v_cvt_pk_bf16_f32 v201, v202, v203\n"
"v_cvt_pk_bf16_f32 v202, v204, v205\n"
"v_cvt_pk_bf16_f32 v203, v206, v207\n"
"buffer_store_dwordx4 v[200:203], v6, s[44:47], s62 offen offset:2048 sc1\n"
"v_cvt_pk_bf16_f32 v208, v208, v209\n"
"v_cvt_pk_bf16_f32 v209, v210, v211\n"
"v_cvt_pk_bf16_f32 v210, v212, v213\n"
"v_cvt_pk_bf16_f32 v211, v214, v215\n"
"buffer_store_dwordx4 v[208:211], v6, s[44:47], s63 offen offset:2048 sc1\n"
"v_cvt_pk_bf16_f32 v216, v216, v217\n"
"v_cvt_pk_bf16_f32 v217, v218, v219\n"
"v_cvt_pk_bf16_f32 v218, v220, v221\n"
"v_cvt_pk_bf16_f32 v219, v222, v223\n"
"buffer_store_dwordx4 v[216:219], v6, s[44:47], s64 offen offset:2048 sc1\n"
"v_cvt_pk_bf16_f32 v232, v232, v233\n"
"v_cvt_pk_bf16_f32 v233, v234, v235\n"
"v_cvt_pk_bf16_f32 v234, v236, v237\n"
"v_cvt_pk_bf16_f32 v235, v238, v239\n"
"buffer_store_dwordx4 v[232:235], v6, s[44:47], s62 offen offset:3072 sc1\n"
"v_cvt_pk_bf16_f32 v240, v240, v241\n"
"v_cvt_pk_bf16_f32 v241, v242, v243\n"
"v_cvt_pk_bf16_f32 v242, v244, v245\n"
"v_cvt_pk_bf16_f32 v243, v246, v247\n"
"buffer_store_dwordx4 v[240:243], v6, s[44:47], s63 offen offset:3072 sc1\n"
"v_cvt_pk_bf16_f32 v248, v248, v249\n"
"v_cvt_pk_bf16_f32 v249, v250, v251\n"
"v_cvt_pk_bf16_f32 v250, v252, v253\n"
"v_cvt_pk_bf16_f32 v251, v254, v255\n"
"buffer_store_dwordx4 v[248:251], v6, s[44:47], s64 offen offset:3072 sc1\n"
"s_waitcnt vmcnt(0)\n"
"s_mov_b64 exec, 15\n"
"global_store_dwordx2 v8, v[14:15], %[xw] sc1\n"
"s_mov_b64 exec, -1\n"
"s_mov_b32 s60, 0x200000\n"
".Lpxc_%=:\n"
"global_load_dwordx2 v[16:17], v7, %[xa] offset:0 sc1\n"
"s_waitcnt vmcnt(0)\n"
"v_cmp_ne_u32 vcc, v16, v15\n"
"s_nop 1\n"
"s_cmp_eq_u64 vcc, 0\n"
"s_cbranch_scc1 .Lexc_%=\n"
"v_mov_b32 v15, v16\n"
"s_mov_b64 exec, 15\n"
"global_store_dwordx2 v8, v[14:15], %[xw] offset:0 sc1\n"
"s_mov_b64 exec, -1\n"
".Lexc_%=:\n"
"v_cmp_ne_u32 vcc, v17, v14\n"
"s_nop 1\n"
"s_cmp_eq_u64 vcc, 0\n"
"s_cbranch_scc1 .Lqxc_%=\n"
"s_sleep 1\n"
"s_sub_u32 s60, s60, 1\n"
"s_cmp_lg_u32 s60, 0\n"
"s_cbranch_scc1 .Lpxc_%=\n"
".Lqxc_%=:\n"
"s_waitcnt vmcnt(0)\n"
"buffer_load_dwordx4 v[136:139], v6, s[44:47], s65 offen offset:0 sc1\n"
"buffer_load_dwordx4 v[144:147], v6, s[44:47], s66 offen offset:0 sc1\n"
"buffer_load_dwordx4 v[152:155], v6, s[44:47], s67 offen offset:0 sc1\n"
"buffer_load_dwordx4 v[168:171], v6, s[44:47], s65 offen offset:1024 sc1\n"
"buffer_load_dwordx4 v[176:179], v6, s[44:47], s66 offen offset:1024 sc1\n"
"buffer_load_dwordx4 v[184:187], v6, s[44:47], s67 offen offset:1024 sc1\n"
"buffer_load_dwordx4 v[200:203], v6, s[44:47], s65 offen offset:2048 sc1\n"
"buffer_load_dwordx4 v[208:211], v6, s[44:47], s66 offen offset:2048 sc1\n"
"buffer_load_dwordx4 v[216:219], v6, s[44:47], s67 offen offset:2048 sc1\n"
"buffer_load_dwordx4 v[232:235], v6, s[44:47], s65 offen offset:3072 sc1\n"
"buffer_load_dwordx4 v[240:243], v6, s[44:47], s66 offen offset:3072 sc1\n"
"buffer_load_dwordx4 v[248:251], v6, s[44:47], s67 offen offset:3072 sc1\n"
"s_waitcnt vmcnt(11)\n"
"v_lshlrev_b32 v140, 16, v136\n"
"v_and_b32 v141, 0xffff0000, v136\n"
"v_add_f32 v128, v128, v140\n"
"v_add_f32 v129, v129, v141\n"
"v_lshlrev_b32 v142, 16, v137\n"
"v_and_b32 v143, 0xffff0000, v137\n"
"v_add_f32 v130, v130, v142\n"
"v_add_f32 v131, v131, v143\n"
"v_lshlrev_b32 v140, 16, v138\n"
"v_and_b32 v141, 0xffff0000, v138\n"
"v_add_f32 v132, v132, v140\n"
"v_add_f32 v133, v133, v141\n"
"v_lshlrev_b32 v142, 16, v139\n"
"v_and_b32 v143, 0xffff0000, v139\n"
"v_add_f32 v134, v134, v142\n"
"v_add_f32 v135, v135, v143\n"
"s_waitcnt vmcnt(10)\n"
"v_lshlrev_b32 v148, 16, v144\n"
"v_and_b32 v149, 0xffff0000, v144\n"
"v_add_f32 v128, v128, v148\n"
"v_add_f32 v129, v129, v149\n"
"v_lshlrev_b32 v150, 16, v145\n"
"v_and_b32 v151, 0xffff0000, v145\n"
"v_add_f32 v130, v130, v150\n"
"v_add_f32 v131, v131, v151\n"
"v_lshlrev_b32 v148, 16, v146\n"
"v_and_b32 v149, 0xffff0000, v146\n"
"v_add_f32 v132, v132, v148\n"
"v_add_f32 v133, v133, v149\n"
"v_lshlrev_b32 v150, 16, v147\n"
"v_and_b32 v151, 0xffff0000, v147\n"
"v_add_f32 v134, v134, v150\n"
"v_add_f32 v135, v135, v151\n"
"s_waitcnt vmcnt(9)\n"
"v_lshlrev_b32 v156, 16, v152\n"
"v_and_b32 v157, 0xffff0000, v152\n"
"v_add_f32 v128, v128, v156\n"
"v_add_f32 v129, v129, v157\n"
"v_lshlrev_b32 v158, 16, v153\n"
"v_and_b32 v159, 0xffff0000, v153\n"
"v_add_f32 v130, v130, v158\n"
"v_add_f32 v131, v131, v159\n"
"v_lshlrev_b32 v156, 16, v154\n"
"v_and_b32 v157, 0xffff0000, v154\n"
"v_add_f32 v132, v132, v156\n"
"v_add_f32 v133, v133, v157\n"
"v_lshlrev_b32 v158, 16, v155\n"
"v_and_b32 v159, 0xffff0000, v155\n"
"v_add_f32 v134, v134, v158\n"
"v_add_f32 v135, v135, v159\n"
"v_cvt_pk_bf16_f32 v128, v128, v129\n"
"v_cvt_pk_bf16_f32 v129, v130, v131\n"
"v_cvt_pk_bf16_f32 v130, v132, v133\n"
"v_cvt_pk_bf16_f32 v131, v134, v135\n"
"s_mov_b32 s59, 0\n"
"buffer_store_dwordx2 v[128:129], v5, s[48:51], s59 offen offset:0\n"
"buffer_store_dwordx2 v[130:131], v5, s[48:51], s59 offen offset:32\n"
"s_waitcnt vmcnt(10)\n"
"v_lshlrev_b32 v172, 16, v168\n"
"v_and_b32 v173, 0xffff0000, v168\n"
"v_add_f32 v160, v160, v172\n"
"v_add_f32 v161, v161, v173\n"
"v_lshlrev_b32 v174, 16, v169\n"
"v_and_b32 v175, 0xffff0000, v169\n"
"v_add_f32 v162, v162, v174\n"
"v_add_f32 v163, v163, v175\n"
"v_lshlrev_b32 v172, 16, v170\n"
"v_and_b32 v173, 0xffff0000, v170\n"
"v_add_f32 v164, v164, v172\n"
"v_add_f32 v165, v165, v173\n"
"v_lshlrev_b32 v174, 16, v171\n"
"v_and_b32 v175, 0xffff0000, v171\n"
"v_add_f32 v166, v166, v174\n"
"v_add_f32 v167, v167, v175\n"
"s_waitcnt vmcnt(9)\n"
"v_lshlrev_b32 v180, 16, v176\n"
"v_and_b32 v181, 0xffff0000, v176\n"
"v_add_f32 v160, v160, v180\n"
"v_add_f32 v161, v161, v181\n"
"v_lshlrev_b32 v182, 16, v177\n"
"v_and_b32 v183, 0xffff0000, v177\n"
"v_add_f32 v162, v162, v182\n"
"v_add_f32 v163, v163, v183\n"
"v_lshlrev_b32 v180, 16, v178\n"
"v_and_b32 v181, 0xffff0000, v178\n"
"v_add_f32 v164, v164, v180\n"
"v_add_f32 v165, v165, v181\n"
"v_lshlrev_b32 v182, 16, v179\n"
"v_and_b32 v183, 0xffff0000, v179\n"
"v_add_f32 v166, v166, v182\n"
"v_add_f32 v167, v167, v183\n"
"s_waitcnt vmcnt(8)\n"
"v_lshlrev_b32 v188, 16, v184\n"
"v_and_b32 v189, 0xffff0000, v184\n"
"v_add_f32 v160, v160, v188\n"
"v_add_f32 v161, v161, v189\n"
"v_lshlrev_b32 v190, 16, v185\n"
"v_and_b32 v191, 0xffff0000, v185\n"
"v_add_f32 v162, v162, v190\n"
"v_add_f32 v163, v163, v191\n"
"v_lshlrev_b32 v188, 16, v186\n"
"v_and_b32 v189, 0xffff0000, v186\n"
"v_add_f32 v164, v164, v188\n"
"v_add_f32 v165, v165, v189\n"
"v_lshlrev_b32 v190, 16, v187\n"
"v_and_b32 v191, 0xffff0000, v187\n"
"v_add_f32 v166, v166, v190\n"
"v_add_f32 v167, v167, v191\n"
"v_cvt_pk_bf16_f32 v160, v160, v161\n"
"v_cvt_pk_bf16_f32 v161, v162, v163\n"
"v_cvt_pk_bf16_f32 v162, v164, v165\n"
"v_cvt_pk_bf16_f32 v163, v166, v167\n"
"s_mov_b32 s59, 32768\n"
"buffer_store_dwordx2 v[160:161], v5, s[48:51], s59 offen offset:0\n"
"buffer_store_dwordx2 v[162:163], v5, s[48:51], s59 offen offset:32\n"
"s_waitcnt vmcnt(9)\n"
"v_lshlrev_b32 v204, 16, v200\n"
"v_and_b32 v205, 0xffff0000, v200\n"
"v_add_f32 v192, v192, v204\n"
"v_add_f32 v193, v193, v205\n"
"v_lshlrev_b32 v206, 16, v201\n"
"v_and_b32 v207, 0xffff0000, v201\n"
"v_add_f32 v194, v194, v206\n"
"v_add_f32 v195, v195, v207\n"
"v_lshlrev_b32 v204, 16, v202\n"
"v_and_b32 v205, 0xffff0000, v202\n"
"v_add_f32 v196, v196, v204\n"
"v_add_f32 v197, v197, v205\n"
"v_lshlrev_b32 v206, 16, v203\n"
"v_and_b32 v207, 0xffff0000, v203\n"
"v_add_f32 v198, v198, v206\n"
"v_add_f32 v199, v199, v207\n"
"s_waitcnt vmcnt(8)\n"
"v_lshlrev_b32 v212, 16, v208\n"
"v_and_b32 v213, 0xffff0000, v208\n"
"v_add_f32 v192, v192, v212\n"
"v_add_f32 v193, v193, v213\n"
"v_lshlrev_b32 v214, 16, v209\n"
"v_and_b32 v215, 0xffff0000, v209\n"
"v_add_f32 v194, v194, v214\n"
"v_add_f32 v195, v195, v215\n"
"v_lshlrev_b32 v212, 16, v210\n"
"v_and_b32 v213, 0xffff0000, v210\n"
"v_add_f32 v196, v196, v212\n"
"v_add_f32 v197, v197, v213\n"
"v_lshlrev_b32 v214, 16, v211\n"
"v_and_b32 v215, 0xffff0000, v211\n"
"v_add_f32 v198, v198, v214\n"
"v_add_f32 v199, v199, v215\n"
"s_waitcnt vmcnt(7)\n"
"v_lshlrev_b32 v220, 16, v216\n"
"v_and_b32 v221, 0xffff0000, v216\n"
"v_add_f32 v192, v192, v220\n"
"v_add_f32 v193, v193, v221\n"
"v_lshlrev_b32 v222, 16, v217\n"
"v_and_b32 v223, 0xffff0000, v217\n"
"v_add_f32 v194, v194, v222\n"
"v_add_f32 v195, v195, v223\n"
"v_lshlrev_b32 v220, 16, v218\n"
"v_and_b32 v221, 0xffff0000, v218\n"
"v_add_f32 v196, v196, v220\n"
"v_add_f32 v197, v197, v221\n"
"v_lshlrev_b32 v222, 16, v219\n"
"v_and_b32 v223, 0xffff0000, v219\n"
"v_add_f32 v198, v198, v222\n"
"v_add_f32 v199, v199, v223\n"
"v_cvt_pk_bf16_f32 v192, v192, v193\n"
"v_cvt_pk_bf16_f32 v193, v194, v195\n"
"v_cvt_pk_bf16_f32 v194, v196, v197\n"
"v_cvt_pk_bf16_f32 v195, v198, v199\n"
"s_mov_b32 s59, 65536\n"
"buffer_store_dwordx2 v[192:193], v5, s[48:51], s59 offen offset:0\n"
"buffer_store_dwordx2 v[194:195], v5, s[48:51], s59 offen offset:32\n"
"s_waitcnt vmcnt(8)\n"
"v_lshlrev_b32 v236, 16, v232\n"
"v_and_b32 v237, 0xffff0000, v232\n"
"v_add_f32 v224, v224, v236\n"
"v_add_f32 v225, v225, v237\n"
"v_lshlrev_b32 v238, 16, v233\n"
"v_and_b32 v239, 0xffff0000, v233\n"
"v_add_f32 v226, v226, v238\n"
"v_add_f32 v227, v227, v239\n"
"v_lshlrev_b32 v236, 16, v234\n"
"v_and_b32 v237, 0xffff0000, v234\n"
"v_add_f32 v228, v228, v236\n"
"v_add_f32 v229, v229, v237\n"
"v_lshlrev_b32 v238, 16, v235\n"
"v_and_b32 v239, 0xffff0000, v235\n"
"v_add_f32 v230, v230, v238\n"
"v_add_f32 v231, v231, v239\n"
"s_waitcnt vmcnt(7)\n"
"v_lshlrev_b32 v244, 16, v240\n"
"v_and_b32 v245, 0xffff0000, v240\n"
"v_add_f32 v224, v224, v244\n"
"v_add_f32 v225, v225, v245\n"
"v_lshlrev_b32 v246, 16, v241\n"
"v_and_b32 v247, 0xffff0000, v241\n"
"v_add_f32 v226, v226, v246\n"
"v_add_f32 v227, v227, v247\n"
"v_lshlrev_b32 v244, 16, v242\n"
"v_and_b32 v245, 0xffff0000, v242\n"
"v_add_f32 v228, v228, v244\n"
"v_add_f32 v229, v229, v245\n"
"v_lshlrev_b32 v246, 16, v243\n"
"v_and_b32 v247, 0xffff0000, v243\n"
"v_add_f32 v230, v230, v246\n"
"v_add_f32 v231, v231, v247\n"
"s_waitcnt vmcnt(6)\n"
"v_lshlrev_b32 v252, 16, v248\n"
"v_and_b32 v253, 0xffff0000, v248\n"
"v_add_f32 v224, v224, v252\n"
"v_add_f32 v225, v225, v253\n"
"v_lshlrev_b32 v254, 16, v249\n"
"v_and_b32 v255, 0xffff0000, v249\n"
"v_add_f32 v226, v226, v254\n"
"v_add_f32 v227, v227, v255\n"
"v_lshlrev_b32 v252, 16, v250\n"
"v_and_b32 v253, 0xffff0000, v250\n"
"v_add_f32 v228, v228, v252\n"
"v_add_f32 v229, v229, v253\n"
"v_lshlrev_b32 v254, 16, v251\n"
"v_and_b32 v255, 0xffff0000, v251\n"
"v_add_f32 v230, v230, v254\n"
"v_add_f32 v231, v231, v255\n"
"v_cvt_pk_bf16_f32 v224, v224, v225\n"
"v_cvt_pk_bf16_f32 v225, v226, v227\n"
"v_cvt_pk_bf16_f32 v226, v228, v229\n"
"v_cvt_pk_bf16_f32 v227, v230, v231\n"
"s_mov_b32 s59, 98304\n"
"buffer_store_dwordx2 v[224:225], v5, s[48:51], s59 offen offset:0\n"
"buffer_store_dwordx2 v[226:227], v5, s[48:51], s59 offen offset:32\n"
"s_waitcnt vmcnt(0)\n"
"s_branch .Lzc_%=\n"
".Ls1c_%=:\n"
"s_mov_b32 s60, 0x200000\n"
".Lpcc_%=:\n"
"global_load_dwordx2 v[16:17], v7, %[ma] offset:128 sc1\n"
"s_waitcnt vmcnt(0)\n"
"v_cmp_ne_u32 vcc, v16, v13\n"
"s_nop 1\n"
"s_cmp_eq_u64 vcc, 0\n"
"s_cbranch_scc1 .Lecc_%=\n"
"v_mov_b32 v13, v16\n"
"s_mov_b64 exec, 15\n"
"global_store_dwordx2 v8, v[12:13], %[mw] offset:128 sc1\n"
"s_mov_b64 exec, -1\n"
".Lecc_%=:\n"
"v_cmp_ne_u32 vcc, v17, v12\n"
"s_nop 1\n"
"s_cmp_eq_u64 vcc, 0\n"
"s_cbranch_scc1 .Lqcc_%=\n"
"s_sleep 1\n"
"s_sub_u32 s60, s60, 1\n"
"s_cmp_lg_u32 s60, 0\n"
"s_cbranch_scc1 .Lpcc_%=\n"
".Lqcc_%=:\n"
"s_waitcnt vmcnt(0)\n"
"s_branch .Lr1c_%=\n"
".Lzc_%=:\n"
"s_branch .Lend_%=\n"
".Lw3_%=:\n"
"s_mov_b32 s36, %[wl]\n"
"s_and_b32 s37, %[wh], 0xffff\n"
"s_mov_b32 s38, 1047808\n"
"s_mov_b32 s39, 0x20000\n"
"s_mov_b32 s40, %[ol]\n"
"s_and_b32 s41, %[oh], 0xffff\n"
"s_mov_b32 s42, %[osz]\n"
"s_mov_b32 s43, 0x20000\n"
"s_mov_b32 s44, %[xl]\n"
"s_and_b32 s45, %[xh], 0xffff\n"
"s_mov_b32 s46, 11796480\n"
"s_mov_b32 s47, 0x20000\n"
"s_mov_b32 s48, %[yl]\n"
"s_and_b32 s49, %[yh], 0xffff\n"
"s_mov_b32 s50, 489536\n"
"s_mov_b32 s51, 0x20000\n"
"v_mbcnt_lo_u32_b32 v20, -1, 0\n"
"v_mbcnt_hi_u32_b32 v20, -1, v20\n"
"s_lshl_b32 s61, %[wv], 12\n"
"v_and_b32 v21, 15, v20\n"
"v_lshrrev_b32 v22, 4, v20\n"
"v_bfe_u32 v23, v20, 1, 2\n"
"v_bfe_u32 v24, v20, 3, 1\n"
"v_and_b32 v25, 1, v20\n"
"v_lshl_add_u32 v26, v22, 1, v24\n"
"v_lshlrev_b32 v26, 5, v26\n"
"v_lshl_add_u32 v26, v25, 4, v26\n"
"s_lshl_b32 s59, %[ksp], 6\n"
"v_add_u32 v27, s59, v26\n"
"v_and_b32 v27, 0xff, v27\n"
"v_lshl_add_u32 v0, v23, 10, v27\n"
"s_lshl_b32 s60, %[wv], 14\n"
"v_add_u32 v0, s60, v0\n"
"v_xor_b32 v26, 32, v26\n"
"v_add_u32 v27, s59, v26\n"
"v_and_b32 v27, 0xff, v27\n"
"v_lshl_add_u32 v1, v23, 10, v27\n"
"s_lshl_b32 s60, %[wv], 14\n"
"v_add_u32 v1, s60, v1\n"
"s_mov_b32 s52, 0xfffd2400\n"
"s_mov_b32 s53, s61\n"
"s_mov_b32 s70, 0xfff4dc00\n"
"s_mov_b32 s72, 0x2000\n"
"s_mov_b32 s73, 0xffffb000\n"
"s_mov_b32 s74, 0x2000\n"
"s_mov_b32 s75, 0x400000\n"
"v_and_b32 v8, 3, v20\n"
"v_lshlrev_b32 v7, 5, v8\n"
"v_lshlrev_b32 v8, 3, v8\n"
"s_lshl_b32 s59, %[wv], 19\n"
"v_lshlrev_b32 v4, 4, v20\n"
"v_add_u32 v4, s59, v4\n"
"s_mov_b32 s54, 0\n"
"s_mov_b32 s55, 131072\n"
"s_mov_b32 s56, 262144\n"
"s_add_u32 m0, s53, 0\n"
"s_add_u32 s52, s52, 187392\n"
"buffer_load_dwordx4 v0, s[36:39], s52 offen offset:0 lds\n"
"s_add_u32 m0, s53, 0\n"
"s_add_u32 s52, s52, 3072\n"
"buffer_load_dwordx4 v1, s[36:39], s52 offen offset:1024 lds\n"
"s_add_u32 m0, s53, 0\n"
"s_add_u32 s52, s52, 3072\n"
"buffer_load_dwordx4 v0, s[36:39], s52 offen offset:2048 lds\n"
"s_add_u32 m0, s53, 0\n"
"s_add_u32 s52, s52, 3072\n"
"buffer_load_dwordx4 v1, s[36:39], s52 offen offset:3072 lds\n"
"s_add_u32 m0, s53, 16384\n"
"s_add_u32 s52, s52, 56320\n"
"buffer_load_dwordx4 v0, s[36:39], s52 offen offset:0 lds\n"
"s_add_u32 m0, s53, 16384\n"
"s_add_u32 s52, s52, 3072\n"
"buffer_load_dwordx4 v1, s[36:39], s52 offen offset:1024 lds\n"
"s_add_u32 m0, s53, 16384\n"
"s_add_u32 s52, s52, 3072\n"
"buffer_load_dwordx4 v0, s[36:39], s52 offen offset:2048 lds\n"
"s_add_u32 m0, s53, 16384\n"
"s_add_u32 s52, s52, 3072\n"
"buffer_load_dwordx4 v1, s[36:39], s52 offen offset:3072 lds\n"
"s_xor_b32 s53, s53, 0x8000\n"
"s_cmp_eq_u32 %[nok], 0\n"
"s_cbranch_scc1 .Lfd_%=\n"
"s_mov_b32 s60, 0x200000\n"
"s_waitcnt vmcnt(0)\n"
"global_load_dwordx2 v[10:11], v8, %[mw] offset:0 sc1\n"
".Lpad_%=:\n"
"global_load_dwordx2 v[16:17], v7, %[ma] offset:0 sc1\n"
"s_waitcnt vmcnt(0)\n"
"v_cmp_ne_u32 vcc, v16, v11\n"
"s_nop 1\n"
"s_cmp_eq_u64 vcc, 0\n"
"s_cbranch_scc1 .Lead_%=\n"
"v_mov_b32 v11, v16\n"
"s_mov_b64 exec, 15\n"
"global_store_dwordx2 v8, v[10:11], %[mw] offset:0 sc1\n"
"s_mov_b64 exec, -1\n"
".Lead_%=:\n"
"v_cmp_ne_u32 vcc, v17, v10\n"
"s_nop 1\n"
"s_cmp_eq_u64 vcc, 0\n"
"s_cbranch_scc1 .Lqad_%=\n"
"s_sleep 1\n"
"s_sub_u32 s60, s60, 1\n"
"s_cmp_lg_u32 s60, 0\n"
"s_cbranch_scc1 .Lpad_%=\n"
".Lqad_%=:\n"
"s_mov_b32 s60, 0x200000\n"
"s_waitcnt vmcnt(0)\n"
"global_load_dwordx2 v[12:13], v8, %[mw] offset:128 sc1\n"
".Lpbd_%=:\n"
"global_load_dwordx2 v[16:17], v7, %[ma] offset:128 sc1\n"
"s_waitcnt vmcnt(0)\n"
"v_cmp_ne_u32 vcc, v16, v13\n"
"s_nop 1\n"
"s_cmp_eq_u64 vcc, 0\n"
"s_cbranch_scc1 .Lebd_%=\n"
"v_mov_b32 v13, v16\n"
"s_mov_b64 exec, 15\n"
"global_store_dwordx2 v8, v[12:13], %[mw] offset:128 sc1\n"
"s_mov_b64 exec, -1\n"
".Lebd_%=:\n"
"v_cmp_ne_u32 vcc, v17, v12\n"
"s_nop 1\n"
"s_cmp_eq_u64 vcc, 0\n"
"s_cbranch_scc1 .Lqbd_%=\n"
"s_sleep 1\n"
"s_sub_u32 s60, s60, 1\n"
"s_cmp_lg_u32 s60, 0\n"
"s_cbranch_scc1 .Lpbd_%=\n"
".Lqbd_%=:\n"
".Lfd_%=:\n"
"global_load_dword v14, v7, %[xa] offset:4 sc1\n"
"buffer_load_dwordx4 a[128:131], v4, s[40:43], s54 offen offset:0 sc1\n"
"buffer_load_dwordx4 a[132:135], v4, s[40:43], s55 offen offset:0 sc1\n"
"buffer_load_dwordx4 a[136:139], v4, s[40:43], s56 offen offset:0 sc1\n"
"buffer_load_dwordx4 a[144:147], v4, s[40:43], s54 offen offset:1024 sc1\n"
"buffer_load_dwordx4 a[148:151], v4, s[40:43], s55 offen offset:1024 sc1\n"
"buffer_load_dwordx4 a[152:155], v4, s[40:43], s56 offen offset:1024 sc1\n"
"buffer_load_dwordx4 a[160:163], v4, s[40:43], s54 offen offset:2048 sc1\n"
"buffer_load_dwordx4 a[164:167], v4, s[40:43], s55 offen offset:2048 sc1\n"
"buffer_load_dwordx4 a[168:171], v4, s[40:43], s56 offen offset:2048 sc1\n"
"buffer_load_dwordx4 a[176:179], v4, s[40:43], s54 offen offset:3072 sc1\n"
"buffer_load_dwordx4 a[180:183], v4, s[40:43], s55 offen offset:3072 sc1\n"
"buffer_load_dwordx4 a[184:187], v4, s[40:43], s56 offen offset:3072 sc1\n"
"s_add_u32 s54, s54, 0x2000\n"
"s_add_u32 s55, s55, 0x2000\n"
"s_add_u32 s56, s56, 0x2000\n"
"s_add_u32 m0, s53, 0\n"
"s_add_u32 s52, s52, 187392\n"
"buffer_load_dwordx4 v0, s[36:39], s52 offen offset:0 lds\n"
"s_add_u32 m0, s53, 0\n"
"s_add_u32 s52, s52, 3072\n"
"buffer_load_dwordx4 v1, s[36:39], s52 offen offset:1024 lds\n"
"s_add_u32 m0, s53, 0\n"
"s_add_u32 s52, s52, 3072\n"
"buffer_load_dwordx4 v0, s[36:39], s52 offen offset:2048 lds\n"
"s_add_u32 m0, s53, 0\n"
"s_add_u32 s52, s52, 3072\n"
"buffer_load_dwordx4 v1, s[36:39], s52 offen offset:3072 lds\n"
"s_add_u32 m0, s53, 16384\n"
"s_add_u32 s52, s52, 56320\n"
"buffer_load_dwordx4 v0, s[36:39], s52 offen offset:0 lds\n"
"s_add_u32 m0, s53, 16384\n"
"s_add_u32 s52, s52, 3072\n"
"buffer_load_dwordx4 v1, s[36:39], s52 offen offset:1024 lds\n"
"s_add_u32 m0, s53, 16384\n"
"s_add_u32 s52, s52, 3072\n"
"buffer_load_dwordx4 v0, s[36:39], s52 offen offset:2048 lds\n"
"s_add_u32 m0, s53, 16384\n"
"s_add_u32 s52, s52, 3072\n"
"buffer_load_dwordx4 v1, s[36:39], s52 offen offset:3072 lds\n"
"s_xor_b32 s53, s53, 0x10000\n"
"buffer_load_dwordx4 a[192:195], v4, s[40:43], s54 offen offset:0 sc1\n"
"buffer_load_dwordx4 a[196:199], v4, s[40:43], s55 offen offset:0 sc1\n"
"buffer_load_dwordx4 a[200:203], v4, s[40:43], s56 offen offset:0 sc1\n"
"buffer_load_dwordx4 a[208:211], v4, s[40:43], s54 offen offset:1024 sc1\n"
"buffer_load_dwordx4 a[212:215], v4, s[40:43], s55 offen offset:1024 sc1\n"
"buffer_load_dwordx4 a[216:219], v4, s[40:43], s56 offen offset:1024 sc1\n"
"s_add_u32 m0, s53, 0\n"
"s_add_u32 s52, s52, 187392\n"
"buffer_load_dwordx4 v0, s[36:39], s52 offen offset:0 lds\n"
"s_add_u32 m0, s53, 0\n"
"s_add_u32 s52, s52, 3072\n"
"buffer_load_dwordx4 v1, s[36:39], s52 offen offset:1024 lds\n"
"s_add_u32 m0, s53, 0\n"
"s_add_u32 s52, s52, 3072\n"
"buffer_load_dwordx4 v0, s[36:39], s52 offen offset:2048 lds\n"
"s_add_u32 m0, s53, 0\n"
"s_add_u32 s52, s52, 3072\n"
"buffer_load_dwordx4 v1, s[36:39], s52 offen offset:3072 lds\n"
"s_add_u32 m0, s53, 16384\n"
"s_add_u32 s52, s52, 56320\n"
"buffer_load_dwordx4 v0, s[36:39], s52 offen offset:0 lds\n"
"s_add_u32 m0, s53, 16384\n"
"s_add_u32 s52, s52, 3072\n"
"buffer_load_dwordx4 v1, s[36:39], s52 offen offset:1024 lds\n"
"s_add_u32 m0, s53, 16384\n"
"s_add_u32 s52, s52, 3072\n"
"buffer_load_dwordx4 v0, s[36:39], s52 offen offset:2048 lds\n"
"s_add_u32 m0, s53, 16384\n"
"s_add_u32 s52, s52, 3072\n"
"buffer_load_dwordx4 v1, s[36:39], s52 offen offset:3072 lds\n"
"s_xor_b32 s53, s53, 0x8000\n"
"s_waitcnt vmcnt(43)\n"
"s_mov_b64 exec, 15\n"
"global_store_dwordx2 v8, v[12:13], %[mw] offset:128 sc1\n"
"s_mov_b64 exec, -1\n"
"v_lshrrev_b32 v30, 2, v21\n"
"v_and_b32 v31, 1, v22\n"
"v_lshl_add_u32 v30, v31, 2, v30\n"
"v_lshlrev_b32 v30, 5, v30\n"
"v_and_b32 v31, 3, v20\n"
"v_lshl_add_u32 v30, v31, 3, v30\n"
"v_lshl_add_u32 v2, v22, 10, v30\n"
"v_xor_b32 v3, 0x80, v2\n"
"v_lshlrev_b32 v5, 11, v21\n"
"v_lshl_add_u32 v5, v22, 3, v5\n"
"s_lshl_b32 s59, %[wv], 17\n"
"v_add_u32 v5, s59, v5\n"
"v_lshlrev_b32 v6, 4, v20\n"
"v_add_u32 v6, s61, v6\n"
"s_add_u32 s59, %[ksp], 1\n"
"s_and_b32 s59, s59, 3\n"
"s_lshl_b32 s60, %[grpp], 2\n"
"s_add_u32 s59, s59, s60\n"
"s_mul_i32 s59, s59, 3\n"
"s_add_u32 s59, s59, 0\n"
"s_mul_i32 s62, s59, 15360\n"
"s_add_u32 s59, %[ksp], 2\n"
"s_and_b32 s59, s59, 3\n"
"s_lshl_b32 s60, %[grpp], 2\n"
"s_add_u32 s59, s59, s60\n"
"s_mul_i32 s59, s59, 3\n"
"s_add_u32 s59, s59, 1\n"
"s_mul_i32 s63, s59, 15360\n"
"s_add_u32 s59, %[ksp], 3\n"
"s_and_b32 s59, s59, 3\n"
"s_lshl_b32 s60, %[grpp], 2\n"
"s_add_u32 s59, s59, s60\n"
"s_mul_i32 s59, s59, 3\n"
"s_add_u32 s59, s59, 2\n"
"s_mul_i32 s64, s59, 15360\n"
"s_lshl_b32 s60, %[grpp], 2\n"
"s_add_u32 s59, s60, %[ksp]\n"
"s_mul_i32 s65, s59, 46080\n"
"s_add_u32 s66, s65, 15360\n"
"s_add_u32 s67, s65, 30720\n"
"s_mov_b32 s58, 4\n"
"v_mov_b32 v128, 0\n"
"v_mov_b32 v129, 0\n"
"v_mov_b32 v130, 0\n"
"v_mov_b32 v131, 0\n"
"v_mov_b32 v132, 0\n"
"v_mov_b32 v133, 0\n"
"v_mov_b32 v134, 0\n"
"v_mov_b32 v135, 0\n"
"v_mov_b32 v136, 0\n"
"v_mov_b32 v137, 0\n"
"v_mov_b32 v138, 0\n"
"v_mov_b32 v139, 0\n"
"v_mov_b32 v140, 0\n"
"v_mov_b32 v141, 0\n"
"v_mov_b32 v142, 0\n"
"v_mov_b32 v143, 0\n"
"v_mov_b32 v144, 0\n"
"v_mov_b32 v145, 0\n"
"v_mov_b32 v146, 0\n"
"v_mov_b32 v147, 0\n"
"v_mov_b32 v148, 0\n"
"v_mov_b32 v149, 0\n"
"v_mov_b32 v150, 0\n"
"v_mov_b32 v151, 0\n"
"v_mov_b32 v152, 0\n"
"v_mov_b32 v153, 0\n"
"v_mov_b32 v154, 0\n"
"v_mov_b32 v155, 0\n"
"v_mov_b32 v156, 0\n"
"v_mov_b32 v157, 0\n"
"v_mov_b32 v158, 0\n"
"v_mov_b32 v159, 0\n"
"v_mov_b32 v160, 0\n"
"v_mov_b32 v161, 0\n"
"v_mov_b32 v162, 0\n"
"v_mov_b32 v163, 0\n"
"v_mov_b32 v164, 0\n"
"v_mov_b32 v165, 0\n"
"v_mov_b32 v166, 0\n"
"v_mov_b32 v167, 0\n"
"v_mov_b32 v168, 0\n"
"v_mov_b32 v169, 0\n"
"v_mov_b32 v170, 0\n"
"v_mov_b32 v171, 0\n"
"v_mov_b32 v172, 0\n"
"v_mov_b32 v173, 0\n"
"v_mov_b32 v174, 0\n"
"v_mov_b32 v175, 0\n"
"v_mov_b32 v176, 0\n"
"v_mov_b32 v177, 0\n"
"v_mov_b32 v178, 0\n"
"v_mov_b32 v179, 0\n"
"v_mov_b32 v180, 0\n"
"v_mov_b32 v181, 0\n"
"v_mov_b32 v182, 0\n"
"v_mov_b32 v183, 0\n"
"v_mov_b32 v184, 0\n"
"v_mov_b32 v185, 0\n"
"v_mov_b32 v186, 0\n"
"v_mov_b32 v187, 0\n"
"v_mov_b32 v188, 0\n"
"v_mov_b32 v189, 0\n"
"v_mov_b32 v190, 0\n"
"v_mov_b32 v191, 0\n"
"v_mov_b32 v192, 0\n"
"v_mov_b32 v193, 0\n"
"v_mov_b32 v194, 0\n"
"v_mov_b32 v195, 0\n"
"v_mov_b32 v196, 0\n"
"v_mov_b32 v197, 0\n"
"v_mov_b32 v198, 0\n"
"v_mov_b32 v199, 0\n"
"v_mov_b32 v200, 0\n"
"v_mov_b32 v201, 0\n"
"v_mov_b32 v202, 0\n"
"v_mov_b32 v203, 0\n"
"v_mov_b32 v204, 0\n"
"v_mov_b32 v205, 0\n"
"v_mov_b32 v206, 0\n"
"v_mov_b32 v207, 0\n"
"v_mov_b32 v208, 0\n"
"v_mov_b32 v209, 0\n"
"v_mov_b32 v210, 0\n"
"v_mov_b32 v211, 0\n"
"v_mov_b32 v212, 0\n"
"v_mov_b32 v213, 0\n"
"v_mov_b32 v214, 0\n"
"v_mov_b32 v215, 0\n"
"v_mov_b32 v216, 0\n"
"v_mov_b32 v217, 0\n"
"v_mov_b32 v218, 0\n"
"v_mov_b32 v219, 0\n"
"v_mov_b32 v220, 0\n"
"v_mov_b32 v221, 0\n"
"v_mov_b32 v222, 0\n"
"v_mov_b32 v223, 0\n"
"s_waitcnt vmcnt(35)\n"
"v_add_u32 v14, 1, v14\n"
"s_mov_b64 exec, 15\n"
"global_store_dword v8, v14, %[xw] sc1\n"
"s_mov_b64 exec, -1\n"
"s_waitcnt vmcnt(63)\n"
"s_barrier\n"
"ds_read_b64_tr_b16 a[0:1], v2 offset:0\n"
"ds_read_b64_tr_b16 a[2:3], v2 offset:4096\n"
"ds_read_b64_tr_b16 a[4:5], v2 offset:8192\n"
"ds_read_b64_tr_b16 a[6:7], v2 offset:12288\n"
"ds_read_b64_tr_b16 a[8:9], v3 offset:0\n"
"ds_read_b64_tr_b16 a[10:11], v3 offset:4096\n"
"ds_read_b64_tr_b16 a[12:13], v3 offset:8192\n"
"ds_read_b64_tr_b16 a[14:15], v3 offset:12288\n"
"ds_read_b64_tr_b16 a[16:17], v2 offset:256\n"
"ds_read_b64_tr_b16 a[18:19], v2 offset:4352\n"
"ds_read_b64_tr_b16 a[20:21], v2 offset:8448\n"
"ds_read_b64_tr_b16 a[22:23], v2 offset:12544\n"
"ds_read_b64_tr_b16 a[24:25], v3 offset:256\n"
"ds_read_b64_tr_b16 a[26:27], v3 offset:4352\n"
"ds_read_b64_tr_b16 a[28:29], v3 offset:8448\n"
"ds_read_b64_tr_b16 a[30:31], v3 offset:12544\n"
"ds_read_b64_tr_b16 a[32:33], v2 offset:512\n"
"ds_read_b64_tr_b16 a[34:35], v2 offset:4608\n"
"ds_read_b64_tr_b16 a[36:37], v2 offset:8704\n"
"ds_read_b64_tr_b16 a[38:39], v2 offset:12800\n"
"ds_read_b64_tr_b16 a[40:41], v3 offset:512\n"
"ds_read_b64_tr_b16 a[42:43], v3 offset:4608\n"
"ds_read_b64_tr_b16 a[44:45], v3 offset:8704\n"
"ds_read_b64_tr_b16 a[46:47], v3 offset:12800\n"
"ds_read_b64_tr_b16 a[48:49], v2 offset:768\n"
"ds_read_b64_tr_b16 a[50:51], v2 offset:4864\n"
"ds_read_b64_tr_b16 a[52:53], v2 offset:8960\n"
"ds_read_b64_tr_b16 a[54:55], v2 offset:13056\n"
"ds_read_b64_tr_b16 a[56:57], v3 offset:768\n"
"ds_read_b64_tr_b16 a[58:59], v3 offset:4864\n"
".LLd_%=:\n"
"s_waitcnt lgkmcnt(15)\n"
"s_waitcnt vmcnt(35)\n"
"v_mfma_f32_16x16x32_bf16 v[128:131], a[0:3], a[128:131], v[128:131]\n"
"ds_read_b64_tr_b16 a[60:61], v3 offset:8960\n"
"ds_read_b64_tr_b16 a[62:63], v3 offset:13056\n"
"s_waitcnt vmcnt(28)\n"
"v_mfma_f32_16x16x32_bf16 v[128:131], a[4:7], a[144:147], v[128:131]\n"
"buffer_load_dwordx4 a[224:227], v4, s[40:43], s54 offen offset:2048 sc1\n"
"v_mfma_f32_16x16x32_bf16 v[160:163], a[0:3], a[132:135], v[160:163]\n"
"s_waitcnt vmcnt(27)\n"
"v_mfma_f32_16x16x32_bf16 v[160:163], a[4:7], a[148:151], v[160:163]\n"
"v_mfma_f32_16x16x32_bf16 v[192:195], a[0:3], a[136:139], v[192:195]\n"
"ds_read_b64_tr_b16 a[0:1], v2 offset:16384\n"
"ds_read_b64_tr_b16 a[2:3], v2 offset:20480\n"
"s_waitcnt vmcnt(25)\n"
"v_mfma_f32_16x16x32_bf16 v[192:195], a[4:7], a[152:155], v[192:195]\n"
"ds_read_b64_tr_b16 a[4:5], v2 offset:24576\n"
"ds_read_b64_tr_b16 a[6:7], v2 offset:28672\n"
"v_mfma_f32_16x16x32_bf16 v[132:135], a[8:11], a[128:131], v[132:135]\n"
"v_mfma_f32_16x16x32_bf16 v[132:135], a[12:15], a[144:147], v[132:135]\n"
"buffer_load_dwordx4 a[228:231], v4, s[40:43], s55 offen offset:2048 sc1\n"
"v_mfma_f32_16x16x32_bf16 v[164:167], a[8:11], a[132:135], v[164:167]\n"
"v_mfma_f32_16x16x32_bf16 v[164:167], a[12:15], a[148:151], v[164:167]\n"
"v_mfma_f32_16x16x32_bf16 v[196:199], a[8:11], a[136:139], v[196:199]\n"
"ds_read_b64_tr_b16 a[8:9], v3 offset:16384\n"
"ds_read_b64_tr_b16 a[10:11], v3 offset:20480\n"
"v_mfma_f32_16x16x32_bf16 v[196:199], a[12:15], a[152:155], v[196:199]\n"
"ds_read_b64_tr_b16 a[12:13], v3 offset:24576\n"
"ds_read_b64_tr_b16 a[14:15], v3 offset:28672\n"
"v_mfma_f32_16x16x32_bf16 v[136:139], a[16:19], a[128:131], v[136:139]\n"
"v_mfma_f32_16x16x32_bf16 v[136:139], a[20:23], a[144:147], v[136:139]\n"
"buffer_load_dwordx4 a[232:235], v4, s[40:43], s56 offen offset:2048 sc1\n"
"v_mfma_f32_16x16x32_bf16 v[168:171], a[16:19], a[132:135], v[168:171]\n"
"v_mfma_f32_16x16x32_bf16 v[168:171], a[20:23], a[148:151], v[168:171]\n"
"v_mfma_f32_16x16x32_bf16 v[200:203], a[16:19], a[136:139], v[200:203]\n"
"ds_read_b64_tr_b16 a[16:17], v2 offset:16640\n"
"ds_read_b64_tr_b16 a[18:19], v2 offset:20736\n"
"v_mfma_f32_16x16x32_bf16 v[200:203], a[20:23], a[152:155], v[200:203]\n"
"ds_read_b64_tr_b16 a[20:21], v2 offset:24832\n"
"ds_read_b64_tr_b16 a[22:23], v2 offset:28928\n"
"v_mfma_f32_16x16x32_bf16 v[140:143], a[24:27], a[128:131], v[140:143]\n"
"s_waitcnt lgkmcnt(15)\n"
"v_mfma_f32_16x16x32_bf16 v[140:143], a[28:31], a[144:147], v[140:143]\n"
"buffer_load_dwordx4 a[240:243], v4, s[40:43], s54 offen offset:3072 sc1\n"
"v_mfma_f32_16x16x32_bf16 v[172:175], a[24:27], a[132:135], v[172:175]\n"
"v_mfma_f32_16x16x32_bf16 v[172:175], a[28:31], a[148:151], v[172:175]\n"
"v_mfma_f32_16x16x32_bf16 v[204:207], a[24:27], a[136:139], v[204:207]\n"
"ds_read_b64_tr_b16 a[24:25], v3 offset:16640\n"
"ds_read_b64_tr_b16 a[26:27], v3 offset:20736\n"
"v_mfma_f32_16x16x32_bf16 v[204:207], a[28:31], a[152:155], v[204:207]\n"
"ds_read_b64_tr_b16 a[28:29], v3 offset:24832\n"
"ds_read_b64_tr_b16 a[30:31], v3 offset:28928\n"
"v_mfma_f32_16x16x32_bf16 v[144:147], a[32:35], a[128:131], v[144:147]\n"
"v_mfma_f32_16x16x32_bf16 v[144:147], a[36:39], a[144:147], v[144:147]\n"
"buffer_load_dwordx4 a[244:247], v4, s[40:43], s55 offen offset:3072 sc1\n"
"v_mfma_f32_16x16x32_bf16 v[176:179], a[32:35], a[132:135], v[176:179]\n"
"v_mfma_f32_16x16x32_bf16 v[176:179], a[36:39], a[148:151], v[176:179]\n"
"v_mfma_f32_16x16x32_bf16 v[208:211], a[32:35], a[136:139], v[208:211]\n"
"ds_read_b64_tr_b16 a[32:33], v2 offset:16896\n"
"ds_read_b64_tr_b16 a[34:35], v2 offset:20992\n"
"v_mfma_f32_16x16x32_bf16 v[208:211], a[36:39], a[152:155], v[208:211]\n"
"ds_read_b64_tr_b16 a[36:37], v2 offset:25088\n"
"ds_read_b64_tr_b16 a[38:39], v2 offset:29184\n"
"v_mfma_f32_16x16x32_bf16 v[148:151], a[40:43], a[128:131], v[148:151]\n"
"v_mfma_f32_16x16x32_bf16 v[148:151], a[44:47], a[144:147], v[148:151]\n"
"buffer_load_dwordx4 a[248:251], v4, s[40:43], s56 offen offset:3072 sc1\n"
"v_mfma_f32_16x16x32_bf16 v[180:183], a[40:43], a[132:135], v[180:183]\n"
"v_mfma_f32_16x16x32_bf16 v[180:183], a[44:47], a[148:151], v[180:183]\n"
"v_mfma_f32_16x16x32_bf16 v[212:215], a[40:43], a[136:139], v[212:215]\n"
"ds_read_b64_tr_b16 a[40:41], v3 offset:16896\n"
"ds_read_b64_tr_b16 a[42:43], v3 offset:20992\n"
"v_mfma_f32_16x16x32_bf16 v[212:215], a[44:47], a[152:155], v[212:215]\n"
"ds_read_b64_tr_b16 a[44:45], v3 offset:25088\n"
"ds_read_b64_tr_b16 a[46:47], v3 offset:29184\n"
"v_mfma_f32_16x16x32_bf16 v[152:155], a[48:51], a[128:131], v[152:155]\n"
"v_mfma_f32_16x16x32_bf16 v[152:155], a[52:55], a[144:147], v[152:155]\n"
"s_add_u32 s54, s54, s72\n"
"s_add_u32 s55, s55, s72\n"
"s_add_u32 s56, s56, s72\n"
"s_mov_b32 s72, s73\n"
"s_mov_b32 s73, s74\n"
"s_mov_b32 s74, s75\n"
"s_waitcnt vmcnt(21)\n"
"v_cmp_ne_u32 vcc, v18, v13\n"
"s_nop 1\n"
"s_mov_b64 s[78:79], vcc\n"
"v_cmp_ne_u32 vcc, v19, v12\n"
"s_cmp_eq_u32 s58, 3\n"
"s_cselect_b64 s[76:77], -1, 0\n"
"s_or_b64 s[78:79], s[78:79], vcc\n"
"s_and_b64 s[78:79], s[78:79], s[76:77]\n"
"s_cbranch_scc1 .Ls1d_%=\n"
".Lr1d_%=:\n"
"v_mfma_f32_16x16x32_bf16 v[184:187], a[48:51], a[132:135], v[184:187]\n"
"v_mfma_f32_16x16x32_bf16 v[184:187], a[52:55], a[148:151], v[184:187]\n"
"v_mfma_f32_16x16x32_bf16 v[216:219], a[48:51], a[136:139], v[216:219]\n"
"ds_read_b64_tr_b16 a[48:49], v2 offset:17152\n"
"ds_read_b64_tr_b16 a[50:51], v2 offset:21248\n"
"v_mfma_f32_16x16x32_bf16 v[216:219], a[52:55], a[152:155], v[216:219]\n"
"ds_read_b64_tr_b16 a[52:53], v2 offset:25344\n"
"ds_read_b64_tr_b16 a[54:55], v2 offset:29440\n"
"s_waitcnt lgkmcnt(15)\n"
"v_mfma_f32_16x16x32_bf16 v[156:159], a[56:59], a[128:131], v[156:159]\n"
"v_mfma_f32_16x16x32_bf16 v[156:159], a[60:63], a[144:147], v[156:159]\n"
"v_mfma_f32_16x16x32_bf16 v[188:191], a[56:59], a[132:135], v[188:191]\n"
"v_mfma_f32_16x16x32_bf16 v[188:191], a[60:63], a[148:151], v[188:191]\n"
"v_mfma_f32_16x16x32_bf16 v[220:223], a[56:59], a[136:139], v[220:223]\n"
"ds_read_b64_tr_b16 a[56:57], v3 offset:17152\n"
"ds_read_b64_tr_b16 a[58:59], v3 offset:21248\n"
"v_mfma_f32_16x16x32_bf16 v[220:223], a[60:63], a[152:155], v[220:223]\n"
"ds_read_b64_tr_b16 a[60:61], v3 offset:25344\n"
"ds_read_b64_tr_b16 a[62:63], v3 offset:29440\n"
"s_waitcnt vmcnt(35)\n"
"v_mfma_f32_16x16x32_bf16 v[128:131], a[0:3], a[160:163], v[128:131]\n"
"buffer_load_dwordx4 a[128:131], v4, s[40:43], s54 offen offset:0 sc1\n"
"s_waitcnt vmcnt(33)\n"
"v_mfma_f32_16x16x32_bf16 v[128:131], a[4:7], a[176:179], v[128:131]\n"
"v_mfma_f32_16x16x32_bf16 v[160:163], a[0:3], a[164:167], v[160:163]\n"
"s_waitcnt vmcnt(32)\n"
"v_mfma_f32_16x16x32_bf16 v[160:163], a[4:7], a[180:183], v[160:163]\n"
"s_waitcnt vmcnt(23)\n"
"s_barrier\n"
"v_xor_b32 v2, 0x8000, v2\n"
"v_xor_b32 v3, 0x8000, v3\n"
"v_mfma_f32_16x16x32_bf16 v[192:195], a[0:3], a[168:171], v[192:195]\n"
"s_add_u32 m0, s53, 0\n"
"s_add_u32 s52, s52, 187392\n"
"buffer_load_dwordx4 v0, s[36:39], s52 offen offset:0 lds\n"
"v_mfma_f32_16x16x32_bf16 v[192:195], a[4:7], a[184:187], v[192:195]\n"
"buffer_load_dwordx4 a[132:135], v4, s[40:43], s55 offen offset:0 sc1\n"
"v_mfma_f32_16x16x32_bf16 v[132:135], a[8:11], a[160:163], v[132:135]\n"
"ds_read_b64_tr_b16 a[0:1], v2 offset:0\n"
"ds_read_b64_tr_b16 a[2:3], v2 offset:4096\n"
"v_mfma_f32_16x16x32_bf16 v[132:135], a[12:15], a[176:179], v[132:135]\n"
"ds_read_b64_tr_b16 a[4:5], v2 offset:8192\n"
"ds_read_b64_tr_b16 a[6:7], v2 offset:12288\n"
"v_mfma_f32_16x16x32_bf16 v[164:167], a[8:11], a[164:167], v[164:167]\n"
"v_mfma_f32_16x16x32_bf16 v[164:167], a[12:15], a[180:183], v[164:167]\n"
"s_add_u32 m0, s53, 0\n"
"s_add_u32 s52, s52, 3072\n"
"buffer_load_dwordx4 v1, s[36:39], s52 offen offset:1024 lds\n"
"v_mfma_f32_16x16x32_bf16 v[196:199], a[8:11], a[168:171], v[196:199]\n"
"ds_read_b64_tr_b16 a[8:9], v3 offset:0\n"
"ds_read_b64_tr_b16 a[10:11], v3 offset:4096\n"
"v_mfma_f32_16x16x32_bf16 v[196:199], a[12:15], a[184:187], v[196:199]\n"
"ds_read_b64_tr_b16 a[12:13], v3 offset:8192\n"
"ds_read_b64_tr_b16 a[14:15], v3 offset:12288\n"
"v_mfma_f32_16x16x32_bf16 v[136:139], a[16:19], a[160:163], v[136:139]\n"
"buffer_load_dwordx4 a[136:139], v4, s[40:43], s56 offen offset:0 sc1\n"
"v_mfma_f32_16x16x32_bf16 v[136:139], a[20:23], a[176:179], v[136:139]\n"
"v_mfma_f32_16x16x32_bf16 v[168:171], a[16:19], a[164:167], v[168:171]\n"
"s_add_u32 m0, s53, 0\n"
"s_add_u32 s52, s52, 3072\n"
"buffer_load_dwordx4 v0, s[36:39], s52 offen offset:2048 lds\n"
"v_mfma_f32_16x16x32_bf16 v[168:171], a[20:23], a[180:183], v[168:171]\n"
"v_mfma_f32_16x16x32_bf16 v[200:203], a[16:19], a[168:171], v[200:203]\n"
"ds_read_b64_tr_b16 a[16:17], v2 offset:256\n"
"ds_read_b64_tr_b16 a[18:19], v2 offset:4352\n"
"v_mfma_f32_16x16x32_bf16 v[200:203], a[20:23], a[184:187], v[200:203]\n"
"ds_read_b64_tr_b16 a[20:21], v2 offset:8448\n"
"ds_read_b64_tr_b16 a[22:23], v2 offset:12544\n"
"s_waitcnt lgkmcnt(15)\n"
"v_mfma_f32_16x16x32_bf16 v[140:143], a[24:27], a[160:163], v[140:143]\n"
"v_mfma_f32_16x16x32_bf16 v[140:143], a[28:31], a[176:179], v[140:143]\n"
"s_add_u32 m0, s53, 0\n"
"s_add_u32 s52, s52, 3072\n"
"buffer_load_dwordx4 v1, s[36:39], s52 offen offset:3072 lds\n"
"v_mfma_f32_16x16x32_bf16 v[172:175], a[24:27], a[164:167], v[172:175]\n"
"buffer_load_dwordx4 a[144:147], v4, s[40:43], s54 offen offset:1024 sc1\n"
"v_mfma_f32_16x16x32_bf16 v[172:175], a[28:31], a[180:183], v[172:175]\n"
"v_mfma_f32_16x16x32_bf16 v[204:207], a[24:27], a[168:171], v[204:207]\n"
"ds_read_b64_tr_b16 a[24:25], v3 offset:256\n"
"ds_read_b64_tr_b16 a[26:27], v3 offset:4352\n"
"v_mfma_f32_16x16x32_bf16 v[204:207], a[28:31], a[184:187], v[204:207]\n"
"ds_read_b64_tr_b16 a[28:29], v3 offset:8448\n"
"ds_read_b64_tr_b16 a[30:31], v3 offset:12544\n"
"v_mfma_f32_16x16x32_bf16 v[144:147], a[32:35], a[160:163], v[144:147]\n"
"s_add_u32 m0, s53, 16384\n"
"s_add_u32 s52, s52, 56320\n"
"buffer_load_dwordx4 v0, s[36:39], s52 offen offset:0 lds\n"
"v_mfma_f32_16x16x32_bf16 v[144:147], a[36:39], a[176:179], v[144:147]\n"
"v_mfma_f32_16x16x32_bf16 v[176:179], a[32:35], a[164:167], v[176:179]\n"
"buffer_load_dwordx4 a[148:151], v4, s[40:43], s55 offen offset:1024 sc1\n"
"v_mfma_f32_16x16x32_bf16 v[176:179], a[36:39], a[180:183], v[176:179]\n"
"v_mfma_f32_16x16x32_bf16 v[208:211], a[32:35], a[168:171], v[208:211]\n"
"ds_read_b64_tr_b16 a[32:33], v2 offset:512\n"
"ds_read_b64_tr_b16 a[34:35], v2 offset:4608\n"
"v_mfma_f32_16x16x32_bf16 v[208:211], a[36:39], a[184:187], v[208:211]\n"
"s_add_u32 m0, s53, 16384\n"
"s_add_u32 s52, s52, 3072\n"
"buffer_load_dwordx4 v1, s[36:39], s52 offen offset:1024 lds\n"
"v_mfma_f32_16x16x32_bf16 v[148:151], a[40:43], a[160:163], v[148:151]\n"
"ds_read_b64_tr_b16 a[36:37], v2 offset:8704\n"
"ds_read_b64_tr_b16 a[38:39], v2 offset:12800\n"
"v_mfma_f32_16x16x32_bf16 v[148:151], a[44:47], a[176:179], v[148:151]\n"
"v_mfma_f32_16x16x32_bf16 v[180:183], a[40:43], a[164:167], v[180:183]\n"
"v_mfma_f32_16x16x32_bf16 v[180:183], a[44:47], a[180:183], v[180:183]\n"
"buffer_load_dwordx4 a[152:155], v4, s[40:43], s56 offen offset:1024 sc1\n"
"v_mfma_f32_16x16x32_bf16 v[212:215], a[40:43], a[168:171], v[212:215]\n"
"s_add_u32 m0, s53, 16384\n"
"s_add_u32 s52, s52, 3072\n"
"buffer_load_dwordx4 v0, s[36:39], s52 offen offset:2048 lds\n"
"v_mfma_f32_16x16x32_bf16 v[212:215], a[44:47], a[184:187], v[212:215]\n"
"ds_read_b64_tr_b16 a[40:41], v3 offset:512\n"
"ds_read_b64_tr_b16 a[42:43], v3 offset:4608\n"
"v_mfma_f32_16x16x32_bf16 v[152:155], a[48:51], a[160:163], v[152:155]\n"
"ds_read_b64_tr_b16 a[44:45], v3 offset:8704\n"
"ds_read_b64_tr_b16 a[46:47], v3 offset:12800\n"
"v_mfma_f32_16x16x32_bf16 v[152:155], a[52:55], a[176:179], v[152:155]\n"
"v_mfma_f32_16x16x32_bf16 v[184:187], a[48:51], a[164:167], v[184:187]\n"
"v_mfma_f32_16x16x32_bf16 v[184:187], a[52:55], a[180:183], v[184:187]\n"
"s_add_u32 m0, s53, 16384\n"
"s_add_u32 s52, s52, 3072\n"
"buffer_load_dwordx4 v1, s[36:39], s52 offen offset:3072 lds\n"
"s_xor_b32 s53, s53, 0x10000\n"
"v_mfma_f32_16x16x32_bf16 v[216:219], a[48:51], a[168:171], v[216:219]\n"
"ds_read_b64_tr_b16 a[48:49], v2 offset:768\n"
"ds_read_b64_tr_b16 a[50:51], v2 offset:4864\n"
"v_mfma_f32_16x16x32_bf16 v[216:219], a[52:55], a[184:187], v[216:219]\n"
"ds_read_b64_tr_b16 a[52:53], v2 offset:8960\n"
"ds_read_b64_tr_b16 a[54:55], v2 offset:13056\n"
"s_waitcnt lgkmcnt(15)\n"
"v_mfma_f32_16x16x32_bf16 v[156:159], a[56:59], a[160:163], v[156:159]\n"
"v_mfma_f32_16x16x32_bf16 v[156:159], a[60:63], a[176:179], v[156:159]\n"
"v_mfma_f32_16x16x32_bf16 v[188:191], a[56:59], a[164:167], v[188:191]\n"
"v_mfma_f32_16x16x32_bf16 v[188:191], a[60:63], a[180:183], v[188:191]\n"
"v_mfma_f32_16x16x32_bf16 v[220:223], a[56:59], a[168:171], v[220:223]\n"
"ds_read_b64_tr_b16 a[56:57], v3 offset:768\n"
"ds_read_b64_tr_b16 a[58:59], v3 offset:4864\n"
"v_mfma_f32_16x16x32_bf16 v[220:223], a[60:63], a[184:187], v[220:223]\n"
"s_waitcnt vmcnt(33)\n"
"v_mfma_f32_16x16x32_bf16 v[128:131], a[0:3], a[192:195], v[128:131]\n"
"ds_read_b64_tr_b16 a[60:61], v3 offset:8960\n"
"ds_read_b64_tr_b16 a[62:63], v3 offset:13056\n"
"s_waitcnt vmcnt(26)\n"
"v_mfma_f32_16x16x32_bf16 v[128:131], a[4:7], a[208:211], v[128:131]\n"
"buffer_load_dwordx4 a[160:163], v4, s[40:43], s54 offen offset:2048 sc1\n"
"v_mfma_f32_16x16x32_bf16 v[160:163], a[0:3], a[196:199], v[160:163]\n"
"s_waitcnt vmcnt(25)\n"
"v_mfma_f32_16x16x32_bf16 v[160:163], a[4:7], a[212:215], v[160:163]\n"
"v_mfma_f32_16x16x32_bf16 v[192:195], a[0:3], a[200:203], v[192:195]\n"
"ds_read_b64_tr_b16 a[0:1], v2 offset:16384\n"
"ds_read_b64_tr_b16 a[2:3], v2 offset:20480\n"
"s_waitcnt vmcnt(23)\n"
"v_mfma_f32_16x16x32_bf16 v[192:195], a[4:7], a[216:219], v[192:195]\n"
"ds_read_b64_tr_b16 a[4:5], v2 offset:24576\n"
"ds_read_b64_tr_b16 a[6:7], v2 offset:28672\n"
"v_mfma_f32_16x16x32_bf16 v[132:135], a[8:11], a[192:195], v[132:135]\n"
"v_mfma_f32_16x16x32_bf16 v[132:135], a[12:15], a[208:211], v[132:135]\n"
"buffer_load_dwordx4 a[164:167], v4, s[40:43], s55 offen offset:2048 sc1\n"
"v_mfma_f32_16x16x32_bf16 v[164:167], a[8:11], a[196:199], v[164:167]\n"
"v_mfma_f32_16x16x32_bf16 v[164:167], a[12:15], a[212:215], v[164:167]\n"
"v_mfma_f32_16x16x32_bf16 v[196:199], a[8:11], a[200:203], v[196:199]\n"
"ds_read_b64_tr_b16 a[8:9], v3 offset:16384\n"
"ds_read_b64_tr_b16 a[10:11], v3 offset:20480\n"
"v_mfma_f32_16x16x32_bf16 v[196:199], a[12:15], a[216:219], v[196:199]\n"
"ds_read_b64_tr_b16 a[12:13], v3 offset:24576\n"
"ds_read_b64_tr_b16 a[14:15], v3 offset:28672\n"
"v_mfma_f32_16x16x32_bf16 v[136:139], a[16:19], a[192:195], v[136:139]\n"
"v_mfma_f32_16x16x32_bf16 v[136:139], a[20:23], a[208:211], v[136:139]\n"
"buffer_load_dwordx4 a[168:171], v4, s[40:43], s56 offen offset:2048 sc1\n"
"v_mfma_f32_16x16x32_bf16 v[168:171], a[16:19], a[196:199], v[168:171]\n"
"v_mfma_f32_16x16x32_bf16 v[168:171], a[20:23], a[212:215], v[168:171]\n"
"v_mfma_f32_16x16x32_bf16 v[200:203], a[16:19], a[200:203], v[200:203]\n"
"ds_read_b64_tr_b16 a[16:17], v2 offset:16640\n"
"ds_read_b64_tr_b16 a[18:19], v2 offset:20736\n"
"v_mfma_f32_16x16x32_bf16 v[200:203], a[20:23], a[216:219], v[200:203]\n"
"ds_read_b64_tr_b16 a[20:21], v2 offset:24832\n"
"ds_read_b64_tr_b16 a[22:23], v2 offset:28928\n"
"s_waitcnt lgkmcnt(15)\n"
"v_mfma_f32_16x16x32_bf16 v[140:143], a[24:27], a[192:195], v[140:143]\n"
"v_mfma_f32_16x16x32_bf16 v[140:143], a[28:31], a[208:211], v[140:143]\n"
"buffer_load_dwordx4 a[176:179], v4, s[40:43], s54 offen offset:3072 sc1\n"
"v_mfma_f32_16x16x32_bf16 v[172:175], a[24:27], a[196:199], v[172:175]\n"
"v_mfma_f32_16x16x32_bf16 v[172:175], a[28:31], a[212:215], v[172:175]\n"
"v_mfma_f32_16x16x32_bf16 v[204:207], a[24:27], a[200:203], v[204:207]\n"
"ds_read_b64_tr_b16 a[24:25], v3 offset:16640\n"
"ds_read_b64_tr_b16 a[26:27], v3 offset:20736\n"
"v_mfma_f32_16x16x32_bf16 v[204:207], a[28:31], a[216:219], v[204:207]\n"
"ds_read_b64_tr_b16 a[28:29], v3 offset:24832\n"
"ds_read_b64_tr_b16 a[30:31], v3 offset:28928\n"
"v_mfma_f32_16x16x32_bf16 v[144:147], a[32:35], a[192:195], v[144:147]\n"
"v_mfma_f32_16x16x32_bf16 v[144:147], a[36:39], a[208:211], v[144:147]\n"
"buffer_load_dwordx4 a[180:183], v4, s[40:43], s55 offen offset:3072 sc1\n"
"v_mfma_f32_16x16x32_bf16 v[176:179], a[32:35], a[196:199], v[176:179]\n"
"v_mfma_f32_16x16x32_bf16 v[176:179], a[36:39], a[212:215], v[176:179]\n"
"v_mfma_f32_16x16x32_bf16 v[208:211], a[32:35], a[200:203], v[208:211]\n"
"ds_read_b64_tr_b16 a[32:33], v2 offset:16896\n"
"ds_read_b64_tr_b16 a[34:35], v2 offset:20992\n"
"v_mfma_f32_16x16x32_bf16 v[208:211], a[36:39], a[216:219], v[208:211]\n"
"ds_read_b64_tr_b16 a[36:37], v2 offset:25088\n"
"ds_read_b64_tr_b16 a[38:39], v2 offset:29184\n"
"v_mfma_f32_16x16x32_bf16 v[148:151], a[40:43], a[192:195], v[148:151]\n"
"v_mfma_f32_16x16x32_bf16 v[148:151], a[44:47], a[208:211], v[148:151]\n"
"buffer_load_dwordx4 a[184:187], v4, s[40:43], s56 offen offset:3072 sc1\n"
"v_mfma_f32_16x16x32_bf16 v[180:183], a[40:43], a[196:199], v[180:183]\n"
"v_mfma_f32_16x16x32_bf16 v[180:183], a[44:47], a[212:215], v[180:183]\n"
"v_mfma_f32_16x16x32_bf16 v[212:215], a[40:43], a[200:203], v[212:215]\n"
"ds_read_b64_tr_b16 a[40:41], v3 offset:16896\n"
"ds_read_b64_tr_b16 a[42:43], v3 offset:20992\n"
"v_mfma_f32_16x16x32_bf16 v[212:215], a[44:47], a[216:219], v[212:215]\n"
"ds_read_b64_tr_b16 a[44:45], v3 offset:25088\n"
"ds_read_b64_tr_b16 a[46:47], v3 offset:29184\n"
"v_mfma_f32_16x16x32_bf16 v[152:155], a[48:51], a[192:195], v[152:155]\n"
"v_mfma_f32_16x16x32_bf16 v[152:155], a[52:55], a[208:211], v[152:155]\n"
"s_add_u32 s54, s54, 0x2000\n"
"s_add_u32 s55, s55, 0x2000\n"
"s_add_u32 s56, s56, 0x2000\n"
"v_mfma_f32_16x16x32_bf16 v[184:187], a[48:51], a[196:199], v[184:187]\n"
"v_mfma_f32_16x16x32_bf16 v[184:187], a[52:55], a[212:215], v[184:187]\n"
"v_mfma_f32_16x16x32_bf16 v[216:219], a[48:51], a[200:203], v[216:219]\n"
"ds_read_b64_tr_b16 a[48:49], v2 offset:17152\n"
"ds_read_b64_tr_b16 a[50:51], v2 offset:21248\n"
"v_mfma_f32_16x16x32_bf16 v[216:219], a[52:55], a[216:219], v[216:219]\n"
"ds_read_b64_tr_b16 a[52:53], v2 offset:25344\n"
"ds_read_b64_tr_b16 a[54:55], v2 offset:29440\n"
"s_waitcnt lgkmcnt(15)\n"
"v_mfma_f32_16x16x32_bf16 v[156:159], a[56:59], a[192:195], v[156:159]\n"
"v_mfma_f32_16x16x32_bf16 v[156:159], a[60:63], a[208:211], v[156:159]\n"
"v_mfma_f32_16x16x32_bf16 v[188:191], a[56:59], a[196:199], v[188:191]\n"
"v_mfma_f32_16x16x32_bf16 v[188:191], a[60:63], a[212:215], v[188:191]\n"
"global_load_dwordx2 v[18:19], v7, %[ma] offset:128 sc1\n"
"global_load_dword v15, v7, %[xa] sc1\n"
"v_mfma_f32_16x16x32_bf16 v[220:223], a[56:59], a[200:203], v[220:223]\n"
"ds_read_b64_tr_b16 a[56:57], v3 offset:17152\n"
"ds_read_b64_tr_b16 a[58:59], v3 offset:21248\n"
"v_mfma_f32_16x16x32_bf16 v[220:223], a[60:63], a[216:219], v[220:223]\n"
"ds_read_b64_tr_b16 a[60:61], v3 offset:25344\n"
"ds_read_b64_tr_b16 a[62:63], v3 offset:29440\n"
"s_waitcnt vmcnt(27)\n"
"v_mfma_f32_16x16x32_bf16 v[128:131], a[0:3], a[224:227], v[128:131]\n"
"buffer_load_dwordx4 a[192:195], v4, s[40:43], s54 offen offset:0 sc1\n"
"s_waitcnt vmcnt(25)\n"
"v_mfma_f32_16x16x32_bf16 v[128:131], a[4:7], a[240:243], v[128:131]\n"
"v_mfma_f32_16x16x32_bf16 v[160:163], a[0:3], a[228:231], v[160:163]\n"
"s_waitcnt vmcnt(24)\n"
"v_mfma_f32_16x16x32_bf16 v[160:163], a[4:7], a[244:247], v[160:163]\n"
"s_waitcnt vmcnt(63)\n"
"s_barrier\n"
"v_xor_b32 v2, 0x10000, v2\n"
"v_xor_b32 v3, 0x10000, v3\n"
"v_mfma_f32_16x16x32_bf16 v[192:195], a[0:3], a[232:235], v[192:195]\n"
"s_add_u32 m0, s53, 0\n"
"s_add_u32 s52, s52, s70\n"
"buffer_load_dwordx4 v0, s[36:39], s52 offen offset:0 lds\n"
"s_mov_b32 s70, 187392\n"
"s_waitcnt vmcnt(24)\n"
"v_mfma_f32_16x16x32_bf16 v[192:195], a[4:7], a[248:251], v[192:195]\n"
"buffer_load_dwordx4 a[196:199], v4, s[40:43], s55 offen offset:0 sc1\n"
"v_mfma_f32_16x16x32_bf16 v[132:135], a[8:11], a[224:227], v[132:135]\n"
"ds_read_b64_tr_b16 a[0:1], v2 offset:0\n"
"ds_read_b64_tr_b16 a[2:3], v2 offset:4096\n"
"v_mfma_f32_16x16x32_bf16 v[132:135], a[12:15], a[240:243], v[132:135]\n"
"ds_read_b64_tr_b16 a[4:5], v2 offset:8192\n"
"ds_read_b64_tr_b16 a[6:7], v2 offset:12288\n"
"v_mfma_f32_16x16x32_bf16 v[164:167], a[8:11], a[228:231], v[164:167]\n"
"v_mfma_f32_16x16x32_bf16 v[164:167], a[12:15], a[244:247], v[164:167]\n"
"s_add_u32 m0, s53, 0\n"
"s_add_u32 s52, s52, 3072\n"
"buffer_load_dwordx4 v1, s[36:39], s52 offen offset:1024 lds\n"
"v_mfma_f32_16x16x32_bf16 v[196:199], a[8:11], a[232:235], v[196:199]\n"
"ds_read_b64_tr_b16 a[8:9], v3 offset:0\n"
"ds_read_b64_tr_b16 a[10:11], v3 offset:4096\n"
"v_mfma_f32_16x16x32_bf16 v[196:199], a[12:15], a[248:251], v[196:199]\n"
"ds_read_b64_tr_b16 a[12:13], v3 offset:8192\n"
"ds_read_b64_tr_b16 a[14:15], v3 offset:12288\n"
"v_mfma_f32_16x16x32_bf16 v[136:139], a[16:19], a[224:227], v[136:139]\n"
"buffer_load_dwordx4 a[200:203], v4, s[40:43], s56 offen offset:0 sc1\n"
"v_mfma_f32_16x16x32_bf16 v[136:139], a[20:23], a[240:243], v[136:139]\n"
"v_mfma_f32_16x16x32_bf16 v[168:171], a[16:19], a[228:231], v[168:171]\n"
"s_add_u32 m0, s53, 0\n"
"s_add_u32 s52, s52, 3072\n"
"buffer_load_dwordx4 v0, s[36:39], s52 offen offset:2048 lds\n"
"v_mfma_f32_16x16x32_bf16 v[168:171], a[20:23], a[244:247], v[168:171]\n"
"v_mfma_f32_16x16x32_bf16 v[200:203], a[16:19], a[232:235], v[200:203]\n"
"ds_read_b64_tr_b16 a[16:17], v2 offset:256\n"
"ds_read_b64_tr_b16 a[18:19], v2 offset:4352\n"
"v_mfma_f32_16x16x32_bf16 v[200:203], a[20:23], a[248:251], v[200:203]\n"
"ds_read_b64_tr_b16 a[20:21], v2 offset:8448\n"
"ds_read_b64_tr_b16 a[22:23], v2 offset:12544\n"
"s_waitcnt lgkmcnt(15)\n"
"v_mfma_f32_16x16x32_bf16 v[140:143], a[24:27], a[224:227], v[140:143]\n"
"v_mfma_f32_16x16x32_bf16 v[140:143], a[28:31], a[240:243], v[140:143]\n"
"s_add_u32 m0, s53, 0\n"
"s_add_u32 s52, s52, 3072\n"
"buffer_load_dwordx4 v1, s[36:39], s52 offen offset:3072 lds\n"
"v_mfma_f32_16x16x32_bf16 v[172:175], a[24:27], a[228:231], v[172:175]\n"
"buffer_load_dwordx4 a[208:211], v4, s[40:43], s54 offen offset:1024 sc1\n"
"v_mfma_f32_16x16x32_bf16 v[172:175], a[28:31], a[244:247], v[172:175]\n"
"v_mfma_f32_16x16x32_bf16 v[204:207], a[24:27], a[232:235], v[204:207]\n"
"ds_read_b64_tr_b16 a[24:25], v3 offset:256\n"
"ds_read_b64_tr_b16 a[26:27], v3 offset:4352\n"
"v_mfma_f32_16x16x32_bf16 v[204:207], a[28:31], a[248:251], v[204:207]\n"
"ds_read_b64_tr_b16 a[28:29], v3 offset:8448\n"
"ds_read_b64_tr_b16 a[30:31], v3 offset:12544\n"
"v_mfma_f32_16x16x32_bf16 v[144:147], a[32:35], a[224:227], v[144:147]\n"
"s_add_u32 m0, s53, 16384\n"
"s_add_u32 s52, s52, 56320\n"
"buffer_load_dwordx4 v0, s[36:39], s52 offen offset:0 lds\n"
"v_mfma_f32_16x16x32_bf16 v[144:147], a[36:39], a[240:243], v[144:147]\n"
"v_mfma_f32_16x16x32_bf16 v[176:179], a[32:35], a[228:231], v[176:179]\n"
"buffer_load_dwordx4 a[212:215], v4, s[40:43], s55 offen offset:1024 sc1\n"
"v_mfma_f32_16x16x32_bf16 v[176:179], a[36:39], a[244:247], v[176:179]\n"
"v_mfma_f32_16x16x32_bf16 v[208:211], a[32:35], a[232:235], v[208:211]\n"
"ds_read_b64_tr_b16 a[32:33], v2 offset:512\n"
"ds_read_b64_tr_b16 a[34:35], v2 offset:4608\n"
"v_mfma_f32_16x16x32_bf16 v[208:211], a[36:39], a[248:251], v[208:211]\n"
"s_add_u32 m0, s53, 16384\n"
"s_add_u32 s52, s52, 3072\n"
"buffer_load_dwordx4 v1, s[36:39], s52 offen offset:1024 lds\n"
"v_mfma_f32_16x16x32_bf16 v[148:151], a[40:43], a[224:227], v[148:151]\n"
"ds_read_b64_tr_b16 a[36:37], v2 offset:8704\n"
"ds_read_b64_tr_b16 a[38:39], v2 offset:12800\n"
"v_mfma_f32_16x16x32_bf16 v[148:151], a[44:47], a[240:243], v[148:151]\n"
"v_mfma_f32_16x16x32_bf16 v[180:183], a[40:43], a[228:231], v[180:183]\n"
"v_mfma_f32_16x16x32_bf16 v[180:183], a[44:47], a[244:247], v[180:183]\n"
"buffer_load_dwordx4 a[216:219], v4, s[40:43], s56 offen offset:1024 sc1\n"
"v_mfma_f32_16x16x32_bf16 v[212:215], a[40:43], a[232:235], v[212:215]\n"
"s_add_u32 m0, s53, 16384\n"
"s_add_u32 s52, s52, 3072\n"
"buffer_load_dwordx4 v0, s[36:39], s52 offen offset:2048 lds\n"
"v_mfma_f32_16x16x32_bf16 v[212:215], a[44:47], a[248:251], v[212:215]\n"
"ds_read_b64_tr_b16 a[40:41], v3 offset:512\n"
"ds_read_b64_tr_b16 a[42:43], v3 offset:4608\n"
"v_mfma_f32_16x16x32_bf16 v[152:155], a[48:51], a[224:227], v[152:155]\n"
"ds_read_b64_tr_b16 a[44:45], v3 offset:8704\n"
"ds_read_b64_tr_b16 a[46:47], v3 offset:12800\n"
"v_mfma_f32_16x16x32_bf16 v[152:155], a[52:55], a[240:243], v[152:155]\n"
"v_mfma_f32_16x16x32_bf16 v[184:187], a[48:51], a[228:231], v[184:187]\n"
"v_mfma_f32_16x16x32_bf16 v[184:187], a[52:55], a[244:247], v[184:187]\n"
"s_add_u32 m0, s53, 16384\n"
"s_add_u32 s52, s52, 3072\n"
"buffer_load_dwordx4 v1, s[36:39], s52 offen offset:3072 lds\n"
"s_xor_b32 s53, s53, 0x8000\n"
"v_mfma_f32_16x16x32_bf16 v[216:219], a[48:51], a[232:235], v[216:219]\n"
"ds_read_b64_tr_b16 a[48:49], v2 offset:768\n"
"ds_read_b64_tr_b16 a[50:51], v2 offset:4864\n"
"v_mfma_f32_16x16x32_bf16 v[216:219], a[52:55], a[248:251], v[216:219]\n"
"ds_read_b64_tr_b16 a[52:53], v2 offset:8960\n"
"ds_read_b64_tr_b16 a[54:55], v2 offset:13056\n"
"s_waitcnt lgkmcnt(15)\n"
"v_mfma_f32_16x16x32_bf16 v[156:159], a[56:59], a[224:227], v[156:159]\n"
"v_mfma_f32_16x16x32_bf16 v[156:159], a[60:63], a[240:243], v[156:159]\n"
"v_mfma_f32_16x16x32_bf16 v[188:191], a[56:59], a[228:231], v[188:191]\n"
"v_mfma_f32_16x16x32_bf16 v[188:191], a[60:63], a[244:247], v[188:191]\n"
"v_mfma_f32_16x16x32_bf16 v[220:223], a[56:59], a[232:235], v[220:223]\n"
"ds_read_b64_tr_b16 a[56:57], v3 offset:768\n"
"ds_read_b64_tr_b16 a[58:59], v3 offset:4864\n"
"v_mfma_f32_16x16x32_bf16 v[220:223], a[60:63], a[248:251], v[220:223]\n"
"s_sub_u32 s58, s58, 1\n"
"s_cmp_lg_u32 s58, 0\n"
"s_cbranch_scc1 .LLd_%=\n"
"v_cvt_pk_bf16_f32 v136, v136, v137\n"
"v_cvt_pk_bf16_f32 v137, v138, v139\n"
"v_cvt_pk_bf16_f32 v138, v140, v141\n"
"v_cvt_pk_bf16_f32 v139, v142, v143\n"
"buffer_store_dwordx4 v[136:139], v6, s[44:47], s62 offen offset:0 sc1\n"
"v_cvt_pk_bf16_f32 v144, v144, v145\n"
"v_cvt_pk_bf16_f32 v145, v146, v147\n"
"v_cvt_pk_bf16_f32 v146, v148, v149\n"
"v_cvt_pk_bf16_f32 v147, v150, v151\n"
"buffer_store_dwordx4 v[144:147], v6, s[44:47], s63 offen offset:0 sc1\n"
"v_cvt_pk_bf16_f32 v152, v152, v153\n"
"v_cvt_pk_bf16_f32 v153, v154, v155\n"
"v_cvt_pk_bf16_f32 v154, v156, v157\n"
"v_cvt_pk_bf16_f32 v155, v158, v159\n"
"buffer_store_dwordx4 v[152:155], v6, s[44:47], s64 offen offset:0 sc1\n"
"v_cvt_pk_bf16_f32 v168, v168, v169\n"
"v_cvt_pk_bf16_f32 v169, v170, v171\n"
"v_cvt_pk_bf16_f32 v170, v172, v173\n"
"v_cvt_pk_bf16_f32 v171, v174, v175\n"
"buffer_store_dwordx4 v[168:171], v6, s[44:47], s62 offen offset:1024 sc1\n"
"v_cvt_pk_bf16_f32 v176, v176, v177\n"
"v_cvt_pk_bf16_f32 v177, v178, v179\n"
"v_cvt_pk_bf16_f32 v178, v180, v181\n"
"v_cvt_pk_bf16_f32 v179, v182, v183\n"
"buffer_store_dwordx4 v[176:179], v6, s[44:47], s63 offen offset:1024 sc1\n"
"v_cvt_pk_bf16_f32 v184, v184, v185\n"
"v_cvt_pk_bf16_f32 v185, v186, v187\n"
"v_cvt_pk_bf16_f32 v186, v188, v189\n"
"v_cvt_pk_bf16_f32 v187, v190, v191\n"
"buffer_store_dwordx4 v[184:187], v6, s[44:47], s64 offen offset:1024 sc1\n"
"v_cvt_pk_bf16_f32 v200, v200, v201\n"
"v_cvt_pk_bf16_f32 v201, v202, v203\n"
"v_cvt_pk_bf16_f32 v202, v204, v205\n"
"v_cvt_pk_bf16_f32 v203, v206, v207\n"
"buffer_store_dwordx4 v[200:203], v6, s[44:47], s62 offen offset:2048 sc1\n"
"v_cvt_pk_bf16_f32 v208, v208, v209\n"
"v_cvt_pk_bf16_f32 v209, v210, v211\n"
"v_cvt_pk_bf16_f32 v210, v212, v213\n"
"v_cvt_pk_bf16_f32 v211, v214, v215\n"
"buffer_store_dwordx4 v[208:211], v6, s[44:47], s63 offen offset:2048 sc1\n"
"v_cvt_pk_bf16_f32 v216, v216, v217\n"
"v_cvt_pk_bf16_f32 v217, v218, v219\n"
"v_cvt_pk_bf16_f32 v218, v220, v221\n"
"v_cvt_pk_bf16_f32 v219, v222, v223\n"
"buffer_store_dwordx4 v[216:219], v6, s[44:47], s64 offen offset:2048 sc1\n"
"s_waitcnt vmcnt(0)\n"
"s_mov_b64 exec, 15\n"
"global_store_dwordx2 v8, v[14:15], %[xw] sc1\n"
"s_mov_b64 exec, -1\n"
"s_mov_b32 s60, 0x200000\n"
".Lpxd_%=:\n"
"global_load_dwordx2 v[16:17], v7, %[xa] offset:0 sc1\n"
"s_waitcnt vmcnt(0)\n"
"v_cmp_ne_u32 vcc, v16, v15\n"
"s_nop 1\n"
"s_cmp_eq_u64 vcc, 0\n"
"s_cbranch_scc1 .Lexd_%=\n"
"v_mov_b32 v15, v16\n"
"s_mov_b64 exec, 15\n"
"global_store_dwordx2 v8, v[14:15], %[xw] offset:0 sc1\n"
"s_mov_b64 exec, -1\n"
".Lexd_%=:\n"
"v_cmp_ne_u32 vcc, v17, v14\n"
"s_nop 1\n"
"s_cmp_eq_u64 vcc, 0\n"
"s_cbranch_scc1 .Lqxd_%=\n"
"s_sleep 1\n"
"s_sub_u32 s60, s60, 1\n"
"s_cmp_lg_u32 s60, 0\n"
"s_cbranch_scc1 .Lpxd_%=\n"
".Lqxd_%=:\n"
"s_waitcnt vmcnt(0)\n"
"buffer_load_dwordx4 v[136:139], v6, s[44:47], s65 offen offset:0 sc1\n"
"buffer_load_dwordx4 v[144:147], v6, s[44:47], s66 offen offset:0 sc1\n"
"buffer_load_dwordx4 v[152:155], v6, s[44:47], s67 offen offset:0 sc1\n"
"buffer_load_dwordx4 v[168:171], v6, s[44:47], s65 offen offset:1024 sc1\n"
"buffer_load_dwordx4 v[176:179], v6, s[44:47], s66 offen offset:1024 sc1\n"
"buffer_load_dwordx4 v[184:187], v6, s[44:47], s67 offen offset:1024 sc1\n"
"buffer_load_dwordx4 v[200:203], v6, s[44:47], s65 offen offset:2048 sc1\n"
"buffer_load_dwordx4 v[208:211], v6, s[44:47], s66 offen offset:2048 sc1\n"
"buffer_load_dwordx4 v[216:219], v6, s[44:47], s67 offen offset:2048 sc1\n"
"s_waitcnt vmcnt(8)\n"
"v_lshlrev_b32 v140, 16, v136\n"
"v_and_b32 v141, 0xffff0000, v136\n"
"v_add_f32 v128, v128, v140\n"
"v_add_f32 v129, v129, v141\n"
"v_lshlrev_b32 v142, 16, v137\n"
"v_and_b32 v143, 0xffff0000, v137\n"
"v_add_f32 v130, v130, v142\n"
"v_add_f32 v131, v131, v143\n"
"v_lshlrev_b32 v140, 16, v138\n"
"v_and_b32 v141, 0xffff0000, v138\n"
"v_add_f32 v132, v132, v140\n"
"v_add_f32 v133, v133, v141\n"
"v_lshlrev_b32 v142, 16, v139\n"
"v_and_b32 v143, 0xffff0000, v139\n"
"v_add_f32 v134, v134, v142\n"
"v_add_f32 v135, v135, v143\n"
"s_waitcnt vmcnt(7)\n"
"v_lshlrev_b32 v148, 16, v144\n"
"v_and_b32 v149, 0xffff0000, v144\n"
"v_add_f32 v128, v128, v148\n"
"v_add_f32 v129, v129, v149\n"
"v_lshlrev_b32 v150, 16, v145\n"
"v_and_b32 v151, 0xffff0000, v145\n"
"v_add_f32 v130, v130, v150\n"
"v_add_f32 v131, v131, v151\n"
"v_lshlrev_b32 v148, 16, v146\n"
"v_and_b32 v149, 0xffff0000, v146\n"
"v_add_f32 v132, v132, v148\n"
"v_add_f32 v133, v133, v149\n"
"v_lshlrev_b32 v150, 16, v147\n"
"v_and_b32 v151, 0xffff0000, v147\n"
"v_add_f32 v134, v134, v150\n"
"v_add_f32 v135, v135, v151\n"
"s_waitcnt vmcnt(6)\n"
"v_lshlrev_b32 v156, 16, v152\n"
"v_and_b32 v157, 0xffff0000, v152\n"
"v_add_f32 v128, v128, v156\n"
"v_add_f32 v129, v129, v157\n"
"v_lshlrev_b32 v158, 16, v153\n"
"v_and_b32 v159, 0xffff0000, v153\n"
"v_add_f32 v130, v130, v158\n"
"v_add_f32 v131, v131, v159\n"
"v_lshlrev_b32 v156, 16, v154\n"
"v_and_b32 v157, 0xffff0000, v154\n"
"v_add_f32 v132, v132, v156\n"
"v_add_f32 v133, v133, v157\n"
"v_lshlrev_b32 v158, 16, v155\n"
"v_and_b32 v159, 0xffff0000, v155\n"
"v_add_f32 v134, v134, v158\n"
"v_add_f32 v135, v135, v159\n"
"v_cvt_pk_bf16_f32 v128, v128, v129\n"
"v_cvt_pk_bf16_f32 v129, v130, v131\n"
"v_cvt_pk_bf16_f32 v130, v132, v133\n"
"v_cvt_pk_bf16_f32 v131, v134, v135\n"
"s_mov_b32 s59, 0\n"
"buffer_store_dwordx2 v[128:129], v5, s[48:51], s59 offen offset:0\n"
"buffer_store_dwordx2 v[130:131], v5, s[48:51], s59 offen offset:32\n"
"s_waitcnt vmcnt(7)\n"
"v_lshlrev_b32 v172, 16, v168\n"
"v_and_b32 v173, 0xffff0000, v168\n"
"v_add_f32 v160, v160, v172\n"
"v_add_f32 v161, v161, v173\n"
"v_lshlrev_b32 v174, 16, v169\n"
"v_and_b32 v175, 0xffff0000, v169\n"
"v_add_f32 v162, v162, v174\n"
"v_add_f32 v163, v163, v175\n"
"v_lshlrev_b32 v172, 16, v170\n"
"v_and_b32 v173, 0xffff0000, v170\n"
"v_add_f32 v164, v164, v172\n"
"v_add_f32 v165, v165, v173\n"
"v_lshlrev_b32 v174, 16, v171\n"
"v_and_b32 v175, 0xffff0000, v171\n"
"v_add_f32 v166, v166, v174\n"
"v_add_f32 v167, v167, v175\n"
"s_waitcnt vmcnt(6)\n"
"v_lshlrev_b32 v180, 16, v176\n"
"v_and_b32 v181, 0xffff0000, v176\n"
"v_add_f32 v160, v160, v180\n"
"v_add_f32 v161, v161, v181\n"
"v_lshlrev_b32 v182, 16, v177\n"
"v_and_b32 v183, 0xffff0000, v177\n"
"v_add_f32 v162, v162, v182\n"
"v_add_f32 v163, v163, v183\n"
"v_lshlrev_b32 v180, 16, v178\n"
"v_and_b32 v181, 0xffff0000, v178\n"
"v_add_f32 v164, v164, v180\n"
"v_add_f32 v165, v165, v181\n"
"v_lshlrev_b32 v182, 16, v179\n"
"v_and_b32 v183, 0xffff0000, v179\n"
"v_add_f32 v166, v166, v182\n"
"v_add_f32 v167, v167, v183\n"
"s_waitcnt vmcnt(5)\n"
"v_lshlrev_b32 v188, 16, v184\n"
"v_and_b32 v189, 0xffff0000, v184\n"
"v_add_f32 v160, v160, v188\n"
"v_add_f32 v161, v161, v189\n"
"v_lshlrev_b32 v190, 16, v185\n"
"v_and_b32 v191, 0xffff0000, v185\n"
"v_add_f32 v162, v162, v190\n"
"v_add_f32 v163, v163, v191\n"
"v_lshlrev_b32 v188, 16, v186\n"
"v_and_b32 v189, 0xffff0000, v186\n"
"v_add_f32 v164, v164, v188\n"
"v_add_f32 v165, v165, v189\n"
"v_lshlrev_b32 v190, 16, v187\n"
"v_and_b32 v191, 0xffff0000, v187\n"
"v_add_f32 v166, v166, v190\n"
"v_add_f32 v167, v167, v191\n"
"v_cvt_pk_bf16_f32 v160, v160, v161\n"
"v_cvt_pk_bf16_f32 v161, v162, v163\n"
"v_cvt_pk_bf16_f32 v162, v164, v165\n"
"v_cvt_pk_bf16_f32 v163, v166, v167\n"
"s_mov_b32 s59, 32768\n"
"buffer_store_dwordx2 v[160:161], v5, s[48:51], s59 offen offset:0\n"
"buffer_store_dwordx2 v[162:163], v5, s[48:51], s59 offen offset:32\n"
"s_waitcnt vmcnt(6)\n"
"v_lshlrev_b32 v204, 16, v200\n"
"v_and_b32 v205, 0xffff0000, v200\n"
"v_add_f32 v192, v192, v204\n"
"v_add_f32 v193, v193, v205\n"
"v_lshlrev_b32 v206, 16, v201\n"
"v_and_b32 v207, 0xffff0000, v201\n"
"v_add_f32 v194, v194, v206\n"
"v_add_f32 v195, v195, v207\n"
"v_lshlrev_b32 v204, 16, v202\n"
"v_and_b32 v205, 0xffff0000, v202\n"
"v_add_f32 v196, v196, v204\n"
"v_add_f32 v197, v197, v205\n"
"v_lshlrev_b32 v206, 16, v203\n"
"v_and_b32 v207, 0xffff0000, v203\n"
"v_add_f32 v198, v198, v206\n"
"v_add_f32 v199, v199, v207\n"
"s_waitcnt vmcnt(5)\n"
"v_lshlrev_b32 v212, 16, v208\n"
"v_and_b32 v213, 0xffff0000, v208\n"
"v_add_f32 v192, v192, v212\n"
"v_add_f32 v193, v193, v213\n"
"v_lshlrev_b32 v214, 16, v209\n"
"v_and_b32 v215, 0xffff0000, v209\n"
"v_add_f32 v194, v194, v214\n"
"v_add_f32 v195, v195, v215\n"
"v_lshlrev_b32 v212, 16, v210\n"
"v_and_b32 v213, 0xffff0000, v210\n"
"v_add_f32 v196, v196, v212\n"
"v_add_f32 v197, v197, v213\n"
"v_lshlrev_b32 v214, 16, v211\n"
"v_and_b32 v215, 0xffff0000, v211\n"
"v_add_f32 v198, v198, v214\n"
"v_add_f32 v199, v199, v215\n"
"s_waitcnt vmcnt(4)\n"
"v_lshlrev_b32 v220, 16, v216\n"
"v_and_b32 v221, 0xffff0000, v216\n"
"v_add_f32 v192, v192, v220\n"
"v_add_f32 v193, v193, v221\n"
"v_lshlrev_b32 v222, 16, v217\n"
"v_and_b32 v223, 0xffff0000, v217\n"
"v_add_f32 v194, v194, v222\n"
"v_add_f32 v195, v195, v223\n"
"v_lshlrev_b32 v220, 16, v218\n"
"v_and_b32 v221, 0xffff0000, v218\n"
"v_add_f32 v196, v196, v220\n"
"v_add_f32 v197, v197, v221\n"
"v_lshlrev_b32 v222, 16, v219\n"
"v_and_b32 v223, 0xffff0000, v219\n"
"v_add_f32 v198, v198, v222\n"
"v_add_f32 v199, v199, v223\n"
"v_cvt_pk_bf16_f32 v192, v192, v193\n"
"v_cvt_pk_bf16_f32 v193, v194, v195\n"
"v_cvt_pk_bf16_f32 v194, v196, v197\n"
"v_cvt_pk_bf16_f32 v195, v198, v199\n"
"s_mov_b32 s59, 65536\n"
"buffer_store_dwordx2 v[192:193], v5, s[48:51], s59 offen offset:0\n"
"buffer_store_dwordx2 v[194:195], v5, s[48:51], s59 offen offset:32\n"
"s_waitcnt vmcnt(0)\n"
"s_branch .Lzd_%=\n"
".Ls1d_%=:\n"
"s_mov_b32 s60, 0x200000\n"
".Lpcd_%=:\n"
"global_load_dwordx2 v[16:17], v7, %[ma] offset:128 sc1\n"
"s_waitcnt vmcnt(0)\n"
"v_cmp_ne_u32 vcc, v16, v13\n"
"s_nop 1\n"
"s_cmp_eq_u64 vcc, 0\n"
"s_cbranch_scc1 .Lecd_%=\n"
"v_mov_b32 v13, v16\n"
"s_mov_b64 exec, 15\n"
"global_store_dwordx2 v8, v[12:13], %[mw] offset:128 sc1\n"
"s_mov_b64 exec, -1\n"
".Lecd_%=:\n"
"v_cmp_ne_u32 vcc, v17, v12\n"
"s_nop 1\n"
"s_cmp_eq_u64 vcc, 0\n"
"s_cbranch_scc1 .Lqcd_%=\n"
"s_sleep 1\n"
"s_sub_u32 s60, s60, 1\n"
"s_cmp_lg_u32 s60, 0\n"
"s_cbranch_scc1 .Lpcd_%=\n"
".Lqcd_%=:\n"
"s_waitcnt vmcnt(0)\n"
"s_branch .Lr1d_%=\n"
".Lzd_%=:\n"
".Lend_%=:\n"
      :
      : [wl] "s"((unsigned)pw), [wh] "s"((unsigned)(pw >> 32)), [ol] "s"((unsigned)po), [oh] "s"((unsigned)(po >> 32)),
        [xl] "s"((unsigned)px), [xh] "s"((unsigned)(px >> 32)), [yl] "s"((unsigned)py), [yh] "s"((unsigned)(py >> 32)),
        [osz] "s"(15u * 131072u - ks * 32768u), [ksp] "s"(ks), [grpp] "s"(grp), [wv] "s"(wv), [nok] "s"(1u),
        [ma] "s"(w3 + (32u << 20) + oA + nt * 8), [mw] "s"(w3 + (32u << 20) + oA + nt * 32), [xa] "s"(w3 + (32u << 20) + oX + ks * 8), [xw] "s"(w3 + (32u << 20) + oX + ks * 32)
      : "v0","v1","v2","v3","v4","v5","v6","v7","v8","v9","v10","v11","v12","v13","v14","v15","v16","v17","v18","v19","v20","v21","v22","v23","v24","v25","v26","v27","v28","v29","v30","v31","v32","v33","v34","v35","v36","v37","v38","v39","v40","v41","v42","v43","v44","v45","v46","v47","v48","v49","v50","v51","v52","v53","v54","v55","v56","v57","v58","v59","v60","v61","v62","v63","v64","v65","v66","v67","v68","v69","v70","v71","v72","v73","v74","v75","v76","v77","v78","v79","v80","v81","v82","v83","v84","v85","v86","v87","v88","v89","v90","v91","v92","v93","v94","v95","v96","v97","v98","v99","v100","v101","v102","v103","v104","v105","v106","v107","v108","v109","v110","v111","v112","v113","v114","v115","v116","v117","v118","v119","v120","v121","v122","v123","v124","v125","v126","v127","v128","v129","v130","v131","v132","v133","v134","v135","v136","v137","v138","v139","v140","v141","v142","v143","v144","v145","v146","v147","v148","v149","v150","v151","v152","v153","v154","v155","v156","v157","v158","v159","v160","v161","v162","v163","v164","v165","v166","v167","v168","v169","v170","v171","v172","v173","v174","v175","v176","v177","v178","v179","v180","v181","v182","v183","v184","v185","v186","v187","v188","v189","v190","v191","v192","v193","v194","v195","v196","v197","v198","v199","v200","v201","v202","v203","v204","v205","v206","v207","v208","v209","v210","v211","v212","v213","v214","v215","v216","v217","v218","v219","v220","v221","v222","v223","v224","v225","v226","v227","v228","v229","v230","v231","v232","v233","v234","v235","v236","v237","v238","v239","v240","v241","v242","v243","v244","v245","v246","v247","v248","v249","v250","v251","v252","v253","v254","v255","a0","a1","a2","a3","a4","a5","a6","a7","a8","a9","a10","a11","a12","a13","a14","a15","a16","a17","a18","a19","a20","a21","a22","a23","a24","a25","a26","a27","a28","a29","a30","a31","a32","a33","a34","a35","a36","a37","a38","a39","a40","a41","a42","a43","a44","a45","a46","a47","a48","a49","a50","a51","a52","a53","a54","a55","a56","a57","a58","a59","a60","a61","a62","a63","a64","a65","a66","a67","a68","a69","a70","a71","a72","a73","a74","a75","a76","a77","a78","a79","a80","a81","a82","a83","a84","a85","a86","a87","a88","a89","a90","a91","a92","a93","a94","a95","a96","a97","a98","a99","a100","a101","a102","a103","a104","a105","a106","a107","a108","a109","a110","a111","a112","a113","a114","a115","a116","a117","a118","a119","a120","a121","a122","a123","a124","a125","a126","a127","a128","a129","a130","a131","a132","a133","a134","a135","a136","a137","a138","a139","a140","a141","a142","a143","a144","a145","a146","a147","a148","a149","a150","a151","a152","a153","a154","a155","a156","a157","a158","a159","a160","a161","a162","a163","a164","a165","a166","a167","a168","a169","a170","a171","a172","a173","a174","a175","a176","a177","a178","a179","a180","a181","a182","a183","a184","a185","a186","a187","a188","a189","a190","a191","a192","a193","a194","a195","a196","a197","a198","a199","a200","a201","a202","a203","a204","a205","a206","a207","a208","a209","a210","a211","a212","a213","a214","a215","a216","a217","a218","a219","a220","a221","a222","a223","a224","a225","a226","a227","a228","a229","a230","a231","a232","a233","a234","a235","a236","a237","a238","a239","a240","a241","a242","a243","a244","a245","a246","a247","a248","a249","a250","a251","a252","a253","a254","a255","s36","s37","s38","s39","s40","s41","s42","s43","s44","s45","s46","s47","s48","s49","s50","s51","s52","s53","s54","s55","s56","s57","s58","s59","s60","s61","s62","s63","s64","s65","s66","s67","s68","s69","s70","s71","s72","s73","s74","s75","s76","s77","s78","s79","vcc","scc","m0","memory");
  }
}
#undef xcd
#undef idx
#undef blk
#undef ks
#undef nt
#undef b
#undef pair
#undef grp
#undef oA
#undef oX
extern "C" void kernel_launch(void* const* d_in, const int* in_sizes, int n_in, void* d_out, int out_size, void* d_ws, size_t ws_size, hipStream_t stream) {
  hipFuncSetAttribute((const void*)kf, hipFuncAttributeMaxDynamicSharedMemorySize, 163840);
  hipLaunchKernelGGL(kf, dim3(256), dim3(256), 163840, stream, (const char*)d_in[0], (const char*)d_in[1], (const char*)d_in[2], (const char*)d_in[3], (const char*)d_in[4],
                     (const char*)d_in[5], (const char*)d_in[6], (const char*)d_in[7], (const char*)d_in[8], (const char*)d_in[9], (char*)d_ws, (char*)d_out);
}
```
